```python
import torch

def _softmax(x, axis=-1):
    return torch.softmax(x, dim=axis)
def _gelu(x):
    return torch.nn.functional.gelu(x, approximate="tanh")
def _silu(x):
    return torch.nn.functional.silu(x)
def _sigmoid(x):
    return torch.sigmoid(x)
def _layernorm(x, axis=-1, eps=1e-5):
    m = x.mean(dim=axis, keepdim=True)
    v = x.var(dim=axis, keepdim=True, unbiased=False)
    return (x - m) / torch.sqrt(v + eps)
def _scatter_add(tgt, idx, src, axis=0):
    return tgt.clone().index_add_(axis, idx, src)
def _segment_sum(src, seg, n):
    out = torch.zeros((n,) + src.shape[1:], dtype=src.dtype)
    return out.index_add_(0, seg, src)
def _pad(x, pw):
    flat = tuple(v for pair in reversed(pw) for v in pair)
    return torch.nn.functional.pad(x, flat)

kernel_name = 'standalone__gqa_block_cached__near_ridge'

def setup_inputs():

    _w = (lambda a: (lambda t: t.to(torch.bfloat16) if t.is_floating_point() else t)(torch.as_tensor(a)))
    B, Sq, D = 16, 64, 576
    Hq, Hkv, Sk, Dh = 32, 16, 1024, 128
    assert Hq % Hkv == 0, (Hq, Hkv)
    return {
        "x": _w((lambda *s: torch.randn(*s, dtype=torch.bfloat16) * 0.8)(B, Sq, D)),
        "k_cache": _w((lambda *s: torch.randn(*s, dtype=torch.bfloat16) * 0.8)(B, Hkv, Sk, Dh)),
        "v_cache": _w((lambda *s: torch.randn(*s, dtype=torch.bfloat16) * 0.05)(B, Hkv, Sk, Dh)),
        "wq": _w((lambda *s: torch.randn(*s, dtype=torch.bfloat16) * 0.8)(D, Hq * Dh) * (D ** -0.5)),
        "wo": _w((lambda *s: torch.randn(*s, dtype=torch.bfloat16) * 0.05)(Hq * Dh, D) * ((Hq * Dh) ** -0.5)),
    }

def reference(x, k_cache, v_cache, wq, wo):
    B, Sq, D = x.shape
    Hq, Dh = 32, 128
    Hkv = k_cache.shape[1]
    rep = Hq // Hkv
    q = (lambda x, *d: x.permute(*d))((x @ wq).reshape(B, Sq, Hq, Dh), 0, 2, 1, 3)
    k = (lambda x, n, axis: torch.repeat_interleave(x, n, dim=axis))(k_cache, rep, axis=1)
    v = (lambda x, n, axis: torch.repeat_interleave(x, n, dim=axis))(v_cache, rep, axis=1)
    scores = torch.einsum("bhqd,bhkd->bhqk", q, k) * (Dh ** -0.5)
    p = _softmax(scores, axis=-1)
    o = torch.einsum("bhqk,bhkd->bhqd", p, v)
    return (lambda x, *d: x.permute(*d))(o, 0, 2, 1, 3).reshape(B, Sq, Hq * Dh) @ wo
```

```cpp
#include <hip/hip_runtime.h>
#include <hip/hip_bf16.h>
#include <stdint.h>

typedef short bf16x8 __attribute__((ext_vector_type(8)));
typedef short s16x4 __attribute__((ext_vector_type(4)));
typedef float f32x16 __attribute__((ext_vector_type(16)));
typedef float f32x4 __attribute__((ext_vector_type(4)));
typedef unsigned u32x4 __attribute__((ext_vector_type(4)));
typedef unsigned u32x2 __attribute__((ext_vector_type(2)));
typedef unsigned short u16;
#define LAS __attribute__((address_space(3)))
typedef LAS const char* lds_cptr;

constexpr int NB = 16, SQ = 64, DM = 576, HQ = 32, HKV = 16, SKV = 1024, DH = 128;
constexpr int NQ = HQ * DH;
constexpr int NT = SKV / 64;
constexpr int SLOT = 32768;
constexpr int LDS_Q = 4 * SLOT;
constexpr int LDS1_BYTES = 5 * SLOT;
constexpr float C2 = 0.088388347648318447f * 1.4426950408889634f;

#define MFMA32(a, b, c) __builtin_amdgcn_mfma_f32_32x32x16_bf16(a, b, c, 0, 0, 0)

typedef __bf16 bf16x2_t __attribute__((ext_vector_type(2)));
typedef float f32x2 __attribute__((ext_vector_type(2)));
__device__ __forceinline__ unsigned cvtpk(float lo, float hi) {
    f32x2 v = {lo, hi};
    return __builtin_bit_cast(unsigned, __builtin_convertvector(v, bf16x2_t));
}
__device__ __forceinline__ void glds16(const void* g, unsigned lds_base) {
    unsigned sv;
    asm volatile("s_mov_b32 %0, m0\n\ts_mov_b32 m0, %2\n\ts_nop 0\n\tglobal_load_lds_dwordx4 %1, off\n\ts_mov_b32 m0, %0"
                 : "=&s"(sv) : "v"(g), "s"(lds_base) : "memory");
}
__device__ __forceinline__ s16x4 vtr(unsigned a) {
    return __builtin_amdgcn_ds_read_tr16_b64_v4i16((LAS s16x4*)(uintptr_t)a);
}
__device__ __forceinline__ bf16x8 ldsr128(unsigned a) { return *(const LAS bf16x8*)(uintptr_t)a; }
__device__ __forceinline__ float swap_max(float v) {
    auto rr = __builtin_amdgcn_permlane32_swap(__float_as_uint(v), __float_as_uint(v), false, false);
    return __builtin_fmaxf(__uint_as_float(rr[0]), __uint_as_float(rr[1]));
}
__device__ __forceinline__ float swap_add(float v) {
    auto rr = __builtin_amdgcn_permlane32_swap(__float_as_uint(v), __float_as_uint(v), false, false);
    return __uint_as_float(rr[0]) + __uint_as_float(rr[1]);
}

__global__ __launch_bounds__(512, 2) void attn_kernel(const u16* __restrict__ x, const u16* __restrict__ Kc,
                                                      const u16* __restrict__ Vc, const u16* __restrict__ wq,
                                                      u16* __restrict__ Og) {
    extern __shared__ __attribute__((aligned(16))) char lds[];
    const unsigned lds0 = (unsigned)(uintptr_t)lds;
    const int tid = threadIdx.x, lane = tid & 63, r32 = lane & 31, hi = lane >> 5;
    const int wid = __builtin_amdgcn_readfirstlane(tid >> 6);
    const int qb = wid & 3, kh = wid >> 2;
    const int bid = blockIdx.x, kvh = bid & 15, b = bid >> 4;
    const int head = 2 * kvh + (qb >> 1);

    const int lrow = lane >> 4;
    const unsigned lane_off = lrow * 256 + ((((lane & 15) ^ ((lrow << 2) | (wid & 3)))) << 4);
    const char* Kg = (const char*)Kc + (size_t)(b * HKV + kvh) * SKV * DH * 2 + lane_off;
    const char* Vg = (const char*)Vc + (size_t)(b * HKV + kvh) * SKV * DH * 2 + lane_off;

    bf16x8 qf[8];
    if (wid >= 4) {
        for (int t = 0; t < 3; ++t) {
#pragma unroll
            for (int i = 0; i < 4; ++i) {
                const int p = (wid & 3) + 4 * i;
                glds16(Kg + t * 16384 + p * 1024, lds0 + t * SLOT + p * 1024);
                glds16(Vg + t * 16384 + p * 1024, lds0 + t * SLOT + 16384 + p * 1024);
            }
        }
    } else {
        constexpr int PD = 6;
        const u16* xrow = x + (size_t)(b * SQ + 32 * (qb & 1) + r32) * DM + 32 * hi;
        const u16* wbase = wq + (size_t)(32 * hi) * NQ + head * DH + 4 * r32;
        f32x16 acc[4] = {};
        u32x2 wb[PD][8];
        bf16x8 xf[3][4];
#pragma unroll
        for (int s = 0; s < 4; ++s) xf[0][s] = *(const bf16x8*)(xrow + 8 * s);
#pragma unroll
        for (int s = 0; s < 4; ++s) xf[1][s] = *(const bf16x8*)(xrow + 64 + 8 * s);
#pragma unroll
        for (int u = 0; u < PD; ++u) {
#pragma unroll
            for (int j = 0; j < 8; ++j) wb[u][j] = *(const u32x2*)(wbase + (size_t)(64 * (u >> 2) + 8 * (u & 3) + j) * NQ);
        }
#pragma unroll
        for (int u = 0; u < 36; ++u) {
            const int kb = u >> 2, s = u & 3;
            if (s == 0 && kb + 2 < 9) {
#pragma unroll
                for (int s2 = 0; s2 < 4; ++s2) xf[(kb + 2) % 3][s2] = *(const bf16x8*)(xrow + 64 * (kb + 2) + 8 * s2);
            }
            u32x4 f0, f1, f2, f3;
#pragma unroll
            for (int m = 0; m < 4; ++m) {
                f0[m] = __builtin_amdgcn_perm(wb[u % PD][2 * m + 1].x, wb[u % PD][2 * m].x, 0x05040100u);
                f1[m] = __builtin_amdgcn_perm(wb[u % PD][2 * m + 1].x, wb[u % PD][2 * m].x, 0x07060302u);
                f2[m] = __builtin_amdgcn_perm(wb[u % PD][2 * m + 1].y, wb[u % PD][2 * m].y, 0x05040100u);
                f3[m] = __builtin_amdgcn_perm(wb[u % PD][2 * m + 1].y, wb[u % PD][2 * m].y, 0x07060302u);
            }
            if (u + PD < 36) {
                const int u2 = u + PD;
#pragma unroll
                for (int j = 0; j < 8; ++j) wb[u % PD][j] = *(const u32x2*)(wbase + (size_t)(64 * (u2 >> 2) + 8 * (u2 & 3) + j) * NQ);
            }
            const bf16x8 xb = xf[kb % 3][s];
            acc[0] = MFMA32(__builtin_bit_cast(bf16x8, f0), xb, acc[0]);
            acc[1] = MFMA32(__builtin_bit_cast(bf16x8, f1), xb, acc[1]);
            acc[2] = MFMA32(__builtin_bit_cast(bf16x8, f2), xb, acc[2]);
            acc[3] = MFMA32(__builtin_bit_cast(bf16x8, f3), xb, acc[3]);
        }
#pragma unroll
        for (int g = 0; g < 4; ++g) {
#pragma unroll
            for (int u = 0; u < 2; ++u) {
                const int r0 = 4 * g + 2 * u;
                u32x4 w = {cvtpk(acc[0][r0], acc[1][r0]), cvtpk(acc[2][r0], acc[3][r0]),
                           cvtpk(acc[0][r0 + 1], acc[1][r0 + 1]), cvtpk(acc[2][r0 + 1], acc[3][r0 + 1])};
                qf[2 * g + u] = __builtin_bit_cast(bf16x8, w);
                *(LAS u32x4*)(uintptr_t)(lds0 + LDS_Q + qb * 8192 + (2 * g + u) * 1024 + lane * 16) = w;
            }
        }
    }

    const unsigned kbase = (32 * kh + r32) * 256 + ((((r32 & 3) << 2) | ((2 * hi) ^ ((r32 >> 2) & 3))) << 4);
    const int gi = lane >> 4, vq = (lane & 15) >> 2, vp = lane & 3;
    const unsigned vbase = 16384 + (32 * kh + 4 * hi + vq) * 256 + (((vq << 2) | ((2 * (gi & 1) + (vp >> 1)) ^ hi)) << 4) + 8 * (vp & 1);

    float m_run = -__builtin_inff(), l_run = 0.f;
    f32x16 o[4] = {};

    for (int t = 0; t < NT; ++t) {
        asm volatile("s_waitcnt vmcnt(8) lgkmcnt(0)\n\ts_barrier" ::: "memory");
        if (t == 0 && wid >= 4) {
#pragma unroll
            for (int f = 0; f < 8; ++f) qf[f] = ldsr128(lds0 + LDS_Q + qb * 8192 + f * 1024 + lane * 16);
        }
        if (t + 3 < NT) {
            const int tn = t + 3;
            const unsigned sl = lds0 + (tn & 3) * SLOT;
            glds16(Kg + tn * 16384 + wid * 1024, sl + wid * 1024);
            glds16(Kg + tn * 16384 + (wid + 8) * 1024, sl + (wid + 8) * 1024);
            glds16(Vg + tn * 16384 + wid * 1024, sl + 16384 + wid * 1024);
            glds16(Vg + tn * 16384 + (wid + 8) * 1024, sl + 16384 + (wid + 8) * 1024);
        } else {
            if (t + 2 < NT) asm volatile("s_waitcnt vmcnt(4)" ::: "memory");
            else asm volatile("s_waitcnt vmcnt(0)" ::: "memory");
        }
        const unsigned sl = lds0 + (t & 3) * SLOT;
        f32x16 s = {};
#pragma unroll
        for (int d0 = 0; d0 < 8; ++d0) {
            const bf16x8 kf = ldsr128(sl + (kbase ^ (((d0 >> 1) << 6) | ((d0 & 1) << 4))));
            s = MFMA32(kf, qf[d0], s);
        }
        float tmax = s[0];
#pragma unroll
        for (int r = 1; r < 16; ++r) tmax = __builtin_fmaxf(tmax, s[r]);
        tmax = swap_max(tmax);
        const float mnew = __builtin_fmaxf(m_run, tmax * C2);
        const float alpha = __builtin_amdgcn_exp2f(m_run - mnew);
        m_run = mnew;
        float ps = 0.f;
#pragma unroll
        for (int r = 0; r < 16; ++r) { s[r] = __builtin_amdgcn_exp2f(__builtin_fmaf(s[r], C2, -mnew)); ps += s[r]; }
        l_run = l_run * alpha + ps;
#pragma unroll
        for (int dt = 0; dt < 4; ++dt)
#pragma unroll
            for (int r = 0; r < 16; ++r) o[dt][r] *= alpha;
        bf16x8 pf[2];
#pragma unroll
        for (int ss = 0; ss < 2; ++ss) {
            u32x4 w = {cvtpk(s[8 * ss + 0], s[8 * ss + 1]), cvtpk(s[8 * ss + 2], s[8 * ss + 3]),
                       cvtpk(s[8 * ss + 4], s[8 * ss + 5]), cvtpk(s[8 * ss + 6], s[8 * ss + 7])};
            pf[ss] = __builtin_bit_cast(bf16x8, w);
        }
#pragma unroll
        for (int dt = 0; dt < 4; ++dt) {
#pragma unroll
            for (int ss = 0; ss < 2; ++ss) {
                const s16x4 lo = vtr(sl + (vbase ^ (dt << 6)) + 4096 * ss);
                const s16x4 hi4 = vtr(sl + (vbase ^ ((dt << 6) | (1 << 5))) + 4096 * ss + 2048);
                const bf16x8 vf = {lo[0], lo[1], lo[2], lo[3], hi4[0], hi4[1], hi4[2], hi4[3]};
                o[dt] = MFMA32(vf, pf[ss], o[dt]);
            }
        }
    }
    l_run = swap_add(l_run);
    {
        f32x16 keep0, keep1, send0, send1;
        if (kh == 0) { keep0 = o[0]; keep1 = o[1]; send0 = o[2]; send1 = o[3]; }
        else         { keep0 = o[2]; keep1 = o[3]; send0 = o[0]; send1 = o[1]; }
        const unsigned xb = lds0 + wid * 9216;
#pragma unroll
        for (int r4 = 0; r4 < 4; ++r4) {
            f32x4 v0 = {send0[4 * r4], send0[4 * r4 + 1], send0[4 * r4 + 2], send0[4 * r4 + 3]};
            f32x4 v1 = {send1[4 * r4], send1[4 * r4 + 1], send1[4 * r4 + 2], send1[4 * r4 + 3]};
            *(LAS f32x4*)(uintptr_t)(xb + r4 * 1024 + lane * 16) = v0;
            *(LAS f32x4*)(uintptr_t)(xb + (4 + r4) * 1024 + lane * 16) = v1;
        }
        *(LAS float*)(uintptr_t)(xb + 8192 + lane * 4) = m_run;
        *(LAS float*)(uintptr_t)(xb + 8192 + 256 + lane * 4) = l_run;
        asm volatile("s_waitcnt lgkmcnt(0)\n\ts_barrier" ::: "memory");
        const unsigned pb = lds0 + (wid ^ 4) * 9216;
        const float m2 = *(const LAS float*)(uintptr_t)(pb + 8192 + lane * 4);
        const float l2 = *(const LAS float*)(uintptr_t)(pb + 8192 + 256 + lane * 4);
        const float mm = __builtin_fmaxf(m_run, m2);
        const float a1 = __builtin_amdgcn_exp2f(m_run - mm), a2 = __builtin_amdgcn_exp2f(m2 - mm);
        const float inv = 1.0f / (l_run * a1 + l2 * a2);
        const float w1 = a1 * inv, w2 = a2 * inv;
        u16* orow = Og + (size_t)(b * SQ + 32 * (qb & 1) + r32) * NQ + head * DH + 64 * kh + 4 * hi;
#pragma unroll
        for (int r4 = 0; r4 < 4; ++r4) {
            const f32x4 v = *(const LAS f32x4*)(uintptr_t)(pb + r4 * 1024 + lane * 16);
            const float e0 = keep0[4 * r4] * w1 + v[0] * w2, e1 = keep0[4 * r4 + 1] * w1 + v[1] * w2;
            const float e2 = keep0[4 * r4 + 2] * w1 + v[2] * w2, e3 = keep0[4 * r4 + 3] * w1 + v[3] * w2;
            u32x2 w = {cvtpk(e0, e1), cvtpk(e2, e3)};
            *(u32x2*)(orow + 8 * r4) = w;
        }
#pragma unroll
        for (int r4 = 0; r4 < 4; ++r4) {
            const f32x4 v = *(const LAS f32x4*)(uintptr_t)(pb + (4 + r4) * 1024 + lane * 16);
            const float e0 = keep1[4 * r4] * w1 + v[0] * w2, e1 = keep1[4 * r4 + 1] * w1 + v[1] * w2;
            const float e2 = keep1[4 * r4 + 2] * w1 + v[2] * w2, e3 = keep1[4 * r4 + 3] * w1 + v[3] * w2;
            u32x2 w = {cvtpk(e0, e1), cvtpk(e2, e3)};
            *(u32x2*)(orow + 32 + 8 * r4) = w;
        }
    }
}

constexpr int K2_A = 16384, K2_B = 12288, K2_SLOT = K2_A + K2_B, K2_NS = 4, LDS2_BYTES = K2_SLOT * K2_NS;
constexpr int SPLITK = 4;
__global__ __launch_bounds__(256, 1) void oproj_kernel(const u16* __restrict__ Og, const u16* __restrict__ wo,
                                                       float* __restrict__ part) {
    extern __shared__ __attribute__((aligned(16))) char lds[];
    const unsigned lds0 = (unsigned)(uintptr_t)lds;
    const int tid = threadIdx.x, lane = tid & 63, r32 = lane & 31, hi = lane >> 5;
    const int wid = __builtin_amdgcn_readfirstlane(tid >> 6);
    const int bid = blockIdx.x;
    const int sk = bid & 3, nt = (bid >> 2) % 6, mt = bid / 24;
    const int arow = lane >> 3;
    const char* Ag[4];
#pragma unroll
    for (int i = 0; i < 4; ++i) {
        const int row = 8 * (4 * wid + i) + arow;
        Ag[i] = (const char*)Og + ((size_t)(128 * mt + row) * NQ + 1024 * sk) * 2 + (((lane & 7) ^ ((row >> 1) & 7)) << 4);
    }
    const char* Bg[3];
#pragma unroll
    for (int i = 0; i < 3; ++i) {
        const int L = (3 * wid + i) * 1024 + lane * 16;
        const int row = L / 192, cb = L % 192;
        Bg[i] = (const char*)wo + ((size_t)(1024 * sk + row) * DM + 96 * nt) * 2 + cb;
    }
#define K2_ISSUE(kb) do { const unsigned s_ = lds0 + ((kb) & 3) * K2_SLOT; \
        _Pragma("unroll") for (int i = 0; i < 4; ++i) glds16(Ag[i] + (kb) * 128, s_ + (4 * wid + i) * 1024); \
        _Pragma("unroll") for (int i = 0; i < 3; ++i) glds16(Bg[i] + (size_t)(kb) * 64 * DM * 2, s_ + K2_A + (3 * wid + i) * 1024); } while (0)
    K2_ISSUE(0); K2_ISSUE(1); K2_ISSUE(2);
    const int arw = 32 * wid + r32;
    const unsigned abase = arw * 128 + (((4 * hi) ^ ((arw >> 1) & 7)) << 4);
    const int gi = lane >> 4, vq = (lane & 15) >> 2, vp = lane & 3;
    const unsigned bbase = K2_A + (32 * hi + vq) * 192 + 32 * (gi & 1) + 8 * vp;
    f32x16 acc[3] = {};
    for (int kb = 0; kb < 16; ++kb) {
        if (kb < 14) asm volatile("s_waitcnt vmcnt(14) lgkmcnt(0)\n\ts_barrier" ::: "memory");
        else if (kb == 14) asm volatile("s_waitcnt vmcnt(7) lgkmcnt(0)\n\ts_barrier" ::: "memory");
        else asm volatile("s_waitcnt vmcnt(0) lgkmcnt(0)\n\ts_barrier" ::: "memory");
        if (kb + 3 < 16) K2_ISSUE(kb + 3);
        const unsigned sl = lds0 + (kb & 3) * K2_SLOT;
#pragma unroll
        for (int s = 0; s < 4; ++s) {
            const bf16x8 af = ldsr128(sl + (abase ^ (s << 4)));
#pragma unroll
            for (int ct = 0; ct < 3; ++ct) {
                const s16x4 lo = vtr(sl + bbase + (8 * s) * 192 + 64 * ct);
                const s16x4 h4 = vtr(sl + bbase + (8 * s + 4) * 192 + 64 * ct);
                const bf16x8 bf = {lo[0], lo[1], lo[2], lo[3], h4[0], h4[1], h4[2], h4[3]};
                acc[ct] = MFMA32(af, bf, acc[ct]);
            }
        }
    }
#undef K2_ISSUE
    float* pp = part + (size_t)sk * (NB * SQ * DM) + (size_t)(128 * mt + 32 * wid) * DM + 96 * nt + r32;
#pragma unroll
    for (int ct = 0; ct < 3; ++ct)
#pragma unroll
        for (int r = 0; r < 16; ++r) pp[(size_t)((r & 3) + 8 * (r >> 2) + 4 * hi) * DM + 32 * ct] = acc[ct][r];
}

__global__ __launch_bounds__(256) void reduce_kernel(const float* __restrict__ part, u16* __restrict__ out) {
    const int i = (blockIdx.x * 256 + threadIdx.x) * 8;
    constexpr int N = NB * SQ * DM;
    f32x4 a = *(const f32x4*)(part + i), c = *(const f32x4*)(part + i + 4);
#pragma unroll
    for (int s = 1; s < SPLITK; ++s) {
        a += *(const f32x4*)(part + (size_t)s * N + i);
        c += *(const f32x4*)(part + (size_t)s * N + i + 4);
    }
    u32x4 w = {cvtpk(a[0], a[1]), cvtpk(a[2], a[3]), cvtpk(c[0], c[1]), cvtpk(c[2], c[3])};
    *(u32x4*)(out + i) = w;
}

extern "C" void kernel_launch(void* const* d_in, const int* in_sizes, int n_in, void* d_out, int out_size,
                              void* d_ws, size_t ws_size, hipStream_t stream) {
    const u16* x = (const u16*)d_in[0];
    const u16* kc = (const u16*)d_in[1];
    const u16* vc = (const u16*)d_in[2];
    const u16* wq = (const u16*)d_in[3];
    const u16* wo = (const u16*)d_in[4];
    u16* Og = (u16*)d_ws;
    float* part = (float*)((char*)d_ws + (size_t)NB * SQ * NQ * 2);
    (void)hipFuncSetAttribute((const void*)attn_kernel, hipFuncAttributeMaxDynamicSharedMemorySize, LDS1_BYTES);
    (void)hipFuncSetAttribute((const void*)oproj_kernel, hipFuncAttributeMaxDynamicSharedMemorySize, LDS2_BYTES);
    attn_kernel<<<NB * HKV, 512, LDS1_BYTES, stream>>>(x, kc, vc, wq, Og);
    oproj_kernel<<<8 * 6 * SPLITK, 256, LDS2_BYTES, stream>>>(Og, wo, part);
    reduce_kernel<<<NB * SQ * DM / 8 / 256, 256, 0, stream>>>(part, (u16*)d_out);
}
```

```cpp
#include <hip/hip_runtime.h>
#include <hip/hip_bf16.h>
#include <stdint.h>

typedef short bf16x8 __attribute__((ext_vector_type(8)));
typedef short s16x4 __attribute__((ext_vector_type(4)));
typedef float f32x16 __attribute__((ext_vector_type(16)));
typedef float f32x4 __attribute__((ext_vector_type(4)));
typedef unsigned u32x4 __attribute__((ext_vector_type(4)));
typedef unsigned u32x2 __attribute__((ext_vector_type(2)));
typedef unsigned short u16;
#define LAS __attribute__((address_space(3)))
typedef LAS const char* lds_cptr;

constexpr int NB = 16, SQ = 64, DM = 576, HQ = 32, HKV = 16, SKV = 1024, DH = 128;
constexpr int NQ = HQ * DH;
constexpr int NT = SKV / 64;
constexpr int SLOT = 32768;
constexpr int LDS_Q = 4 * SLOT;
constexpr int LDS1_BYTES = 5 * SLOT;
constexpr float RTHR = 8.0f;
constexpr float C2 = 0.088388347648318447f * 1.4426950408889634f;

#define MFMA32(a, b, c) __builtin_amdgcn_mfma_f32_32x32x16_bf16(a, b, c, 0, 0, 0)

typedef __bf16 bf16x2_t __attribute__((ext_vector_type(2)));
typedef float f32x2 __attribute__((ext_vector_type(2)));
__device__ __forceinline__ unsigned cvtpk(float lo, float hi) {
    f32x2 v = {lo, hi};
    return __builtin_bit_cast(unsigned, __builtin_convertvector(v, bf16x2_t));
}
__device__ __forceinline__ void glds16(const void* g, unsigned lds_base) {
    unsigned sv;
    asm volatile("s_mov_b32 %0, m0\n\ts_mov_b32 m0, %2\n\ts_nop 0\n\tglobal_load_lds_dwordx4 %1, off\n\ts_mov_b32 m0, %0"
                 : "=&s"(sv) : "v"(g), "s"(lds_base) : "memory");
}
__device__ __forceinline__ void glds16nt(const void* g, unsigned lds_base) {
    unsigned sv;
    asm volatile("s_mov_b32 %0, m0\n\ts_mov_b32 m0, %2\n\ts_nop 0\n\tglobal_load_lds_dwordx4 %1, off nt\n\ts_mov_b32 m0, %0"
                 : "=&s"(sv) : "v"(g), "s"(lds_base) : "memory");
}
#define TS_DECL
#define TS(i)
#define TS_STORE
__device__ __forceinline__ s16x4 vtr(unsigned a) {
    return __builtin_amdgcn_ds_read_tr16_b64_v4i16((LAS s16x4*)(uintptr_t)a);
}
__device__ __forceinline__ bf16x8 ldsr128(unsigned a) { return *(const LAS bf16x8*)(uintptr_t)a; }
__device__ __forceinline__ float swap_max(float v) {
    auto rr = __builtin_amdgcn_permlane32_swap(__float_as_uint(v), __float_as_uint(v), false, false);
    return __builtin_fmaxf(__uint_as_float(rr[0]), __uint_as_float(rr[1]));
}
__device__ __forceinline__ float swap_add(float v) {
    auto rr = __builtin_amdgcn_permlane32_swap(__float_as_uint(v), __float_as_uint(v), false, false);
    return __uint_as_float(rr[0]) + __uint_as_float(rr[1]);
}

constexpr int XIMG = 0;
constexpr int WRING = 73728;
TS_DECL
__global__ __launch_bounds__(512, 2) void attn_kernel(const u16* __restrict__ x, const u16* __restrict__ Kc,
                                                      const u16* __restrict__ Vc, const u16* __restrict__ wq,
                                                      u16* __restrict__ Og) {
    extern __shared__ __attribute__((aligned(16))) char lds[];
    TS(0)
    const unsigned lds0 = (unsigned)(uintptr_t)lds;
    const int tid = threadIdx.x, lane = tid & 63, r32 = lane & 31, hi = lane >> 5;
    const int wid = __builtin_amdgcn_readfirstlane(tid >> 6);
    const int qb = wid & 3, kh = wid >> 2;
    const int bid = blockIdx.x, kvh = bid & 15, b = bid >> 4;
    const int head = 2 * kvh + (qb >> 1);
    const int gi = lane >> 4, vq = (lane & 15) >> 2, vp = lane & 3;

    const unsigned lane_off = gi * 256 + ((((lane & 15) ^ ((gi << 2) | (wid & 3)))) << 4);
    const char* Kg = (const char*)Kc + (size_t)(b * HKV + kvh) * SKV * DH * 2 + lane_off;
    const char* Vg = (const char*)Vc + (size_t)(b * HKV + kvh) * SKV * DH * 2 + lane_off;

    bf16x8 qf[8];
    {
        const char* xb = (const char*)x + (size_t)b * SQ * DM * 2;
#pragma unroll
        for (int i = 0; i < 9; ++i) {
            const int L = (wid * 9 + i) * 1024 + lane * 16;
            const int row = L / 1152, ch = (L % 1152) >> 4;
            glds16(xb + row * 1152 + ((ch ^ ((row >> 1) & 7)) << 4), lds0 + XIMG + (wid * 9 + i) * 1024);
        }
        const char* Wg = (const char*)wq + ((size_t)(lane >> 2) * NQ + kvh * 256 + wid * 32) * 2 + ((lane & 3) << 4);
        const unsigned wr = lds0 + WRING + wid * 8192;
        const int rb = b % 9;
#define KROT(ks) (4 * ((((ks) >> 2) + rb) % 9) + ((ks) & 3))
#define WQ_ISSUE(ks) glds16(Wg + (size_t)KROT(ks) * 16 * NQ * 2, wr + ((ks) & 7) * 1024)
#pragma unroll
        for (int ks = 0; ks < 7; ++ks) WQ_ISSUE(ks);
        const int pp = ((vp & 1) << 1) | (vp >> 1);
        const unsigned abase = wr + (8 * hi + vq) * 64 + (2 * (gi & 1) + (pp >> 1)) * 16 + 8 * (pp & 1);
        const unsigned xbase = lds0 + XIMG + r32 * 1152 + ((hi ^ ((r32 >> 1) & 7)) << 4);
        f32x16 acc0 = {}, acc1 = {};
        u32x4 kv0, kv1, kv2, kv3, kv4, kv5, kv6, kv7;
        asm volatile("s_waitcnt vmcnt(7)\n\ts_barrier" ::: "memory");
#pragma unroll
        for (int ks = 0; ks < 36; ++ks) {
            if (ks < 30) asm volatile("s_waitcnt vmcnt(6) lgkmcnt(0)" ::: "memory");
            else if (ks == 30) asm volatile("s_waitcnt vmcnt(13)" ::: "memory");
            else if (ks == 31) asm volatile("s_waitcnt vmcnt(12)" ::: "memory");
            else if (ks == 32) asm volatile("s_waitcnt vmcnt(11)" ::: "memory");
            else if (ks == 33) asm volatile("s_waitcnt vmcnt(10)" ::: "memory");
            else if (ks == 34) asm volatile("s_waitcnt vmcnt(9)" ::: "memory");
            else asm volatile("s_waitcnt vmcnt(8)" ::: "memory");
            const unsigned a = abase + (ks & 7) * 1024;
            const s16x4 lo0 = vtr(a), hi0 = vtr(a + 256);
            const unsigned xa = (xbase ^ ((ks & 3) << 5)) + (KROT(ks) >> 2) * 128;
            const bf16x8 x0 = ldsr128(xa), x1 = ldsr128(xa + 32 * 1152);
            if (ks + 7 < 36) WQ_ISSUE(ks + 7);
            if (ks == 29) {
#define KVLD(r, ptr) asm volatile("global_load_dwordx4 %0, %1, off nt" : "=v"(r) : "v"(ptr) : "memory")
                KVLD(kv0, Kg + wid * 1024); KVLD(kv1, Kg + (wid + 8) * 1024);
                KVLD(kv2, Vg + wid * 1024); KVLD(kv3, Vg + (wid + 8) * 1024);
                KVLD(kv4, Kg + 16384 + wid * 1024); KVLD(kv5, Kg + 16384 + (wid + 8) * 1024);
                KVLD(kv6, Vg + 16384 + wid * 1024); KVLD(kv7, Vg + 16384 + (wid + 8) * 1024);
#undef KVLD
            }
            const bf16x8 a0 = {lo0[0], lo0[1], lo0[2], lo0[3], hi0[0], hi0[1], hi0[2], hi0[3]};
            acc0 = MFMA32(a0, x0, acc0);
            acc1 = MFMA32(a0, x1, acc1);
        }
#undef WQ_ISSUE
#undef KROT
        asm volatile("s_waitcnt lgkmcnt(0)\n\ts_barrier" ::: "memory");
#pragma unroll
        for (int t = 2; t < 4; ++t) {
            const unsigned sl = lds0 + t * SLOT;
            glds16nt(Kg + t * 16384 + wid * 1024, sl + wid * 1024);
            glds16nt(Kg + t * 16384 + (wid + 8) * 1024, sl + (wid + 8) * 1024);
            glds16nt(Vg + t * 16384 + wid * 1024, sl + 16384 + wid * 1024);
            glds16nt(Vg + t * 16384 + (wid + 8) * 1024, sl + 16384 + (wid + 8) * 1024);
        }
#define QW(ACC, qblk) do { _Pragma("unroll") for (int s = 0; s < 2; ++s) { \
            u32x4 w_ = {cvtpk(ACC[8 * s], ACC[8 * s + 1]), cvtpk(ACC[8 * s + 2], ACC[8 * s + 3]), \
                        cvtpk(ACC[8 * s + 4], ACC[8 * s + 5]), cvtpk(ACC[8 * s + 6], ACC[8 * s + 7])}; \
            *(LAS u32x4*)(uintptr_t)(lds0 + LDS_Q + (wid >> 2) * 16384 + (qblk) * 8192 + (2 * (wid & 3) + s) * 1024 + lane * 16) = w_; } } while (0)
        QW(acc0, 0); QW(acc1, 1);
#undef QW
        asm volatile("s_waitcnt vmcnt(8)" : "+v"(kv0), "+v"(kv1), "+v"(kv2), "+v"(kv3), "+v"(kv4), "+v"(kv5), "+v"(kv6), "+v"(kv7) :: "memory");
        const unsigned kw = lds0 + wid * 1024 + lane * 16;
        *(LAS u32x4*)(uintptr_t)(kw) = kv0;                 *(LAS u32x4*)(uintptr_t)(kw + 8192) = kv1;
        *(LAS u32x4*)(uintptr_t)(kw + 16384) = kv2;         *(LAS u32x4*)(uintptr_t)(kw + 16384 + 8192) = kv3;
        *(LAS u32x4*)(uintptr_t)(kw + SLOT) = kv4;          *(LAS u32x4*)(uintptr_t)(kw + SLOT + 8192) = kv5;
        *(LAS u32x4*)(uintptr_t)(kw + SLOT + 16384) = kv6;  *(LAS u32x4*)(uintptr_t)(kw + SLOT + 16384 + 8192) = kv7;
    }
    asm volatile("s_waitcnt lgkmcnt(0)\n\ts_barrier" ::: "memory");
#pragma unroll
    for (int f = 0; f < 8; ++f) qf[f] = ldsr128(lds0 + LDS_Q + qb * 8192 + f * 1024 + lane * 16);
    TS(1)

    const unsigned kbase = (32 * kh + r32) * 256 + ((((r32 & 3) << 2) | (hi ^ ((r32 >> 2) & 3))) << 4);
    const unsigned vbase = 16384 + (32 * kh + 4 * hi + vq) * 256 + (((vq << 2) | ((2 * (gi & 1) + (vp >> 1)) ^ hi)) << 4) + 8 * (vp & 1);

    float m_run = -__builtin_inff(), l_run = 0.f;
    f32x16 o[4] = {};
    f32x16 s;
    s16x4 vlo[8], vhi[8];

#define TILE_WAIT(t) do { \
        if ((t) < NT - 2) asm volatile("s_waitcnt vmcnt(8) lgkmcnt(0)\n\ts_barrier" ::: "memory"); \
        else if ((t) == NT - 2) asm volatile("s_waitcnt vmcnt(4) lgkmcnt(0)\n\ts_barrier" ::: "memory"); \
        else asm volatile("s_waitcnt vmcnt(0) lgkmcnt(0)\n\ts_barrier" ::: "memory"); } while (0)
#define TILE_DMA(t) do { if ((t) >= 1 && (t) + 3 < NT) { const int tn = (t) + 3; const unsigned sl_ = lds0 + (tn & 3) * SLOT; \
            glds16nt(Kg + tn * 16384 + wid * 1024, sl_ + wid * 1024); \
            glds16nt(Kg + tn * 16384 + (wid + 8) * 1024, sl_ + (wid + 8) * 1024); \
            glds16nt(Vg + tn * 16384 + wid * 1024, sl_ + 16384 + wid * 1024); \
            glds16nt(Vg + tn * 16384 + (wid + 8) * 1024, sl_ + 16384 + (wid + 8) * 1024); } } while (0)
#define TILE_QK(t) do { const unsigned sl = lds0 + ((t) & 3) * SLOT; bf16x8 kf[8]; \
        _Pragma("unroll") for (int d0 = 0; d0 < 8; ++d0) kf[d0] = ldsr128(sl + (kbase ^ (d0 << 5))); \
        __builtin_amdgcn_sched_barrier(0); \
        TILE_DMA(t); \
        __builtin_amdgcn_sched_barrier(0); \
        s = f32x16{}; \
        _Pragma("unroll") for (int d0 = 0; d0 < 8; ++d0) s = MFMA32(kf[d0], qf[d0], s); \
        _Pragma("unroll") for (int dt = 0; dt < 4; ++dt) { _Pragma("unroll") for (int ss = 0; ss < 2; ++ss) { \
                vlo[2 * dt + ss] = vtr(sl + (vbase ^ (dt << 6)) + 4096 * ss); \
                vhi[2 * dt + ss] = vtr(sl + (vbase ^ ((dt << 6) | (1 << 5))) + 4096 * ss + 2048); } } \
        __builtin_amdgcn_sched_barrier(0); } while (0)
#define TILE_SMPV() do { \
        float tmax = s[0]; \
        _Pragma("unroll") for (int r = 1; r < 16; ++r) tmax = __builtin_fmaxf(tmax, s[r]); \
        tmax = swap_max(tmax) * C2; \
        if (__any(tmax > m_run + RTHR)) { \
            const float mnew = __builtin_fmaxf(m_run, tmax); \
            const float alpha = __builtin_amdgcn_exp2f(m_run - mnew); \
            m_run = mnew; l_run *= alpha; \
            _Pragma("unroll") for (int dt = 0; dt < 4; ++dt) { _Pragma("unroll") for (int r = 0; r < 16; ++r) o[dt][r] *= alpha; } \
        } \
        float ps = 0.f; \
        _Pragma("unroll") for (int r = 0; r < 16; ++r) { s[r] = __builtin_amdgcn_exp2f(__builtin_fmaf(s[r], C2, -m_run)); ps += s[r]; } \
        l_run += ps; \
        bf16x8 pf[2]; \
        _Pragma("unroll") for (int ss = 0; ss < 2; ++ss) { \
            u32x4 w = {cvtpk(s[8 * ss + 0], s[8 * ss + 1]), cvtpk(s[8 * ss + 2], s[8 * ss + 3]), \
                       cvtpk(s[8 * ss + 4], s[8 * ss + 5]), cvtpk(s[8 * ss + 6], s[8 * ss + 7])}; \
            pf[ss] = __builtin_bit_cast(bf16x8, w); } \
        _Pragma("unroll") for (int dt = 0; dt < 4; ++dt) { _Pragma("unroll") for (int ss = 0; ss < 2; ++ss) { \
                const s16x4 lo = vlo[2 * dt + ss], hi4 = vhi[2 * dt + ss]; \
                const bf16x8 vf = {lo[0], lo[1], lo[2], lo[3], hi4[0], hi4[1], hi4[2], hi4[3]}; \
                o[dt] = MFMA32(vf, pf[ss], o[dt]); } } \
        __builtin_amdgcn_sched_barrier(0); } while (0)

    if (kh == 0) {
        for (int t = 0; t < NT; ++t) {
            TILE_WAIT(t);
            TILE_QK(t);
            TILE_SMPV();
        }
    } else {
        TILE_WAIT(0);
        TILE_QK(0);
        for (int t = 1; t < NT; ++t) {
            TILE_WAIT(t);
            TILE_SMPV();
            TILE_QK(t);
        }
        TILE_SMPV();
    }
    TS(2)
    l_run = swap_add(l_run);
    {
        f32x16 keep0, keep1, send0, send1;
        if (kh == 0) { keep0 = o[0]; keep1 = o[1]; send0 = o[2]; send1 = o[3]; }
        else         { keep0 = o[2]; keep1 = o[3]; send0 = o[0]; send1 = o[1]; }
        const unsigned xb = lds0 + wid * 9216;
#pragma unroll
        for (int r4 = 0; r4 < 4; ++r4) {
            f32x4 v0 = {send0[4 * r4], send0[4 * r4 + 1], send0[4 * r4 + 2], send0[4 * r4 + 3]};
            f32x4 v1 = {send1[4 * r4], send1[4 * r4 + 1], send1[4 * r4 + 2], send1[4 * r4 + 3]};
            *(LAS f32x4*)(uintptr_t)(xb + r4 * 1024 + lane * 16) = v0;
            *(LAS f32x4*)(uintptr_t)(xb + (4 + r4) * 1024 + lane * 16) = v1;
        }
        *(LAS float*)(uintptr_t)(xb + 8192 + lane * 4) = m_run;
        *(LAS float*)(uintptr_t)(xb + 8192 + 256 + lane * 4) = l_run;
        asm volatile("s_waitcnt lgkmcnt(0)\n\ts_barrier" ::: "memory");
        const unsigned pb = lds0 + (wid ^ 4) * 9216;
        const float m2 = *(const LAS float*)(uintptr_t)(pb + 8192 + lane * 4);
        const float l2 = *(const LAS float*)(uintptr_t)(pb + 8192 + 256 + lane * 4);
        const float mm = __builtin_fmaxf(m_run, m2);
        const float a1 = __builtin_amdgcn_exp2f(m_run - mm), a2 = __builtin_amdgcn_exp2f(m2 - mm);
        const float inv = 1.0f / (l_run * a1 + l2 * a2);
        const float w1 = a1 * inv, w2 = a2 * inv;
        u16* orow = Og + (size_t)(b * SQ + 32 * (qb & 1) + r32) * NQ + head * DH + 64 * kh + 4 * hi;
#pragma unroll
        for (int r4 = 0; r4 < 4; ++r4) {
            const f32x4 v = *(const LAS f32x4*)(uintptr_t)(pb + r4 * 1024 + lane * 16);
            const float e0 = keep0[4 * r4] * w1 + v[0] * w2, e1 = keep0[4 * r4 + 1] * w1 + v[1] * w2;
            const float e2 = keep0[4 * r4 + 2] * w1 + v[2] * w2, e3 = keep0[4 * r4 + 3] * w1 + v[3] * w2;
            u32x2 w = {cvtpk(e0, e1), cvtpk(e2, e3)};
            *(u32x2*)(orow + 8 * r4) = w;
        }
#pragma unroll
        for (int r4 = 0; r4 < 4; ++r4) {
            const f32x4 v = *(const LAS f32x4*)(uintptr_t)(pb + (4 + r4) * 1024 + lane * 16);
            const float e0 = keep1[4 * r4] * w1 + v[0] * w2, e1 = keep1[4 * r4 + 1] * w1 + v[1] * w2;
            const float e2 = keep1[4 * r4 + 2] * w1 + v[2] * w2, e3 = keep1[4 * r4 + 3] * w1 + v[3] * w2;
            u32x2 w = {cvtpk(e0, e1), cvtpk(e2, e3)};
            *(u32x2*)(orow + 32 + 8 * r4) = w;
        }
    }
    TS(3)
    TS_STORE
}

constexpr int K2_A = 16384, K2_B = 12288, K2_SLOT = K2_A + K2_B, K2_NS = 4, LDS2_BYTES = K2_SLOT * K2_NS;
constexpr int SPLITK = 4;
__global__ __launch_bounds__(256, 1) void oproj_kernel(const u16* __restrict__ Og, const u16* __restrict__ wo,
                                                       float* __restrict__ part) {
    extern __shared__ __attribute__((aligned(16))) char lds[];
    const unsigned lds0 = (unsigned)(uintptr_t)lds;
    const int tid = threadIdx.x, lane = tid & 63, r32 = lane & 31, hi = lane >> 5;
    const int wid = __builtin_amdgcn_readfirstlane(tid >> 6);
    const int bid = blockIdx.x;
    const int xcd = bid & 7, j = bid >> 3;
    const int sk = xcd >> 1, nt = j % 6, mt = 4 * (xcd & 1) + j / 6;
    const int arow = lane >> 3;
    const char* Ag[4];
#pragma unroll
    for (int i = 0; i < 4; ++i) {
        const int row = 8 * (4 * wid + i) + arow;
        Ag[i] = (const char*)Og + ((size_t)(128 * mt + row) * NQ + 1024 * sk) * 2 + (((lane & 7) ^ ((row >> 1) & 7)) << 4);
    }
    const char* Bg[3];
#pragma unroll
    for (int i = 0; i < 3; ++i) {
        const int L = (3 * wid + i) * 1024 + lane * 16;
        const int row = L / 192, cb = L % 192;
        Bg[i] = (const char*)wo + ((size_t)(1024 * sk + row) * DM + 96 * nt) * 2 + cb;
    }
#define K2_ISSUE(kb) do { const unsigned s_ = lds0 + ((kb) & 3) * K2_SLOT; \
        _Pragma("unroll") for (int i = 0; i < 4; ++i) glds16(Ag[i] + (kb) * 128, s_ + (4 * wid + i) * 1024); \
        _Pragma("unroll") for (int i = 0; i < 3; ++i) glds16(Bg[i] + (size_t)(kb) * 64 * DM * 2, s_ + K2_A + (3 * wid + i) * 1024); } while (0)
    K2_ISSUE(0); K2_ISSUE(1); K2_ISSUE(2);
    const int arw = 32 * wid + r32;
    const unsigned abase = arw * 128 + (((4 * hi) ^ ((arw >> 1) & 7)) << 4);
    const int gi = lane >> 4, vq = (lane & 15) >> 2, vp = lane & 3;
    const unsigned bbase = K2_A + (32 * hi + vq) * 192 + 32 * (gi & 1) + 8 * vp;
    f32x16 acc[3] = {};
    for (int kb = 0; kb < 16; ++kb) {
        if (kb < 14) asm volatile("s_waitcnt vmcnt(14) lgkmcnt(0)\n\ts_barrier" ::: "memory");
        else if (kb == 14) asm volatile("s_waitcnt vmcnt(7) lgkmcnt(0)\n\ts_barrier" ::: "memory");
        else asm volatile("s_waitcnt vmcnt(0) lgkmcnt(0)\n\ts_barrier" ::: "memory");
        if (kb + 3 < 16) K2_ISSUE(kb + 3);
        const unsigned sl = lds0 + (kb & 3) * K2_SLOT;
#pragma unroll
        for (int s = 0; s < 4; ++s) {
            const bf16x8 af = ldsr128(sl + (abase ^ (s << 4)));
#pragma unroll
            for (int ct = 0; ct < 3; ++ct) {
                const s16x4 lo = vtr(sl + bbase + (8 * s) * 192 + 64 * ct);
                const s16x4 h4 = vtr(sl + bbase + (8 * s + 4) * 192 + 64 * ct);
                const bf16x8 bf = {lo[0], lo[1], lo[2], lo[3], h4[0], h4[1], h4[2], h4[3]};
                acc[ct] = MFMA32(af, bf, acc[ct]);
            }
        }
    }
#undef K2_ISSUE
    float* pp = part + (size_t)sk * (NB * SQ * DM) + (size_t)(128 * mt + 32 * wid) * DM + 96 * nt + r32;
#pragma unroll
    for (int ct = 0; ct < 3; ++ct)
#pragma unroll
        for (int r = 0; r < 16; ++r) pp[(size_t)((r & 3) + 8 * (r >> 2) + 4 * hi) * DM + 32 * ct] = acc[ct][r];
}

__global__ __launch_bounds__(256) void reduce_kernel(const float* __restrict__ part, u16* __restrict__ out) {
    const int i = (blockIdx.x * 256 + threadIdx.x) * 8;
    constexpr int N = NB * SQ * DM;
    f32x4 a = *(const f32x4*)(part + i), c = *(const f32x4*)(part + i + 4);
#pragma unroll
    for (int s = 1; s < SPLITK; ++s) {
        a += *(const f32x4*)(part + (size_t)s * N + i);
        c += *(const f32x4*)(part + (size_t)s * N + i + 4);
    }
    u32x4 w = {cvtpk(a[0], a[1]), cvtpk(a[2], a[3]), cvtpk(c[0], c[1]), cvtpk(c[2], c[3])};
    *(u32x4*)(out + i) = w;
}

extern "C" void kernel_launch(void* const* d_in, const int* in_sizes, int n_in, void* d_out, int out_size,
                              void* d_ws, size_t ws_size, hipStream_t stream) {
    const u16* x = (const u16*)d_in[0];
    const u16* kc = (const u16*)d_in[1];
    const u16* vc = (const u16*)d_in[2];
    const u16* wq = (const u16*)d_in[3];
    const u16* wo = (const u16*)d_in[4];
    u16* Og = (u16*)d_ws;
    float* part = (float*)((char*)d_ws + (size_t)NB * SQ * NQ * 2);
    (void)hipFuncSetAttribute((const void*)attn_kernel, hipFuncAttributeMaxDynamicSharedMemorySize, LDS1_BYTES);
    (void)hipFuncSetAttribute((const void*)oproj_kernel, hipFuncAttributeMaxDynamicSharedMemorySize, LDS2_BYTES);
    attn_kernel<<<NB * HKV, 512, LDS1_BYTES, stream>>>(x, kc, vc, wq, Og);
    oproj_kernel<<<8 * 6 * SPLITK, 256, LDS2_BYTES, stream>>>(Og, wo, part);
    reduce_kernel<<<NB * SQ * DM / 8 / 256, 256, 0, stream>>>(part, (u16*)d_out);
}
```

```cpp
#include <hip/hip_runtime.h>
#include <hip/hip_bf16.h>
#include <stdint.h>

typedef short bf16x8 __attribute__((ext_vector_type(8)));
typedef short s16x4 __attribute__((ext_vector_type(4)));
typedef float f32x16 __attribute__((ext_vector_type(16)));
typedef float f32x4 __attribute__((ext_vector_type(4)));
typedef unsigned u32x4 __attribute__((ext_vector_type(4)));
typedef unsigned u32x2 __attribute__((ext_vector_type(2)));
typedef unsigned short u16;
#define LAS __attribute__((address_space(3)))
typedef LAS const char* lds_cptr;

constexpr int NB = 16, SQ = 64, DM = 576, HQ = 32, HKV = 16, SKV = 1024, DH = 128;
constexpr int NQ = HQ * DH;
constexpr int NT = SKV / 64;
constexpr int SLOT = 32768;
constexpr int LDS_Q = 4 * SLOT;
constexpr int LDS1_BYTES = 5 * SLOT;
constexpr float RTHR = 8.0f;
constexpr float C2 = 0.088388347648318447f * 1.4426950408889634f;

#define MFMA32(a, b, c) __builtin_amdgcn_mfma_f32_32x32x16_bf16(a, b, c, 0, 0, 0)

typedef __bf16 bf16x2_t __attribute__((ext_vector_type(2)));
typedef float f32x2 __attribute__((ext_vector_type(2)));
__device__ __forceinline__ unsigned cvtpk(float lo, float hi) {
    f32x2 v = {lo, hi};
    return __builtin_bit_cast(unsigned, __builtin_convertvector(v, bf16x2_t));
}
__device__ __forceinline__ void glds16(const void* g, unsigned lds_base) {
    unsigned sv;
    asm volatile("s_mov_b32 %0, m0\n\ts_mov_b32 m0, %2\n\ts_nop 0\n\tglobal_load_lds_dwordx4 %1, off\n\ts_mov_b32 m0, %0"
                 : "=&s"(sv) : "v"(g), "s"(lds_base) : "memory");
}
__device__ __forceinline__ void glds16nt(const void* g, unsigned lds_base) {
    unsigned sv;
    asm volatile("s_mov_b32 %0, m0\n\ts_mov_b32 m0, %2\n\ts_nop 0\n\tglobal_load_lds_dwordx4 %1, off nt\n\ts_mov_b32 m0, %0"
                 : "=&s"(sv) : "v"(g), "s"(lds_base) : "memory");
}
#define TS_DECL
#define TS(i)
#define TS_STORE
__device__ __forceinline__ s16x4 vtr(unsigned a) {
    return __builtin_amdgcn_ds_read_tr16_b64_v4i16((LAS s16x4*)(uintptr_t)a);
}
__device__ __forceinline__ bf16x8 ldsr128(unsigned a) { return *(const LAS bf16x8*)(uintptr_t)a; }
__device__ __forceinline__ float swap_max(float v) {
    auto rr = __builtin_amdgcn_permlane32_swap(__float_as_uint(v), __float_as_uint(v), false, false);
    return __builtin_fmaxf(__uint_as_float(rr[0]), __uint_as_float(rr[1]));
}
__device__ __forceinline__ float swap_add(float v) {
    auto rr = __builtin_amdgcn_permlane32_swap(__float_as_uint(v), __float_as_uint(v), false, false);
    return __uint_as_float(rr[0]) + __uint_as_float(rr[1]);
}

constexpr int XIMG = 0;
constexpr int WRING = 73728;
TS_DECL
__global__ __launch_bounds__(512, 2) void attn_kernel(const u16* __restrict__ x, const u16* __restrict__ Kc,
                                                      const u16* __restrict__ Vc, const u16* __restrict__ wq,
                                                      u16* __restrict__ Og, const u16* __restrict__ wo) {
    extern __shared__ __attribute__((aligned(16))) char lds[];
    TS(0)
    const unsigned lds0 = (unsigned)(uintptr_t)lds;
    const int tid = threadIdx.x, lane = tid & 63, r32 = lane & 31, hi = lane >> 5;
    const int wid = __builtin_amdgcn_readfirstlane(tid >> 6);
    const int qb = wid & 3, kh = wid >> 2;
    const int bid = blockIdx.x, kvh = bid & 15, b = bid >> 4;
    const int head = 2 * kvh + (qb >> 1);
    const int gi = lane >> 4, vq = (lane & 15) >> 2, vp = lane & 3;

    const unsigned lane_off = gi * 256 + ((((lane & 15) ^ ((gi << 2) | (wid & 3)))) << 4);
    const char* Kg = (const char*)Kc + (size_t)(b * HKV + kvh) * SKV * DH * 2 + lane_off;
    const char* Vg = (const char*)Vc + (size_t)(b * HKV + kvh) * SKV * DH * 2 + lane_off;

    bf16x8 qf[8];
    {
        const char* xb = (const char*)x + (size_t)b * SQ * DM * 2;
#pragma unroll
        for (int i = 0; i < 9; ++i) {
            const int L = (wid * 9 + i) * 1024 + lane * 16;
            const int row = L / 1152, ch = (L % 1152) >> 4;
            glds16(xb + row * 1152 + ((ch ^ ((row >> 1) & 7)) << 4), lds0 + XIMG + (wid * 9 + i) * 1024);
        }
        const char* Wg = (const char*)wq + ((size_t)(lane >> 2) * NQ + kvh * 256 + wid * 32) * 2 + ((lane & 3) << 4);
        const unsigned wr = lds0 + WRING + wid * 8192;
        const int rb = b % 9;
#define KROT(ks) (4 * ((((ks) >> 2) + rb) % 9) + ((ks) & 3))
#define WQ_ISSUE(ks) glds16(Wg + (size_t)KROT(ks) * 16 * NQ * 2, wr + ((ks) & 7) * 1024)
#pragma unroll
        for (int ks = 0; ks < 7; ++ks) WQ_ISSUE(ks);
        const int pp = ((vp & 1) << 1) | (vp >> 1);
        const unsigned abase = wr + (8 * hi + vq) * 64 + (2 * (gi & 1) + (pp >> 1)) * 16 + 8 * (pp & 1);
        const unsigned xbase = lds0 + XIMG + r32 * 1152 + ((hi ^ ((r32 >> 1) & 7)) << 4);
        f32x16 acc0 = {}, acc1 = {};
        u32x4 kv0, kv1, kv2, kv3, kv4, kv5, kv6, kv7;
        asm volatile("s_waitcnt vmcnt(7)\n\ts_barrier" ::: "memory");
#pragma unroll
        for (int ks = 0; ks < 36; ++ks) {
            if (ks < 30) asm volatile("s_waitcnt vmcnt(6) lgkmcnt(0)" ::: "memory");
            else if (ks == 30) asm volatile("s_waitcnt vmcnt(13)" ::: "memory");
            else if (ks == 31) asm volatile("s_waitcnt vmcnt(12)" ::: "memory");
            else if (ks == 32) asm volatile("s_waitcnt vmcnt(11)" ::: "memory");
            else if (ks == 33) asm volatile("s_waitcnt vmcnt(10)" ::: "memory");
            else if (ks == 34) asm volatile("s_waitcnt vmcnt(9)" ::: "memory");
            else asm volatile("s_waitcnt vmcnt(8)" ::: "memory");
            const unsigned a = abase + (ks & 7) * 1024;
            const s16x4 lo0 = vtr(a), hi0 = vtr(a + 256);
            const unsigned xa = (xbase ^ ((ks & 3) << 5)) + (KROT(ks) >> 2) * 128;
            const bf16x8 x0 = ldsr128(xa), x1 = ldsr128(xa + 32 * 1152);
            if (ks + 7 < 36) WQ_ISSUE(ks + 7);
            if (ks == 29) {
#define KVLD(r, ptr) asm volatile("global_load_dwordx4 %0, %1, off nt" : "=v"(r) : "v"(ptr) : "memory")
                KVLD(kv0, Kg + wid * 1024); KVLD(kv1, Kg + (wid + 8) * 1024);
                KVLD(kv2, Vg + wid * 1024); KVLD(kv3, Vg + (wid + 8) * 1024);
                KVLD(kv4, Kg + 16384 + wid * 1024); KVLD(kv5, Kg + 16384 + (wid + 8) * 1024);
                KVLD(kv6, Vg + 16384 + wid * 1024); KVLD(kv7, Vg + 16384 + (wid + 8) * 1024);
#undef KVLD
            }
            const bf16x8 a0 = {lo0[0], lo0[1], lo0[2], lo0[3], hi0[0], hi0[1], hi0[2], hi0[3]};
            acc0 = MFMA32(a0, x0, acc0);
            acc1 = MFMA32(a0, x1, acc1);
        }
#undef WQ_ISSUE
#undef KROT
        asm volatile("s_waitcnt lgkmcnt(0)\n\ts_barrier" ::: "memory");
#pragma unroll
        for (int t = 2; t < 4; ++t) {
            const unsigned sl = lds0 + t * SLOT;
            glds16nt(Kg + t * 16384 + wid * 1024, sl + wid * 1024);
            glds16nt(Kg + t * 16384 + (wid + 8) * 1024, sl + (wid + 8) * 1024);
            glds16nt(Vg + t * 16384 + wid * 1024, sl + 16384 + wid * 1024);
            glds16nt(Vg + t * 16384 + (wid + 8) * 1024, sl + 16384 + (wid + 8) * 1024);
        }
#define QW(ACC, qblk) do { _Pragma("unroll") for (int s = 0; s < 2; ++s) { \
            u32x4 w_ = {cvtpk(ACC[8 * s], ACC[8 * s + 1]), cvtpk(ACC[8 * s + 2], ACC[8 * s + 3]), \
                        cvtpk(ACC[8 * s + 4], ACC[8 * s + 5]), cvtpk(ACC[8 * s + 6], ACC[8 * s + 7])}; \
            *(LAS u32x4*)(uintptr_t)(lds0 + LDS_Q + (wid >> 2) * 16384 + (qblk) * 8192 + (2 * (wid & 3) + s) * 1024 + lane * 16) = w_; } } while (0)
        QW(acc0, 0); QW(acc1, 1);
#undef QW
        asm volatile("s_waitcnt vmcnt(8)" : "+v"(kv0), "+v"(kv1), "+v"(kv2), "+v"(kv3), "+v"(kv4), "+v"(kv5), "+v"(kv6), "+v"(kv7) :: "memory");
        const unsigned kw = lds0 + wid * 1024 + lane * 16;
        *(LAS u32x4*)(uintptr_t)(kw) = kv0;                 *(LAS u32x4*)(uintptr_t)(kw + 8192) = kv1;
        *(LAS u32x4*)(uintptr_t)(kw + 16384) = kv2;         *(LAS u32x4*)(uintptr_t)(kw + 16384 + 8192) = kv3;
        *(LAS u32x4*)(uintptr_t)(kw + SLOT) = kv4;          *(LAS u32x4*)(uintptr_t)(kw + SLOT + 8192) = kv5;
        *(LAS u32x4*)(uintptr_t)(kw + SLOT + 16384) = kv6;  *(LAS u32x4*)(uintptr_t)(kw + SLOT + 16384 + 8192) = kv7;
    }
    asm volatile("s_waitcnt lgkmcnt(0)\n\ts_barrier" ::: "memory");
#pragma unroll
    for (int f = 0; f < 8; ++f) qf[f] = ldsr128(lds0 + LDS_Q + qb * 8192 + f * 1024 + lane * 16);
    TS(1)

    const unsigned kbase = (32 * kh + r32) * 256 + ((((r32 & 3) << 2) | (hi ^ ((r32 >> 2) & 3))) << 4);
    const unsigned vbase = 16384 + (32 * kh + 4 * hi + vq) * 256 + (((vq << 2) | ((2 * (gi & 1) + (vp >> 1)) ^ hi)) << 4) + 8 * (vp & 1);

    float m_run = -__builtin_inff(), l_run = 0.f;
    f32x16 o[4] = {};
    f32x16 s;
    s16x4 vlo[8], vhi[8];

#define TILE_WAIT(t) do { \
        if ((t) < NT - 2) asm volatile("s_waitcnt vmcnt(8) lgkmcnt(0)\n\ts_barrier" ::: "memory"); \
        else if ((t) == NT - 2) asm volatile("s_waitcnt vmcnt(4) lgkmcnt(0)\n\ts_barrier" ::: "memory"); \
        else asm volatile("s_waitcnt vmcnt(0) lgkmcnt(0)\n\ts_barrier" ::: "memory"); } while (0)
#define TILE_DMA(t) do { if ((t) >= 1 && (t) + 3 < NT) { const int tn = (t) + 3; const unsigned sl_ = lds0 + (tn & 3) * SLOT; \
            glds16nt(Kg + tn * 16384 + wid * 1024, sl_ + wid * 1024); \
            glds16nt(Kg + tn * 16384 + (wid + 8) * 1024, sl_ + (wid + 8) * 1024); \
            glds16nt(Vg + tn * 16384 + wid * 1024, sl_ + 16384 + wid * 1024); \
            glds16nt(Vg + tn * 16384 + (wid + 8) * 1024, sl_ + 16384 + (wid + 8) * 1024); } } while (0)
#define TILE_QK(t) do { const unsigned sl = lds0 + ((t) & 3) * SLOT; bf16x8 kf[8]; \
        _Pragma("unroll") for (int d0 = 0; d0 < 8; ++d0) kf[d0] = ldsr128(sl + (kbase ^ (d0 << 5))); \
        __builtin_amdgcn_sched_barrier(0); \
        TILE_DMA(t); \
        __builtin_amdgcn_sched_barrier(0); \
        s = f32x16{}; \
        _Pragma("unroll") for (int d0 = 0; d0 < 8; ++d0) s = MFMA32(kf[d0], qf[d0], s); \
        _Pragma("unroll") for (int dt = 0; dt < 4; ++dt) { _Pragma("unroll") for (int ss = 0; ss < 2; ++ss) { \
                vlo[2 * dt + ss] = vtr(sl + (vbase ^ (dt << 6)) + 4096 * ss); \
                vhi[2 * dt + ss] = vtr(sl + (vbase ^ ((dt << 6) | (1 << 5))) + 4096 * ss + 2048); } } \
        __builtin_amdgcn_sched_barrier(0); } while (0)
#define TILE_SMPV() do { \
        float tmax = s[0]; \
        _Pragma("unroll") for (int r = 1; r < 16; ++r) tmax = __builtin_fmaxf(tmax, s[r]); \
        tmax = swap_max(tmax) * C2; \
        if (__any(tmax > m_run + RTHR)) { \
            const float mnew = __builtin_fmaxf(m_run, tmax); \
            const float alpha = __builtin_amdgcn_exp2f(m_run - mnew); \
            m_run = mnew; l_run *= alpha; \
            _Pragma("unroll") for (int dt = 0; dt < 4; ++dt) { _Pragma("unroll") for (int r = 0; r < 16; ++r) o[dt][r] *= alpha; } \
        } \
        float ps = 0.f; \
        _Pragma("unroll") for (int r = 0; r < 16; ++r) { s[r] = __builtin_amdgcn_exp2f(__builtin_fmaf(s[r], C2, -m_run)); ps += s[r]; } \
        l_run += ps; \
        bf16x8 pf[2]; \
        _Pragma("unroll") for (int ss = 0; ss < 2; ++ss) { \
            u32x4 w = {cvtpk(s[8 * ss + 0], s[8 * ss + 1]), cvtpk(s[8 * ss + 2], s[8 * ss + 3]), \
                       cvtpk(s[8 * ss + 4], s[8 * ss + 5]), cvtpk(s[8 * ss + 6], s[8 * ss + 7])}; \
            pf[ss] = __builtin_bit_cast(bf16x8, w); } \
        _Pragma("unroll") for (int dt = 0; dt < 4; ++dt) { _Pragma("unroll") for (int ss = 0; ss < 2; ++ss) { \
                const s16x4 lo = vlo[2 * dt + ss], hi4 = vhi[2 * dt + ss]; \
                const bf16x8 vf = {lo[0], lo[1], lo[2], lo[3], hi4[0], hi4[1], hi4[2], hi4[3]}; \
                o[dt] = MFMA32(vf, pf[ss], o[dt]); } } \
        __builtin_amdgcn_sched_barrier(0); } while (0)

    if (kh == 0) {
        for (int t = 0; t < NT; ++t) {
            TILE_WAIT(t);
            TILE_QK(t);
            TILE_SMPV();
        }
    } else {
        TILE_WAIT(0);
        TILE_QK(0);
        for (int t = 1; t < NT; ++t) {
            TILE_WAIT(t);
            TILE_SMPV();
            TILE_QK(t);
        }
        TILE_SMPV();
    }
    TS(2)
    {
        const char* wp = (const char*)wo + (size_t)(256 * kvh + 16 * b) * DM * 2 + wid * 1024 + lane * 16;
        glds16(wp, lds0 + LDS_Q + wid * 1024);
        glds16(wp + 8192, lds0 + LDS_Q + 8192 + wid * 1024);
        if (wid < 2) glds16(wp + 16384, lds0 + LDS_Q + 16384 + wid * 1024);
    }
    l_run = swap_add(l_run);
    {
        f32x16 keep0, keep1, send0, send1;
        if (kh == 0) { keep0 = o[0]; keep1 = o[1]; send0 = o[2]; send1 = o[3]; }
        else         { keep0 = o[2]; keep1 = o[3]; send0 = o[0]; send1 = o[1]; }
        const unsigned xb = lds0 + wid * 9216;
#pragma unroll
        for (int r4 = 0; r4 < 4; ++r4) {
            f32x4 v0 = {send0[4 * r4], send0[4 * r4 + 1], send0[4 * r4 + 2], send0[4 * r4 + 3]};
            f32x4 v1 = {send1[4 * r4], send1[4 * r4 + 1], send1[4 * r4 + 2], send1[4 * r4 + 3]};
            *(LAS f32x4*)(uintptr_t)(xb + r4 * 1024 + lane * 16) = v0;
            *(LAS f32x4*)(uintptr_t)(xb + (4 + r4) * 1024 + lane * 16) = v1;
        }
        *(LAS float*)(uintptr_t)(xb + 8192 + lane * 4) = m_run;
        *(LAS float*)(uintptr_t)(xb + 8192 + 256 + lane * 4) = l_run;
        asm volatile("s_waitcnt lgkmcnt(0)\n\ts_barrier" ::: "memory");
        const unsigned pb = lds0 + (wid ^ 4) * 9216;
        const float m2 = *(const LAS float*)(uintptr_t)(pb + 8192 + lane * 4);
        const float l2 = *(const LAS float*)(uintptr_t)(pb + 8192 + 256 + lane * 4);
        const float mm = __builtin_fmaxf(m_run, m2);
        const float a1 = __builtin_amdgcn_exp2f(m_run - mm), a2 = __builtin_amdgcn_exp2f(m2 - mm);
        const float inv = 1.0f / (l_run * a1 + l2 * a2);
        const float w1 = a1 * inv, w2 = a2 * inv;
        const int row = b * SQ + 32 * (qb & 1) + r32;
        char* ob = (char*)Og + ((size_t)((row >> 4) * 128 + head * 4 + 2 * kh) * 64 + hi * 16 + (row & 15)) * 16;
#define OSTORE(KEEP, i) do { u32x2 w_[4]; \
            _Pragma("unroll") for (int r4 = 0; r4 < 4; ++r4) { \
                const f32x4 v = *(const LAS f32x4*)(uintptr_t)(pb + (4 * (i) + r4) * 1024 + lane * 16); \
                const float e0 = KEEP[4 * r4] * w1 + v[0] * w2, e1 = KEEP[4 * r4 + 1] * w1 + v[1] * w2; \
                const float e2 = KEEP[4 * r4 + 2] * w1 + v[2] * w2, e3 = KEEP[4 * r4 + 3] * w1 + v[3] * w2; \
                w_[r4] = u32x2{cvtpk(e0, e1), cvtpk(e2, e3)}; } \
            _Pragma("unroll") for (int g = 0; g < 4; g += 2) { \
                auto rx = __builtin_amdgcn_permlane32_swap(w_[g].x, w_[g + 1].x, false, false); \
                auto ry = __builtin_amdgcn_permlane32_swap(w_[g].y, w_[g + 1].y, false, false); \
                u32x4 o4 = {rx[0], ry[0], rx[1], ry[1]}; \
                *(u32x4*)(ob + (i) * 1024 + g * 256) = o4; } } while (0)
        OSTORE(keep0, 0); OSTORE(keep1, 1);
#undef OSTORE
    }
    TS(3)
    TS_STORE
}

constexpr int LDS2_BYTES = 512 * 320;
#define MFMA16(a, b, c) __builtin_amdgcn_mfma_f32_16x16x32_bf16(a, b, c, 0, 0, 0)
__global__ __launch_bounds__(512, 2) void oproj_kernel(const u16* __restrict__ Og, const u16* __restrict__ wo,
                                                       float* __restrict__ part) {
    extern __shared__ __attribute__((aligned(16))) char lds[];
    const unsigned lds0 = (unsigned)(uintptr_t)lds;
    const int tid = threadIdx.x, lane = tid & 63;
    const int wid = __builtin_amdgcn_readfirstlane(tid >> 6);
    const int rb = wid & 3, kp = wid >> 2;
    const int bid = blockIdx.x;
    const int sk = bid & 7, nt = (bid >> 3) & 3, mt = bid >> 5;
    const char* Bg[5];
#pragma unroll
    for (int i = 0; i < 5; ++i) {
        const int L = i * 1024 + lane * 16;
        const int row = L / 320, cp = (L % 320) >> 4;
        int c = cp ^ (((row >> 3) & 1) << 1);
        c = c < 18 ? c : 0;
        Bg[i] = (const char*)wo + ((size_t)(256 * sk + 16 * wid + row) * DM + 144 * nt) * 2 + c * 16;
    }
    const char* Ag = (const char*)Og + ((size_t)((8 * mt + 2 * rb) * 128 + 8 * sk + kp) * 64 + lane) * 16;
    u32x4 af[16];
#define K2_ISSUE(c) do { \
        _Pragma("unroll") for (int i = 0; i < 5; ++i) glds16(Bg[i] + (size_t)(((c) >> 1) * 2048 + ((c) & 1) * 128) * DM * 2, lds0 + (40 * (c) + 5 * wid + i) * 1024); \
        _Pragma("unroll") for (int i = 0; i < 4; ++i) \
            asm volatile("global_load_dwordx4 %0, %1, off" : "=v"(af[4 * (c) + i]) : "v"(Ag + (size_t)(i & 1) * 131072 + (((c) >> 1) * 64 + ((c) & 1) * 4 + 2 * (i >> 1)) * 1024) : "memory"); } while (0)
    K2_ISSUE(0); K2_ISSUE(1);
    const int gi = lane >> 4, vq = (lane & 15) >> 2, vp = lane & 3;
    const unsigned bb = lds0 + (32 * kp + 8 * gi + vq) * 320 + 8 * vp;
    const unsigned bbE = bb + 32 * (gi & 1), bbO = bb - 32 * (gi & 1);
    f32x4 acc[2][9];
#pragma unroll
    for (int ct = 0; ct < 9; ++ct) { acc[0][ct] = f32x4{0.f, 0.f, 0.f, 0.f}; acc[1][ct] = f32x4{0.f, 0.f, 0.f, 0.f}; }
#pragma unroll
    for (int c = 0; c < 4; ++c) {
        if (c < 3) asm volatile("s_waitcnt vmcnt(9)\n\ts_barrier" : "+v"(af[4 * c]), "+v"(af[4 * c + 1]), "+v"(af[4 * c + 2]), "+v"(af[4 * c + 3]) :: "memory");
        else asm volatile("s_waitcnt vmcnt(0)\n\ts_barrier" : "+v"(af[12]), "+v"(af[13]), "+v"(af[14]), "+v"(af[15]) :: "memory");
        if (c + 2 < 4) K2_ISSUE(c + 2);
#pragma unroll
        for (int j = 0; j < 2; ++j) {
            const bf16x8 a0 = __builtin_bit_cast(bf16x8, af[4 * c + 2 * j]), a1 = __builtin_bit_cast(bf16x8, af[4 * c + 2 * j + 1]);
#pragma unroll
            for (int ct = 0; ct < 9; ++ct) {
                const unsigned ad = ((ct & 1) ? bbO : bbE) + (4 * c + 2 * j) * 10240 + 32 * ct;
                const s16x4 lo = vtr(ad), h4 = vtr(ad + 1280);
                const bf16x8 bf = {lo[0], lo[1], lo[2], lo[3], h4[0], h4[1], h4[2], h4[3]};
                acc[0][ct] = MFMA16(bf, a0, acc[0][ct]);
                acc[1][ct] = MFMA16(bf, a1, acc[1][ct]);
            }
        }
    }
#undef K2_ISSUE
    asm volatile("s_waitcnt lgkmcnt(0)\n\ts_barrier" ::: "memory");
#pragma unroll
    for (int ct = 0; ct < 9; ++ct)
        *(LAS f32x4*)(uintptr_t)(lds0 + (wid * 9 + ct) * 1024 + lane * 16) = kp ? acc[0][ct] : acc[1][ct];
    asm volatile("s_waitcnt lgkmcnt(0)\n\ts_barrier" ::: "memory");
    float* pp = part + ((size_t)(sk * 64 + 8 * mt + 2 * rb + kp) * 36 + 9 * nt) * 256 + lane * 4;
#pragma unroll
    for (int ct = 0; ct < 9; ++ct) {
        const f32x4 v = *(const LAS f32x4*)(uintptr_t)(lds0 + ((wid ^ 4) * 9 + ct) * 1024 + lane * 16);
        *(f32x4*)(pp + ct * 256) = (kp ? acc[1][ct] : acc[0][ct]) + v;
    }
}

__global__ __launch_bounds__(256) void reduce_kernel(const float* __restrict__ part, u16* __restrict__ out) {
    const int lane = threadIdx.x & 63;
    const int u = blockIdx.x * 4 + (threadIdx.x >> 6);
    const float* p = part + (size_t)u * 256 + lane * 4;
    f32x4 t = *(const f32x4*)p;
#pragma unroll
    for (int s = 1; s < 8; ++s) t += *(const f32x4*)(p + (size_t)s * 64 * 36 * 256);
    const int rt = u / 36, ct = u % 36;
    u32x2 w = {cvtpk(t[0], t[1]), cvtpk(t[2], t[3])};
    *(u32x2*)(out + (size_t)(16 * rt + (lane & 15)) * DM + 16 * ct + 4 * (lane >> 4)) = w;
}

extern "C" void kernel_launch(void* const* d_in, const int* in_sizes, int n_in, void* d_out, int out_size,
                              void* d_ws, size_t ws_size, hipStream_t stream) {
    const u16* x = (const u16*)d_in[0];
    const u16* kc = (const u16*)d_in[1];
    const u16* vc = (const u16*)d_in[2];
    const u16* wq = (const u16*)d_in[3];
    const u16* wo = (const u16*)d_in[4];
    u16* Og = (u16*)d_ws;
    float* part = (float*)((char*)d_ws + (size_t)NB * SQ * NQ * 2);
    (void)hipFuncSetAttribute((const void*)attn_kernel, hipFuncAttributeMaxDynamicSharedMemorySize, LDS1_BYTES);
    (void)hipFuncSetAttribute((const void*)oproj_kernel, hipFuncAttributeMaxDynamicSharedMemorySize, LDS2_BYTES);
    attn_kernel<<<NB * HKV, 512, LDS1_BYTES, stream>>>(x, kc, vc, wq, Og, wo);
    oproj_kernel<<<256, 512, LDS2_BYTES, stream>>>(Og, wo, part);
    reduce_kernel<<<576, 256, 0, stream>>>(part, (u16*)d_out);
}
```

```cpp
#include <hip/hip_runtime.h>
#include <hip/hip_bf16.h>
#include <stdint.h>

typedef short bf16x8 __attribute__((ext_vector_type(8)));
typedef short s16x4 __attribute__((ext_vector_type(4)));
typedef float f32x16 __attribute__((ext_vector_type(16)));
typedef float f32x4 __attribute__((ext_vector_type(4)));
typedef unsigned u32x4 __attribute__((ext_vector_type(4)));
typedef unsigned u32x2 __attribute__((ext_vector_type(2)));
typedef unsigned short u16;
#define LAS __attribute__((address_space(3)))
typedef LAS const char* lds_cptr;

constexpr int NB = 16, SQ = 64, DM = 576, HQ = 32, HKV = 16, SKV = 1024, DH = 128;
constexpr int NQ = HQ * DH;
constexpr int NT = SKV / 64;
constexpr int SLOT = 32768;
constexpr int LDS_Q = 4 * SLOT;
constexpr int LDS1_BYTES = 5 * SLOT;
constexpr float RTHR = 8.0f;
constexpr float C2 = 0.088388347648318447f * 1.4426950408889634f;

#define MFMA32(a, b, c) __builtin_amdgcn_mfma_f32_32x32x16_bf16(a, b, c, 0, 0, 0)

typedef __bf16 bf16x2_t __attribute__((ext_vector_type(2)));
typedef float f32x2 __attribute__((ext_vector_type(2)));
__device__ __forceinline__ unsigned cvtpk(float lo, float hi) {
    f32x2 v = {lo, hi};
    return __builtin_bit_cast(unsigned, __builtin_convertvector(v, bf16x2_t));
}
__device__ __forceinline__ void glds16(const void* g, unsigned lds_base) {
    unsigned sv;
    asm volatile("s_mov_b32 %0, m0\n\ts_mov_b32 m0, %2\n\ts_nop 0\n\tglobal_load_lds_dwordx4 %1, off\n\ts_mov_b32 m0, %0"
                 : "=&s"(sv) : "v"(g), "s"(lds_base) : "memory");
}
__device__ __forceinline__ void glds16nt(const void* g, unsigned lds_base) {
    unsigned sv;
    asm volatile("s_mov_b32 %0, m0\n\ts_mov_b32 m0, %2\n\ts_nop 0\n\tglobal_load_lds_dwordx4 %1, off nt\n\ts_mov_b32 m0, %0"
                 : "=&s"(sv) : "v"(g), "s"(lds_base) : "memory");
}
#define TS_DECL
#define TS(i)
#define TS_STORE
__device__ __forceinline__ s16x4 vtr(unsigned a) {
    return __builtin_amdgcn_ds_read_tr16_b64_v4i16((LAS s16x4*)(uintptr_t)a);
}
__device__ __forceinline__ bf16x8 ldsr128(unsigned a) { return *(const LAS bf16x8*)(uintptr_t)a; }
__device__ __forceinline__ float swap_max(float v) {
    auto rr = __builtin_amdgcn_permlane32_swap(__float_as_uint(v), __float_as_uint(v), false, false);
    return __builtin_fmaxf(__uint_as_float(rr[0]), __uint_as_float(rr[1]));
}
__device__ __forceinline__ float swap_add(float v) {
    auto rr = __builtin_amdgcn_permlane32_swap(__float_as_uint(v), __float_as_uint(v), false, false);
    return __uint_as_float(rr[0]) + __uint_as_float(rr[1]);
}

constexpr int XIMG = 0;
constexpr int WRING = 73728;
TS_DECL
__global__ __launch_bounds__(512, 2) void attn_kernel(const u16* __restrict__ x, const u16* __restrict__ Kc,
                                                      const u16* __restrict__ Vc, const u16* __restrict__ wq,
                                                      u16* __restrict__ Og, const u16* __restrict__ wo) {
    extern __shared__ __attribute__((aligned(16))) char lds[];
    TS(0)
    const unsigned lds0 = (unsigned)(uintptr_t)lds;
    const int tid = threadIdx.x, lane = tid & 63, r32 = lane & 31, hi = lane >> 5;
    const int wid = __builtin_amdgcn_readfirstlane(tid >> 6);
    const int qb = wid & 3, kh = wid >> 2;
    const int bid = blockIdx.x, kvh = bid & 15, b = bid >> 4;
    const int head = 2 * kvh + (qb >> 1);
    const int gi = lane >> 4, vq = (lane & 15) >> 2, vp = lane & 3;

    const unsigned lane_off = gi * 256 + ((((lane & 15) ^ ((gi << 2) | (wid & 3)))) << 4);
    const char* Kg = (const char*)Kc + (size_t)(b * HKV + kvh) * SKV * DH * 2 + lane_off;
    const char* Vg = (const char*)Vc + (size_t)(b * HKV + kvh) * SKV * DH * 2 + lane_off;

    bf16x8 qf[8];
    {
        u32x4 kv0, kv1, kv2, kv3, kv4, kv5, kv6, kv7;
        const int rb = b % 9;
#define KBR(j) (((j) + rb) % 9)
#define KROT(ks) (4 * KBR((ks) >> 2) + ((ks) & 3))
        const char* Xg = (const char*)x + ((size_t)b * SQ + 8 * wid + (lane >> 3)) * DM * 2 + (((lane & 7) ^ ((4 * wid + (lane >> 4)) & 7)) << 4);
#define X_ISSUE(j) glds16(Xg + KBR(j) * 128, lds0 + XIMG + (j) * 8192 + wid * 1024)
        const char* Wg = (const char*)wq + ((size_t)(lane >> 2) * NQ + kvh * 256 + wid * 32) * 2 + ((lane & 3) << 4);
        const unsigned wr = lds0 + WRING + wid * 8192;
#define WQ_ISSUE(ks) glds16(Wg + (size_t)KROT(ks) * 16 * NQ * 2, wr + ((ks) & 7) * 1024)
        X_ISSUE(0); X_ISSUE(1); X_ISSUE(2);
#pragma unroll
        for (int ks = 0; ks < 7; ++ks) WQ_ISSUE(ks);
#pragma unroll
        for (int j = 3; j < 9; ++j) X_ISSUE(j);
        const int pp = ((vp & 1) << 1) | (vp >> 1);
        const unsigned abase = wr + (8 * hi + vq) * 64 + (2 * (gi & 1) + (pp >> 1)) * 16 + 8 * (pp & 1);
        const unsigned xbase = lds0 + XIMG + r32 * 128 + ((hi ^ ((r32 >> 1) & 7)) << 4);
        f32x16 acc0 = {}, acc1 = {};
        asm volatile("s_waitcnt vmcnt(13)\n\ts_barrier" ::: "memory");
#define QSTEP(ks, slot, WAITS, ISSUE) do { \
            WAITS; \
            const unsigned a = abase + (slot) * 1024; \
            const s16x4 lo0 = vtr(a), hi0 = vtr(a + 256); \
            const unsigned xa = (xbase ^ (((ks) & 3) << 5)) + ((ks) >> 2) * 8192; \
            const bf16x8 x0 = ldsr128(xa), x1 = ldsr128(xa + 4096); \
            ISSUE; \
            const bf16x8 a0 = {lo0[0], lo0[1], lo0[2], lo0[3], hi0[0], hi0[1], hi0[2], hi0[3]}; \
            acc0 = MFMA32(a0, x0, acc0); \
            acc1 = MFMA32(a0, x1, acc1); } while (0)
#pragma unroll
        for (int ks = 0; ks < 8; ++ks) {
            if (ks < 7) QSTEP(ks, ks, asm volatile("s_waitcnt vmcnt(12) lgkmcnt(0)" ::: "memory"), WQ_ISSUE(ks + 7));
            else QSTEP(ks, ks, asm volatile("s_waitcnt vmcnt(6) lgkmcnt(0)" ::: "memory"), WQ_ISSUE(ks + 7));
        }
#pragma unroll 1
        for (int k4 = 8; k4 < 28; k4 += 4) {
            if (k4 == 12) asm volatile("s_barrier" ::: "memory");
#pragma unroll
            for (int i = 0; i < 4; ++i)
                QSTEP(k4 + i, (k4 + i) & 7, asm volatile("s_waitcnt vmcnt(6) lgkmcnt(0)" ::: "memory"), WQ_ISSUE(k4 + i + 7));
        }
#define KVLD(r, ptr) asm volatile("global_load_dwordx4 %0, %1, off nt" : "=v"(r) : "v"(ptr) : "memory")
        QSTEP(28, 4, asm volatile("s_waitcnt vmcnt(6) lgkmcnt(0)" ::: "memory"), WQ_ISSUE(35));
        QSTEP(29, 5, asm volatile("s_waitcnt vmcnt(6) lgkmcnt(0)" ::: "memory"),
              KVLD(kv0, Kg + wid * 1024); KVLD(kv1, Kg + (wid + 8) * 1024);
              KVLD(kv2, Vg + wid * 1024); KVLD(kv3, Vg + (wid + 8) * 1024);
              KVLD(kv4, Kg + 16384 + wid * 1024); KVLD(kv5, Kg + 16384 + (wid + 8) * 1024);
              KVLD(kv6, Kg + 32768 + wid * 1024); KVLD(kv7, Kg + 32768 + (wid + 8) * 1024));
#undef KVLD
        QSTEP(30, 6, asm volatile("s_waitcnt vmcnt(13)" ::: "memory"), (void)0);
        QSTEP(31, 7, asm volatile("s_waitcnt vmcnt(12)" ::: "memory"), (void)0);
        QSTEP(32, 0, asm volatile("s_waitcnt vmcnt(11)" ::: "memory"), (void)0);
        QSTEP(33, 1, asm volatile("s_waitcnt vmcnt(10)" ::: "memory"), (void)0);
        QSTEP(34, 2, asm volatile("s_waitcnt vmcnt(9)" ::: "memory"), (void)0);
        QSTEP(35, 3, asm volatile("s_waitcnt vmcnt(8)" ::: "memory"), (void)0);
#undef QSTEP
#undef X_ISSUE
#undef KBR
#undef WQ_ISSUE
#undef KROT
        asm volatile("s_waitcnt lgkmcnt(0)\n\ts_barrier" ::: "memory");
#pragma unroll
        for (int u = 1; u < 3; ++u) {
            glds16nt(Vg + u * 16384 + wid * 1024, lds0 + u * SLOT + 16384 + wid * 1024);
            glds16nt(Vg + u * 16384 + (wid + 8) * 1024, lds0 + u * SLOT + 16384 + (wid + 8) * 1024);
            if (u == 1) {
                glds16nt(Kg + (u + 2) * 16384 + wid * 1024, lds0 + ((u + 2) & 3) * SLOT + wid * 1024);
                glds16nt(Kg + (u + 2) * 16384 + (wid + 8) * 1024, lds0 + ((u + 2) & 3) * SLOT + (wid + 8) * 1024);
            }
        }
#define QW(ACC, qblk) do { _Pragma("unroll") for (int s = 0; s < 2; ++s) { \
            u32x4 w_ = {cvtpk(ACC[8 * s], ACC[8 * s + 1]), cvtpk(ACC[8 * s + 2], ACC[8 * s + 3]), \
                        cvtpk(ACC[8 * s + 4], ACC[8 * s + 5]), cvtpk(ACC[8 * s + 6], ACC[8 * s + 7])}; \
            *(LAS u32x4*)(uintptr_t)(lds0 + LDS_Q + (wid >> 2) * 16384 + (qblk) * 8192 + (2 * (wid & 3) + s) * 1024 + lane * 16) = w_; } } while (0)
        QW(acc0, 0); QW(acc1, 1);
#undef QW
        asm volatile("s_waitcnt vmcnt(6)" : "+v"(kv0), "+v"(kv1), "+v"(kv2), "+v"(kv3), "+v"(kv4), "+v"(kv5), "+v"(kv6), "+v"(kv7) :: "memory");
        const unsigned kw = lds0 + wid * 1024 + lane * 16;
        *(LAS u32x4*)(uintptr_t)(kw) = kv0;                 *(LAS u32x4*)(uintptr_t)(kw + 8192) = kv1;
        *(LAS u32x4*)(uintptr_t)(kw + 16384) = kv2;         *(LAS u32x4*)(uintptr_t)(kw + 16384 + 8192) = kv3;
        *(LAS u32x4*)(uintptr_t)(kw + SLOT) = kv4;          *(LAS u32x4*)(uintptr_t)(kw + SLOT + 8192) = kv5;
        *(LAS u32x4*)(uintptr_t)(kw + 2 * SLOT) = kv6;      *(LAS u32x4*)(uintptr_t)(kw + 2 * SLOT + 8192) = kv7;
    }
    asm volatile("s_waitcnt lgkmcnt(0)\n\ts_barrier" ::: "memory");
#pragma unroll
    for (int f = 0; f < 8; ++f) qf[f] = ldsr128(lds0 + LDS_Q + qb * 8192 + f * 1024 + lane * 16);
    TS(1)

    const unsigned kbase = (32 * kh + r32) * 256 + ((((r32 & 3) << 2) | (hi ^ ((r32 >> 2) & 3))) << 4);
    const unsigned vbase = 16384 + (32 * kh + 4 * hi + vq) * 256 + (((vq << 2) | ((2 * (gi & 1) + (vp >> 1)) ^ hi)) << 4) + 8 * (vp & 1);

    float m_run = -__builtin_inff(), l_run = 0.f;
    f32x16 o[4] = {};
    f32x16 sA, sB;

#define DMA_V(tn) do { const unsigned sl_ = lds0 + ((tn) & 3) * SLOT + 16384; \
            glds16nt(Vg + (tn) * 16384 + wid * 1024, sl_ + wid * 1024); \
            glds16nt(Vg + (tn) * 16384 + (wid + 8) * 1024, sl_ + (wid + 8) * 1024); } while (0)
#define DMA_K(tn) do { const unsigned sl_ = lds0 + ((tn) & 3) * SLOT; \
            glds16nt(Kg + (tn) * 16384 + wid * 1024, sl_ + wid * 1024); \
            glds16nt(Kg + (tn) * 16384 + (wid + 8) * 1024, sl_ + (wid + 8) * 1024); } while (0)
#define STEP(SC, SN, t, HASQK, HASKRD, DOV, DOK, VMW) do { \
        s16x4 vlo[8], vhi[8]; \
        asm volatile("s_waitcnt vmcnt(" #VMW ") lgkmcnt(0)\n\ts_barrier" ::: "memory"); \
        float tmax = SC[0]; \
        _Pragma("unroll") for (int r = 1; r < 16; ++r) tmax = __builtin_fmaxf(tmax, SC[r]); \
        tmax = swap_max(tmax) * C2; \
        if (__any(tmax > m_run + RTHR)) { \
            const float mnew = __builtin_fmaxf(m_run, tmax); \
            const float alpha = __builtin_amdgcn_exp2f(m_run - mnew); \
            m_run = mnew; l_run *= alpha; \
            _Pragma("unroll") for (int dt = 0; dt < 4; ++dt) { _Pragma("unroll") for (int r = 0; r < 16; ++r) o[dt][r] *= alpha; } \
        } \
        __builtin_amdgcn_sched_barrier(0); \
        const unsigned sl = lds0 + ((t) & 3) * SLOT; \
        _Pragma("unroll") for (int ss = 0; ss < 2; ++ss) { _Pragma("unroll") for (int dt = 0; dt < 4; ++dt) { \
                vlo[2 * dt + ss] = vtr(sl + (vbase ^ (dt << 6)) + 4096 * ss); \
                vhi[2 * dt + ss] = vtr(sl + (vbase ^ ((dt << 6) | (1 << 5))) + 4096 * ss + 2048); } } \
        if (HASQK) { SN = f32x16{}; _Pragma("unroll") for (int d0 = 0; d0 < 8; ++d0) SN = MFMA32(kf[d0], qf[d0], SN); } \
        float ps = 0.f; \
        _Pragma("unroll") for (int r = 0; r < 16; ++r) { SC[r] = __builtin_amdgcn_exp2f(__builtin_fmaf(SC[r], C2, -m_run)); ps += SC[r]; } \
        l_run += ps; \
        bf16x8 pf[2]; \
        _Pragma("unroll") for (int ss = 0; ss < 2; ++ss) { \
            u32x4 w = {cvtpk(SC[8 * ss + 0], SC[8 * ss + 1]), cvtpk(SC[8 * ss + 2], SC[8 * ss + 3]), \
                       cvtpk(SC[8 * ss + 4], SC[8 * ss + 5]), cvtpk(SC[8 * ss + 6], SC[8 * ss + 7])}; \
            pf[ss] = __builtin_bit_cast(bf16x8, w); } \
        asm volatile("" : "+v"(pf[0]), "+v"(pf[1]), "+v"(l_run)); \
        if (HASQK) { _Pragma("unroll") for (int i = 0; i < 8; ++i) { \
            __builtin_amdgcn_sched_group_barrier(0x008, 1, 0); __builtin_amdgcn_sched_group_barrier(0x100, 2, 0); \
            __builtin_amdgcn_sched_group_barrier(0x002, 8, 0); } } \
        __builtin_amdgcn_sched_barrier(0); \
        if (HASKRD) { const unsigned sk_ = lds0 + (((t) + 2) & 3) * SLOT; \
            _Pragma("unroll") for (int d0 = 0; d0 < 8; ++d0) kf[d0] = ldsr128(sk_ + (kbase ^ (d0 << 5))); } \
        _Pragma("unroll") for (int ss = 0; ss < 2; ++ss) { _Pragma("unroll") for (int dt = 0; dt < 4; ++dt) { \
                const s16x4 lo = vlo[2 * dt + ss], hi4 = vhi[2 * dt + ss]; \
                const bf16x8 vf = {lo[0], lo[1], lo[2], lo[3], hi4[0], hi4[1], hi4[2], hi4[3]}; \
                o[dt] = MFMA32(vf, pf[ss], o[dt]); } } \
        if (HASKRD) { _Pragma("unroll") for (int i = 0; i < 8; ++i) { \
            __builtin_amdgcn_sched_group_barrier(0x008, 1, 0); __builtin_amdgcn_sched_group_barrier(0x100, 1, 0); \
            __builtin_amdgcn_sched_group_barrier(0x002, 1, 0); } } \
        __builtin_amdgcn_sched_barrier(0); \
        if (DOV) DMA_V((t) + 3); \
        if (DOK) DMA_K((t) + 5); \
        __builtin_amdgcn_sched_barrier(0); } while (0)

    bf16x8 kf[8];
    {
#pragma unroll
        for (int d0 = 0; d0 < 8; ++d0) kf[d0] = ldsr128(lds0 + (kbase ^ (d0 << 5)));
        sA = f32x16{};
#pragma unroll
        for (int d0 = 0; d0 < 8; ++d0) sA = MFMA32(kf[d0], qf[d0], sA);
#pragma unroll
        for (int d0 = 0; d0 < 8; ++d0) kf[d0] = ldsr128(lds0 + SLOT + (kbase ^ (d0 << 5)));
        asm volatile("s_waitcnt lgkmcnt(0)\n\ts_barrier" ::: "memory");
        DMA_K(4);
    }
    STEP(sA, sB, 0, true, true, true, true, 8);
    for (int t = 1; t < 11; t += 2) {
        STEP(sB, sA, t, true, true, true, true, 8);
        STEP(sA, sB, t + 1, true, true, true, true, 8);
    }
    STEP(sB, sA, 11, true, true, true, false, 8);
    STEP(sA, sB, 12, true, true, true, false, 6);
    STEP(sB, sA, 13, true, true, false, false, 4);
    STEP(sA, sB, 14, true, false, false, false, 2);
    STEP(sB, sA, 15, false, false, false, false, 0);
    TS(2)
    {
        const char* wp = (const char*)wo + (size_t)(256 * kvh + 16 * b) * DM * 2 + wid * 1024 + lane * 16;
        glds16(wp, lds0 + LDS_Q + wid * 1024);
        glds16(wp + 8192, lds0 + LDS_Q + 8192 + wid * 1024);
        if (wid < 2) glds16(wp + 16384, lds0 + LDS_Q + 16384 + wid * 1024);
    }
    l_run = swap_add(l_run);
    asm volatile("s_waitcnt lgkmcnt(0)\n\ts_barrier" ::: "memory");
    {
        f32x16 keep0, keep1, send0, send1;
        if (kh == 0) { keep0 = o[0]; keep1 = o[1]; send0 = o[2]; send1 = o[3]; }
        else         { keep0 = o[2]; keep1 = o[3]; send0 = o[0]; send1 = o[1]; }
        const unsigned xb = lds0 + wid * 9216;
#pragma unroll
        for (int r4 = 0; r4 < 4; ++r4) {
            f32x4 v0 = {send0[4 * r4], send0[4 * r4 + 1], send0[4 * r4 + 2], send0[4 * r4 + 3]};
            f32x4 v1 = {send1[4 * r4], send1[4 * r4 + 1], send1[4 * r4 + 2], send1[4 * r4 + 3]};
            *(LAS f32x4*)(uintptr_t)(xb + r4 * 1024 + lane * 16) = v0;
            *(LAS f32x4*)(uintptr_t)(xb + (4 + r4) * 1024 + lane * 16) = v1;
        }
        *(LAS float*)(uintptr_t)(xb + 8192 + lane * 4) = m_run;
        *(LAS float*)(uintptr_t)(xb + 8192 + 256 + lane * 4) = l_run;
        asm volatile("s_waitcnt lgkmcnt(0)\n\ts_barrier" ::: "memory");
        const unsigned pb = lds0 + (wid ^ 4) * 9216;
        const float m2 = *(const LAS float*)(uintptr_t)(pb + 8192 + lane * 4);
        const float l2 = *(const LAS float*)(uintptr_t)(pb + 8192 + 256 + lane * 4);
        const float mm = __builtin_fmaxf(m_run, m2);
        const float a1 = __builtin_amdgcn_exp2f(m_run - mm), a2 = __builtin_amdgcn_exp2f(m2 - mm);
        const float inv = 1.0f / (l_run * a1 + l2 * a2);
        const float w1 = a1 * inv, w2 = a2 * inv;
        const int row = b * SQ + 32 * (qb & 1) + r32;
        char* ob = (char*)Og + ((size_t)((row >> 4) * 128 + head * 4 + 2 * kh) * 64 + hi * 16 + (row & 15)) * 16;
#define OSTORE(KEEP, i) do { u32x2 w_[4]; \
            _Pragma("unroll") for (int r4 = 0; r4 < 4; ++r4) { \
                const f32x4 v = *(const LAS f32x4*)(uintptr_t)(pb + (4 * (i) + r4) * 1024 + lane * 16); \
                const float e0 = KEEP[4 * r4] * w1 + v[0] * w2, e1 = KEEP[4 * r4 + 1] * w1 + v[1] * w2; \
                const float e2 = KEEP[4 * r4 + 2] * w1 + v[2] * w2, e3 = KEEP[4 * r4 + 3] * w1 + v[3] * w2; \
                w_[r4] = u32x2{cvtpk(e0, e1), cvtpk(e2, e3)}; } \
            _Pragma("unroll") for (int g = 0; g < 4; g += 2) { \
                auto rx = __builtin_amdgcn_permlane32_swap(w_[g].x, w_[g + 1].x, false, false); \
                auto ry = __builtin_amdgcn_permlane32_swap(w_[g].y, w_[g + 1].y, false, false); \
                u32x4 o4 = {rx[0], ry[0], rx[1], ry[1]}; \
                *(u32x4*)(ob + (i) * 1024 + g * 256) = o4; } } while (0)
        OSTORE(keep0, 0); OSTORE(keep1, 1);
#undef OSTORE
    }
    TS(3)
    TS_STORE
}

constexpr int LDS2_BYTES = 512 * 320;
#define MFMA16(a, b, c) __builtin_amdgcn_mfma_f32_16x16x32_bf16(a, b, c, 0, 0, 0)
__global__ __launch_bounds__(512, 2) void oproj_kernel(const u16* __restrict__ Og, const u16* __restrict__ wo,
                                                       float* __restrict__ part) {
    extern __shared__ __attribute__((aligned(16))) char lds[];
    const unsigned lds0 = (unsigned)(uintptr_t)lds;
    const int tid = threadIdx.x, lane = tid & 63;
    const int wid = __builtin_amdgcn_readfirstlane(tid >> 6);
    const int rb = wid & 3, kp = wid >> 2;
    const int bid = blockIdx.x;
    const int sk = bid & 7, nt = (bid >> 3) & 3, mt = bid >> 5;
    const char* Bg[5];
#pragma unroll
    for (int i = 0; i < 5; ++i) {
        const int L = i * 1024 + lane * 16;
        const int row = L / 320, cp = (L % 320) >> 4;
        int c = cp ^ (((row >> 3) & 1) << 1);
        c = c < 18 ? c : 0;
        Bg[i] = (const char*)wo + ((size_t)(256 * sk + 16 * wid + row) * DM + 144 * nt) * 2 + c * 16;
    }
    const char* Ag = (const char*)Og + ((size_t)((8 * mt + 2 * rb) * 128 + 8 * sk + kp) * 64 + lane) * 16;
    u32x4 af[16];
#define K2_ISSUE(c) do { \
        _Pragma("unroll") for (int i = 0; i < 5; ++i) glds16(Bg[i] + (size_t)(((c) >> 1) * 2048 + ((c) & 1) * 128) * DM * 2, lds0 + (40 * (c) + 5 * wid + i) * 1024); \
        _Pragma("unroll") for (int i = 0; i < 4; ++i) \
            asm volatile("global_load_dwordx4 %0, %1, off" : "=v"(af[4 * (c) + i]) : "v"(Ag + (size_t)(i & 1) * 131072 + (((c) >> 1) * 64 + ((c) & 1) * 4 + 2 * (i >> 1)) * 1024) : "memory"); } while (0)
    K2_ISSUE(0); K2_ISSUE(1);
    const int gi = lane >> 4, vq = (lane & 15) >> 2, vp = lane & 3;
    const unsigned bb = lds0 + (32 * kp + 8 * gi + vq) * 320 + 8 * vp;
    const unsigned bbE = bb + 32 * (gi & 1), bbO = bb - 32 * (gi & 1);
    f32x4 acc[2][9];
#pragma unroll
    for (int ct = 0; ct < 9; ++ct) { acc[0][ct] = f32x4{0.f, 0.f, 0.f, 0.f}; acc[1][ct] = f32x4{0.f, 0.f, 0.f, 0.f}; }
#pragma unroll
    for (int c = 0; c < 4; ++c) {
        if (c < 3) asm volatile("s_waitcnt vmcnt(9)\n\ts_barrier" : "+v"(af[4 * c]), "+v"(af[4 * c + 1]), "+v"(af[4 * c + 2]), "+v"(af[4 * c + 3]) :: "memory");
        else asm volatile("s_waitcnt vmcnt(0)\n\ts_barrier" : "+v"(af[12]), "+v"(af[13]), "+v"(af[14]), "+v"(af[15]) :: "memory");
        if (c + 2 < 4) K2_ISSUE(c + 2);
#pragma unroll
        for (int j = 0; j < 2; ++j) {
            const bf16x8 a0 = __builtin_bit_cast(bf16x8, af[4 * c + 2 * j]), a1 = __builtin_bit_cast(bf16x8, af[4 * c + 2 * j + 1]);
#pragma unroll
            for (int ct = 0; ct < 9; ++ct) {
                const unsigned ad = ((ct & 1) ? bbO : bbE) + (4 * c + 2 * j) * 10240 + 32 * ct;
                const s16x4 lo = vtr(ad), h4 = vtr(ad + 1280);
                const bf16x8 bf = {lo[0], lo[1], lo[2], lo[3], h4[0], h4[1], h4[2], h4[3]};
                acc[0][ct] = MFMA16(bf, a0, acc[0][ct]);
                acc[1][ct] = MFMA16(bf, a1, acc[1][ct]);
            }
        }
    }
#undef K2_ISSUE
    asm volatile("s_waitcnt lgkmcnt(0)\n\ts_barrier" ::: "memory");
#pragma unroll
    for (int ct = 0; ct < 9; ++ct)
        *(LAS f32x4*)(uintptr_t)(lds0 + (wid * 9 + ct) * 1024 + lane * 16) = kp ? acc[0][ct] : acc[1][ct];
    asm volatile("s_waitcnt lgkmcnt(0)\n\ts_barrier" ::: "memory");
    float* pp = part + ((size_t)(sk * 64 + 8 * mt + 2 * rb + kp) * 36 + 9 * nt) * 256 + lane * 4;
#pragma unroll
    for (int ct = 0; ct < 9; ++ct) {
        const f32x4 v = *(const LAS f32x4*)(uintptr_t)(lds0 + ((wid ^ 4) * 9 + ct) * 1024 + lane * 16);
        *(f32x4*)(pp + ct * 256) = (kp ? acc[1][ct] : acc[0][ct]) + v;
    }
}

__global__ __launch_bounds__(256) void reduce_kernel(const float* __restrict__ part, u16* __restrict__ out) {
    const int lane = threadIdx.x & 63;
    const int u = blockIdx.x * 4 + (threadIdx.x >> 6);
    const float* p = part + (size_t)u * 256 + lane * 4;
    f32x4 t = *(const f32x4*)p;
#pragma unroll
    for (int s = 1; s < 8; ++s) t += *(const f32x4*)(p + (size_t)s * 64 * 36 * 256);
    const int rt = u / 36, ct = u % 36;
    u32x2 w = {cvtpk(t[0], t[1]), cvtpk(t[2], t[3])};
    *(u32x2*)(out + (size_t)(16 * rt + (lane & 15)) * DM + 16 * ct + 4 * (lane >> 4)) = w;
}

extern "C" void kernel_launch(void* const* d_in, const int* in_sizes, int n_in, void* d_out, int out_size,
                              void* d_ws, size_t ws_size, hipStream_t stream) {
    const u16* x = (const u16*)d_in[0];
    const u16* kc = (const u16*)d_in[1];
    const u16* vc = (const u16*)d_in[2];
    const u16* wq = (const u16*)d_in[3];
    const u16* wo = (const u16*)d_in[4];
    u16* Og = (u16*)d_ws;
    float* part = (float*)((char*)d_ws + (size_t)NB * SQ * NQ * 2);
    (void)hipFuncSetAttribute((const void*)attn_kernel, hipFuncAttributeMaxDynamicSharedMemorySize, LDS1_BYTES);
    (void)hipFuncSetAttribute((const void*)oproj_kernel, hipFuncAttributeMaxDynamicSharedMemorySize, LDS2_BYTES);
    attn_kernel<<<NB * HKV, 512, LDS1_BYTES, stream>>>(x, kc, vc, wq, Og, wo);
    oproj_kernel<<<256, 512, LDS2_BYTES, stream>>>(Og, wo, part);
    reduce_kernel<<<576, 256, 0, stream>>>(part, (u16*)d_out);
}
```

```cpp
#include <hip/hip_runtime.h>
#include <hip/hip_bf16.h>
#include <stdint.h>

typedef short bf16x8 __attribute__((ext_vector_type(8)));
typedef short s16x4 __attribute__((ext_vector_type(4)));
typedef float f32x16 __attribute__((ext_vector_type(16)));
typedef float f32x4 __attribute__((ext_vector_type(4)));
typedef unsigned u32x4 __attribute__((ext_vector_type(4)));
typedef unsigned u32x2 __attribute__((ext_vector_type(2)));
typedef unsigned short u16;
#define LAS __attribute__((address_space(3)))
typedef LAS const char* lds_cptr;

constexpr int NB = 16, SQ = 64, DM = 576, HQ = 32, HKV = 16, SKV = 1024, DH = 128;
constexpr int NQ = HQ * DH;
constexpr int NT = SKV / 64;
constexpr int SLOT = 32768;
constexpr int LDS_Q = 4 * SLOT;
constexpr int LDS1_BYTES = 5 * SLOT;
constexpr float RTHR = 8.0f;
constexpr float C2 = 0.088388347648318447f * 1.4426950408889634f;

#define MFMA32(a, b, c) __builtin_amdgcn_mfma_f32_32x32x16_bf16(a, b, c, 0, 0, 0)

typedef __bf16 bf16x2_t __attribute__((ext_vector_type(2)));
typedef float f32x2 __attribute__((ext_vector_type(2)));
__device__ __forceinline__ unsigned cvtpk(float lo, float hi) {
    f32x2 v = {lo, hi};
    return __builtin_bit_cast(unsigned, __builtin_convertvector(v, bf16x2_t));
}
__device__ __forceinline__ void glds16(const void* g, unsigned lds_base) {
    unsigned sv;
    asm volatile("s_mov_b32 %0, m0\n\ts_mov_b32 m0, %2\n\ts_nop 0\n\tglobal_load_lds_dwordx4 %1, off\n\ts_mov_b32 m0, %0"
                 : "=&s"(sv) : "v"(g), "s"(lds_base) : "memory");
}
__device__ __forceinline__ void glds16nt(const void* g, unsigned lds_base) {
    unsigned sv;
    asm volatile("s_mov_b32 %0, m0\n\ts_mov_b32 m0, %2\n\ts_nop 0\n\tglobal_load_lds_dwordx4 %1, off nt\n\ts_mov_b32 m0, %0"
                 : "=&s"(sv) : "v"(g), "s"(lds_base) : "memory");
}
#define TS_DECL
#define TS(i)
#define TS_STORE
__device__ __forceinline__ s16x4 vtr(unsigned a) {
    return __builtin_amdgcn_ds_read_tr16_b64_v4i16((LAS s16x4*)(uintptr_t)a);
}
__device__ __forceinline__ bf16x8 ldsr128(unsigned a) { return *(const LAS bf16x8*)(uintptr_t)a; }
__device__ __forceinline__ float swap_max(float v) {
    auto rr = __builtin_amdgcn_permlane32_swap(__float_as_uint(v), __float_as_uint(v), false, false);
    return __builtin_fmaxf(__uint_as_float(rr[0]), __uint_as_float(rr[1]));
}
__device__ __forceinline__ float swap_add(float v) {
    auto rr = __builtin_amdgcn_permlane32_swap(__float_as_uint(v), __float_as_uint(v), false, false);
    return __uint_as_float(rr[0]) + __uint_as_float(rr[1]);
}

constexpr int XIMG = 0;
constexpr int WRING = 73728;
TS_DECL
__global__ __launch_bounds__(512, 2) void attn_kernel(const u16* __restrict__ x, const u16* __restrict__ Kc,
                                                      const u16* __restrict__ Vc, const u16* __restrict__ wq,
                                                      u16* __restrict__ Og, const u16* __restrict__ wo) {
    extern __shared__ __attribute__((aligned(16))) char lds[];
    TS(0)
    const unsigned lds0 = (unsigned)(uintptr_t)lds;
    const int tid = threadIdx.x, lane = tid & 63, r32 = lane & 31, hi = lane >> 5;
    const int wid = __builtin_amdgcn_readfirstlane(tid >> 6);
    const int qb = wid & 3, kh = wid >> 2;
    const int bid = blockIdx.x, kvh = bid & 15, b = bid >> 4;
    const int head = 2 * kvh + (qb >> 1);
    const int gi = lane >> 4, vq = (lane & 15) >> 2, vp = lane & 3;

    const unsigned lane_off = gi * 256 + ((((lane & 15) ^ ((gi << 2) | (wid & 3)))) << 4);
    const char* Kg = (const char*)Kc + (size_t)(b * HKV + kvh) * SKV * DH * 2 + lane_off;
    const char* Vg = (const char*)Vc + (size_t)(b * HKV + kvh) * SKV * DH * 2 + lane_off;

    bf16x8 qf[8];
    {
        u32x4 kv0, kv1, kv2, kv3, kv4, kv5, kv6, kv7;
        const int rb = b % 9;
#define KBR(j) (((j) + rb) % 9)
#define KROT(ks) (4 * KBR((ks) >> 2) + ((ks) & 3))
        const char* Xg = (const char*)x + ((size_t)b * SQ + 8 * wid + (lane >> 3)) * DM * 2 + (((lane & 7) ^ ((4 * wid + (lane >> 4)) & 7)) << 4);
#define X_ISSUE(j) glds16(Xg + KBR(j) * 128, lds0 + XIMG + (j) * 8192 + wid * 1024)
        const char* Wg = (const char*)wq + ((size_t)(lane >> 2) * NQ + kvh * 256 + wid * 32) * 2 + ((lane & 3) << 4);
        const unsigned wr = lds0 + WRING + wid * 8192;
#define WQ_ISSUE(ks) glds16(Wg + (size_t)KROT(ks) * 16 * NQ * 2, wr + ((ks) & 7) * 1024)
        X_ISSUE(0); X_ISSUE(1); X_ISSUE(2);
#pragma unroll
        for (int ks = 0; ks < 7; ++ks) WQ_ISSUE(ks);
#pragma unroll
        for (int j = 3; j < 9; ++j) X_ISSUE(j);
        const int pp = ((vp & 1) << 1) | (vp >> 1);
        const unsigned abase = wr + (8 * hi + vq) * 64 + (2 * (gi & 1) + (pp >> 1)) * 16 + 8 * (pp & 1);
        const unsigned xbase = lds0 + XIMG + r32 * 128 + ((hi ^ ((r32 >> 1) & 7)) << 4);
        f32x16 acc0 = {}, acc1 = {};
        asm volatile("s_waitcnt vmcnt(13)\n\ts_barrier" ::: "memory");
#define QSTEP(ks, slot, WAITS, ISSUE) do { \
            WAITS; \
            const unsigned a = abase + (slot) * 1024; \
            const s16x4 lo0 = vtr(a), hi0 = vtr(a + 256); \
            const unsigned xa = (xbase ^ (((ks) & 3) << 5)) + ((ks) >> 2) * 8192; \
            const bf16x8 x0 = ldsr128(xa), x1 = ldsr128(xa + 4096); \
            ISSUE; \
            const bf16x8 a0 = {lo0[0], lo0[1], lo0[2], lo0[3], hi0[0], hi0[1], hi0[2], hi0[3]}; \
            acc0 = MFMA32(a0, x0, acc0); \
            acc1 = MFMA32(a0, x1, acc1); } while (0)
#pragma unroll
        for (int ks = 0; ks < 8; ++ks) {
            if (ks < 7) QSTEP(ks, ks, asm volatile("s_waitcnt vmcnt(12) lgkmcnt(0)" ::: "memory"), WQ_ISSUE(ks + 7));
            else QSTEP(ks, ks, asm volatile("s_waitcnt vmcnt(6) lgkmcnt(0)" ::: "memory"), WQ_ISSUE(ks + 7));
        }
#pragma unroll 1
        for (int k4 = 8; k4 < 28; k4 += 4) {
            if (k4 == 12) asm volatile("s_barrier" ::: "memory");
#pragma unroll
            for (int i = 0; i < 4; ++i)
                QSTEP(k4 + i, (k4 + i) & 7, asm volatile("s_waitcnt vmcnt(6) lgkmcnt(0)" ::: "memory"), WQ_ISSUE(k4 + i + 7));
        }
#define KVLD(r, ptr) asm volatile("global_load_dwordx4 %0, %1, off nt" : "=v"(r) : "v"(ptr) : "memory")
        QSTEP(28, 4, asm volatile("s_waitcnt vmcnt(6) lgkmcnt(0)" ::: "memory"), WQ_ISSUE(35));
        QSTEP(29, 5, asm volatile("s_waitcnt vmcnt(6) lgkmcnt(0)" ::: "memory"),
              KVLD(kv0, Kg + wid * 1024); KVLD(kv1, Kg + (wid + 8) * 1024);
              KVLD(kv2, Vg + wid * 1024); KVLD(kv3, Vg + (wid + 8) * 1024);
              KVLD(kv4, Kg + 16384 + wid * 1024); KVLD(kv5, Kg + 16384 + (wid + 8) * 1024);
              KVLD(kv6, Kg + 32768 + wid * 1024); KVLD(kv7, Kg + 32768 + (wid + 8) * 1024));
#undef KVLD
        QSTEP(30, 6, asm volatile("s_waitcnt vmcnt(13)" ::: "memory"), (void)0);
        QSTEP(31, 7, asm volatile("s_waitcnt vmcnt(12)" ::: "memory"), (void)0);
        QSTEP(32, 0, asm volatile("s_waitcnt vmcnt(11)" ::: "memory"), (void)0);
        QSTEP(33, 1, asm volatile("s_waitcnt vmcnt(10)" ::: "memory"), (void)0);
        QSTEP(34, 2, asm volatile("s_waitcnt vmcnt(9)" ::: "memory"), (void)0);
        QSTEP(35, 3, asm volatile("s_waitcnt vmcnt(8)" ::: "memory"), (void)0);
#undef QSTEP
#undef X_ISSUE
#undef KBR
#undef WQ_ISSUE
#undef KROT
        asm volatile("s_waitcnt lgkmcnt(0)\n\ts_barrier" ::: "memory");
#pragma unroll
        for (int u = 1; u < 3; ++u) {
            glds16nt(Vg + u * 16384 + wid * 1024, lds0 + u * SLOT + 16384 + wid * 1024);
            glds16nt(Vg + u * 16384 + (wid + 8) * 1024, lds0 + u * SLOT + 16384 + (wid + 8) * 1024);
            if (u == 1) {
                glds16nt(Kg + (u + 2) * 16384 + wid * 1024, lds0 + ((u + 2) & 3) * SLOT + wid * 1024);
                glds16nt(Kg + (u + 2) * 16384 + (wid + 8) * 1024, lds0 + ((u + 2) & 3) * SLOT + (wid + 8) * 1024);
            }
        }
#define QW(ACC, qblk) do { _Pragma("unroll") for (int s = 0; s < 2; ++s) { \
            u32x4 w_ = {cvtpk(ACC[8 * s], ACC[8 * s + 1]), cvtpk(ACC[8 * s + 2], ACC[8 * s + 3]), \
                        cvtpk(ACC[8 * s + 4], ACC[8 * s + 5]), cvtpk(ACC[8 * s + 6], ACC[8 * s + 7])}; \
            *(LAS u32x4*)(uintptr_t)(lds0 + LDS_Q + (wid >> 2) * 16384 + (qblk) * 8192 + (2 * (wid & 3) + s) * 1024 + lane * 16) = w_; } } while (0)
        QW(acc0, 0); QW(acc1, 1);
#undef QW
        asm volatile("s_waitcnt vmcnt(6)" : "+v"(kv0), "+v"(kv1), "+v"(kv2), "+v"(kv3), "+v"(kv4), "+v"(kv5), "+v"(kv6), "+v"(kv7) :: "memory");
        const unsigned kw = lds0 + wid * 1024 + lane * 16;
        *(LAS u32x4*)(uintptr_t)(kw) = kv0;                 *(LAS u32x4*)(uintptr_t)(kw + 8192) = kv1;
        *(LAS u32x4*)(uintptr_t)(kw + 16384) = kv2;         *(LAS u32x4*)(uintptr_t)(kw + 16384 + 8192) = kv3;
        *(LAS u32x4*)(uintptr_t)(kw + SLOT) = kv4;          *(LAS u32x4*)(uintptr_t)(kw + SLOT + 8192) = kv5;
        *(LAS u32x4*)(uintptr_t)(kw + 2 * SLOT) = kv6;      *(LAS u32x4*)(uintptr_t)(kw + 2 * SLOT + 8192) = kv7;
    }
    asm volatile("s_waitcnt lgkmcnt(0)\n\ts_barrier" ::: "memory");
#pragma unroll
    for (int f = 0; f < 8; ++f) qf[f] = ldsr128(lds0 + LDS_Q + qb * 8192 + f * 1024 + lane * 16);
    TS(1)

    const unsigned kbase = (32 * kh + r32) * 256 + ((((r32 & 3) << 2) | (hi ^ ((r32 >> 2) & 3))) << 4);
    const unsigned vbase = 16384 + (32 * kh + 4 * hi + vq) * 256 + (((vq << 2) | ((2 * (gi & 1) + (vp >> 1)) ^ hi)) << 4) + 8 * (vp & 1);

    float m_run = -__builtin_inff(), l_run = 0.f;
    f32x16 o[4] = {};
    f32x16 sA, sB;

#define DMA_V(tn) do { const unsigned sl_ = lds0 + ((tn) & 3) * SLOT + 16384; \
            glds16nt(Vg + (tn) * 16384 + wid * 1024, sl_ + wid * 1024); \
            glds16nt(Vg + (tn) * 16384 + (wid + 8) * 1024, sl_ + (wid + 8) * 1024); } while (0)
#define DMA_K(tn) do { const unsigned sl_ = lds0 + ((tn) & 3) * SLOT; \
            glds16nt(Kg + (tn) * 16384 + wid * 1024, sl_ + wid * 1024); \
            glds16nt(Kg + (tn) * 16384 + (wid + 8) * 1024, sl_ + (wid + 8) * 1024); } while (0)
    bf16x8 kf[8];
    {
#pragma unroll
        for (int d0 = 0; d0 < 8; ++d0) kf[d0] = ldsr128(lds0 + (kbase ^ (d0 << 5)));
        sA = f32x16{};
#pragma unroll
        for (int d0 = 0; d0 < 8; ++d0) sA = MFMA32(kf[d0], qf[d0], sA);
#pragma unroll
        for (int d0 = 0; d0 < 8; ++d0) kf[d0] = ldsr128(lds0 + SLOT + (kbase ^ (d0 << 5)));
        asm volatile("s_waitcnt lgkmcnt(0)\n\ts_barrier" ::: "memory");
        DMA_K(4);
    }
#pragma unroll 1
    for (int t = 0; t < NT; ++t) {
        s16x4 vlo[8], vhi[8];
        if (t < 12) asm volatile("s_waitcnt vmcnt(8)" ::: "memory");
        else if (t == 12) asm volatile("s_waitcnt vmcnt(6)" ::: "memory");
        else if (t == 13) asm volatile("s_waitcnt vmcnt(4)" ::: "memory");
        else if (t == 14) asm volatile("s_waitcnt vmcnt(2)" ::: "memory");
        else asm volatile("s_waitcnt vmcnt(0)" ::: "memory");
        asm volatile("s_waitcnt lgkmcnt(0)\n\ts_barrier" ::: "memory");
        float tmax = sA[0];
#pragma unroll
        for (int r = 1; r < 16; ++r) tmax = __builtin_fmaxf(tmax, sA[r]);
        tmax = swap_max(tmax) * C2;
        if (__any(tmax > m_run + RTHR)) {
            const float mnew = __builtin_fmaxf(m_run, tmax);
            const float alpha = __builtin_amdgcn_exp2f(m_run - mnew);
            m_run = mnew; l_run *= alpha;
#pragma unroll
            for (int dt = 0; dt < 4; ++dt)
#pragma unroll
                for (int r = 0; r < 16; ++r) o[dt][r] *= alpha;
        }
        __builtin_amdgcn_sched_barrier(0);
        const unsigned sl = lds0 + (t & 3) * SLOT;
#pragma unroll
        for (int ss = 0; ss < 2; ++ss)
#pragma unroll
            for (int dt = 0; dt < 4; ++dt) {
                vlo[2 * dt + ss] = vtr(sl + (vbase ^ (dt << 6)) + 4096 * ss);
                vhi[2 * dt + ss] = vtr(sl + (vbase ^ ((dt << 6) | (1 << 5))) + 4096 * ss + 2048);
            }
        sB = f32x16{};
#pragma unroll
        for (int d0 = 0; d0 < 8; ++d0) sB = MFMA32(kf[d0], qf[d0], sB);
        float ps = 0.f;
#pragma unroll
        for (int r = 0; r < 16; ++r) { sA[r] = __builtin_amdgcn_exp2f(__builtin_fmaf(sA[r], C2, -m_run)); ps += sA[r]; }
        l_run += ps;
        bf16x8 pf[2];
#pragma unroll
        for (int ss = 0; ss < 2; ++ss) {
            u32x4 w = {cvtpk(sA[8 * ss + 0], sA[8 * ss + 1]), cvtpk(sA[8 * ss + 2], sA[8 * ss + 3]),
                       cvtpk(sA[8 * ss + 4], sA[8 * ss + 5]), cvtpk(sA[8 * ss + 6], sA[8 * ss + 7])};
            pf[ss] = __builtin_bit_cast(bf16x8, w);
        }
        asm volatile("" : "+v"(pf[0]), "+v"(pf[1]), "+v"(l_run));
#pragma unroll
        for (int i = 0; i < 8; ++i) {
            __builtin_amdgcn_sched_group_barrier(0x008, 1, 0); __builtin_amdgcn_sched_group_barrier(0x100, 2, 0);
            __builtin_amdgcn_sched_group_barrier(0x002, 8, 0);
        }
        __builtin_amdgcn_sched_barrier(0);
        {
            const unsigned sk_ = lds0 + ((t + 2) & 3) * SLOT;
#pragma unroll
            for (int d0 = 0; d0 < 8; ++d0) kf[d0] = ldsr128(sk_ + (kbase ^ (d0 << 5)));
        }
#pragma unroll
        for (int ss = 0; ss < 2; ++ss)
#pragma unroll
            for (int dt = 0; dt < 4; ++dt) {
                const s16x4 lo = vlo[2 * dt + ss], hi4 = vhi[2 * dt + ss];
                const bf16x8 vf = {lo[0], lo[1], lo[2], lo[3], hi4[0], hi4[1], hi4[2], hi4[3]};
                o[dt] = MFMA32(vf, pf[ss], o[dt]);
            }
#pragma unroll
        for (int i = 0; i < 8; ++i) {
            __builtin_amdgcn_sched_group_barrier(0x008, 1, 0); __builtin_amdgcn_sched_group_barrier(0x100, 1, 0);
            __builtin_amdgcn_sched_group_barrier(0x002, 1, 0);
        }
        __builtin_amdgcn_sched_barrier(0);
        if (t < 13) DMA_V(t + 3);
        if (t < 11) DMA_K(t + 5);
        sA = sB;
    }
    TS(2)
    {
        const char* wp = (const char*)wo + (size_t)(256 * kvh + 16 * b) * DM * 2 + wid * 1024 + lane * 16;
        glds16(wp, lds0 + LDS_Q + wid * 1024);
        glds16(wp + 8192, lds0 + LDS_Q + 8192 + wid * 1024);
        if (wid < 2) glds16(wp + 16384, lds0 + LDS_Q + 16384 + wid * 1024);
    }
    l_run = swap_add(l_run);
    asm volatile("s_waitcnt lgkmcnt(0)\n\ts_barrier" ::: "memory");
    {
        f32x16 keep0, keep1, send0, send1;
        if (kh == 0) { keep0 = o[0]; keep1 = o[1]; send0 = o[2]; send1 = o[3]; }
        else         { keep0 = o[2]; keep1 = o[3]; send0 = o[0]; send1 = o[1]; }
        const unsigned xb = lds0 + wid * 9216;
#pragma unroll
        for (int r4 = 0; r4 < 4; ++r4) {
            f32x4 v0 = {send0[4 * r4], send0[4 * r4 + 1], send0[4 * r4 + 2], send0[4 * r4 + 3]};
            f32x4 v1 = {send1[4 * r4], send1[4 * r4 + 1], send1[4 * r4 + 2], send1[4 * r4 + 3]};
            *(LAS f32x4*)(uintptr_t)(xb + r4 * 1024 + lane * 16) = v0;
            *(LAS f32x4*)(uintptr_t)(xb + (4 + r4) * 1024 + lane * 16) = v1;
        }
        *(LAS float*)(uintptr_t)(xb + 8192 + lane * 4) = m_run;
        *(LAS float*)(uintptr_t)(xb + 8192 + 256 + lane * 4) = l_run;
        asm volatile("s_waitcnt lgkmcnt(0)\n\ts_barrier" ::: "memory");
        const unsigned pb = lds0 + (wid ^ 4) * 9216;
        const float m2 = *(const LAS float*)(uintptr_t)(pb + 8192 + lane * 4);
        const float l2 = *(const LAS float*)(uintptr_t)(pb + 8192 + 256 + lane * 4);
        const float mm = __builtin_fmaxf(m_run, m2);
        const float a1 = __builtin_amdgcn_exp2f(m_run - mm), a2 = __builtin_amdgcn_exp2f(m2 - mm);
        const float inv = 1.0f / (l_run * a1 + l2 * a2);
        const float w1 = a1 * inv, w2 = a2 * inv;
        const int row = b * SQ + 32 * (qb & 1) + r32;
        char* ob = (char*)Og + ((size_t)((row >> 4) * 128 + head * 4 + 2 * kh) * 64 + hi * 16 + (row & 15)) * 16;
#define OSTORE(KEEP, i) do { u32x2 w_[4]; \
            _Pragma("unroll") for (int r4 = 0; r4 < 4; ++r4) { \
                const f32x4 v = *(const LAS f32x4*)(uintptr_t)(pb + (4 * (i) + r4) * 1024 + lane * 16); \
                const float e0 = KEEP[4 * r4] * w1 + v[0] * w2, e1 = KEEP[4 * r4 + 1] * w1 + v[1] * w2; \
                const float e2 = KEEP[4 * r4 + 2] * w1 + v[2] * w2, e3 = KEEP[4 * r4 + 3] * w1 + v[3] * w2; \
                w_[r4] = u32x2{cvtpk(e0, e1), cvtpk(e2, e3)}; } \
            _Pragma("unroll") for (int g = 0; g < 4; g += 2) { \
                auto rx = __builtin_amdgcn_permlane32_swap(w_[g].x, w_[g + 1].x, false, false); \
                auto ry = __builtin_amdgcn_permlane32_swap(w_[g].y, w_[g + 1].y, false, false); \
                u32x4 o4 = {rx[0], ry[0], rx[1], ry[1]}; \
                *(u32x4*)(ob + (i) * 1024 + g * 256) = o4; } } while (0)
        OSTORE(keep0, 0); OSTORE(keep1, 1);
#undef OSTORE
    }
    TS(3)
    TS_STORE
}

constexpr int LDS2_BYTES = 512 * 320;
#define MFMA16(a, b, c) __builtin_amdgcn_mfma_f32_16x16x32_bf16(a, b, c, 0, 0, 0)
__global__ __launch_bounds__(512, 2) void oproj_kernel(const u16* __restrict__ Og, const u16* __restrict__ wo,
                                                       float* __restrict__ part) {
    extern __shared__ __attribute__((aligned(16))) char lds[];
    const unsigned lds0 = (unsigned)(uintptr_t)lds;
    const int tid = threadIdx.x, lane = tid & 63;
    const int wid = __builtin_amdgcn_readfirstlane(tid >> 6);
    const int rb = wid & 3, kp = wid >> 2;
    const int bid = blockIdx.x;
    const int sk = bid & 7, nt = (bid >> 3) & 3, mt = bid >> 5;
    const char* Bg[5];
#pragma unroll
    for (int i = 0; i < 5; ++i) {
        const int L = i * 1024 + lane * 16;
        const int row = L / 320, cp = (L % 320) >> 4;
        int c = cp ^ (((row >> 3) & 1) << 1);
        c = c < 18 ? c : 0;
        Bg[i] = (const char*)wo + ((size_t)(256 * sk + 16 * wid + row) * DM + 144 * nt) * 2 + c * 16;
    }
    const char* Ag = (const char*)Og + ((size_t)((8 * mt + 2 * rb) * 128 + 8 * sk + kp) * 64 + lane) * 16;
    u32x4 af[16];
#define K2_ISSUE(c) do { \
        _Pragma("unroll") for (int i = 0; i < 5; ++i) glds16(Bg[i] + (size_t)(((c) >> 1) * 2048 + ((c) & 1) * 128) * DM * 2, lds0 + (40 * (c) + 5 * wid + i) * 1024); \
        _Pragma("unroll") for (int i = 0; i < 4; ++i) \
            asm volatile("global_load_dwordx4 %0, %1, off" : "=v"(af[4 * (c) + i]) : "v"(Ag + (size_t)(i & 1) * 131072 + (((c) >> 1) * 64 + ((c) & 1) * 4 + 2 * (i >> 1)) * 1024) : "memory"); } while (0)
    K2_ISSUE(0); K2_ISSUE(1);
    const int gi = lane >> 4, vq = (lane & 15) >> 2, vp = lane & 3;
    const unsigned bb = lds0 + (32 * kp + 8 * gi + vq) * 320 + 8 * vp;
    const unsigned bbE = bb + 32 * (gi & 1), bbO = bb - 32 * (gi & 1);
    f32x4 acc[2][9];
#pragma unroll
    for (int ct = 0; ct < 9; ++ct) { acc[0][ct] = f32x4{0.f, 0.f, 0.f, 0.f}; acc[1][ct] = f32x4{0.f, 0.f, 0.f, 0.f}; }
#pragma unroll
    for (int c = 0; c < 4; ++c) {
        if (c < 3) asm volatile("s_waitcnt vmcnt(9)\n\ts_barrier" : "+v"(af[4 * c]), "+v"(af[4 * c + 1]), "+v"(af[4 * c + 2]), "+v"(af[4 * c + 3]) :: "memory");
        else asm volatile("s_waitcnt vmcnt(0)\n\ts_barrier" : "+v"(af[12]), "+v"(af[13]), "+v"(af[14]), "+v"(af[15]) :: "memory");
        if (c + 2 < 4) K2_ISSUE(c + 2);
#pragma unroll
        for (int j = 0; j < 2; ++j) {
            const bf16x8 a0 = __builtin_bit_cast(bf16x8, af[4 * c + 2 * j]), a1 = __builtin_bit_cast(bf16x8, af[4 * c + 2 * j + 1]);
#pragma unroll
            for (int ct = 0; ct < 9; ++ct) {
                const unsigned ad = ((ct & 1) ? bbO : bbE) + (4 * c + 2 * j) * 10240 + 32 * ct;
                const s16x4 lo = vtr(ad), h4 = vtr(ad + 1280);
                const bf16x8 bf = {lo[0], lo[1], lo[2], lo[3], h4[0], h4[1], h4[2], h4[3]};
                acc[0][ct] = MFMA16(bf, a0, acc[0][ct]);
                acc[1][ct] = MFMA16(bf, a1, acc[1][ct]);
            }
        }
    }
#undef K2_ISSUE
    asm volatile("s_waitcnt lgkmcnt(0)\n\ts_barrier" ::: "memory");
#pragma unroll
    for (int ct = 0; ct < 9; ++ct)
        *(LAS f32x4*)(uintptr_t)(lds0 + (wid * 9 + ct) * 1024 + lane * 16) = kp ? acc[0][ct] : acc[1][ct];
    asm volatile("s_waitcnt lgkmcnt(0)\n\ts_barrier" ::: "memory");
    float* pp = part + ((size_t)(sk * 64 + 8 * mt + 2 * rb + kp) * 36 + 9 * nt) * 256 + lane * 4;
#pragma unroll
    for (int ct = 0; ct < 9; ++ct) {
        const f32x4 v = *(const LAS f32x4*)(uintptr_t)(lds0 + ((wid ^ 4) * 9 + ct) * 1024 + lane * 16);
        *(f32x4*)(pp + ct * 256) = (kp ? acc[1][ct] : acc[0][ct]) + v;
    }
}

__global__ __launch_bounds__(256) void reduce_kernel(const float* __restrict__ part, u16* __restrict__ out) {
    const int lane = threadIdx.x & 63;
    const int u = blockIdx.x * 4 + (threadIdx.x >> 6);
    const float* p = part + (size_t)u * 256 + lane * 4;
    f32x4 t = *(const f32x4*)p;
#pragma unroll
    for (int s = 1; s < 8; ++s) t += *(const f32x4*)(p + (size_t)s * 64 * 36 * 256);
    const int rt = u / 36, ct = u % 36;
    u32x2 w = {cvtpk(t[0], t[1]), cvtpk(t[2], t[3])};
    *(u32x2*)(out + (size_t)(16 * rt + (lane & 15)) * DM + 16 * ct + 4 * (lane >> 4)) = w;
}

extern "C" void kernel_launch(void* const* d_in, const int* in_sizes, int n_in, void* d_out, int out_size,
                              void* d_ws, size_t ws_size, hipStream_t stream) {
    const u16* x = (const u16*)d_in[0];
    const u16* kc = (const u16*)d_in[1];
    const u16* vc = (const u16*)d_in[2];
    const u16* wq = (const u16*)d_in[3];
    const u16* wo = (const u16*)d_in[4];
    u16* Og = (u16*)d_ws;
    float* part = (float*)((char*)d_ws + (size_t)NB * SQ * NQ * 2);
    (void)hipFuncSetAttribute((const void*)attn_kernel, hipFuncAttributeMaxDynamicSharedMemorySize, LDS1_BYTES);
    (void)hipFuncSetAttribute((const void*)oproj_kernel, hipFuncAttributeMaxDynamicSharedMemorySize, LDS2_BYTES);
    attn_kernel<<<NB * HKV, 512, LDS1_BYTES, stream>>>(x, kc, vc, wq, Og, wo);
    oproj_kernel<<<256, 512, LDS2_BYTES, stream>>>(Og, wo, part);
    reduce_kernel<<<576, 256, 0, stream>>>(part, (u16*)d_out);
}
```

```cpp
#include <hip/hip_runtime.h>
#include <hip/hip_bf16.h>
#include <stdint.h>

typedef short bf16x8 __attribute__((ext_vector_type(8)));
typedef short s16x4 __attribute__((ext_vector_type(4)));
typedef float f32x16 __attribute__((ext_vector_type(16)));
typedef float f32x4 __attribute__((ext_vector_type(4)));
typedef unsigned u32x4 __attribute__((ext_vector_type(4)));
typedef unsigned u32x2 __attribute__((ext_vector_type(2)));
typedef unsigned short u16;
#define LAS __attribute__((address_space(3)))
typedef LAS const char* lds_cptr;

constexpr int NB = 16, SQ = 64, DM = 576, HQ = 32, HKV = 16, SKV = 1024, DH = 128;
constexpr int NQ = HQ * DH;
constexpr int NT = SKV / 64;
constexpr int SLOT = 32768;
constexpr int LDS_Q = 4 * SLOT;
constexpr int LDS1_BYTES = 5 * SLOT;
constexpr float RTHR = 8.0f;
constexpr float C2 = 0.088388347648318447f * 1.4426950408889634f;

#define MFMA32(a, b, c) __builtin_amdgcn_mfma_f32_32x32x16_bf16(a, b, c, 0, 0, 0)

typedef __bf16 bf16x2_t __attribute__((ext_vector_type(2)));
typedef float f32x2 __attribute__((ext_vector_type(2)));
__device__ __forceinline__ unsigned cvtpk(float lo, float hi) {
    f32x2 v = {lo, hi};
    return __builtin_bit_cast(unsigned, __builtin_convertvector(v, bf16x2_t));
}
__device__ __forceinline__ void glds16(const void* g, unsigned lds_base) {
    unsigned sv;
    asm volatile("s_mov_b32 %0, m0\n\ts_mov_b32 m0, %2\n\ts_nop 0\n\tglobal_load_lds_dwordx4 %1, off\n\ts_mov_b32 m0, %0"
                 : "=&s"(sv) : "v"(g), "s"(lds_base) : "memory");
}
__device__ __forceinline__ void glds16nt(const void* g, unsigned lds_base) {
    unsigned sv;
    asm volatile("s_mov_b32 %0, m0\n\ts_mov_b32 m0, %2\n\ts_nop 0\n\tglobal_load_lds_dwordx4 %1, off nt\n\ts_mov_b32 m0, %0"
                 : "=&s"(sv) : "v"(g), "s"(lds_base) : "memory");
}
#define TS_DECL
#define TS(i)
#define TS_STORE
__device__ __forceinline__ s16x4 vtr(unsigned a) {
    return __builtin_amdgcn_ds_read_tr16_b64_v4i16((LAS s16x4*)(uintptr_t)a);
}
__device__ __forceinline__ bf16x8 ldsr128(unsigned a) { return *(const LAS bf16x8*)(uintptr_t)a; }
__device__ __forceinline__ float swap_max(float v) {
    auto rr = __builtin_amdgcn_permlane32_swap(__float_as_uint(v), __float_as_uint(v), false, false);
    return __builtin_fmaxf(__uint_as_float(rr[0]), __uint_as_float(rr[1]));
}
__device__ __forceinline__ float swap_add(float v) {
    auto rr = __builtin_amdgcn_permlane32_swap(__float_as_uint(v), __float_as_uint(v), false, false);
    return __uint_as_float(rr[0]) + __uint_as_float(rr[1]);
}

constexpr int XIMG = 0;
constexpr int WRING = 73728;
TS_DECL
__global__ __launch_bounds__(512, 2) void attn_kernel(const u16* __restrict__ x, const u16* __restrict__ Kc,
                                                      const u16* __restrict__ Vc, const u16* __restrict__ wq,
                                                      u16* __restrict__ Og, const u16* __restrict__ wo) {
    extern __shared__ __attribute__((aligned(16))) char lds[];
    TS(0)
    const unsigned lds0 = (unsigned)(uintptr_t)lds;
    const int tid = threadIdx.x, lane = tid & 63, r32 = lane & 31, hi = lane >> 5;
    const int wid = __builtin_amdgcn_readfirstlane(tid >> 6);
    const int qb = wid & 3, kh = wid >> 2;
    const int bid = blockIdx.x, kvh = bid & 15, b = bid >> 4;
    const int head = 2 * kvh + (qb >> 1);
    const int gi = lane >> 4, vq = (lane & 15) >> 2, vp = lane & 3;

    const unsigned lane_off = gi * 256 + ((((lane & 15) ^ ((gi << 2) | (wid & 3)))) << 4);
    const char* Kg = (const char*)Kc + (size_t)(b * HKV + kvh) * SKV * DH * 2 + lane_off;
    const char* Vg = (const char*)Vc + (size_t)(b * HKV + kvh) * SKV * DH * 2 + lane_off;

    bf16x8 qf[8];
    {
        u32x4 kv0, kv1, kv2, kv3, kv4, kv5, kv6, kv7;
        const int rb = b % 9;
        const char* Xg = (const char*)x + ((size_t)b * SQ + 8 * wid + (lane >> 3)) * DM * 2 + (((lane & 7) ^ ((4 * wid + (lane >> 4)) & 7)) << 4);
        {
            int kb = rb;
#pragma unroll 1
            for (int j = 0; j < 9; ++j) {
                glds16(Xg + kb * 128, lds0 + XIMG + j * 8192 + wid * 1024);
                kb = kb == 8 ? 0 : kb + 1;
            }
        }
        const char* Wg = (const char*)wq + ((size_t)(lane >> 2) * NQ + kvh * 256 + wid * 32) * 2 + ((lane & 3) << 4);
        const unsigned wr = lds0 + WRING + wid * 8192;
        int woff = rb * 64 * NQ * 2;
        unsigned wslot = 0;
#define WQ_ISSUE() do { glds16(Wg + woff, wr + wslot); woff += 16 * NQ * 2; woff = woff == DM * NQ * 2 ? 0 : woff; wslot = (wslot + 1024) & 7168; } while (0)
#pragma unroll 1
        for (int ks = 0; ks < 7; ++ks) WQ_ISSUE();
        const int pp = ((vp & 1) << 1) | (vp >> 1);
        const unsigned abase = wr + (8 * hi + vq) * 64 + (2 * (gi & 1) + (pp >> 1)) * 16 + 8 * (pp & 1);
        const unsigned xbase = lds0 + XIMG + r32 * 128 + ((hi ^ ((r32 >> 1) & 7)) << 4);
        f32x16 acc0 = {}, acc1 = {};
        asm volatile("s_waitcnt vmcnt(13)\n\ts_barrier" ::: "memory");
#define QSTEP(ks, WAITS, ISSUE) do { \
            WAITS; \
            const unsigned a = abase + ((ks) & 7) * 1024; \
            const s16x4 lo0 = vtr(a), hi0 = vtr(a + 256); \
            const unsigned xa = (xbase ^ (((ks) & 3) << 5)) + ((ks) >> 2) * 8192; \
            const bf16x8 x0 = ldsr128(xa), x1 = ldsr128(xa + 4096); \
            ISSUE; \
            const bf16x8 a0 = {lo0[0], lo0[1], lo0[2], lo0[3], hi0[0], hi0[1], hi0[2], hi0[3]}; \
            acc0 = MFMA32(a0, x0, acc0); \
            acc1 = MFMA32(a0, x1, acc1); } while (0)
#pragma unroll 1
        for (int k4 = 0; k4 < 28; k4 += 4) {
            if (k4 == 12) asm volatile("s_barrier" ::: "memory");
#pragma unroll
            for (int i = 0; i < 4; ++i)
                QSTEP(k4 + i, asm volatile("s_waitcnt vmcnt(6) lgkmcnt(0)" ::: "memory"), WQ_ISSUE());
        }
#define KVLD(r, ptr) asm volatile("global_load_dwordx4 %0, %1, off nt" : "=v"(r) : "v"(ptr) : "memory")
        QSTEP(28, asm volatile("s_waitcnt vmcnt(6) lgkmcnt(0)" ::: "memory"), WQ_ISSUE());
        QSTEP(29, asm volatile("s_waitcnt vmcnt(6) lgkmcnt(0)" ::: "memory"),
              KVLD(kv0, Kg + wid * 1024); KVLD(kv1, Kg + (wid + 8) * 1024);
              KVLD(kv2, Vg + wid * 1024); KVLD(kv3, Vg + (wid + 8) * 1024);
              KVLD(kv4, Kg + 16384 + wid * 1024); KVLD(kv5, Kg + 16384 + (wid + 8) * 1024);
              KVLD(kv6, Kg + 32768 + wid * 1024); KVLD(kv7, Kg + 32768 + (wid + 8) * 1024));
#undef KVLD
        asm volatile("s_waitcnt vmcnt(8)" ::: "memory");
#pragma unroll 1
        for (int k2 = 30; k2 < 36; k2 += 2) {
            QSTEP(k2, (void)0, (void)0);
            QSTEP(k2 + 1, (void)0, (void)0);
        }
#undef QSTEP
#undef WQ_ISSUE
        asm volatile("s_waitcnt lgkmcnt(0)\n\ts_barrier" ::: "memory");
#pragma unroll
        for (int u = 1; u < 3; ++u) {
            glds16nt(Vg + u * 16384 + wid * 1024, lds0 + u * SLOT + 16384 + wid * 1024);
            glds16nt(Vg + u * 16384 + (wid + 8) * 1024, lds0 + u * SLOT + 16384 + (wid + 8) * 1024);
            if (u == 1) {
                glds16nt(Kg + (u + 2) * 16384 + wid * 1024, lds0 + ((u + 2) & 3) * SLOT + wid * 1024);
                glds16nt(Kg + (u + 2) * 16384 + (wid + 8) * 1024, lds0 + ((u + 2) & 3) * SLOT + (wid + 8) * 1024);
            }
        }
#define QW(ACC, qblk) do { _Pragma("unroll") for (int s = 0; s < 2; ++s) { \
            u32x4 w_ = {cvtpk(ACC[8 * s], ACC[8 * s + 1]), cvtpk(ACC[8 * s + 2], ACC[8 * s + 3]), \
                        cvtpk(ACC[8 * s + 4], ACC[8 * s + 5]), cvtpk(ACC[8 * s + 6], ACC[8 * s + 7])}; \
            *(LAS u32x4*)(uintptr_t)(lds0 + LDS_Q + (wid >> 2) * 16384 + (qblk) * 8192 + (2 * (wid & 3) + s) * 1024 + lane * 16) = w_; } } while (0)
        QW(acc0, 0); QW(acc1, 1);
#undef QW
        asm volatile("s_waitcnt vmcnt(6)" : "+v"(kv0), "+v"(kv1), "+v"(kv2), "+v"(kv3), "+v"(kv4), "+v"(kv5), "+v"(kv6), "+v"(kv7) :: "memory");
        const unsigned kw = lds0 + wid * 1024 + lane * 16;
        *(LAS u32x4*)(uintptr_t)(kw) = kv0;                 *(LAS u32x4*)(uintptr_t)(kw + 8192) = kv1;
        *(LAS u32x4*)(uintptr_t)(kw + 16384) = kv2;         *(LAS u32x4*)(uintptr_t)(kw + 16384 + 8192) = kv3;
        *(LAS u32x4*)(uintptr_t)(kw + SLOT) = kv4;          *(LAS u32x4*)(uintptr_t)(kw + SLOT + 8192) = kv5;
        *(LAS u32x4*)(uintptr_t)(kw + 2 * SLOT) = kv6;      *(LAS u32x4*)(uintptr_t)(kw + 2 * SLOT + 8192) = kv7;
    }
    asm volatile("s_waitcnt lgkmcnt(0)\n\ts_barrier" ::: "memory");
#pragma unroll
    for (int f = 0; f < 8; ++f) qf[f] = ldsr128(lds0 + LDS_Q + qb * 8192 + f * 1024 + lane * 16);
    TS(1)

    const unsigned kbase = (32 * kh + r32) * 256 + ((((r32 & 3) << 2) | (hi ^ ((r32 >> 2) & 3))) << 4);
    const unsigned vbase = 16384 + (32 * kh + 4 * hi + vq) * 256 + (((vq << 2) | ((2 * (gi & 1) + (vp >> 1)) ^ hi)) << 4) + 8 * (vp & 1);

    float m_run = -__builtin_inff(), l_run = 0.f;
    f32x16 o[4] = {};
    f32x16 sA, sB;

#define DMA_V(tn) do { const unsigned sl_ = lds0 + ((tn) & 3) * SLOT + 16384; \
            glds16nt(Vg + (tn) * 16384 + wid * 1024, sl_ + wid * 1024); \
            glds16nt(Vg + (tn) * 16384 + (wid + 8) * 1024, sl_ + (wid + 8) * 1024); } while (0)
#define DMA_K(tn) do { const unsigned sl_ = lds0 + ((tn) & 3) * SLOT; \
            glds16nt(Kg + (tn) * 16384 + wid * 1024, sl_ + wid * 1024); \
            glds16nt(Kg + (tn) * 16384 + (wid + 8) * 1024, sl_ + (wid + 8) * 1024); } while (0)
    bf16x8 kf[8];
    {
#pragma unroll
        for (int d0 = 0; d0 < 8; ++d0) kf[d0] = ldsr128(lds0 + (kbase ^ (d0 << 5)));
        sA = f32x16{};
#pragma unroll
        for (int d0 = 0; d0 < 8; ++d0) sA = MFMA32(kf[d0], qf[d0], sA);
#pragma unroll
        for (int d0 = 0; d0 < 8; ++d0) kf[d0] = ldsr128(lds0 + SLOT + (kbase ^ (d0 << 5)));
        asm volatile("s_waitcnt lgkmcnt(0)\n\ts_barrier" ::: "memory");
        DMA_K(4);
    }
#pragma unroll 1
    for (int t = 0; t < NT; ++t) {
        s16x4 vlo[8], vhi[8];
        if (t < 12) asm volatile("s_waitcnt vmcnt(8)" ::: "memory");
        else if (t == 12) asm volatile("s_waitcnt vmcnt(6)" ::: "memory");
        else if (t == 13) asm volatile("s_waitcnt vmcnt(4)" ::: "memory");
        else if (t == 14) asm volatile("s_waitcnt vmcnt(2)" ::: "memory");
        else asm volatile("s_waitcnt vmcnt(0)" ::: "memory");
        asm volatile("s_waitcnt lgkmcnt(0)\n\ts_barrier" ::: "memory");
        float tmax = sA[0];
#pragma unroll
        for (int r = 1; r < 16; ++r) tmax = __builtin_fmaxf(tmax, sA[r]);
        tmax = swap_max(tmax) * C2;
        if (__any(tmax > m_run + RTHR)) {
            const float mnew = __builtin_fmaxf(m_run, tmax);
            const float alpha = __builtin_amdgcn_exp2f(m_run - mnew);
            m_run = mnew; l_run *= alpha;
#pragma unroll
            for (int dt = 0; dt < 4; ++dt)
#pragma unroll
                for (int r = 0; r < 16; ++r) o[dt][r] *= alpha;
        }
        __builtin_amdgcn_sched_barrier(0);
        const unsigned sl = lds0 + (t & 3) * SLOT;
#pragma unroll
        for (int ss = 0; ss < 2; ++ss)
#pragma unroll
            for (int dt = 0; dt < 4; ++dt) {
                vlo[2 * dt + ss] = vtr(sl + (vbase ^ (dt << 6)) + 4096 * ss);
                vhi[2 * dt + ss] = vtr(sl + (vbase ^ ((dt << 6) | (1 << 5))) + 4096 * ss + 2048);
            }
        sB = f32x16{};
#pragma unroll
        for (int d0 = 0; d0 < 8; ++d0) sB = MFMA32(kf[d0], qf[d0], sB);
        float ps = 0.f;
#pragma unroll
        for (int r = 0; r < 16; ++r) { sA[r] = __builtin_amdgcn_exp2f(__builtin_fmaf(sA[r], C2, -m_run)); ps += sA[r]; }
        l_run += ps;
        bf16x8 pf[2];
#pragma unroll
        for (int ss = 0; ss < 2; ++ss) {
            u32x4 w = {cvtpk(sA[8 * ss + 0], sA[8 * ss + 1]), cvtpk(sA[8 * ss + 2], sA[8 * ss + 3]),
                       cvtpk(sA[8 * ss + 4], sA[8 * ss + 5]), cvtpk(sA[8 * ss + 6], sA[8 * ss + 7])};
            pf[ss] = __builtin_bit_cast(bf16x8, w);
        }
        asm volatile("" : "+v"(pf[0]), "+v"(pf[1]), "+v"(l_run));
#pragma unroll
        for (int i = 0; i < 8; ++i) {
            __builtin_amdgcn_sched_group_barrier(0x008, 1, 0); __builtin_amdgcn_sched_group_barrier(0x100, 2, 0);
            __builtin_amdgcn_sched_group_barrier(0x002, 8, 0);
        }
        __builtin_amdgcn_sched_barrier(0);
        {
            const unsigned sk_ = lds0 + ((t + 2) & 3) * SLOT;
#pragma unroll
            for (int d0 = 0; d0 < 8; ++d0) kf[d0] = ldsr128(sk_ + (kbase ^ (d0 << 5)));
        }
#pragma unroll
        for (int ss = 0; ss < 2; ++ss)
#pragma unroll
            for (int dt = 0; dt < 4; ++dt) {
                const s16x4 lo = vlo[2 * dt + ss], hi4 = vhi[2 * dt + ss];
                const bf16x8 vf = {lo[0], lo[1], lo[2], lo[3], hi4[0], hi4[1], hi4[2], hi4[3]};
                o[dt] = MFMA32(vf, pf[ss], o[dt]);
            }
#pragma unroll
        for (int i = 0; i < 8; ++i) {
            __builtin_amdgcn_sched_group_barrier(0x008, 1, 0); __builtin_amdgcn_sched_group_barrier(0x100, 1, 0);
            __builtin_amdgcn_sched_group_barrier(0x002, 1, 0);
        }
        __builtin_amdgcn_sched_barrier(0);
        if (t < 13) DMA_V(t + 3);
        if (t < 11) DMA_K(t + 5);
        sA = sB;
    }
    TS(2)
    {
        const char* wp = (const char*)wo + (size_t)(256 * kvh + 16 * b) * DM * 2 + wid * 1024 + lane * 16;
        glds16(wp, lds0 + LDS_Q + wid * 1024);
        glds16(wp + 8192, lds0 + LDS_Q + 8192 + wid * 1024);
        if (wid < 2) glds16(wp + 16384, lds0 + LDS_Q + 16384 + wid * 1024);
    }
    l_run = swap_add(l_run);
    asm volatile("s_waitcnt lgkmcnt(0)\n\ts_barrier" ::: "memory");
    {
        f32x16 keep0, keep1, send0, send1;
        if (kh == 0) { keep0 = o[0]; keep1 = o[1]; send0 = o[2]; send1 = o[3]; }
        else         { keep0 = o[2]; keep1 = o[3]; send0 = o[0]; send1 = o[1]; }
        const unsigned xb = lds0 + wid * 9216;
#pragma unroll
        for (int r4 = 0; r4 < 4; ++r4) {
            f32x4 v0 = {send0[4 * r4], send0[4 * r4 + 1], send0[4 * r4 + 2], send0[4 * r4 + 3]};
            f32x4 v1 = {send1[4 * r4], send1[4 * r4 + 1], send1[4 * r4 + 2], send1[4 * r4 + 3]};
            *(LAS f32x4*)(uintptr_t)(xb + r4 * 1024 + lane * 16) = v0;
            *(LAS f32x4*)(uintptr_t)(xb + (4 + r4) * 1024 + lane * 16) = v1;
        }
        *(LAS float*)(uintptr_t)(xb + 8192 + lane * 4) = m_run;
        *(LAS float*)(uintptr_t)(xb + 8192 + 256 + lane * 4) = l_run;
        asm volatile("s_waitcnt lgkmcnt(0)\n\ts_barrier" ::: "memory");
        const unsigned pb = lds0 + (wid ^ 4) * 9216;
        const float m2 = *(const LAS float*)(uintptr_t)(pb + 8192 + lane * 4);
        const float l2 = *(const LAS float*)(uintptr_t)(pb + 8192 + 256 + lane * 4);
        const float mm = __builtin_fmaxf(m_run, m2);
        const float a1 = __builtin_amdgcn_exp2f(m_run - mm), a2 = __builtin_amdgcn_exp2f(m2 - mm);
        const float inv = 1.0f / (l_run * a1 + l2 * a2);
        const float w1 = a1 * inv, w2 = a2 * inv;
        const int row = b * SQ + 32 * (qb & 1) + r32;
        char* ob = (char*)Og + ((size_t)((row >> 4) * 128 + head * 4 + 2 * kh) * 64 + hi * 16 + (row & 15)) * 16;
#define OSTORE(KEEP, i) do { u32x2 w_[4]; \
            _Pragma("unroll") for (int r4 = 0; r4 < 4; ++r4) { \
                const f32x4 v = *(const LAS f32x4*)(uintptr_t)(pb + (4 * (i) + r4) * 1024 + lane * 16); \
                const float e0 = KEEP[4 * r4] * w1 + v[0] * w2, e1 = KEEP[4 * r4 + 1] * w1 + v[1] * w2; \
                const float e2 = KEEP[4 * r4 + 2] * w1 + v[2] * w2, e3 = KEEP[4 * r4 + 3] * w1 + v[3] * w2; \
                w_[r4] = u32x2{cvtpk(e0, e1), cvtpk(e2, e3)}; } \
            _Pragma("unroll") for (int g = 0; g < 4; g += 2) { \
                auto rx = __builtin_amdgcn_permlane32_swap(w_[g].x, w_[g + 1].x, false, false); \
                auto ry = __builtin_amdgcn_permlane32_swap(w_[g].y, w_[g + 1].y, false, false); \
                u32x4 o4 = {rx[0], ry[0], rx[1], ry[1]}; \
                *(u32x4*)(ob + (i) * 1024 + g * 256) = o4; } } while (0)
        OSTORE(keep0, 0); OSTORE(keep1, 1);
#undef OSTORE
    }
    TS(3)
    TS_STORE
}

constexpr int LDS2_BYTES = 512 * 320;
#define MFMA16(a, b, c) __builtin_amdgcn_mfma_f32_16x16x32_bf16(a, b, c, 0, 0, 0)
__global__ __launch_bounds__(512, 2) void oproj_kernel(const u16* __restrict__ Og, const u16* __restrict__ wo,
                                                       float* __restrict__ part) {
    extern __shared__ __attribute__((aligned(16))) char lds[];
    const unsigned lds0 = (unsigned)(uintptr_t)lds;
    const int tid = threadIdx.x, lane = tid & 63;
    const int wid = __builtin_amdgcn_readfirstlane(tid >> 6);
    const int rb = wid & 3, kp = wid >> 2;
    const int bid = blockIdx.x;
    const int sk = bid & 7, nt = (bid >> 3) & 3, mt = bid >> 5;
    const char* Bg[5];
#pragma unroll
    for (int i = 0; i < 5; ++i) {
        const int L = i * 1024 + lane * 16;
        const int row = L / 320, cp = (L % 320) >> 4;
        int c = cp ^ (((row >> 3) & 1) << 1);
        c = c < 18 ? c : 0;
        Bg[i] = (const char*)wo + ((size_t)(256 * sk + 16 * wid + row) * DM + 144 * nt) * 2 + c * 16;
    }
    const char* Ag = (const char*)Og + ((size_t)((8 * mt + 2 * rb) * 128 + 8 * sk + kp) * 64 + lane) * 16;
    u32x4 an0, an1, an2, an3;
#define K2_B(c) do { const size_t ro_ = (size_t)(((c) >> 1) * 2048 + ((c) & 1) * 128) * DM * 2; \
        _Pragma("unroll") for (int i = 0; i < 5; ++i) glds16(Bg[i] + ro_, lds0 + (40 * (c) + 5 * wid + i) * 1024); } while (0)
#define K2_A(c) do { const char* a_ = Ag + (((c) >> 1) * 64 + ((c) & 1) * 4) * 1024; \
        asm volatile("global_load_dwordx4 %0, %1, off" : "=v"(an0) : "v"(a_) : "memory"); \
        asm volatile("global_load_dwordx4 %0, %1, off" : "=v"(an1) : "v"(a_ + 131072) : "memory"); \
        asm volatile("global_load_dwordx4 %0, %1, off offset:2048" : "=v"(an2) : "v"(a_) : "memory"); \
        asm volatile("global_load_dwordx4 %0, %1, off offset:2048" : "=v"(an3) : "v"(a_ + 131072) : "memory"); } while (0)
    K2_B(0); K2_A(0); K2_B(1);
    const int gi = lane >> 4, vq = (lane & 15) >> 2, vp = lane & 3;
    unsigned bbE = lds0 + (32 * kp + 8 * gi + vq) * 320 + 8 * vp + 32 * (gi & 1);
    unsigned bbO = bbE - 64 * (gi & 1);
    f32x4 acc[2][9];
#pragma unroll
    for (int ct = 0; ct < 9; ++ct) { acc[0][ct] = f32x4{0.f, 0.f, 0.f, 0.f}; acc[1][ct] = f32x4{0.f, 0.f, 0.f, 0.f}; }
#pragma unroll 1
    for (int c = 0; c < 4; ++c) {
        if (c < 3) asm volatile("s_waitcnt vmcnt(5)" ::: "memory");
        else asm volatile("s_waitcnt vmcnt(0)" ::: "memory");
        asm volatile("s_barrier" : "+v"(an0), "+v"(an1), "+v"(an2), "+v"(an3) :: "memory");
        const bf16x8 a[4] = {__builtin_bit_cast(bf16x8, an0), __builtin_bit_cast(bf16x8, an1),
                             __builtin_bit_cast(bf16x8, an2), __builtin_bit_cast(bf16x8, an3)};
        bf16x8 ac[4];
#pragma unroll
        for (int i = 0; i < 4; ++i) { ac[i] = a[i]; asm volatile("" : "+v"(ac[i])); }
        if (c < 3) K2_A(c + 1);
        if (c < 2) K2_B(c + 2);
#pragma unroll
        for (int j = 0; j < 2; ++j) {
#pragma unroll
            for (int ct = 0; ct < 9; ++ct) {
                const unsigned ad = ((ct & 1) ? bbO : bbE) + 2 * j * 10240 + 32 * ct;
                const s16x4 lo = vtr(ad), h4 = vtr(ad + 1280);
                const bf16x8 bf = {lo[0], lo[1], lo[2], lo[3], h4[0], h4[1], h4[2], h4[3]};
                acc[0][ct] = MFMA16(bf, ac[2 * j], acc[0][ct]);
                acc[1][ct] = MFMA16(bf, ac[2 * j + 1], acc[1][ct]);
            }
        }
        bbE += 40960; bbO += 40960;
    }
#undef K2_A
#undef K2_B
    asm volatile("s_waitcnt lgkmcnt(0)\n\ts_barrier" ::: "memory");
#pragma unroll
    for (int ct = 0; ct < 9; ++ct)
        *(LAS f32x4*)(uintptr_t)(lds0 + (wid * 9 + ct) * 1024 + lane * 16) = kp ? acc[0][ct] : acc[1][ct];
    asm volatile("s_waitcnt lgkmcnt(0)\n\ts_barrier" ::: "memory");
    float* pp = part + ((size_t)(sk * 64 + 8 * mt + 2 * rb + kp) * 36 + 9 * nt) * 256 + lane * 4;
#pragma unroll
    for (int ct = 0; ct < 9; ++ct) {
        const f32x4 v = *(const LAS f32x4*)(uintptr_t)(lds0 + ((wid ^ 4) * 9 + ct) * 1024 + lane * 16);
        *(f32x4*)(pp + ct * 256) = (kp ? acc[1][ct] : acc[0][ct]) + v;
    }
}

__global__ __launch_bounds__(256) void reduce_kernel(const float* __restrict__ part, u16* __restrict__ out) {
    const int lane = threadIdx.x & 63;
    const int u = blockIdx.x * 4 + (threadIdx.x >> 6);
    const float* p = part + (size_t)u * 256 + lane * 4;
    f32x4 t = *(const f32x4*)p;
#pragma unroll
    for (int s = 1; s < 8; ++s) t += *(const f32x4*)(p + (size_t)s * 64 * 36 * 256);
    const int rt = u / 36, ct = u % 36;
    u32x2 w = {cvtpk(t[0], t[1]), cvtpk(t[2], t[3])};
    *(u32x2*)(out + (size_t)(16 * rt + (lane & 15)) * DM + 16 * ct + 4 * (lane >> 4)) = w;
}

extern "C" void kernel_launch(void* const* d_in, const int* in_sizes, int n_in, void* d_out, int out_size,
                              void* d_ws, size_t ws_size, hipStream_t stream) {
    const u16* x = (const u16*)d_in[0];
    const u16* kc = (const u16*)d_in[1];
    const u16* vc = (const u16*)d_in[2];
    const u16* wq = (const u16*)d_in[3];
    const u16* wo = (const u16*)d_in[4];
    u16* Og = (u16*)d_ws;
    float* part = (float*)((char*)d_ws + (size_t)NB * SQ * NQ * 2);
    (void)hipFuncSetAttribute((const void*)attn_kernel, hipFuncAttributeMaxDynamicSharedMemorySize, LDS1_BYTES);
    (void)hipFuncSetAttribute((const void*)oproj_kernel, hipFuncAttributeMaxDynamicSharedMemorySize, LDS2_BYTES);
    attn_kernel<<<NB * HKV, 512, LDS1_BYTES, stream>>>(x, kc, vc, wq, Og, wo);
    oproj_kernel<<<256, 512, LDS2_BYTES, stream>>>(Og, wo, part);
    reduce_kernel<<<576, 256, 0, stream>>>(part, (u16*)d_out);
}
```

```cpp
#include <hip/hip_runtime.h>
#include <hip/hip_bf16.h>
#include <stdint.h>

typedef short bf16x8 __attribute__((ext_vector_type(8)));
typedef short s16x4 __attribute__((ext_vector_type(4)));
typedef float f32x16 __attribute__((ext_vector_type(16)));
typedef float f32x4 __attribute__((ext_vector_type(4)));
typedef unsigned u32x4 __attribute__((ext_vector_type(4)));
typedef unsigned u32x2 __attribute__((ext_vector_type(2)));
typedef unsigned short u16;
#define LAS __attribute__((address_space(3)))
typedef LAS const char* lds_cptr;

constexpr int NB = 16, SQ = 64, DM = 576, HQ = 32, HKV = 16, SKV = 1024, DH = 128;
constexpr int NQ = HQ * DH;
constexpr int NT = SKV / 64;
constexpr int SLOT = 32768;
constexpr int LDS_Q = 4 * SLOT;
constexpr int LDS1_BYTES = 5 * SLOT;
constexpr float RTHR = 8.0f;
constexpr float C2 = 0.088388347648318447f * 1.4426950408889634f;

#define MFMA32(a, b, c) __builtin_amdgcn_mfma_f32_32x32x16_bf16(a, b, c, 0, 0, 0)

typedef __bf16 bf16x2_t __attribute__((ext_vector_type(2)));
typedef float f32x2 __attribute__((ext_vector_type(2)));
__device__ __forceinline__ unsigned cvtpk(float lo, float hi) {
    f32x2 v = {lo, hi};
    return __builtin_bit_cast(unsigned, __builtin_convertvector(v, bf16x2_t));
}
__device__ __forceinline__ void glds16(const void* g, unsigned lds_base) {
    unsigned sv;
    asm volatile("s_mov_b32 %0, m0\n\ts_mov_b32 m0, %2\n\ts_nop 0\n\tglobal_load_lds_dwordx4 %1, off\n\ts_mov_b32 m0, %0"
                 : "=&s"(sv) : "v"(g), "s"(lds_base) : "memory");
}
__device__ __forceinline__ void glds16nt(const void* g, unsigned lds_base) {
    unsigned sv;
    asm volatile("s_mov_b32 %0, m0\n\ts_mov_b32 m0, %2\n\ts_nop 0\n\tglobal_load_lds_dwordx4 %1, off nt\n\ts_mov_b32 m0, %0"
                 : "=&s"(sv) : "v"(g), "s"(lds_base) : "memory");
}
#define TS_DECL
#define TS(i)
#define TS_STORE
__device__ __forceinline__ s16x4 vtr(unsigned a) {
    return __builtin_amdgcn_ds_read_tr16_b64_v4i16((LAS s16x4*)(uintptr_t)a);
}
__device__ __forceinline__ bf16x8 ldsr128(unsigned a) { return *(const LAS bf16x8*)(uintptr_t)a; }
__device__ __forceinline__ float swap_max(float v) {
    auto rr = __builtin_amdgcn_permlane32_swap(__float_as_uint(v), __float_as_uint(v), false, false);
    return __builtin_fmaxf(__uint_as_float(rr[0]), __uint_as_float(rr[1]));
}
__device__ __forceinline__ float swap_add(float v) {
    auto rr = __builtin_amdgcn_permlane32_swap(__float_as_uint(v), __float_as_uint(v), false, false);
    return __uint_as_float(rr[0]) + __uint_as_float(rr[1]);
}

constexpr int XIMG = 0;
constexpr int WRING = 73728;
TS_DECL
__global__ __launch_bounds__(512, 2) void attn_kernel(const u16* __restrict__ x, const u16* __restrict__ Kc,
                                                      const u16* __restrict__ Vc, const u16* __restrict__ wq,
                                                      u16* __restrict__ Og, const u16* __restrict__ wo) {
    extern __shared__ __attribute__((aligned(16))) char lds[];
    TS(0)
    const unsigned lds0 = (unsigned)(uintptr_t)lds;
    const int tid = threadIdx.x, lane = tid & 63, r32 = lane & 31, hi = lane >> 5;
    const int wid = __builtin_amdgcn_readfirstlane(tid >> 6);
    const int qb = wid & 3, kh = wid >> 2;
    const int bid = blockIdx.x, kvh = bid & 15, b = bid >> 4;
    const int head = 2 * kvh + (qb >> 1);
    const int gi = lane >> 4, vq = (lane & 15) >> 2, vp = lane & 3;

    const unsigned lane_off = gi * 256 + ((((lane & 15) ^ ((gi << 2) | (wid & 3)))) << 4);
    const char* Kg = (const char*)Kc + (size_t)(b * HKV + kvh) * SKV * DH * 2 + lane_off;
    const char* Vg = (const char*)Vc + (size_t)(b * HKV + kvh) * SKV * DH * 2 + lane_off;

    bf16x8 qf[8];
    {
        u32x4 kv0, kv1, kv2, kv3, kv4, kv5, kv6, kv7;
        int kb = b % 9;
        const char* Xg = (const char*)x + ((size_t)b * SQ + 8 * wid + (lane >> 3)) * DM * 2 + (((lane & 7) ^ ((4 * wid + (lane >> 4)) & 7)) << 4);
        const char* Wg0 = (const char*)wq + ((size_t)(8 * wid + hi) * NQ + kvh * 256) * 2 + ((r32 ^ (hi << 2)) << 4);
        const char* Wg1 = (const char*)wq + ((size_t)(8 * wid + 2 + hi) * NQ + kvh * 256) * 2 + ((r32 ^ ((2 + hi) << 2)) << 4);
#define QS_ISSUE(slot) do { const unsigned s_ = lds0 + (slot) * 40960 + wid * 4096; const size_t go_ = (size_t)kb * 64 * NQ * 2; \
            glds16(Wg0 + go_, s_); glds16(Wg1 + go_, s_ + 1024); \
            glds16(Wg0 + go_ + 4 * NQ * 2, s_ + 2048); glds16(Wg1 + go_ + 4 * NQ * 2, s_ + 3072); \
            glds16(Xg + kb * 128, lds0 + (slot) * 40960 + 32768 + wid * 1024); \
            kb = kb == 8 ? 0 : kb + 1; } while (0)
#pragma unroll 1
        for (int j = 0; j < 3; ++j) QS_ISSUE(j);
        const int pp = ((vp & 1) << 1) | (vp >> 1);
        const unsigned abase = lds0 + (8 * hi + vq) * 512 + (((4 * wid + 2 * (gi & 1) + (pp >> 1)) ^ (vq << 2)) << 4) + 8 * (pp & 1);
        const unsigned xbase = lds0 + 32768 + r32 * 128 + ((hi ^ ((r32 >> 1) & 7)) << 4);
        f32x16 acc0 = {}, acc1 = {};
#pragma unroll 1
        for (int st = 0; st < 9; ++st) {
            if (st < 7) asm volatile("s_waitcnt vmcnt(10)" ::: "memory");
            else if (st == 7) asm volatile("s_waitcnt vmcnt(13)" ::: "memory");
            else asm volatile("s_waitcnt vmcnt(8)" ::: "memory");
            asm volatile("s_waitcnt lgkmcnt(0)\n\ts_barrier" ::: "memory");
            const unsigned so = (st & 3) * 40960;
            s16x4 lo[4], h4[4]; bf16x8 x0[4], x1[4];
#pragma unroll
            for (int i = 0; i < 4; ++i) {
                lo[i] = vtr(abase + so + i * 8192); h4[i] = vtr(abase + so + i * 8192 + 2048);
                x0[i] = ldsr128((xbase ^ (i << 5)) + so); x1[i] = ldsr128((xbase ^ (i << 5)) + so + 4096);
            }
            __builtin_amdgcn_sched_barrier(0);
            if (st < 6) QS_ISSUE((st + 3) & 3);
            if (st == 6) {
#define KVLD(r, ptr) asm volatile("global_load_dwordx4 %0, %1, off nt" : "=v"(r) : "v"(ptr) : "memory")
                KVLD(kv0, Kg + wid * 1024); KVLD(kv1, Kg + (wid + 8) * 1024);
                KVLD(kv2, Vg + wid * 1024); KVLD(kv3, Vg + (wid + 8) * 1024);
                KVLD(kv4, Kg + 16384 + wid * 1024); KVLD(kv5, Kg + 16384 + (wid + 8) * 1024);
                KVLD(kv6, Kg + 32768 + wid * 1024); KVLD(kv7, Kg + 32768 + (wid + 8) * 1024);
#undef KVLD
            }
            __builtin_amdgcn_sched_barrier(0);
#pragma unroll
            for (int i = 0; i < 4; ++i) {
                const bf16x8 a0 = {lo[i][0], lo[i][1], lo[i][2], lo[i][3], h4[i][0], h4[i][1], h4[i][2], h4[i][3]};
                acc0 = MFMA32(a0, x0[i], acc0);
                acc1 = MFMA32(a0, x1[i], acc1);
            }
        }
#define WQ_ISSUE
#undef WQ_ISSUE
        asm volatile("s_waitcnt lgkmcnt(0)\n\ts_barrier" ::: "memory");
#pragma unroll
        for (int u = 1; u < 3; ++u) {
            glds16nt(Vg + u * 16384 + wid * 1024, lds0 + u * SLOT + 16384 + wid * 1024);
            glds16nt(Vg + u * 16384 + (wid + 8) * 1024, lds0 + u * SLOT + 16384 + (wid + 8) * 1024);
            if (u == 1) {
                glds16nt(Kg + (u + 2) * 16384 + wid * 1024, lds0 + ((u + 2) & 3) * SLOT + wid * 1024);
                glds16nt(Kg + (u + 2) * 16384 + (wid + 8) * 1024, lds0 + ((u + 2) & 3) * SLOT + (wid + 8) * 1024);
            }
        }
#define QW(ACC, qblk) do { _Pragma("unroll") for (int s = 0; s < 2; ++s) { \
            u32x4 w_ = {cvtpk(ACC[8 * s], ACC[8 * s + 1]), cvtpk(ACC[8 * s + 2], ACC[8 * s + 3]), \
                        cvtpk(ACC[8 * s + 4], ACC[8 * s + 5]), cvtpk(ACC[8 * s + 6], ACC[8 * s + 7])}; \
            *(LAS u32x4*)(uintptr_t)(lds0 + LDS_Q + (wid >> 2) * 16384 + (qblk) * 8192 + (2 * (wid & 3) + s) * 1024 + lane * 16) = w_; } } while (0)
        QW(acc0, 0); QW(acc1, 1);
#undef QW
        asm volatile("s_waitcnt vmcnt(6)" : "+v"(kv0), "+v"(kv1), "+v"(kv2), "+v"(kv3), "+v"(kv4), "+v"(kv5), "+v"(kv6), "+v"(kv7) :: "memory");
        const unsigned kw = lds0 + wid * 1024 + lane * 16;
        *(LAS u32x4*)(uintptr_t)(kw) = kv0;                 *(LAS u32x4*)(uintptr_t)(kw + 8192) = kv1;
        *(LAS u32x4*)(uintptr_t)(kw + 16384) = kv2;         *(LAS u32x4*)(uintptr_t)(kw + 16384 + 8192) = kv3;
        *(LAS u32x4*)(uintptr_t)(kw + SLOT) = kv4;          *(LAS u32x4*)(uintptr_t)(kw + SLOT + 8192) = kv5;
        *(LAS u32x4*)(uintptr_t)(kw + 2 * SLOT) = kv6;      *(LAS u32x4*)(uintptr_t)(kw + 2 * SLOT + 8192) = kv7;
    }
    asm volatile("s_waitcnt lgkmcnt(0)\n\ts_barrier" ::: "memory");
#pragma unroll
    for (int f = 0; f < 8; ++f) qf[f] = ldsr128(lds0 + LDS_Q + qb * 8192 + f * 1024 + lane * 16);
    TS(1)

    const unsigned kbase = (32 * kh + r32) * 256 + ((((r32 & 3) << 2) | (hi ^ ((r32 >> 2) & 3))) << 4);
    const unsigned vbase = 16384 + (32 * kh + 4 * hi + vq) * 256 + (((vq << 2) | ((2 * (gi & 1) + (vp >> 1)) ^ hi)) << 4) + 8 * (vp & 1);

    float m_run = -__builtin_inff(), l_run = 0.f;
    f32x16 o[4] = {};
    f32x16 sA;

#define DMA_V(tn) do { const unsigned sl_ = lds0 + ((tn) & 3) * SLOT + 16384; \
            glds16nt(Vg + (tn) * 16384 + wid * 1024, sl_ + wid * 1024); \
            glds16nt(Vg + (tn) * 16384 + (wid + 8) * 1024, sl_ + (wid + 8) * 1024); } while (0)
#define DMA_K(tn) do { const unsigned sl_ = lds0 + ((tn) & 3) * SLOT; \
            glds16nt(Kg + (tn) * 16384 + wid * 1024, sl_ + wid * 1024); \
            glds16nt(Kg + (tn) * 16384 + (wid + 8) * 1024, sl_ + (wid + 8) * 1024); } while (0)
    bf16x8 kf[8];
    {
#pragma unroll
        for (int d0 = 0; d0 < 8; ++d0) kf[d0] = ldsr128(lds0 + (kbase ^ (d0 << 5)));
        sA = f32x16{};
#pragma unroll
        for (int d0 = 0; d0 < 8; ++d0) sA = MFMA32(kf[d0], qf[d0], sA);
#pragma unroll
        for (int d0 = 0; d0 < 8; ++d0) kf[d0] = ldsr128(lds0 + SLOT + (kbase ^ (d0 << 5)));
        asm volatile("s_waitcnt lgkmcnt(0)\n\ts_barrier" ::: "memory");
        DMA_K(4);
    }
    float e[16], tmax;
    {
        tmax = sA[0];
#pragma unroll
        for (int r = 1; r < 16; ++r) tmax = __builtin_fmaxf(tmax, sA[r]);
        tmax = swap_max(tmax) * C2;
        m_run = tmax;
#pragma unroll
        for (int r = 0; r < 16; ++r) e[r] = __builtin_fmaf(sA[r], C2, -m_run);
    }
#pragma unroll 1
    for (int t = 0; t < NT; ++t) {
        s16x4 vlo[8], vhi[8];
        if (t < 12) asm volatile("s_waitcnt vmcnt(8)" ::: "memory");
        else if (t == 12) asm volatile("s_waitcnt vmcnt(6)" ::: "memory");
        else if (t == 13) asm volatile("s_waitcnt vmcnt(4)" ::: "memory");
        else if (t == 14) asm volatile("s_waitcnt vmcnt(2)" ::: "memory");
        else asm volatile("s_waitcnt vmcnt(0)" ::: "memory");
        asm volatile("s_waitcnt lgkmcnt(0)\n\ts_barrier" ::: "memory");
        if (__any(tmax > m_run + RTHR)) {
            const float mnew = __builtin_fmaxf(m_run, tmax);
            const float dm = m_run - mnew;
            const float alpha = __builtin_amdgcn_exp2f(dm);
            m_run = mnew; l_run *= alpha;
#pragma unroll
            for (int r = 0; r < 16; ++r) e[r] += dm;
#pragma unroll
            for (int dt = 0; dt < 4; ++dt)
#pragma unroll
                for (int r = 0; r < 16; ++r) o[dt][r] *= alpha;
        }
        const unsigned sl = lds0 + (t & 3) * SLOT;
        const unsigned sk_ = lds0 + ((t + 2) & 3) * SLOT;
        unsigned pw[8];
        float ps = 0.f;
        __builtin_amdgcn_sched_barrier(0);
#pragma unroll
        for (int i = 0; i < 8; ++i) {
            sA = MFMA32(kf[i], qf[i], i == 0 ? f32x16{} : sA);
            const int ss = i >> 2, dt = i & 3;
            vlo[2 * dt + ss] = vtr(sl + (vbase ^ (dt << 6)) + 4096 * ss);
            vhi[2 * dt + ss] = vtr(sl + (vbase ^ ((dt << 6) | (1 << 5))) + 4096 * ss + 2048);
            e[2 * i] = __builtin_amdgcn_exp2f(e[2 * i]); e[2 * i + 1] = __builtin_amdgcn_exp2f(e[2 * i + 1]);
            if (i > 0) {
                ps += e[2 * i - 2]; ps += e[2 * i - 1];
                pw[i - 1] = cvtpk(e[2 * i - 2], e[2 * i - 1]);
                asm volatile("" : "+v"(pw[i - 1]), "+v"(ps));
            }
            asm volatile("" : "+v"(e[2 * i]), "+v"(e[2 * i + 1]), "+v"(sA));
            __builtin_amdgcn_sched_barrier(0);
        }
        ps += e[14]; ps += e[15];
        pw[7] = cvtpk(e[14], e[15]);
        l_run += ps;
        const bf16x8 pf0 = __builtin_bit_cast(bf16x8, u32x4{pw[0], pw[1], pw[2], pw[3]});
        const bf16x8 pf1 = __builtin_bit_cast(bf16x8, u32x4{pw[4], pw[5], pw[6], pw[7]});
#pragma unroll
        for (int i = 0; i < 8; ++i) {
            const int ss = i >> 2, dt = i & 3;
            const s16x4 lo = vlo[2 * dt + ss], hi4 = vhi[2 * dt + ss];
            const bf16x8 vf = {lo[0], lo[1], lo[2], lo[3], hi4[0], hi4[1], hi4[2], hi4[3]};
            o[dt] = MFMA32(vf, ss ? pf1 : pf0, o[dt]);
            kf[i] = ldsr128(sk_ + (kbase ^ (i << 5)));
            if (i == 2) {
                tmax = __builtin_fmaxf(__builtin_fmaxf(sA[0], sA[1]), sA[2]);
                float t2 = __builtin_fmaxf(__builtin_fmaxf(sA[3], sA[4]), sA[5]);
                tmax = __builtin_fmaxf(__builtin_fmaxf(tmax, sA[6]), sA[7]);
                t2 = __builtin_fmaxf(__builtin_fmaxf(t2, sA[8]), sA[9]);
                tmax = __builtin_fmaxf(tmax, t2);
                asm volatile("" : "+v"(tmax));
            }
            if (i == 3) {
                float t2 = __builtin_fmaxf(__builtin_fmaxf(sA[10], sA[11]), sA[12]);
                tmax = __builtin_fmaxf(__builtin_fmaxf(tmax, sA[13]), sA[14]);
                tmax = __builtin_fmaxf(__builtin_fmaxf(tmax, sA[15]), t2);
                asm volatile("" : "+v"(tmax));
            }
            if (i == 4) { tmax = swap_max(tmax) * C2; asm volatile("" : "+v"(tmax)); }
            if (i >= 4) {
#pragma unroll
                for (int r = 4 * (i - 4); r < 4 * (i - 3); ++r) e[r] = __builtin_fmaf(sA[r], C2, -m_run);
                asm volatile("" : "+v"(e[4 * (i - 4)]), "+v"(e[4 * (i - 4) + 1]), "+v"(e[4 * (i - 4) + 2]), "+v"(e[4 * (i - 4) + 3]));
            }
            __builtin_amdgcn_sched_barrier(0);
        }
        if (t < 13) DMA_V(t + 3);
        if (t < 11) DMA_K(t + 5);
    }
    TS(2)
    {
        const char* wp = (const char*)wo + (size_t)(256 * kvh + 16 * b) * DM * 2 + wid * 1024 + lane * 16;
        glds16(wp, lds0 + LDS_Q + wid * 1024);
        glds16(wp + 8192, lds0 + LDS_Q + 8192 + wid * 1024);
        if (wid < 2) glds16(wp + 16384, lds0 + LDS_Q + 16384 + wid * 1024);
    }
    l_run = swap_add(l_run);
    asm volatile("s_waitcnt lgkmcnt(0)\n\ts_barrier" ::: "memory");
    {
        f32x16 keep0, keep1, send0, send1;
        if (kh == 0) { keep0 = o[0]; keep1 = o[1]; send0 = o[2]; send1 = o[3]; }
        else         { keep0 = o[2]; keep1 = o[3]; send0 = o[0]; send1 = o[1]; }
        const unsigned xb = lds0 + wid * 9216;
#pragma unroll
        for (int r4 = 0; r4 < 4; ++r4) {
            f32x4 v0 = {send0[4 * r4], send0[4 * r4 + 1], send0[4 * r4 + 2], send0[4 * r4 + 3]};
            f32x4 v1 = {send1[4 * r4], send1[4 * r4 + 1], send1[4 * r4 + 2], send1[4 * r4 + 3]};
            *(LAS f32x4*)(uintptr_t)(xb + r4 * 1024 + lane * 16) = v0;
            *(LAS f32x4*)(uintptr_t)(xb + (4 + r4) * 1024 + lane * 16) = v1;
        }
        *(LAS float*)(uintptr_t)(xb + 8192 + lane * 4) = m_run;
        *(LAS float*)(uintptr_t)(xb + 8192 + 256 + lane * 4) = l_run;
        asm volatile("s_waitcnt lgkmcnt(0)\n\ts_barrier" ::: "memory");
        const unsigned pb = lds0 + (wid ^ 4) * 9216;
        const float m2 = *(const LAS float*)(uintptr_t)(pb + 8192 + lane * 4);
        const float l2 = *(const LAS float*)(uintptr_t)(pb + 8192 + 256 + lane * 4);
        const float mm = __builtin_fmaxf(m_run, m2);
        const float a1 = __builtin_amdgcn_exp2f(m_run - mm), a2 = __builtin_amdgcn_exp2f(m2 - mm);
        const float inv = 1.0f / (l_run * a1 + l2 * a2);
        const float w1 = a1 * inv, w2 = a2 * inv;
        const int row = b * SQ + 32 * (qb & 1) + r32;
        char* ob = (char*)Og + ((size_t)((row >> 4) * 128 + head * 4 + 2 * kh) * 64 + hi * 16 + (row & 15)) * 16;
#define OSTORE(KEEP, i) do { u32x2 w_[4]; \
            _Pragma("unroll") for (int r4 = 0; r4 < 4; ++r4) { \
                const f32x4 v = *(const LAS f32x4*)(uintptr_t)(pb + (4 * (i) + r4) * 1024 + lane * 16); \
                const float e0 = KEEP[4 * r4] * w1 + v[0] * w2, e1 = KEEP[4 * r4 + 1] * w1 + v[1] * w2; \
                const float e2 = KEEP[4 * r4 + 2] * w1 + v[2] * w2, e3 = KEEP[4 * r4 + 3] * w1 + v[3] * w2; \
                w_[r4] = u32x2{cvtpk(e0, e1), cvtpk(e2, e3)}; } \
            _Pragma("unroll") for (int g = 0; g < 4; g += 2) { \
                auto rx = __builtin_amdgcn_permlane32_swap(w_[g].x, w_[g + 1].x, false, false); \
                auto ry = __builtin_amdgcn_permlane32_swap(w_[g].y, w_[g + 1].y, false, false); \
                u32x4 o4 = {rx[0], ry[0], rx[1], ry[1]}; \
                *(u32x4*)(ob + (i) * 1024 + g * 256) = o4; } } while (0)
        OSTORE(keep0, 0); OSTORE(keep1, 1);
#undef OSTORE
    }
    TS(3)
    TS_STORE
}

constexpr int LDS2_BYTES = 512 * 320;
#define MFMA16(a, b, c) __builtin_amdgcn_mfma_f32_16x16x32_bf16(a, b, c, 0, 0, 0)
__global__ __launch_bounds__(512, 2) void oproj_kernel(const u16* __restrict__ Og, const u16* __restrict__ wo,
                                                       float* __restrict__ part) {
    extern __shared__ __attribute__((aligned(16))) char lds[];
    const unsigned lds0 = (unsigned)(uintptr_t)lds;
    const int tid = threadIdx.x, lane = tid & 63;
    const int wid = __builtin_amdgcn_readfirstlane(tid >> 6);
    const int rb = wid & 3, kp = wid >> 2;
    const int bid = blockIdx.x;
    const int sk = bid & 7, nt = (bid >> 3) & 3, mt = bid >> 5;
    const char* Bg[5];
#pragma unroll
    for (int i = 0; i < 5; ++i) {
        const int L = i * 1024 + lane * 16;
        const int row = L / 320, cp = (L % 320) >> 4;
        int c = cp ^ (((row >> 3) & 1) << 1);
        c = c < 18 ? c : 0;
        Bg[i] = (const char*)wo + ((size_t)(256 * sk + 16 * wid + row) * DM + 144 * nt) * 2 + c * 16;
    }
    const char* Ag = (const char*)Og + ((size_t)((8 * mt + 2 * rb) * 128 + 8 * sk + kp) * 64 + lane) * 16;
    u32x4 an0, an1, an2, an3;
#define K2_B(c) do { const size_t ro_ = (size_t)(((c) >> 1) * 2048 + ((c) & 1) * 128) * DM * 2; \
        _Pragma("unroll") for (int i = 0; i < 5; ++i) glds16(Bg[i] + ro_, lds0 + (40 * (c) + 5 * wid + i) * 1024); } while (0)
#define K2_A(c) do { const char* a_ = Ag + (((c) >> 1) * 64 + ((c) & 1) * 4) * 1024; \
        asm volatile("global_load_dwordx4 %0, %1, off" : "=v"(an0) : "v"(a_) : "memory"); \
        asm volatile("global_load_dwordx4 %0, %1, off" : "=v"(an1) : "v"(a_ + 131072) : "memory"); \
        asm volatile("global_load_dwordx4 %0, %1, off offset:2048" : "=v"(an2) : "v"(a_) : "memory"); \
        asm volatile("global_load_dwordx4 %0, %1, off offset:2048" : "=v"(an3) : "v"(a_ + 131072) : "memory"); } while (0)
    K2_B(0); K2_A(0); K2_B(1);
    const int gi = lane >> 4, vq = (lane & 15) >> 2, vp = lane & 3;
    unsigned bbE = lds0 + (32 * kp + 8 * gi + vq) * 320 + 8 * vp + 32 * (gi & 1);
    unsigned bbO = bbE - 64 * (gi & 1);
    f32x4 acc[2][9];
#pragma unroll
    for (int ct = 0; ct < 9; ++ct) { acc[0][ct] = f32x4{0.f, 0.f, 0.f, 0.f}; acc[1][ct] = f32x4{0.f, 0.f, 0.f, 0.f}; }
#pragma unroll 1
    for (int c = 0; c < 4; ++c) {
        if (c < 3) asm volatile("s_waitcnt vmcnt(5)" ::: "memory");
        else asm volatile("s_waitcnt vmcnt(0)" ::: "memory");
        asm volatile("s_barrier" : "+v"(an0), "+v"(an1), "+v"(an2), "+v"(an3) :: "memory");
        const bf16x8 a[4] = {__builtin_bit_cast(bf16x8, an0), __builtin_bit_cast(bf16x8, an1),
                             __builtin_bit_cast(bf16x8, an2), __builtin_bit_cast(bf16x8, an3)};
        bf16x8 ac[4];
#pragma unroll
        for (int i = 0; i < 4; ++i) { ac[i] = a[i]; asm volatile("" : "+v"(ac[i])); }
        if (c < 3) K2_A(c + 1);
        if (c < 2) K2_B(c + 2);
#pragma unroll
        for (int j = 0; j < 2; ++j) {
#pragma unroll
            for (int ct = 0; ct < 9; ++ct) {
                const unsigned ad = ((ct & 1) ? bbO : bbE) + 2 * j * 10240 + 32 * ct;
                const s16x4 lo = vtr(ad), h4 = vtr(ad + 1280);
                const bf16x8 bf = {lo[0], lo[1], lo[2], lo[3], h4[0], h4[1], h4[2], h4[3]};
                acc[0][ct] = MFMA16(bf, ac[2 * j], acc[0][ct]);
                acc[1][ct] = MFMA16(bf, ac[2 * j + 1], acc[1][ct]);
            }
        }
        bbE += 40960; bbO += 40960;
    }
#undef K2_A
#undef K2_B
    asm volatile("s_waitcnt lgkmcnt(0)\n\ts_barrier" ::: "memory");
#pragma unroll
    for (int ct = 0; ct < 9; ++ct)
        *(LAS f32x4*)(uintptr_t)(lds0 + (wid * 9 + ct) * 1024 + lane * 16) = kp ? acc[0][ct] : acc[1][ct];
    asm volatile("s_waitcnt lgkmcnt(0)\n\ts_barrier" ::: "memory");
    float* pp = part + ((size_t)(sk * 64 + 8 * mt + 2 * rb + kp) * 36 + 9 * nt) * 256 + lane * 4;
#pragma unroll
    for (int ct = 0; ct < 9; ++ct) {
        const f32x4 v = *(const LAS f32x4*)(uintptr_t)(lds0 + ((wid ^ 4) * 9 + ct) * 1024 + lane * 16);
        *(f32x4*)(pp + ct * 256) = (kp ? acc[1][ct] : acc[0][ct]) + v;
    }
}

__global__ __launch_bounds__(256) void reduce_kernel(const float* __restrict__ part, u16* __restrict__ out) {
    const int lane = threadIdx.x & 63;
    const int u = blockIdx.x * 4 + (threadIdx.x >> 6);
    const float* p = part + (size_t)u * 256 + lane * 4;
    f32x4 t = *(const f32x4*)p;
#pragma unroll
    for (int s = 1; s < 8; ++s) t += *(const f32x4*)(p + (size_t)s * 64 * 36 * 256);
    const int rt = u / 36, ct = u % 36;
    u32x2 w = {cvtpk(t[0], t[1]), cvtpk(t[2], t[3])};
    *(u32x2*)(out + (size_t)(16 * rt + (lane & 15)) * DM + 16 * ct + 4 * (lane >> 4)) = w;
}

extern "C" void kernel_launch(void* const* d_in, const int* in_sizes, int n_in, void* d_out, int out_size,
                              void* d_ws, size_t ws_size, hipStream_t stream) {
    const u16* x = (const u16*)d_in[0];
    const u16* kc = (const u16*)d_in[1];
    const u16* vc = (const u16*)d_in[2];
    const u16* wq = (const u16*)d_in[3];
    const u16* wo = (const u16*)d_in[4];
    u16* Og = (u16*)d_ws;
    float* part = (float*)((char*)d_ws + (size_t)NB * SQ * NQ * 2);
    (void)hipFuncSetAttribute((const void*)attn_kernel, hipFuncAttributeMaxDynamicSharedMemorySize, LDS1_BYTES);
    (void)hipFuncSetAttribute((const void*)oproj_kernel, hipFuncAttributeMaxDynamicSharedMemorySize, LDS2_BYTES);
    attn_kernel<<<NB * HKV, 512, LDS1_BYTES, stream>>>(x, kc, vc, wq, Og, wo);
    oproj_kernel<<<256, 512, LDS2_BYTES, stream>>>(Og, wo, part);
    reduce_kernel<<<576, 256, 0, stream>>>(part, (u16*)d_out);
}
```

```cpp
#define XS0 5
#define XS1 7
#define YS0 5
#define YS1 7
#include <hip/hip_runtime.h>
#include <hip/hip_bf16.h>
#include <stdint.h>

typedef short bf16x8 __attribute__((ext_vector_type(8)));
typedef short s16x4 __attribute__((ext_vector_type(4)));
typedef float f32x16 __attribute__((ext_vector_type(16)));
typedef float f32x4 __attribute__((ext_vector_type(4)));
typedef unsigned u32x4 __attribute__((ext_vector_type(4)));
typedef unsigned u32x2 __attribute__((ext_vector_type(2)));
typedef unsigned short u16;
#define LAS __attribute__((address_space(3)))
typedef LAS const char* lds_cptr;

constexpr int NB = 16, SQ = 64, DM = 576, HQ = 32, HKV = 16, SKV = 1024, DH = 128;
constexpr int NQ = HQ * DH;
constexpr int NT = SKV / 64;
constexpr int SLOT = 32768;
constexpr int LDS_Q = 4 * SLOT;
constexpr int LDS1_BYTES = 5 * SLOT;
constexpr float RTHR = 8.0f;
constexpr float C2 = 0.088388347648318447f * 1.4426950408889634f;

#define MFMA32(a, b, c) __builtin_amdgcn_mfma_f32_32x32x16_bf16(a, b, c, 0, 0, 0)

typedef __bf16 bf16x2_t __attribute__((ext_vector_type(2)));
typedef float f32x2 __attribute__((ext_vector_type(2)));
__device__ __forceinline__ unsigned cvtpk(float lo, float hi) {
    f32x2 v = {lo, hi};
    return __builtin_bit_cast(unsigned, __builtin_convertvector(v, bf16x2_t));
}
__device__ __forceinline__ void glds16(const void* g, unsigned lds_base) {
    unsigned sv;
    asm volatile("s_mov_b32 %0, m0\n\ts_mov_b32 m0, %2\n\ts_nop 0\n\tglobal_load_lds_dwordx4 %1, off\n\ts_mov_b32 m0, %0"
                 : "=&s"(sv) : "v"(g), "s"(lds_base) : "memory");
}
__device__ __forceinline__ void glds16nt(const void* g, unsigned lds_base) {
    unsigned sv;
    asm volatile("s_mov_b32 %0, m0\n\ts_mov_b32 m0, %2\n\ts_nop 0\n\tglobal_load_lds_dwordx4 %1, off nt\n\ts_mov_b32 m0, %0"
                 : "=&s"(sv) : "v"(g), "s"(lds_base) : "memory");
}
__device__ __forceinline__ s16x4 vtr(unsigned a) {
    return __builtin_amdgcn_ds_read_tr16_b64_v4i16((LAS s16x4*)(uintptr_t)a);
}
__device__ __forceinline__ bf16x8 ldsr128(unsigned a) { return *(const LAS bf16x8*)(uintptr_t)a; }
__device__ __forceinline__ float swap_max(float v) {
    auto rr = __builtin_amdgcn_permlane32_swap(__float_as_uint(v), __float_as_uint(v), false, false);
    return __builtin_fmaxf(__uint_as_float(rr[0]), __uint_as_float(rr[1]));
}
__device__ __forceinline__ float max3f(float a, float b, float c) { float r; asm("v_max3_f32 %0, %1, %2, %3" : "=v"(r) : "v"(a), "v"(b), "v"(c)); return r; }
__device__ __forceinline__ float swap_max3(float v) {
    auto rr = __builtin_amdgcn_permlane32_swap(__float_as_uint(v), __float_as_uint(v), false, false);
    return max3f(__uint_as_float(rr[0]), __uint_as_float(rr[1]), __uint_as_float(rr[1]));
}
__device__ __forceinline__ float swap_add(float v) {
    auto rr = __builtin_amdgcn_permlane32_swap(__float_as_uint(v), __float_as_uint(v), false, false);
    return __uint_as_float(rr[0]) + __uint_as_float(rr[1]);
}

constexpr int XIMG = 0;
constexpr int WRING = 73728;
__global__ __launch_bounds__(512, 2) void attn_kernel(const u16* __restrict__ x, const u16* __restrict__ Kc,
                                                      const u16* __restrict__ Vc, const u16* __restrict__ wq,
                                                      u16* __restrict__ Og, const u16* __restrict__ wo) {
    extern __shared__ __attribute__((aligned(16))) char lds[];
    const unsigned lds0 = (unsigned)(uintptr_t)lds;
    const int tid = threadIdx.x, lane = tid & 63, r32 = lane & 31, hi = lane >> 5;
    const int wid = __builtin_amdgcn_readfirstlane(tid >> 6);
    const int qb = wid & 3, kh = wid >> 2;
    const int bid = blockIdx.x, b = 8 * (bid & 1) + ((bid >> 3) & 7), kvh = 4 * ((bid >> 1) & 3) + (bid >> 6);
    const int head = 2 * kvh + (qb >> 1);
    const int gi = lane >> 4, vq = (lane & 15) >> 2, vp = lane & 3;

    const unsigned vsrc = (16 * (wid >> 1) + ((lane >> 2) & 7)) * 256 + (wid & 1) * 128 + hi * 64;
    const unsigned voffV = vsrc + vp * 16;
    const unsigned voffK0 = vsrc + ((vp ^ (wid & 2)) << 4), voffK1 = vsrc + ((vp ^ ((wid & 2) | 1)) << 4);
    const char* Kg = (const char*)Kc + (size_t)(b * HKV + kvh) * SKV * DH * 2;
    const char* Vg = (const char*)Vc + (size_t)(b * HKV + kvh) * SKV * DH * 2;
    const unsigned dwo = lds0 + (wid >> 1) * 4096 + (wid & 1) * 1024;
#define KVDMA(voff, gp, dst, OFF) asm volatile("s_mov_b32 m0, %2\n\ts_nop 0\n\tglobal_load_lds_dwordx4 %0, %1 offset:" #OFF " nt" \
            :: "v"(voff), "s"(gp), "s"(dst) : "memory")
#define VSO(tn) ((((tn) + 3) & 3) * 16384)
#define KSO(tn) (65536 + ((tn) & 3) * 16384)
#define DMA_V0(tn) KVDMA(voffV, Vg + (tn) * 16384, dwo + VSO(tn), 0)
#define DMA_V1(tn) KVDMA(voffV, Vg + (tn) * 16384, dwo + VSO(tn), 2048)
#define DMA_K0(tn) KVDMA(voffK0, Kg + (tn) * 16384, dwo + KSO(tn), 0)
#define DMA_K1(tn) KVDMA(voffK1, Kg + (tn) * 16384, dwo + KSO(tn), 2048)
#define DMA_V(tn) do { DMA_V0(tn); DMA_V1(tn); } while (0)
#define DMA_K(tn) do { DMA_K0(tn); DMA_K1(tn); } while (0)
    bf16x8 qf[8];
    {
        int kb = (b + 2 * (kvh & 3)) % 9;
        const char* Xg = (const char*)x + ((size_t)b * SQ + 8 * wid + (lane >> 3)) * DM * 2 + (((lane & 7) ^ ((4 * wid + (lane >> 4)) & 7)) << 4);
        const char* Wg0 = (const char*)wq + ((size_t)(8 * wid + hi) * NQ + kvh * 256) * 2 + ((r32 ^ (hi << 2)) << 4);
        const char* Wg1 = (const char*)wq + ((size_t)(8 * wid + 2 + hi) * NQ + kvh * 256) * 2 + ((r32 ^ ((2 + hi) << 2)) << 4);
#define QS_PIECE(slot, p) do { const unsigned s_ = lds0 + (slot) * 40960 + wid * 4096; const size_t go_ = (size_t)kb * 64 * NQ * 2; \
            if ((p) == 0) glds16(Wg0 + go_, s_); \
            if ((p) == 1) glds16(Wg1 + go_, s_ + 1024); \
            if ((p) == 2) glds16(Wg0 + go_ + 4 * NQ * 2, s_ + 2048); \
            if ((p) == 3) glds16(Wg1 + go_ + 4 * NQ * 2, s_ + 3072); \
            if ((p) == 4) { glds16(Xg + kb * 128, lds0 + (slot) * 40960 + 32768 + wid * 1024); kb = kb == 8 ? 0 : kb + 1; } } while (0)
#define QS_ISSUE(slot) do { QS_PIECE(slot, 0); QS_PIECE(slot, 1); QS_PIECE(slot, 2); QS_PIECE(slot, 3); QS_PIECE(slot, 4); } while (0)
#pragma unroll 1
        for (int j = 0; j < 2; ++j) { QS_ISSUE(j); asm volatile("s_barrier" ::: "memory"); }
        const int pp = ((vp & 1) << 1) | (vp >> 1);
        const unsigned abase = lds0 + (8 * hi + vq) * 512 + (((4 * wid + 2 * (gi & 1) + (pp >> 1)) ^ (vq << 2)) << 4) + 8 * (pp & 1);
        const unsigned xbase = lds0 + 32768 + r32 * 128 + ((hi ^ ((r32 >> 1) & 7)) << 4);
        f32x16 acc0 = {}, acc1 = {};
        s16x4 lo[2][4], h4[2][4]; bf16x8 x0[2][4], x1[2][4];
        asm volatile("s_waitcnt vmcnt(5)\n\ts_barrier" ::: "memory");
        QS_ISSUE(2);
#pragma unroll
        for (int i = 0; i < 4; ++i) {
            lo[0][i] = vtr(abase + i * 8192); h4[0][i] = vtr(abase + i * 8192 + 2048);
            x0[0][i] = ldsr128(xbase ^ (i << 5)); x1[0][i] = ldsr128((xbase ^ (i << 5)) + 4096);
        }
#pragma unroll
        for (int st = 0; st < 9; ++st) {
            if (st == 0) asm volatile("s_waitcnt vmcnt(5)" ::: "memory");
            else if (st < 6) asm volatile("s_waitcnt vmcnt(10)" ::: "memory");
            else if (st == 6) asm volatile("s_waitcnt vmcnt(9)" ::: "memory");
            else if (st == 7) asm volatile("s_waitcnt vmcnt(8)" ::: "memory");
            asm volatile("s_waitcnt lgkmcnt(0)\n\ts_barrier" ::: "memory");
            const unsigned so = ((st + 1) & 3) * 40960;
            const int cu = st & 1, nx = cu ^ 1;
            __builtin_amdgcn_sched_barrier(0);
            if (st == 0) QS_ISSUE(3);
#pragma unroll
            for (int i = 0; i < 4; ++i) {
                const bf16x8 a0 = {lo[cu][i][0], lo[cu][i][1], lo[cu][i][2], lo[cu][i][3], h4[cu][i][0], h4[cu][i][1], h4[cu][i][2], h4[cu][i][3]};
                acc0 = MFMA32(a0, x0[cu][i], acc0);
                if (st < 8) { lo[nx][i] = vtr(abase + so + i * 8192); h4[nx][i] = vtr(abase + so + i * 8192 + 2048); }
                if (st < 5) QS_PIECE(st & 3, i);
                __builtin_amdgcn_sched_barrier(0);
                acc1 = MFMA32(a0, x1[cu][i], acc1);
                if (st < 8) { x0[nx][i] = ldsr128((xbase ^ (i << 5)) + so); x1[nx][i] = ldsr128((xbase ^ (i << 5)) + so + 4096); }
                if (st < 5 && i == 3) QS_PIECE(st & 3, 4);
                if (st == 5 && i == 3) { DMA_K(0); DMA_V(0); }
                if (st == 6 && i == 3) { DMA_K(1); DMA_K(2); }
                if (st == 7 && i == 3) DMA_K(3);
                __builtin_amdgcn_sched_barrier(0);
            }
        }
#undef QS_ISSUE
#undef QS_PIECE
#define QW(ACC, qblk) do { _Pragma("unroll") for (int s = 0; s < 2; ++s) { \
            u32x4 w_ = {cvtpk(ACC[8 * s] * C2, ACC[8 * s + 1] * C2), cvtpk(ACC[8 * s + 2] * C2, ACC[8 * s + 3] * C2), \
                        cvtpk(ACC[8 * s + 4] * C2, ACC[8 * s + 5] * C2), cvtpk(ACC[8 * s + 6] * C2, ACC[8 * s + 7] * C2)}; \
            *(LAS u32x4*)(uintptr_t)(lds0 + LDS_Q + (wid >> 2) * 16384 + (qblk) * 8192 + (2 * (wid & 3) + s) * 1024 + lane * 16) = w_; } } while (0)
        QW(acc0, 0); QW(acc1, 1);
#undef QW
        DMA_V(1); DMA_V(2);
    }
    asm volatile("s_waitcnt vmcnt(8) lgkmcnt(0)\n\ts_barrier" ::: "memory");
#pragma unroll
    for (int f = 0; f < 8; ++f) qf[f] = ldsr128(lds0 + LDS_Q + qb * 8192 + f * 1024 + lane * 16);

    const unsigned kb0 = lds0 + (4 * kh + (r32 >> 3)) * 2048 + (r32 & 7) * 64 + ((hi ^ (r32 >> 3)) << 4);
    const unsigned kb1 = kb0 ^ 32;
    const unsigned vb = lds0 + kh * 8192 + (4 * hi + vq) * 64 + (gi & 1) * 32 + vp * 8;
#define KRD(d0, so) ldsr128((((d0) & 1) ? kb1 : kb0) + (so) + ((d0) >> 1) * 512)

    float m_run, l_run = 0.f, tmax = 0.f;
    f32x16 o[4] = {};
    f32x16 sA, sB, negm;

    bf16x8 kf[8];
    {
#pragma unroll
        for (int d0 = 0; d0 < 8; ++d0) kf[d0] = KRD(d0, KSO(0));
        sA = f32x16{};
#pragma unroll
        for (int d0 = 0; d0 < 8; ++d0) sA = MFMA32(kf[d0], qf[d0], sA);
#pragma unroll
        for (int d0 = 0; d0 < 8; ++d0) kf[d0] = KRD(d0, KSO(1));
        asm volatile("s_waitcnt lgkmcnt(0)\n\ts_barrier" ::: "memory");
        DMA_K(4);
        m_run = sA[0];
#pragma unroll
        for (int r = 1; r < 16; ++r) m_run = __builtin_fmaxf(m_run, sA[r]);
        m_run = swap_max(m_run);
#pragma unroll
        for (int r = 0; r < 16; ++r) { sA[r] -= m_run; negm[r] = -m_run; }
    }
#define EXP1(r) do { sc[r] = __builtin_amdgcn_exp2f(sc[r]); } while (0)
#define SUM1(k) do { ps0 += sc[2 * (k)]; ps1 += sc[2 * (k) + 1]; pw[k] = cvtpk(sc[2 * (k)], sc[2 * (k) + 1]); asm volatile("" : "+v"(pw[k]), "+v"(ps0), "+v"(ps1)); } while (0)
#define STEP(t, SC_, SN_, TAIL) do { \
        f32x16& sc = SC_; f32x16& sn = SN_; \
        s16x4 vlo[8], vhi[8]; \
        if (!TAIL || (t) < 12) asm volatile("s_waitcnt vmcnt(8)" ::: "memory"); \
        else if ((t) == 12) asm volatile("s_waitcnt vmcnt(6)" ::: "memory"); \
        else if ((t) == 13) asm volatile("s_waitcnt vmcnt(4)" ::: "memory"); \
        else if ((t) == 14) asm volatile("s_waitcnt vmcnt(2)" ::: "memory"); \
        else asm volatile("s_waitcnt vmcnt(0)" ::: "memory"); \
        asm volatile("s_waitcnt lgkmcnt(0)\n\ts_barrier" ::: "memory"); \
        if (__builtin_expect(__any(tmax > RTHR), 0)) { \
            const float dl = __builtin_fmaxf(tmax, 0.f); \
            const float alpha = __builtin_amdgcn_exp2f(-dl); \
            m_run += dl; l_run *= alpha; \
            _Pragma("unroll") for (int r = 0; r < 16; ++r) { sc[r] -= dl; negm[r] = -m_run; } \
            _Pragma("unroll") for (int dt = 0; dt < 4; ++dt) \
                _Pragma("unroll") for (int r = 0; r < 16; ++r) o[dt][r] *= alpha; \
        } \
        const unsigned va = vb + VSO(t); \
        const unsigned ka0 = kb0 + KSO((t) + 2), ka1 = kb1 + KSO((t) + 2);       \
        unsigned pw[8]; \
        float ps0 = 0.f, ps1 = 0.f, t2; \
        __builtin_amdgcn_sched_barrier(0); \
          \
        _Pragma("unroll") for (int i = 0; i < 8; ++i) { \
            sn = MFMA32(kf[i], qf[i], i == 0 ? negm : sn);                   \
            const int ss = i >> 2, dt = i & 3; \
            vlo[2 * dt + ss] = vtr(va + ss * 4096 + dt * 512); \
            vhi[2 * dt + ss] = vtr(va + ss * 4096 + 2048 + dt * 512); \
            if (i < 4) { EXP1(2 * i); EXP1(2 * i + 1); } else EXP1(4 + i); \
            if (i >= 1 && i <= 4) SUM1(i - 1); \
            if (i == 6) SUM1(4); \
            asm volatile("" : "+v"(sn), "+v"(sc)); \
            if (i == XS0 && (!TAIL || (t) < 13)) DMA_V0((t) + 3); \
            if (i == XS1 && (!TAIL || (t) < 13)) DMA_V1((t) + 3); \
            __builtin_amdgcn_sched_barrier(0); \
        } \
          \
        _Pragma("unroll") for (int i = 0; i < 8; ++i) { \
            const int ss = i >> 2, dt = i & 3; \
            const s16x4 lo = vlo[2 * dt + ss], hi4 = vhi[2 * dt + ss]; \
            const bf16x8 vf = {lo[0], lo[1], lo[2], lo[3], hi4[0], hi4[1], hi4[2], hi4[3]}; \
            const bf16x8 pf = __builtin_bit_cast(bf16x8, u32x4{pw[4 * ss], pw[4 * ss + 1], pw[4 * ss + 2], pw[4 * ss + 3]}); \
            o[dt] = MFMA32(vf, pf, o[dt]); \
            if (i < 4) { kf[2 * i] = ldsr128(ka0 + i * 512); kf[2 * i + 1] = ldsr128(ka1 + i * 512); } \
            if (i < 2) { EXP1(12 + 2 * i); EXP1(13 + 2 * i); asm volatile("" : "+v"(sc)); } \
            if (i < 3) SUM1(5 + i); \
            if (i == 3) { l_run += ps0 + ps1; asm volatile("" : "+v"(sn), "+v"(l_run)); \
                          tmax = max3f(sn[0], sn[1], sn[2]); t2 = max3f(sn[3], sn[4], sn[5]); asm volatile("" : "+v"(tmax), "+v"(t2)); } \
            if (i == 4) { tmax = max3f(tmax, sn[6], sn[7]); t2 = max3f(t2, sn[8], sn[9]); tmax = max3f(tmax, sn[10], sn[11]); asm volatile("" : "+v"(tmax), "+v"(t2)); } \
            if (i == 5) { t2 = max3f(t2, sn[12], sn[13]); tmax = max3f(tmax, sn[14], sn[15]); tmax = max3f(tmax, t2, t2); asm volatile("" : "+v"(tmax)); } \
            if (i == 6) { tmax = swap_max3(tmax); asm volatile("" : "+v"(tmax)); } \
            if (i == YS0 && (!TAIL || (t) < 11)) DMA_K0((t) + 5); \
            if (i == YS1 && (!TAIL || (t) < 11)) DMA_K1((t) + 5); \
            __builtin_amdgcn_sched_barrier(0); \
        } } while (0)
#pragma unroll 1
    for (int t = 0; t < NT; t += 2) { STEP(t, sA, sB, true); STEP(t + 1, sB, sA, true); }
#undef STEP
#undef EXP1
#undef SUM1
    {
        const char* wp = (const char*)wo + (size_t)(1024 * ((bid >> 1) & 3) + 32 * (bid >> 3)) * DM * 2 + wid * 1024 + lane * 16;
#pragma unroll
        for (int i = 0; i < 4; ++i) glds16(wp + i * 8192, lds0 + LDS_Q + i * 8192 + wid * 1024);
        if (wid < 4) glds16(wp + 32768, lds0 + LDS_Q + wid * 1024);
    }
    l_run = swap_add(l_run);
    asm volatile("s_waitcnt lgkmcnt(0)\n\ts_barrier" ::: "memory");
    {
        f32x16 keep0, keep1, send0, send1;
        if (kh == 0) { keep0 = o[0]; keep1 = o[1]; send0 = o[2]; send1 = o[3]; }
        else         { keep0 = o[2]; keep1 = o[3]; send0 = o[0]; send1 = o[1]; }
        const unsigned xb = lds0 + wid * 9216;
#pragma unroll
        for (int r4 = 0; r4 < 4; ++r4) {
            f32x4 v0 = {send0[4 * r4], send0[4 * r4 + 1], send0[4 * r4 + 2], send0[4 * r4 + 3]};
            f32x4 v1 = {send1[4 * r4], send1[4 * r4 + 1], send1[4 * r4 + 2], send1[4 * r4 + 3]};
            *(LAS f32x4*)(uintptr_t)(xb + r4 * 1024 + lane * 16) = v0;
            *(LAS f32x4*)(uintptr_t)(xb + (4 + r4) * 1024 + lane * 16) = v1;
        }
        *(LAS float*)(uintptr_t)(xb + 8192 + lane * 4) = m_run;
        *(LAS float*)(uintptr_t)(xb + 8192 + 256 + lane * 4) = l_run;
        asm volatile("s_waitcnt lgkmcnt(0)\n\ts_barrier" ::: "memory");
        const unsigned pb = lds0 + (wid ^ 4) * 9216;
        const float m2 = *(const LAS float*)(uintptr_t)(pb + 8192 + lane * 4);
        const float l2 = *(const LAS float*)(uintptr_t)(pb + 8192 + 256 + lane * 4);
        const float mm = __builtin_fmaxf(m_run, m2);
        const float a1 = __builtin_amdgcn_exp2f(m_run - mm), a2 = __builtin_amdgcn_exp2f(m2 - mm);
        const float inv = 1.0f / (l_run * a1 + l2 * a2);
        const float w1 = a1 * inv, w2 = a2 * inv;
        const int row = b * SQ + 32 * (qb & 1) + r32;
        char* ob = (char*)Og + ((size_t)((row >> 4) * 128 + head * 4 + 2 * kh) * 64 + hi * 16 + (row & 15)) * 16;
#define OSTORE(KEEP, i) do { u32x2 w_[4]; \
            _Pragma("unroll") for (int r4 = 0; r4 < 4; ++r4) { \
                const f32x4 v = *(const LAS f32x4*)(uintptr_t)(pb + (4 * (i) + r4) * 1024 + lane * 16); \
                const float e0 = KEEP[4 * r4] * w1 + v[0] * w2, e1 = KEEP[4 * r4 + 1] * w1 + v[1] * w2; \
                const float e2 = KEEP[4 * r4 + 2] * w1 + v[2] * w2, e3 = KEEP[4 * r4 + 3] * w1 + v[3] * w2; \
                w_[r4] = u32x2{cvtpk(e0, e1), cvtpk(e2, e3)}; } \
            _Pragma("unroll") for (int g = 0; g < 2; ++g) { \
                u32x4 o4 = {w_[g].x, w_[g].y, w_[g + 2].x, w_[g + 2].y}; \
                asm volatile("global_store_dwordx4 %0, %1, off sc1" :: "v"(ob + (i) * 1024 + g * 512), "v"(o4) : "memory"); } } while (0)
        OSTORE(keep0, 0); OSTORE(keep1, 1);
#undef OSTORE
    }
}

constexpr int LDS2_BYTES = 1024 * 160;
#define MFMA16(a, b, c) __builtin_amdgcn_mfma_f32_16x16x32_bf16(a, b, c, 0, 0, 0)
__global__ __launch_bounds__(512, 2) void oproj_kernel(const u16* __restrict__ Og, const u16* __restrict__ wo,
                                                       float* __restrict__ part) {
    extern __shared__ __attribute__((aligned(16))) char lds[];
    const unsigned lds0 = (unsigned)(uintptr_t)lds;
    const int tid = threadIdx.x, lane = tid & 63;
    const int wid = __builtin_amdgcn_readfirstlane(tid >> 6);
    const int rb = wid & 3, kp = wid >> 2;
    const int bid = blockIdx.x;
    const int sk = (bid >> 1) & 3, mt = 4 * (bid & 1) + ((bid >> 3) & 3), nt = bid >> 5;
    const char* Bg[5];
#pragma unroll
    for (int i = 0; i < 5; ++i) {
        const int L = i * 1024 + lane * 16;
        const int row = L / 160;
        int c = (L % 160) >> 4;
        c = c < 9 ? c : 0;
        Bg[i] = (const char*)wo + ((size_t)(1024 * sk + 32 * wid + row) * DM + 72 * nt) * 2 + c * 16;
    }
    const char* Ag = (const char*)Og + ((size_t)((8 * mt + 2 * rb) * 128 + 32 * sk + kp) * 64 + lane) * 16;
    u32x4 an[4][8];
#define K2_B(c) do { _Pragma("unroll") for (int i = 0; i < 5; ++i) \
        glds16(Bg[i] + (size_t)(c) * 256 * DM * 2, lds0 + (40 * (c) + 5 * wid + i) * 1024); } while (0)
#define K2_A(c) do { _Pragma("unroll") for (int j = 0; j < 4; j += 2) { const char* a_ = Ag + (8 * (c) + 2 * j) * 1024; \
        asm volatile("global_load_dwordx4 %0, %1, off" : "=v"(an[c][2 * j]) : "v"(a_) : "memory"); \
        asm volatile("global_load_dwordx4 %0, %1, off" : "=v"(an[c][2 * j + 1]) : "v"(a_ + 131072) : "memory"); \
        asm volatile("global_load_dwordx4 %0, %1, off offset:2048" : "=v"(an[c][2 * j + 2]) : "v"(a_) : "memory"); \
        asm volatile("global_load_dwordx4 %0, %1, off offset:2048" : "=v"(an[c][2 * j + 3]) : "v"(a_ + 131072) : "memory"); } } while (0)
    K2_B(0);
    asm volatile("s_barrier" ::: "memory");
    K2_A(0); K2_B(1);
    const int gi = lane >> 4, vq = (lane & 15) >> 2, vp = lane & 3;
    const unsigned bb = lds0 + (32 * kp + 4 * gi + vq) * 160 + 8 * vp;
    f32x4 acc[2][5];
#pragma unroll
    for (int ct = 0; ct < 5; ++ct) { acc[0][ct] = f32x4{0.f, 0.f, 0.f, 0.f}; acc[1][ct] = f32x4{0.f, 0.f, 0.f, 0.f}; }
    s16x4 blo[2][5], bhi[2][5];
    asm volatile("s_waitcnt vmcnt(13)\n\ts_barrier" ::: "memory");
#pragma unroll
    for (int ct = 0; ct < 5; ++ct) { blo[0][ct] = vtr(bb + 32 * ct); bhi[0][ct] = vtr(bb + 32 * ct + 2560); }
#pragma unroll
    for (int s = 0; s < 16; ++s) {
        const int c = s >> 2, j = s & 3;
        const int young = (6 - 2 * j) + (c < 3 ? 5 + (j < 2 ? 4 * j : 8) + (j == 3 && c < 2 ? 5 : 0) : 0);
        asm volatile("s_waitcnt vmcnt(%2)" : "+v"(an[c][2 * j]), "+v"(an[c][2 * j + 1]) : "n"(young) : "memory");

        if (j == 3 && c < 3) asm volatile("s_waitcnt vmcnt(%0)\n\ts_barrier" :: "n"(c < 2 ? 13 : 8) : "memory");
        __builtin_amdgcn_sched_barrier(0);
#pragma unroll
        for (int ct = 0; ct < 5; ++ct) {
            if (s < 15) {
                const unsigned ad = bb + (s + 1) * 10240 + 32 * ct;
                blo[(s + 1) & 1][ct] = vtr(ad); bhi[(s + 1) & 1][ct] = vtr(ad + 2560);
            }
            const s16x4 lo = blo[s & 1][ct], h4 = bhi[s & 1][ct];
            const bf16x8 bf = {lo[0], lo[1], lo[2], lo[3], h4[0], h4[1], h4[2], h4[3]};
            acc[0][ct] = MFMA16(bf, __builtin_bit_cast(bf16x8, an[c][2 * j]), acc[0][ct]);
            acc[1][ct] = MFMA16(bf, __builtin_bit_cast(bf16x8, an[c][2 * j + 1]), acc[1][ct]);
            if (j < 2 && c < 3 && ct < 4) {
                const char* a_ = Ag + (8 * (c + 1) + 4 * j + (ct >> 1) * 2) * 1024 + (ct & 1) * 131072;
                asm volatile("global_load_dwordx4 %0, %1, off" : "=v"(an[(c + 1) & 3][4 * j + ct]) : "v"(a_) : "memory");
            }
            if (j == 2 && c < 2) glds16(Bg[ct] + (size_t)(c + 2) * 256 * DM * 2, lds0 + (40 * (c + 2) + 5 * wid + ct) * 1024);
            __builtin_amdgcn_sched_barrier(0);
        }
    }
#undef K2_A
#undef K2_B
    asm volatile("s_waitcnt lgkmcnt(0)\n\ts_barrier" ::: "memory");
#pragma unroll
    for (int ct = 0; ct < 5; ++ct)
        *(LAS f32x4*)(uintptr_t)(lds0 + (wid * 5 + ct) * 1024 + lane * 16) = kp ? acc[0][ct] : acc[1][ct];
    asm volatile("s_waitcnt lgkmcnt(0)\n\ts_barrier" ::: "memory");
    float* pp = part + ((size_t)(sk * 64 + 8 * mt + 2 * rb + kp) * 8 + nt) * 1152 + lane * 4;
#pragma unroll
    for (int ct = 0; ct < 5; ++ct) {
        const f32x4 v = *(const LAS f32x4*)(uintptr_t)(lds0 + ((wid ^ 4) * 5 + ct) * 1024 + lane * 16);
        const f32x4 r_ = (kp ? acc[1][ct] : acc[0][ct]) + v;
        if (ct < 4 || lane < 32)
            asm volatile("global_store_dwordx4 %0, %1, off sc1" :: "v"(pp + ct * 256), "v"(r_) : "memory");
    }
}

__global__ __launch_bounds__(256) void reduce_kernel(const float* __restrict__ part, u16* __restrict__ out) {
    const int u = blockIdx.x * 256 + threadIdx.x;
    const float* p = part + (size_t)u * 4;
    f32x4 t = *(const f32x4*)p;
#pragma unroll
    for (int s = 1; s < 4; ++s) t += *(const f32x4*)(p + (size_t)s * 64 * 8 * 1152);
    const int g = u / 288, e = u % 288;
    u32x2 w = {cvtpk(t[0], t[1]), cvtpk(t[2], t[3])};
    *(u32x2*)(out + (size_t)(16 * (g >> 3) + (e & 15)) * DM + 72 * (g & 7) + 4 * (e >> 4)) = w;
}

extern "C" void kernel_launch(void* const* d_in, const int* in_sizes, int n_in, void* d_out, int out_size,
                              void* d_ws, size_t ws_size, hipStream_t stream) {
    const u16* x = (const u16*)d_in[0];
    const u16* kc = (const u16*)d_in[1];
    const u16* vc = (const u16*)d_in[2];
    const u16* wq = (const u16*)d_in[3];
    const u16* wo = (const u16*)d_in[4];
    u16* Og = (u16*)d_ws;
    float* part = (float*)((char*)d_ws + (size_t)NB * SQ * NQ * 2);
    (void)hipFuncSetAttribute((const void*)attn_kernel, hipFuncAttributeMaxDynamicSharedMemorySize, LDS1_BYTES);
    (void)hipFuncSetAttribute((const void*)oproj_kernel, hipFuncAttributeMaxDynamicSharedMemorySize, LDS2_BYTES);
    attn_kernel<<<NB * HKV, 512, LDS1_BYTES, stream>>>(x, kc, vc, wq, Og, wo);
    oproj_kernel<<<256, 512, LDS2_BYTES, stream>>>(Og, wo, part);
    reduce_kernel<<<576, 256, 0, stream>>>(part, (u16*)d_out);
}
```

```cpp
#define XS0 5
#define XS1 7
#define YS0 5
#define YS1 7
#include <hip/hip_runtime.h>
#include <hip/hip_bf16.h>
#include <stdint.h>

typedef short bf16x8 __attribute__((ext_vector_type(8)));
typedef short s16x4 __attribute__((ext_vector_type(4)));
typedef float f32x16 __attribute__((ext_vector_type(16)));
typedef float f32x4 __attribute__((ext_vector_type(4)));
typedef unsigned u32x4 __attribute__((ext_vector_type(4)));
typedef unsigned u32x2 __attribute__((ext_vector_type(2)));
typedef unsigned short u16;
#define LAS __attribute__((address_space(3)))
typedef LAS const char* lds_cptr;
typedef __attribute__((address_space(1))) unsigned gu32;
#define RLX_AGENT __ATOMIC_RELAXED, __HIP_MEMORY_SCOPE_AGENT

constexpr int NB = 16, SQ = 64, DM = 576, HQ = 32, HKV = 16, SKV = 1024, DH = 128;
constexpr int NQ = HQ * DH;
constexpr int NT = SKV / 64;
constexpr int SLOT = 32768;
constexpr int LDS_Q = 4 * SLOT;
constexpr int LDS1_BYTES = 5 * SLOT;
constexpr size_t CNT_OFF = (size_t)20 << 20;
constexpr float RTHR = 8.0f;
constexpr float C2 = 0.088388347648318447f * 1.4426950408889634f;

#define MFMA32(a, b, c) __builtin_amdgcn_mfma_f32_32x32x16_bf16(a, b, c, 0, 0, 0)

typedef __bf16 bf16x2_t __attribute__((ext_vector_type(2)));
typedef float f32x2 __attribute__((ext_vector_type(2)));
__device__ __forceinline__ unsigned cvtpk(float lo, float hi) {
    f32x2 v = {lo, hi};
    return __builtin_bit_cast(unsigned, __builtin_convertvector(v, bf16x2_t));
}
__device__ __forceinline__ void glds16(const void* g, unsigned lds_base) {
    unsigned sv;
    asm volatile("s_mov_b32 %0, m0\n\ts_mov_b32 m0, %2\n\ts_nop 0\n\tglobal_load_lds_dwordx4 %1, off\n\ts_mov_b32 m0, %0"
                 : "=&s"(sv) : "v"(g), "s"(lds_base) : "memory");
}
__device__ __forceinline__ void glds16nt(const void* g, unsigned lds_base) {
    unsigned sv;
    asm volatile("s_mov_b32 %0, m0\n\ts_mov_b32 m0, %2\n\ts_nop 0\n\tglobal_load_lds_dwordx4 %1, off nt\n\ts_mov_b32 m0, %0"
                 : "=&s"(sv) : "v"(g), "s"(lds_base) : "memory");
}
__device__ __forceinline__ s16x4 vtr(unsigned a) {
    return __builtin_amdgcn_ds_read_tr16_b64_v4i16((LAS s16x4*)(uintptr_t)a);
}
__device__ __forceinline__ bf16x8 ldsr128(unsigned a) { return *(const LAS bf16x8*)(uintptr_t)a; }
__device__ __forceinline__ float swap_max(float v) {
    auto rr = __builtin_amdgcn_permlane32_swap(__float_as_uint(v), __float_as_uint(v), false, false);
    return __builtin_fmaxf(__uint_as_float(rr[0]), __uint_as_float(rr[1]));
}
__device__ __forceinline__ float max3f(float a, float b, float c) { float r; asm("v_max3_f32 %0, %1, %2, %3" : "=v"(r) : "v"(a), "v"(b), "v"(c)); return r; }
__device__ __forceinline__ float swap_max3(float v) {
    auto rr = __builtin_amdgcn_permlane32_swap(__float_as_uint(v), __float_as_uint(v), false, false);
    return max3f(__uint_as_float(rr[0]), __uint_as_float(rr[1]), __uint_as_float(rr[1]));
}
__device__ __forceinline__ float swap_add(float v) {
    auto rr = __builtin_amdgcn_permlane32_swap(__float_as_uint(v), __float_as_uint(v), false, false);
    return __uint_as_float(rr[0]) + __uint_as_float(rr[1]);
}

constexpr int XIMG = 0;
constexpr int WRING = 73728;
__global__ __launch_bounds__(512, 2) void attn_kernel(const u16* __restrict__ x, const u16* __restrict__ Kc,
                                                      const u16* __restrict__ Vc, const u16* __restrict__ wq,
                                                      u16* __restrict__ Og, const u16* __restrict__ wo) {
    extern __shared__ __attribute__((aligned(16))) char lds[];
    const unsigned lds0 = (unsigned)(uintptr_t)lds;
    const int tid = threadIdx.x, lane = tid & 63, r32 = lane & 31, hi = lane >> 5;
    const int wid = __builtin_amdgcn_readfirstlane(tid >> 6);
    const int qb = wid & 3, kh = wid >> 2;
    const int bid = blockIdx.x, b = 8 * (bid & 1) + ((bid >> 3) & 7), kvh = 4 * ((bid >> 1) & 3) + (bid >> 6);
    const int head = 2 * kvh + (qb >> 1);
    if (tid == 0 && bid < 64) ((unsigned*)((char*)Og + CNT_OFF))[bid * 32] = 0u;
    const int gi = lane >> 4, vq = (lane & 15) >> 2, vp = lane & 3;

    const unsigned vsrc = (16 * (wid >> 1) + ((lane >> 2) & 7)) * 256 + (wid & 1) * 128 + hi * 64;
    const unsigned voffV = vsrc + vp * 16;
    const unsigned voffK0 = vsrc + ((vp ^ (wid & 2)) << 4), voffK1 = vsrc + ((vp ^ ((wid & 2) | 1)) << 4);
    const char* Kg = (const char*)Kc + (size_t)(b * HKV + kvh) * SKV * DH * 2;
    const char* Vg = (const char*)Vc + (size_t)(b * HKV + kvh) * SKV * DH * 2;
    const unsigned dwo = lds0 + (wid >> 1) * 4096 + (wid & 1) * 1024;
#define KVDMA(voff, gp, dst, OFF) asm volatile("s_mov_b32 m0, %2\n\ts_nop 0\n\tglobal_load_lds_dwordx4 %0, %1 offset:" #OFF " nt" \
            :: "v"(voff), "s"(gp), "s"(dst) : "memory")
#define VSO(tn) ((((tn) + 3) & 3) * 16384)
#define KSO(tn) (65536 + ((tn) & 3) * 16384)
#define DMA_V0(tn) KVDMA(voffV, Vg + (tn) * 16384, dwo + VSO(tn), 0)
#define DMA_V1(tn) KVDMA(voffV, Vg + (tn) * 16384, dwo + VSO(tn), 2048)
#define DMA_K0(tn) KVDMA(voffK0, Kg + (tn) * 16384, dwo + KSO(tn), 0)
#define DMA_K1(tn) KVDMA(voffK1, Kg + (tn) * 16384, dwo + KSO(tn), 2048)
#define DMA_V(tn) do { DMA_V0(tn); DMA_V1(tn); } while (0)
#define DMA_K(tn) do { DMA_K0(tn); DMA_K1(tn); } while (0)
    bf16x8 qf[8];
    {
        int kb = (b + 2 * (kvh & 3)) % 9;
        const unsigned xo = (8 * wid + (lane >> 3)) * DM * 2 + (((lane & 7) ^ ((4 * wid + (lane >> 4)) & 7)) << 4);
        const unsigned wo0 = (8 * wid + hi) * NQ * 2 + ((r32 ^ (hi << 2)) << 4);
        const unsigned wo1 = (8 * wid + 2 + hi) * NQ * 2 + ((r32 ^ ((2 + hi) << 2)) << 4);
        const unsigned wo2 = wo0 + 4 * NQ * 2, wo3 = wo1 + 4 * NQ * 2;
        const char* Xs = (const char*)x + (size_t)b * SQ * DM * 2;
        const char* Ws = (const char*)wq + kvh * 512;
#define QDMA(voff, gp, dst) asm volatile("s_mov_b32 m0, %2\n\ts_nop 0\n\tglobal_load_lds_dwordx4 %0, %1" :: "v"(voff), "s"(gp), "s"(dst) : "memory")
#define QS_PIECE(slot, p) do { const unsigned s_ = lds0 + (slot) * 40960 + wid * 4096; const char* w_ = Ws + (size_t)kb * 64 * NQ * 2; \
            if ((p) == 0) QDMA(wo0, w_, s_); \
            if ((p) == 1) QDMA(wo1, w_, s_ + 1024); \
            if ((p) == 2) QDMA(wo2, w_, s_ + 2048); \
            if ((p) == 3) QDMA(wo3, w_, s_ + 3072); \
            if ((p) == 4) { QDMA(xo, Xs + kb * 128, lds0 + (slot) * 40960 + 32768 + wid * 1024); kb = kb == 8 ? 0 : kb + 1; } } while (0)
#define QS_ISSUE(slot) do { QS_PIECE(slot, 0); QS_PIECE(slot, 1); QS_PIECE(slot, 2); QS_PIECE(slot, 3); QS_PIECE(slot, 4); } while (0)
#pragma unroll 1
        for (int j = 0; j < 2; ++j) { QS_ISSUE(j); asm volatile("s_barrier" ::: "memory"); }
        const int pp = ((vp & 1) << 1) | (vp >> 1);
        const unsigned abase = lds0 + (8 * hi + vq) * 512 + (((4 * wid + 2 * (gi & 1) + (pp >> 1)) ^ (vq << 2)) << 4) + 8 * (pp & 1);
        const unsigned xbase = lds0 + 32768 + r32 * 128 + ((hi ^ ((r32 >> 1) & 7)) << 4);
        f32x16 acc0 = {}, acc1 = {};
        s16x4 lo[2][4], h4[2][4]; bf16x8 x0[2][4], x1[2][4];
        asm volatile("s_waitcnt vmcnt(5)\n\ts_barrier" ::: "memory");
        QS_ISSUE(2);
#pragma unroll
        for (int i = 0; i < 4; ++i) {
            lo[0][i] = vtr(abase + i * 8192); h4[0][i] = vtr(abase + i * 8192 + 2048);
            x0[0][i] = ldsr128(xbase ^ (i << 5)); x1[0][i] = ldsr128((xbase ^ (i << 5)) + 4096);
        }
#pragma unroll
        for (int st = 0; st < 9; ++st) {
            if (st == 0) asm volatile("s_waitcnt vmcnt(5)" ::: "memory");
            else if (st < 6) asm volatile("s_waitcnt vmcnt(10)" ::: "memory");
            else if (st == 6) asm volatile("s_waitcnt vmcnt(9)" ::: "memory");
            else if (st == 7) asm volatile("s_waitcnt vmcnt(8)" ::: "memory");
            asm volatile("s_waitcnt lgkmcnt(0)\n\ts_barrier" ::: "memory");
            const unsigned so = ((st + 1) & 3) * 40960;
            const int cu = st & 1, nx = cu ^ 1;
            unsigned aa = abase + so;
            asm volatile("" : "+v"(aa));
            __builtin_amdgcn_sched_barrier(0);
            if (st == 0) QS_ISSUE(3);
#pragma unroll
            for (int i = 0; i < 4; ++i) {
                const bf16x8 a0 = {lo[cu][i][0], lo[cu][i][1], lo[cu][i][2], lo[cu][i][3], h4[cu][i][0], h4[cu][i][1], h4[cu][i][2], h4[cu][i][3]};
                acc0 = MFMA32(a0, x0[cu][i], acc0);
                if (st < 8) { lo[nx][i] = vtr(aa + i * 8192); h4[nx][i] = vtr(aa + i * 8192 + 2048); }
                if (st < 5) QS_PIECE(st & 3, i);
                __builtin_amdgcn_sched_barrier(0);
                acc1 = MFMA32(a0, x1[cu][i], acc1);
                if (st < 8) { unsigned xa = (xbase ^ (i << 5)) + so; asm volatile("" : "+v"(xa)); x0[nx][i] = ldsr128(xa); x1[nx][i] = ldsr128(xa + 4096); }
                if (st < 5 && i == 3) QS_PIECE(st & 3, 4);
                if (st == 5 && i == 3) { DMA_K(0); DMA_V(0); }
                if (st == 6 && i == 3) { DMA_K(1); DMA_K(2); }
                if (st == 7 && i == 3) DMA_K(3);
                __builtin_amdgcn_sched_barrier(0);
            }
        }
#undef QS_ISSUE
#undef QS_PIECE
#define QW(ACC, qblk) do { _Pragma("unroll") for (int s = 0; s < 2; ++s) { \
            u32x4 w_ = {cvtpk(ACC[8 * s] * C2, ACC[8 * s + 1] * C2), cvtpk(ACC[8 * s + 2] * C2, ACC[8 * s + 3] * C2), \
                        cvtpk(ACC[8 * s + 4] * C2, ACC[8 * s + 5] * C2), cvtpk(ACC[8 * s + 6] * C2, ACC[8 * s + 7] * C2)}; \
            *(LAS u32x4*)(uintptr_t)(lds0 + LDS_Q + (wid >> 2) * 16384 + (qblk) * 8192 + (2 * (wid & 3) + s) * 1024 + lane * 16) = w_; } } while (0)
        QW(acc0, 0); QW(acc1, 1);
#undef QW
        DMA_V(1); DMA_V(2);
    }
    asm volatile("s_waitcnt vmcnt(8) lgkmcnt(0)\n\ts_barrier" ::: "memory");
#pragma unroll
    for (int f = 0; f < 8; ++f) qf[f] = ldsr128(lds0 + LDS_Q + qb * 8192 + f * 1024 + lane * 16);

    const unsigned kb0 = lds0 + (4 * kh + (r32 >> 3)) * 2048 + (r32 & 7) * 64 + ((hi ^ (r32 >> 3)) << 4);
    const unsigned kb1 = kb0 ^ 32;
    const unsigned vb = lds0 + kh * 8192 + (4 * hi + vq) * 64 + (gi & 1) * 32 + vp * 8;
#define KRD(d0, so) ldsr128((((d0) & 1) ? kb1 : kb0) + (so) + ((d0) >> 1) * 512)

    float m_run, l_run = 0.f, tmax = 0.f;
    f32x16 o[4] = {};
    f32x16 sA, sB, negm;

    bf16x8 kf[8];
    {
        bf16x8 k0[8];
        unsigned ka0 = kb0 + KSO(0), ka1 = kb1 + KSO(0);
        asm volatile("" : "+v"(ka0), "+v"(ka1));
#pragma unroll
        for (int d0 = 0; d0 < 8; ++d0) k0[d0] = ldsr128(((d0 & 1) ? ka1 : ka0) + (d0 >> 1) * 512);
#pragma unroll
        for (int d0 = 0; d0 < 8; ++d0) kf[d0] = ldsr128(((d0 & 1) ? ka1 : ka0) + 16384 + (d0 >> 1) * 512);
        __builtin_amdgcn_sched_barrier(0);
        asm volatile("s_waitcnt lgkmcnt(0)\n\ts_barrier" ::: "memory");
        DMA_K(4);
        sA = f32x16{};
#pragma unroll
        for (int d0 = 0; d0 < 8; ++d0) sA = MFMA32(k0[d0], qf[d0], sA);
        m_run = sA[0];
#pragma unroll
        for (int r = 1; r < 16; ++r) m_run = __builtin_fmaxf(m_run, sA[r]);
        m_run = swap_max(m_run);
#pragma unroll
        for (int r = 0; r < 16; ++r) { sA[r] -= m_run; negm[r] = -m_run; }
    }
#define EXP1(r) do { sc[r] = __builtin_amdgcn_exp2f(sc[r]); } while (0)
#define SUM1(k) do { ps0 += sc[2 * (k)]; ps1 += sc[2 * (k) + 1]; pw[k] = cvtpk(sc[2 * (k)], sc[2 * (k) + 1]); asm volatile("" : "+v"(pw[k]), "+v"(ps0), "+v"(ps1)); } while (0)
#define STEP(t, SC_, SN_, TAIL) do { \
        f32x16& sc = SC_; f32x16& sn = SN_; \
        s16x4 vlo[8], vhi[8]; \
        if (!TAIL || (t) < 12) asm volatile("s_waitcnt vmcnt(8)" ::: "memory"); \
        else if ((t) == 12) asm volatile("s_waitcnt vmcnt(6)" ::: "memory"); \
        else if ((t) == 13) asm volatile("s_waitcnt vmcnt(4)" ::: "memory"); \
        else if ((t) == 14) asm volatile("s_waitcnt vmcnt(2)" ::: "memory"); \
        else asm volatile("s_waitcnt vmcnt(0)" ::: "memory"); \
        asm volatile("s_waitcnt lgkmcnt(0)\n\ts_barrier" ::: "memory"); \
        if (__builtin_expect(__any(tmax > RTHR), 0)) { \
            const float dl = __builtin_fmaxf(tmax, 0.f); \
            const float alpha = __builtin_amdgcn_exp2f(-dl); \
            m_run += dl; l_run *= alpha; \
            _Pragma("unroll") for (int r = 0; r < 16; ++r) { sc[r] -= dl; negm[r] = -m_run; } \
            _Pragma("unroll") for (int dt = 0; dt < 4; ++dt) \
                _Pragma("unroll") for (int r = 0; r < 16; ++r) o[dt][r] *= alpha; \
        } \
        const unsigned va = vb + VSO(t); \
        const unsigned ka0 = kb0 + KSO((t) + 2), ka1 = kb1 + KSO((t) + 2);       \
        unsigned pw[8]; \
        float ps0 = 0.f, ps1 = 0.f, t2; \
        __builtin_amdgcn_sched_barrier(0); \
          \
        _Pragma("unroll") for (int i = 0; i < 8; ++i) { \
            sn = MFMA32(kf[i], qf[i], i == 0 ? negm : sn);                   \
            const int ss = i >> 2, dt = i & 3; \
            vlo[2 * dt + ss] = vtr(va + ss * 4096 + dt * 512); \
            vhi[2 * dt + ss] = vtr(va + ss * 4096 + 2048 + dt * 512); \
            if (i < 4) { EXP1(2 * i); EXP1(2 * i + 1); } else EXP1(4 + i); \
            if (i >= 1 && i <= 4) SUM1(i - 1); \
            if (i == 6) SUM1(4); \
            asm volatile("" : "+v"(sn), "+v"(sc)); \
            if (i == XS0 && (!TAIL || (t) < 13)) DMA_V0((t) + 3); \
            if (i == XS1 && (!TAIL || (t) < 13)) DMA_V1((t) + 3); \
            __builtin_amdgcn_sched_barrier(0); \
        } \
          \
        _Pragma("unroll") for (int i = 0; i < 8; ++i) { \
            const int ss = i >> 2, dt = i & 3; \
            const s16x4 lo = vlo[2 * dt + ss], hi4 = vhi[2 * dt + ss]; \
            const bf16x8 vf = {lo[0], lo[1], lo[2], lo[3], hi4[0], hi4[1], hi4[2], hi4[3]}; \
            const bf16x8 pf = __builtin_bit_cast(bf16x8, u32x4{pw[4 * ss], pw[4 * ss + 1], pw[4 * ss + 2], pw[4 * ss + 3]}); \
            o[dt] = MFMA32(vf, pf, o[dt]); \
            if (i < 4) { kf[2 * i] = ldsr128(ka0 + i * 512); kf[2 * i + 1] = ldsr128(ka1 + i * 512); } \
            if (i < 2) { EXP1(12 + 2 * i); EXP1(13 + 2 * i); asm volatile("" : "+v"(sc)); } \
            if (i < 3) SUM1(5 + i); \
            if (i == 3) { l_run += ps0 + ps1; asm volatile("" : "+v"(sn), "+v"(l_run)); \
                          tmax = max3f(sn[0], sn[1], sn[2]); t2 = max3f(sn[3], sn[4], sn[5]); asm volatile("" : "+v"(tmax), "+v"(t2)); } \
            if (i == 4) { tmax = max3f(tmax, sn[6], sn[7]); t2 = max3f(t2, sn[8], sn[9]); tmax = max3f(tmax, sn[10], sn[11]); asm volatile("" : "+v"(tmax), "+v"(t2)); } \
            if (i == 5) { t2 = max3f(t2, sn[12], sn[13]); tmax = max3f(tmax, sn[14], sn[15]); tmax = max3f(tmax, t2, t2); asm volatile("" : "+v"(tmax)); } \
            if (i == 6) { tmax = swap_max3(tmax); asm volatile("" : "+v"(tmax)); } \
            if (i == YS0 && (!TAIL || (t) < 11)) DMA_K0((t) + 5); \
            if (i == YS1 && (!TAIL || (t) < 11)) DMA_K1((t) + 5); \
            __builtin_amdgcn_sched_barrier(0); \
        } } while (0)
#pragma unroll 1
    for (int t = 0; t < 10; t += 2) { STEP(t, sA, sB, false); STEP(t + 1, sB, sA, false); }
#pragma unroll 1
    for (int t = 10; t < NT; t += 2) { STEP(t, sA, sB, true); STEP(t + 1, sB, sA, true); }
#undef STEP
#undef EXP1
#undef SUM1
    {
        const char* wp = (const char*)wo + (size_t)(1024 * ((bid >> 1) & 3) + 32 * (bid >> 3)) * DM * 2 + wid * 1024 + lane * 16;
#pragma unroll
        for (int i = 0; i < 4; ++i) glds16(wp + i * 8192, lds0 + LDS_Q + i * 8192 + wid * 1024);
        if (wid < 4) glds16(wp + 32768, lds0 + LDS_Q + wid * 1024);
    }
    l_run = swap_add(l_run);
    asm volatile("s_waitcnt lgkmcnt(0)\n\ts_barrier" ::: "memory");
    {
        f32x16 keep0, keep1, send0, send1;
        if (kh == 0) { keep0 = o[0]; keep1 = o[1]; send0 = o[2]; send1 = o[3]; }
        else         { keep0 = o[2]; keep1 = o[3]; send0 = o[0]; send1 = o[1]; }
        const unsigned xb = lds0 + wid * 9216;
#pragma unroll
        for (int r4 = 0; r4 < 4; ++r4) {
            f32x4 v0 = {send0[4 * r4], send0[4 * r4 + 1], send0[4 * r4 + 2], send0[4 * r4 + 3]};
            f32x4 v1 = {send1[4 * r4], send1[4 * r4 + 1], send1[4 * r4 + 2], send1[4 * r4 + 3]};
            *(LAS f32x4*)(uintptr_t)(xb + r4 * 1024 + lane * 16) = v0;
            *(LAS f32x4*)(uintptr_t)(xb + (4 + r4) * 1024 + lane * 16) = v1;
        }
        *(LAS float*)(uintptr_t)(xb + 8192 + lane * 4) = m_run;
        *(LAS float*)(uintptr_t)(xb + 8192 + 256 + lane * 4) = l_run;
        asm volatile("s_waitcnt lgkmcnt(0)\n\ts_barrier" ::: "memory");
        const unsigned pb = lds0 + (wid ^ 4) * 9216;
        const float m2 = *(const LAS float*)(uintptr_t)(pb + 8192 + lane * 4);
        const float l2 = *(const LAS float*)(uintptr_t)(pb + 8192 + 256 + lane * 4);
        const float mm = __builtin_fmaxf(m_run, m2);
        const float a1 = __builtin_amdgcn_exp2f(m_run - mm), a2 = __builtin_amdgcn_exp2f(m2 - mm);
        const float inv = 1.0f / (l_run * a1 + l2 * a2);
        const float w1 = a1 * inv, w2 = a2 * inv;
        const int row = b * SQ + 32 * (qb & 1) + r32;
        char* ob = (char*)Og + ((size_t)((row >> 4) * 128 + head * 4 + 2 * kh) * 64 + hi * 16 + (row & 15)) * 16;
#define OSTORE(KEEP, i) do { u32x2 w_[4]; \
            _Pragma("unroll") for (int r4 = 0; r4 < 4; ++r4) { \
                const f32x4 v = *(const LAS f32x4*)(uintptr_t)(pb + (4 * (i) + r4) * 1024 + lane * 16); \
                const float e0 = KEEP[4 * r4] * w1 + v[0] * w2, e1 = KEEP[4 * r4 + 1] * w1 + v[1] * w2; \
                const float e2 = KEEP[4 * r4 + 2] * w1 + v[2] * w2, e3 = KEEP[4 * r4 + 3] * w1 + v[3] * w2; \
                w_[r4] = u32x2{cvtpk(e0, e1), cvtpk(e2, e3)}; } \
            _Pragma("unroll") for (int g = 0; g < 2; ++g) { \
                u32x4 o4 = {w_[g].x, w_[g].y, w_[g + 2].x, w_[g + 2].y}; \
                asm volatile("global_store_dwordx4 %0, %1, off sc1" :: "v"(ob + (i) * 1024 + g * 512), "v"(o4) : "memory"); } } while (0)
        OSTORE(keep0, 0); OSTORE(keep1, 1);
#undef OSTORE
    }
}

constexpr int LDS2_BYTES = 1024 * 160;
#define MFMA16(a, b, c) __builtin_amdgcn_mfma_f32_16x16x32_bf16(a, b, c, 0, 0, 0)
__global__ __launch_bounds__(512, 2) void oproj_kernel(const u16* __restrict__ Og, const u16* __restrict__ wo,
                                                       float* __restrict__ part, u16* __restrict__ out) {
    extern __shared__ __attribute__((aligned(16))) char lds[];
    const unsigned lds0 = (unsigned)(uintptr_t)lds;
    const int tid = threadIdx.x, lane = tid & 63;
    const int wid = __builtin_amdgcn_readfirstlane(tid >> 6);
    const int rb = wid & 3, kp = wid >> 2;
    const int bid = blockIdx.x;
    const int sk = (bid >> 1) & 3, mt = 4 * (bid & 1) + ((bid >> 3) & 3), nt = bid >> 5;
    const char* Bg[5];
#pragma unroll
    for (int i = 0; i < 5; ++i) {
        const int L = i * 1024 + lane * 16;
        const int row = L / 160;
        int c = (L % 160) >> 4;
        c = c < 9 ? c : 0;
        Bg[i] = (const char*)wo + ((size_t)(1024 * sk + 32 * wid + row) * DM + 72 * nt) * 2 + c * 16;
    }
    const char* Ag = (const char*)Og + ((size_t)((8 * mt + 2 * rb) * 128 + 32 * sk + kp) * 64 + lane) * 16;
    u32x4 an[4][8];
#define K2_B(c) do { _Pragma("unroll") for (int i = 0; i < 5; ++i) \
        glds16(Bg[i] + (size_t)(c) * 256 * DM * 2, lds0 + (40 * (c) + 5 * wid + i) * 1024); } while (0)
#define K2_A(c) do { _Pragma("unroll") for (int j = 0; j < 4; j += 2) { const char* a_ = Ag + (8 * (c) + 2 * j) * 1024; \
        asm volatile("global_load_dwordx4 %0, %1, off" : "=v"(an[c][2 * j]) : "v"(a_) : "memory"); \
        asm volatile("global_load_dwordx4 %0, %1, off" : "=v"(an[c][2 * j + 1]) : "v"(a_ + 131072) : "memory"); \
        asm volatile("global_load_dwordx4 %0, %1, off offset:2048" : "=v"(an[c][2 * j + 2]) : "v"(a_) : "memory"); \
        asm volatile("global_load_dwordx4 %0, %1, off offset:2048" : "=v"(an[c][2 * j + 3]) : "v"(a_ + 131072) : "memory"); } } while (0)
    K2_B(0);
    asm volatile("s_barrier" ::: "memory");
    K2_A(0); K2_B(1);
    const int gi = lane >> 4, vq = (lane & 15) >> 2, vp = lane & 3;
    const unsigned bb = lds0 + (32 * kp + 4 * gi + vq) * 160 + 8 * vp;
    f32x4 acc[2][5];
#pragma unroll
    for (int ct = 0; ct < 5; ++ct) { acc[0][ct] = f32x4{0.f, 0.f, 0.f, 0.f}; acc[1][ct] = f32x4{0.f, 0.f, 0.f, 0.f}; }
    s16x4 blo[2][5], bhi[2][5];
    asm volatile("s_waitcnt vmcnt(13)\n\ts_barrier" ::: "memory");
#pragma unroll
    for (int ct = 0; ct < 5; ++ct) { blo[0][ct] = vtr(bb + 32 * ct); bhi[0][ct] = vtr(bb + 32 * ct + 2560); }
#pragma unroll
    for (int s = 0; s < 16; ++s) {
        const int c = s >> 2, j = s & 3;
        const int young = (6 - 2 * j) + (c < 3 ? 5 + (j < 2 ? 4 * j : 8) + (j == 3 && c < 2 ? 5 : 0) : 0);
        asm volatile("s_waitcnt vmcnt(%2)" : "+v"(an[c][2 * j]), "+v"(an[c][2 * j + 1]) : "n"(young) : "memory");

        if (j == 3 && c < 3) asm volatile("s_waitcnt vmcnt(%0)\n\ts_barrier" :: "n"(c < 2 ? 13 : 8) : "memory");
        __builtin_amdgcn_sched_barrier(0);
#pragma unroll
        for (int ct = 0; ct < 5; ++ct) {
            if (s < 15) {
                const unsigned ad = bb + (s + 1) * 10240 + 32 * ct;
                blo[(s + 1) & 1][ct] = vtr(ad); bhi[(s + 1) & 1][ct] = vtr(ad + 2560);
            }
            const s16x4 lo = blo[s & 1][ct], h4 = bhi[s & 1][ct];
            const bf16x8 bf = {lo[0], lo[1], lo[2], lo[3], h4[0], h4[1], h4[2], h4[3]};
            acc[0][ct] = MFMA16(bf, __builtin_bit_cast(bf16x8, an[c][2 * j]), acc[0][ct]);
            acc[1][ct] = MFMA16(bf, __builtin_bit_cast(bf16x8, an[c][2 * j + 1]), acc[1][ct]);
            if (j < 2 && c < 3 && ct < 4) {
                const char* a_ = Ag + (8 * (c + 1) + 4 * j + (ct >> 1) * 2) * 1024 + (ct & 1) * 131072;
                asm volatile("global_load_dwordx4 %0, %1, off" : "=v"(an[(c + 1) & 3][4 * j + ct]) : "v"(a_) : "memory");
            }
            if (j == 2 && c < 2) glds16(Bg[ct] + (size_t)(c + 2) * 256 * DM * 2, lds0 + (40 * (c + 2) + 5 * wid + ct) * 1024);
            __builtin_amdgcn_sched_barrier(0);
        }
    }
#undef K2_A
#undef K2_B
    asm volatile("s_waitcnt lgkmcnt(0)\n\ts_barrier" ::: "memory");
#pragma unroll
    for (int ct = 0; ct < 5; ++ct)
        *(LAS f32x4*)(uintptr_t)(lds0 + (wid * 5 + ct) * 1024 + lane * 16) = kp ? acc[0][ct] : acc[1][ct];
    asm volatile("s_waitcnt lgkmcnt(0)\n\ts_barrier" ::: "memory");
    float* pp = part + ((size_t)(sk * 64 + 8 * mt + 2 * rb + kp) * 8 + nt) * 1152 + lane * 4;
#pragma unroll
    for (int ct = 0; ct < 5; ++ct) {
        const f32x4 v = *(const LAS f32x4*)(uintptr_t)(lds0 + ((wid ^ 4) * 5 + ct) * 1024 + lane * 16);
        const f32x4 r_ = (kp ? acc[1][ct] : acc[0][ct]) + v;
        if (ct < 4 || lane < 32)
            asm volatile("global_store_dwordx4 %0, %1, off sc1" :: "v"(pp + ct * 256), "v"(r_) : "memory");
    }
    asm volatile("s_waitcnt vmcnt(0)\n\ts_barrier" ::: "memory");
    if (wid == 0) {
        gu32* cw = (gu32*)((const char*)Og + CNT_OFF) + (mt * 8 + nt) * 32;
        unsigned todo = 1u << sk;
        unsigned old = 0;
        if (lane == 0) old = __hip_atomic_fetch_add(cw, 1u, RLX_AGENT);
        old = __builtin_amdgcn_readfirstlane(old);
        if ((old & 255u) == 3u) todo |= (old >> 8) & 15u;
        else {
            const unsigned long long t0 = __builtin_amdgcn_s_memrealtime();
            for (;;) {
                const unsigned v = __builtin_amdgcn_readfirstlane(__hip_atomic_load(cw, RLX_AGENT));
                if ((v & 255u) == 4u) break;
                if (__builtin_amdgcn_s_memrealtime() - t0 > 5000ull) {
                    unsigned o2 = 0;
                    if (lane == 0) o2 = __hip_atomic_fetch_or(cw, 256u << sk, RLX_AGENT);
                    if ((__builtin_amdgcn_readfirstlane(o2) & 255u) != 4u) todo = 0;
                    break;
                }
                __builtin_amdgcn_s_sleep(1);
            }
        }
        if (lane == 0) *(LAS unsigned*)(uintptr_t)lds0 = todo;
    }
    asm volatile("s_waitcnt lgkmcnt(0)\n\ts_barrier" ::: "memory");
    unsigned todo = __builtin_amdgcn_readfirstlane(*(const LAS unsigned*)(uintptr_t)lds0);
    while (todo) {
        const int q = __builtin_ctz(todo);
        todo &= todo - 1;
        if (tid < 288) {
            const float* p = part + ((size_t)(8 * mt + 2 * q) * 8 + nt) * 1152 + tid * 4;
            f32x4 v[2][4];
#pragma unroll
            for (int r = 0; r < 2; ++r)
#pragma unroll
                for (int s = 0; s < 4; ++s)
                    asm volatile("global_load_dwordx4 %0, %1, off sc1" : "=v"(v[r][s]) : "v"(p + (size_t)s * 64 * 8 * 1152 + r * 8 * 1152) : "memory");
            asm volatile("s_waitcnt vmcnt(0)" : "+v"(v[0][0]), "+v"(v[0][1]), "+v"(v[0][2]), "+v"(v[0][3]),
                                                "+v"(v[1][0]), "+v"(v[1][1]), "+v"(v[1][2]), "+v"(v[1][3]) :: "memory");
#pragma unroll
            for (int r = 0; r < 2; ++r) {
                const f32x4 t = ((v[r][0] + v[r][1]) + v[r][2]) + v[r][3];
                u32x2 w = {cvtpk(t[0], t[1]), cvtpk(t[2], t[3])};
                *(u32x2*)(out + (size_t)(16 * (8 * mt + 2 * q + r) + (tid & 15)) * DM + 72 * nt + 4 * (tid >> 4)) = w;
            }
        }
    }
}

extern "C" void kernel_launch(void* const* d_in, const int* in_sizes, int n_in, void* d_out, int out_size,
                              void* d_ws, size_t ws_size, hipStream_t stream) {
    const u16* x = (const u16*)d_in[0];
    const u16* kc = (const u16*)d_in[1];
    const u16* vc = (const u16*)d_in[2];
    const u16* wq = (const u16*)d_in[3];
    const u16* wo = (const u16*)d_in[4];
    u16* Og = (u16*)d_ws;
    float* part = (float*)((char*)d_ws + (size_t)NB * SQ * NQ * 2);
    (void)hipFuncSetAttribute((const void*)attn_kernel, hipFuncAttributeMaxDynamicSharedMemorySize, LDS1_BYTES);
    (void)hipFuncSetAttribute((const void*)oproj_kernel, hipFuncAttributeMaxDynamicSharedMemorySize, LDS2_BYTES);
    attn_kernel<<<NB * HKV, 512, LDS1_BYTES, stream>>>(x, kc, vc, wq, Og, wo);
    oproj_kernel<<<256, 512, LDS2_BYTES, stream>>>(Og, wo, part, (u16*)d_out);
}
```

```cpp
#define XS0 5
#define XS1 7
#define YS0 5
#define YS1 7
#include <hip/hip_runtime.h>
#include <hip/hip_bf16.h>
#include <stdint.h>

typedef short bf16x8 __attribute__((ext_vector_type(8)));
typedef short s16x4 __attribute__((ext_vector_type(4)));
typedef float f32x16 __attribute__((ext_vector_type(16)));
typedef float f32x4 __attribute__((ext_vector_type(4)));
typedef unsigned u32x4 __attribute__((ext_vector_type(4)));
typedef unsigned u32x2 __attribute__((ext_vector_type(2)));
typedef unsigned short u16;
#define LAS __attribute__((address_space(3)))
typedef LAS const char* lds_cptr;
typedef __attribute__((address_space(1))) unsigned gu32;
#define RLX_AGENT __ATOMIC_RELAXED, __HIP_MEMORY_SCOPE_AGENT

constexpr int NB = 16, SQ = 64, DM = 576, HQ = 32, HKV = 16, SKV = 1024, DH = 128;
constexpr int NQ = HQ * DH;
constexpr int NT = SKV / 64;
constexpr int SLOT = 32768;
constexpr int LDS_Q = 4 * SLOT;
constexpr int LDS1_BYTES = 5 * SLOT;
constexpr size_t CNT_OFF = (size_t)20 << 20;
constexpr float RTHR = 8.0f;
constexpr float C2 = 0.088388347648318447f * 1.4426950408889634f;

#define MFMA32(a, b, c) __builtin_amdgcn_mfma_f32_32x32x16_bf16(a, b, c, 0, 0, 0)

typedef __bf16 bf16x2_t __attribute__((ext_vector_type(2)));
typedef float f32x2 __attribute__((ext_vector_type(2)));
__device__ __forceinline__ unsigned cvtpk(float lo, float hi) {
    f32x2 v = {lo, hi};
    return __builtin_bit_cast(unsigned, __builtin_convertvector(v, bf16x2_t));
}
__device__ __forceinline__ void glds16(const void* g, unsigned lds_base) {
    unsigned sv;
    asm volatile("s_mov_b32 %0, m0\n\ts_mov_b32 m0, %2\n\ts_nop 0\n\tglobal_load_lds_dwordx4 %1, off\n\ts_mov_b32 m0, %0"
                 : "=&s"(sv) : "v"(g), "s"(lds_base) : "memory");
}
__device__ __forceinline__ void glds16nt(const void* g, unsigned lds_base) {
    unsigned sv;
    asm volatile("s_mov_b32 %0, m0\n\ts_mov_b32 m0, %2\n\ts_nop 0\n\tglobal_load_lds_dwordx4 %1, off nt\n\ts_mov_b32 m0, %0"
                 : "=&s"(sv) : "v"(g), "s"(lds_base) : "memory");
}
__device__ __forceinline__ void dbg_delay(int units) {
    const unsigned long long t0 = __builtin_amdgcn_s_memrealtime();
    while (__builtin_amdgcn_s_memrealtime() - t0 < (unsigned long long)units * 200ull) __builtin_amdgcn_s_sleep(1);
}
__device__ __forceinline__ int dbg_v() { unsigned x; asm volatile("s_getreg_b32 %0, hwreg(HW_REG_XCC_ID)" : "=s"(x)); return ((int)(x & 15) - (int)blockIdx.x) & 7; }
__device__ __forceinline__ s16x4 vtr(unsigned a) {
    return __builtin_amdgcn_ds_read_tr16_b64_v4i16((LAS s16x4*)(uintptr_t)a);
}
__device__ __forceinline__ bf16x8 ldsr128(unsigned a) { return *(const LAS bf16x8*)(uintptr_t)a; }
__device__ __forceinline__ float swap_max(float v) {
    auto rr = __builtin_amdgcn_permlane32_swap(__float_as_uint(v), __float_as_uint(v), false, false);
    return __builtin_fmaxf(__uint_as_float(rr[0]), __uint_as_float(rr[1]));
}
__device__ __forceinline__ float max3f(float a, float b, float c) { float r; asm("v_max3_f32 %0, %1, %2, %3" : "=v"(r) : "v"(a), "v"(b), "v"(c)); return r; }
__device__ __forceinline__ float swap_max3(float v) {
    auto rr = __builtin_amdgcn_permlane32_swap(__float_as_uint(v), __float_as_uint(v), false, false);
    return max3f(__uint_as_float(rr[0]), __uint_as_float(rr[1]), __uint_as_float(rr[1]));
}
__device__ __forceinline__ float swap_add(float v) {
    auto rr = __builtin_amdgcn_permlane32_swap(__float_as_uint(v), __float_as_uint(v), false, false);
    return __uint_as_float(rr[0]) + __uint_as_float(rr[1]);
}

constexpr int XIMG = 0;
constexpr int WRING = 73728;
__global__ __launch_bounds__(512, 2) void attn_kernel(const u16* __restrict__ x, const u16* __restrict__ Kc,
                                                      const u16* __restrict__ Vc, const u16* __restrict__ wq,
                                                      u16* __restrict__ Og, const u16* __restrict__ wo) {
    extern __shared__ __attribute__((aligned(16))) char lds[];
    dbg_delay(dbg_v());
    const unsigned lds0 = (unsigned)(uintptr_t)lds;
    const int tid = threadIdx.x, lane = tid & 63, r32 = lane & 31, hi = lane >> 5;
    const int wid = __builtin_amdgcn_readfirstlane(tid >> 6);
    const int qb = wid & 3, kh = wid >> 2;
    const int bid = blockIdx.x, b = 8 * (bid & 1) + ((bid >> 3) & 7), kvh = 4 * ((bid >> 1) & 3) + (bid >> 6);
    const int head = 2 * kvh + (qb >> 1);
    if (tid == 0 && bid < 64) ((unsigned*)((char*)Og + CNT_OFF))[bid * 32] = 0u;
    const int gi = lane >> 4, vq = (lane & 15) >> 2, vp = lane & 3;

    const unsigned vsrc = (16 * (wid >> 1) + ((lane >> 2) & 7)) * 256 + (wid & 1) * 128 + hi * 64;
    const unsigned voffV = vsrc + vp * 16;
    const unsigned voffK0 = vsrc + ((vp ^ (wid & 2)) << 4), voffK1 = vsrc + ((vp ^ ((wid & 2) | 1)) << 4);
    const char* Kg = (const char*)Kc + (size_t)(b * HKV + kvh) * SKV * DH * 2;
    const char* Vg = (const char*)Vc + (size_t)(b * HKV + kvh) * SKV * DH * 2;
    const unsigned dwo = lds0 + (wid >> 1) * 4096 + (wid & 1) * 1024;
#define KVDMA(voff, gp, dst, OFF) asm volatile("s_mov_b32 m0, %2\n\ts_nop 0\n\tglobal_load_lds_dwordx4 %0, %1 offset:" #OFF " sc1 nt" \
            :: "v"(voff), "s"(gp), "s"(dst) : "memory")
#define VSO(tn) ((((tn) + 3) & 3) * 16384)
#define KSO(tn) (65536 + ((tn) & 3) * 16384)
#define DMA_V0(tn) KVDMA(voffV, Vg + (tn) * 16384, dwo + VSO(tn), 0)
#define DMA_V1(tn) KVDMA(voffV, Vg + (tn) * 16384, dwo + VSO(tn), 2048)
#define DMA_K0(tn) KVDMA(voffK0, Kg + (tn) * 16384, dwo + KSO(tn), 0)
#define DMA_K1(tn) KVDMA(voffK1, Kg + (tn) * 16384, dwo + KSO(tn), 2048)
#define DMA_V(tn) do { DMA_V0(tn); DMA_V1(tn); } while (0)
#define DMA_K(tn) do { DMA_K0(tn); DMA_K1(tn); } while (0)
    bf16x8 qf[8];
    {
        int kb = (b + 2 * (kvh & 3)) % 9;
        const unsigned xo = (8 * wid + (lane >> 3)) * DM * 2 + (((lane & 7) ^ ((4 * wid + (lane >> 4)) & 7)) << 4);
        const unsigned wo0 = (8 * wid + hi) * NQ * 2 + ((r32 ^ (hi << 2)) << 4);
        const unsigned wo1 = (8 * wid + 2 + hi) * NQ * 2 + ((r32 ^ ((2 + hi) << 2)) << 4);
        const unsigned wo2 = wo0 + 4 * NQ * 2, wo3 = wo1 + 4 * NQ * 2;
        const char* Xs = (const char*)x + (size_t)b * SQ * DM * 2;
        const char* Ws = (const char*)wq + kvh * 512;
#define QDMA(voff, gp, dst) asm volatile("s_mov_b32 m0, %2\n\ts_nop 0\n\tglobal_load_lds_dwordx4 %0, %1" :: "v"(voff), "s"(gp), "s"(dst) : "memory")
#define QS_PIECE(slot, p) do { const unsigned s_ = lds0 + (slot) * 40960 + wid * 4096; const char* w_ = Ws + (size_t)kb * 64 * NQ * 2; \
            if ((p) == 0) QDMA(wo0, w_, s_); \
            if ((p) == 1) QDMA(wo1, w_, s_ + 1024); \
            if ((p) == 2) QDMA(wo2, w_, s_ + 2048); \
            if ((p) == 3) QDMA(wo3, w_, s_ + 3072); \
            if ((p) == 4) { QDMA(xo, Xs + kb * 128, lds0 + (slot) * 40960 + 32768 + wid * 1024); kb = kb == 8 ? 0 : kb + 1; } } while (0)
#define QS_ISSUE(slot) do { QS_PIECE(slot, 0); QS_PIECE(slot, 1); QS_PIECE(slot, 2); QS_PIECE(slot, 3); QS_PIECE(slot, 4); } while (0)
#pragma unroll 1
        for (int j = 0; j < 2; ++j) { QS_ISSUE(j); asm volatile("s_barrier" ::: "memory"); }
        const int pp = ((vp & 1) << 1) | (vp >> 1);
        const unsigned abase = lds0 + (8 * hi + vq) * 512 + (((4 * wid + 2 * (gi & 1) + (pp >> 1)) ^ (vq << 2)) << 4) + 8 * (pp & 1);
        const unsigned xbase = lds0 + 32768 + r32 * 128 + ((hi ^ ((r32 >> 1) & 7)) << 4);
        f32x16 acc0 = {}, acc1 = {};
        s16x4 lo[2][4], h4[2][4]; bf16x8 x0[2][4], x1[2][4];
        asm volatile("s_waitcnt vmcnt(5)\n\ts_barrier" ::: "memory");
        QS_ISSUE(2);
#pragma unroll
        for (int i = 0; i < 4; ++i) {
            lo[0][i] = vtr(abase + i * 8192); h4[0][i] = vtr(abase + i * 8192 + 2048);
            x0[0][i] = ldsr128(xbase ^ (i << 5)); x1[0][i] = ldsr128((xbase ^ (i << 5)) + 4096);
        }
#pragma unroll
        for (int st = 0; st < 9; ++st) {
            if (st == 0) asm volatile("s_waitcnt vmcnt(5)" ::: "memory");
            else if (st < 6) asm volatile("s_waitcnt vmcnt(10)" ::: "memory");
            else if (st == 6) asm volatile("s_waitcnt vmcnt(9)" ::: "memory");
            else if (st == 7) asm volatile("s_waitcnt vmcnt(8)" ::: "memory");
            asm volatile("s_waitcnt lgkmcnt(0)\n\ts_barrier" ::: "memory");
            const unsigned so = ((st + 1) & 3) * 40960;
            const int cu = st & 1, nx = cu ^ 1;
            unsigned aa = abase + so;
            asm volatile("" : "+v"(aa));
            __builtin_amdgcn_sched_barrier(0);
            if (st == 0) QS_ISSUE(3);
#pragma unroll
            for (int i = 0; i < 4; ++i) {
                const bf16x8 a0 = {lo[cu][i][0], lo[cu][i][1], lo[cu][i][2], lo[cu][i][3], h4[cu][i][0], h4[cu][i][1], h4[cu][i][2], h4[cu][i][3]};
                acc0 = MFMA32(a0, x0[cu][i], acc0);
                if (st < 8) { lo[nx][i] = vtr(aa + i * 8192); h4[nx][i] = vtr(aa + i * 8192 + 2048); }
                if (st < 5) QS_PIECE(st & 3, i);
                __builtin_amdgcn_sched_barrier(0);
                acc1 = MFMA32(a0, x1[cu][i], acc1);
                if (st < 8) { unsigned xa = (xbase ^ (i << 5)) + so; asm volatile("" : "+v"(xa)); x0[nx][i] = ldsr128(xa); x1[nx][i] = ldsr128(xa + 4096); }
                if (st < 5 && i == 3) QS_PIECE(st & 3, 4);
                if (st == 5 && i == 3) { DMA_K(0); DMA_V(0); }
                if (st == 6 && i == 3) { DMA_K(1); DMA_K(2); }
                if (st == 7 && i == 3) DMA_K(3);
                __builtin_amdgcn_sched_barrier(0);
            }
        }
#undef QS_ISSUE
#undef QS_PIECE
#define QW(ACC, qblk) do { _Pragma("unroll") for (int s = 0; s < 2; ++s) { \
            u32x4 w_ = {cvtpk(ACC[8 * s] * C2, ACC[8 * s + 1] * C2), cvtpk(ACC[8 * s + 2] * C2, ACC[8 * s + 3] * C2), \
                        cvtpk(ACC[8 * s + 4] * C2, ACC[8 * s + 5] * C2), cvtpk(ACC[8 * s + 6] * C2, ACC[8 * s + 7] * C2)}; \
            *(LAS u32x4*)(uintptr_t)(lds0 + LDS_Q + (wid >> 2) * 16384 + (qblk) * 8192 + (2 * (wid & 3) + s) * 1024 + lane * 16) = w_; } } while (0)
        QW(acc0, 0); QW(acc1, 1);
#undef QW
        DMA_V(1); DMA_V(2);
    }
    asm volatile("s_waitcnt vmcnt(8) lgkmcnt(0)\n\ts_barrier" ::: "memory");
#pragma unroll
    for (int f = 0; f < 8; ++f) qf[f] = ldsr128(lds0 + LDS_Q + qb * 8192 + f * 1024 + lane * 16);

    const unsigned kb0 = lds0 + (4 * kh + (r32 >> 3)) * 2048 + (r32 & 7) * 64 + ((hi ^ (r32 >> 3)) << 4);
    const unsigned kb1 = kb0 ^ 32;
    const unsigned vb = lds0 + kh * 8192 + (4 * hi + vq) * 64 + (gi & 1) * 32 + vp * 8;
#define KRD(d0, so) ldsr128((((d0) & 1) ? kb1 : kb0) + (so) + ((d0) >> 1) * 512)

    float m_run, l_run = 0.f, tmax = 0.f;
    f32x16 o[4] = {};
    f32x16 sA, sB, negm;

    bf16x8 kf[8];
    {
        bf16x8 k0[8];
        unsigned ka0 = kb0 + KSO(0), ka1 = kb1 + KSO(0);
        asm volatile("" : "+v"(ka0), "+v"(ka1));
#pragma unroll
        for (int d0 = 0; d0 < 8; ++d0) k0[d0] = ldsr128(((d0 & 1) ? ka1 : ka0) + (d0 >> 1) * 512);
#pragma unroll
        for (int d0 = 0; d0 < 8; ++d0) kf[d0] = ldsr128(((d0 & 1) ? ka1 : ka0) + 16384 + (d0 >> 1) * 512);
        __builtin_amdgcn_sched_barrier(0);
        asm volatile("s_waitcnt lgkmcnt(0)\n\ts_barrier" ::: "memory");
        DMA_K(4);
        sA = f32x16{};
#pragma unroll
        for (int d0 = 0; d0 < 8; ++d0) sA = MFMA32(k0[d0], qf[d0], sA);
        m_run = sA[0];
#pragma unroll
        for (int r = 1; r < 16; ++r) m_run = __builtin_fmaxf(m_run, sA[r]);
        m_run = swap_max(m_run);
#pragma unroll
        for (int r = 0; r < 16; ++r) { sA[r] -= m_run; negm[r] = -m_run; }
    }
#define EXP1(r) do { sc[r] = __builtin_amdgcn_exp2f(sc[r]); } while (0)
#define SUM1(k) do { ps0 += sc[2 * (k)]; ps1 += sc[2 * (k) + 1]; pw[k] = cvtpk(sc[2 * (k)], sc[2 * (k) + 1]); asm volatile("" : "+v"(pw[k]), "+v"(ps0), "+v"(ps1)); } while (0)
#define STEP(t, SC_, SN_, TAIL) do { \
        f32x16& sc = SC_; f32x16& sn = SN_; \
        s16x4 vlo[8], vhi[8]; \
        if (!TAIL || (t) < 12) asm volatile("s_waitcnt vmcnt(8)" ::: "memory"); \
        else if ((t) == 12) asm volatile("s_waitcnt vmcnt(6)" ::: "memory"); \
        else if ((t) == 13) asm volatile("s_waitcnt vmcnt(4)" ::: "memory"); \
        else if ((t) == 14) asm volatile("s_waitcnt vmcnt(2)" ::: "memory"); \
        else asm volatile("s_waitcnt vmcnt(0)" ::: "memory"); \
        asm volatile("s_waitcnt lgkmcnt(0)\n\ts_barrier" ::: "memory"); \
        if (__builtin_expect(__any(tmax > RTHR), 0)) { \
            const float dl = __builtin_fmaxf(tmax, 0.f); \
            const float alpha = __builtin_amdgcn_exp2f(-dl); \
            m_run += dl; l_run *= alpha; \
            _Pragma("unroll") for (int r = 0; r < 16; ++r) { sc[r] -= dl; negm[r] = -m_run; } \
            _Pragma("unroll") for (int dt = 0; dt < 4; ++dt) \
                _Pragma("unroll") for (int r = 0; r < 16; ++r) o[dt][r] *= alpha; \
        } \
        const unsigned va = vb + VSO(t); \
        const unsigned ka0 = kb0 + KSO((t) + 2), ka1 = kb1 + KSO((t) + 2);       \
        unsigned pw[8]; \
        float ps0 = 0.f, ps1 = 0.f, t2; \
        __builtin_amdgcn_sched_barrier(0); \
          \
        _Pragma("unroll") for (int i = 0; i < 8; ++i) { \
            sn = MFMA32(kf[i], qf[i], i == 0 ? negm : sn);                   \
            const int ss = i >> 2, dt = i & 3; \
            vlo[2 * dt + ss] = vtr(va + ss * 4096 + dt * 512); \
            vhi[2 * dt + ss] = vtr(va + ss * 4096 + 2048 + dt * 512); \
            if (i < 4) { EXP1(2 * i); EXP1(2 * i + 1); } else EXP1(4 + i); \
            if (i >= 1 && i <= 4) SUM1(i - 1); \
            if (i == 6) SUM1(4); \
            asm volatile("" : "+v"(sn), "+v"(sc)); \
            if (i == XS0 && (!TAIL || (t) < 13)) DMA_V0((t) + 3); \
            if (i == XS1 && (!TAIL || (t) < 13)) DMA_V1((t) + 3); \
            __builtin_amdgcn_sched_barrier(0); \
        } \
          \
        _Pragma("unroll") for (int i = 0; i < 8; ++i) { \
            const int ss = i >> 2, dt = i & 3; \
            const s16x4 lo = vlo[2 * dt + ss], hi4 = vhi[2 * dt + ss]; \
            const bf16x8 vf = {lo[0], lo[1], lo[2], lo[3], hi4[0], hi4[1], hi4[2], hi4[3]}; \
            const bf16x8 pf = __builtin_bit_cast(bf16x8, u32x4{pw[4 * ss], pw[4 * ss + 1], pw[4 * ss + 2], pw[4 * ss + 3]}); \
            o[dt] = MFMA32(vf, pf, o[dt]); \
            if (i < 4) { kf[2 * i] = ldsr128(ka0 + i * 512); kf[2 * i + 1] = ldsr128(ka1 + i * 512); } \
            if (i < 2) { EXP1(12 + 2 * i); EXP1(13 + 2 * i); asm volatile("" : "+v"(sc)); } \
            if (i < 3) SUM1(5 + i); \
            if (i == 3) { l_run += ps0 + ps1; asm volatile("" : "+v"(sn), "+v"(l_run)); \
                          tmax = max3f(sn[0], sn[1], sn[2]); t2 = max3f(sn[3], sn[4], sn[5]); asm volatile("" : "+v"(tmax), "+v"(t2)); } \
            if (i == 4) { tmax = max3f(tmax, sn[6], sn[7]); t2 = max3f(t2, sn[8], sn[9]); tmax = max3f(tmax, sn[10], sn[11]); asm volatile("" : "+v"(tmax), "+v"(t2)); } \
            if (i == 5) { t2 = max3f(t2, sn[12], sn[13]); tmax = max3f(tmax, sn[14], sn[15]); tmax = max3f(tmax, t2, t2); asm volatile("" : "+v"(tmax)); } \
            if (i == 6) { tmax = swap_max3(tmax); asm volatile("" : "+v"(tmax)); } \
            if (i == YS0 && (!TAIL || (t) < 11)) DMA_K0((t) + 5); \
            if (i == YS1 && (!TAIL || (t) < 11)) DMA_K1((t) + 5); \
            __builtin_amdgcn_sched_barrier(0); \
        } } while (0)
#pragma unroll 1
    for (int t = 0; t < 10; t += 2) { STEP(t, sA, sB, false); STEP(t + 1, sB, sA, false); }
#pragma unroll 1
    for (int t = 10; t < NT; t += 2) { STEP(t, sA, sB, true); STEP(t + 1, sB, sA, true); }
#undef STEP
#undef EXP1
#undef SUM1
    {
        const char* wp = (const char*)wo + (size_t)(1024 * ((bid >> 1) & 3) + 32 * (bid >> 3)) * DM * 2 + wid * 1024 + lane * 16;
#pragma unroll
        for (int i = 0; i < 4; ++i) glds16(wp + i * 8192, lds0 + LDS_Q + i * 8192 + wid * 1024);
        if (wid < 4) glds16(wp + 32768, lds0 + LDS_Q + wid * 1024);
    }
    l_run = swap_add(l_run);
    asm volatile("s_waitcnt lgkmcnt(0)\n\ts_barrier" ::: "memory");
    {
        f32x16 keep0, keep1, send0, send1;
        if (kh == 0) { keep0 = o[0]; keep1 = o[1]; send0 = o[2]; send1 = o[3]; }
        else         { keep0 = o[2]; keep1 = o[3]; send0 = o[0]; send1 = o[1]; }
        const unsigned xb = lds0 + wid * 9216;
#pragma unroll
        for (int r4 = 0; r4 < 4; ++r4) {
            f32x4 v0 = {send0[4 * r4], send0[4 * r4 + 1], send0[4 * r4 + 2], send0[4 * r4 + 3]};
            f32x4 v1 = {send1[4 * r4], send1[4 * r4 + 1], send1[4 * r4 + 2], send1[4 * r4 + 3]};
            *(LAS f32x4*)(uintptr_t)(xb + r4 * 1024 + lane * 16) = v0;
            *(LAS f32x4*)(uintptr_t)(xb + (4 + r4) * 1024 + lane * 16) = v1;
        }
        *(LAS float*)(uintptr_t)(xb + 8192 + lane * 4) = m_run;
        *(LAS float*)(uintptr_t)(xb + 8192 + 256 + lane * 4) = l_run;
        asm volatile("s_waitcnt lgkmcnt(0)\n\ts_barrier" ::: "memory");
        const unsigned pb = lds0 + (wid ^ 4) * 9216;
        const float m2 = *(const LAS float*)(uintptr_t)(pb + 8192 + lane * 4);
        const float l2 = *(const LAS float*)(uintptr_t)(pb + 8192 + 256 + lane * 4);
        const float mm = __builtin_fmaxf(m_run, m2);
        const float a1 = __builtin_amdgcn_exp2f(m_run - mm), a2 = __builtin_amdgcn_exp2f(m2 - mm);
        const float inv = 1.0f / (l_run * a1 + l2 * a2);
        const float w1 = a1 * inv, w2 = a2 * inv;
        const int row = b * SQ + 32 * (qb & 1) + r32;
        char* ob = (char*)Og + ((size_t)((row >> 4) * 128 + head * 4 + 2 * kh) * 64 + hi * 16 + (row & 15)) * 16;
#define OSTORE(KEEP, i) do { u32x2 w_[4]; \
            _Pragma("unroll") for (int r4 = 0; r4 < 4; ++r4) { \
                const f32x4 v = *(const LAS f32x4*)(uintptr_t)(pb + (4 * (i) + r4) * 1024 + lane * 16); \
                const float e0 = KEEP[4 * r4] * w1 + v[0] * w2, e1 = KEEP[4 * r4 + 1] * w1 + v[1] * w2; \
                const float e2 = KEEP[4 * r4 + 2] * w1 + v[2] * w2, e3 = KEEP[4 * r4 + 3] * w1 + v[3] * w2; \
                w_[r4] = u32x2{cvtpk(e0, e1), cvtpk(e2, e3)}; } \
            _Pragma("unroll") for (int g = 0; g < 2; ++g) { \
                u32x4 o4 = {w_[g].x, w_[g].y, w_[g + 2].x, w_[g + 2].y}; \
                asm volatile("global_store_dwordx4 %0, %1, off sc1" :: "v"(ob + (i) * 1024 + g * 512), "v"(o4) : "memory"); } } while (0)
        OSTORE(keep0, 0); OSTORE(keep1, 1);
#undef OSTORE
    }
}

constexpr int LDS2_BYTES = 1024 * 160;
#define MFMA16(a, b, c) __builtin_amdgcn_mfma_f32_16x16x32_bf16(a, b, c, 0, 0, 0)
__global__ __launch_bounds__(512, 2) void oproj_kernel(const u16* __restrict__ Og, const u16* __restrict__ wo,
                                                       float* __restrict__ part, u16* __restrict__ out) {
    extern __shared__ __attribute__((aligned(16))) char lds[];
    const unsigned lds0 = (unsigned)(uintptr_t)lds;
    const int tid = threadIdx.x, lane = tid & 63;
    const int wid = __builtin_amdgcn_readfirstlane(tid >> 6);
    const int rb = wid & 3, kp = wid >> 2;
    const int bid = blockIdx.x;
    const int sk = (bid >> 1) & 3, mt = 4 * (bid & 1) + ((bid >> 3) & 3), nt = bid >> 5;
    const char* Bg[5];
#pragma unroll
    for (int i = 0; i < 5; ++i) {
        const int L = i * 1024 + lane * 16;
        const int row = L / 160;
        int c = (L % 160) >> 4;
        c = c < 9 ? c : 0;
        Bg[i] = (const char*)wo + ((size_t)(1024 * sk + 32 * wid + row) * DM + 72 * nt) * 2 + c * 16;
    }
    const char* Ag = (const char*)Og + ((size_t)((8 * mt + 2 * rb) * 128 + 32 * sk + kp) * 64 + lane) * 16;
    u32x4 an[4][8];
#define K2_B(c) do { _Pragma("unroll") for (int i = 0; i < 5; ++i) \
        glds16(Bg[i] + (size_t)(c) * 256 * DM * 2, lds0 + (40 * (c) + 5 * wid + i) * 1024); } while (0)
#define K2_A(c) do { _Pragma("unroll") for (int j = 0; j < 4; j += 2) { const char* a_ = Ag + (8 * (c) + 2 * j) * 1024; \
        asm volatile("global_load_dwordx4 %0, %1, off" : "=v"(an[c][2 * j]) : "v"(a_) : "memory"); \
        asm volatile("global_load_dwordx4 %0, %1, off" : "=v"(an[c][2 * j + 1]) : "v"(a_ + 131072) : "memory"); \
        asm volatile("global_load_dwordx4 %0, %1, off offset:2048" : "=v"(an[c][2 * j + 2]) : "v"(a_) : "memory"); \
        asm volatile("global_load_dwordx4 %0, %1, off offset:2048" : "=v"(an[c][2 * j + 3]) : "v"(a_ + 131072) : "memory"); } } while (0)
    K2_B(0);
    asm volatile("s_barrier" ::: "memory");
    K2_A(0); K2_B(1);
    const int gi = lane >> 4, vq = (lane & 15) >> 2, vp = lane & 3;
    const unsigned bb = lds0 + (32 * kp + 4 * gi + vq) * 160 + 8 * vp;
    f32x4 acc[2][5];
#pragma unroll
    for (int ct = 0; ct < 5; ++ct) { acc[0][ct] = f32x4{0.f, 0.f, 0.f, 0.f}; acc[1][ct] = f32x4{0.f, 0.f, 0.f, 0.f}; }
    s16x4 blo[2][5], bhi[2][5];
    asm volatile("s_waitcnt vmcnt(13)\n\ts_barrier" ::: "memory");
#pragma unroll
    for (int ct = 0; ct < 5; ++ct) { blo[0][ct] = vtr(bb + 32 * ct); bhi[0][ct] = vtr(bb + 32 * ct + 2560); }
#pragma unroll
    for (int s = 0; s < 16; ++s) {
        const int c = s >> 2, j = s & 3;
        const int young = (6 - 2 * j) + (c < 3 ? 5 + (j < 2 ? 4 * j : 8) + (j == 3 && c < 2 ? 5 : 0) : 0);
        asm volatile("s_waitcnt vmcnt(%2)" : "+v"(an[c][2 * j]), "+v"(an[c][2 * j + 1]) : "n"(young) : "memory");

        if (j == 3 && c < 3) asm volatile("s_waitcnt vmcnt(%0)\n\ts_barrier" :: "n"(c < 2 ? 13 : 8) : "memory");
        __builtin_amdgcn_sched_barrier(0);
#pragma unroll
        for (int ct = 0; ct < 5; ++ct) {
            if (s < 15) {
                const unsigned ad = bb + (s + 1) * 10240 + 32 * ct;
                blo[(s + 1) & 1][ct] = vtr(ad); bhi[(s + 1) & 1][ct] = vtr(ad + 2560);
            }
            const s16x4 lo = blo[s & 1][ct], h4 = bhi[s & 1][ct];
            const bf16x8 bf = {lo[0], lo[1], lo[2], lo[3], h4[0], h4[1], h4[2], h4[3]};
            acc[0][ct] = MFMA16(bf, __builtin_bit_cast(bf16x8, an[c][2 * j]), acc[0][ct]);
            acc[1][ct] = MFMA16(bf, __builtin_bit_cast(bf16x8, an[c][2 * j + 1]), acc[1][ct]);
            if (j < 2 && c < 3 && ct < 4) {
                const char* a_ = Ag + (8 * (c + 1) + 4 * j + (ct >> 1) * 2) * 1024 + (ct & 1) * 131072;
                asm volatile("global_load_dwordx4 %0, %1, off" : "=v"(an[(c + 1) & 3][4 * j + ct]) : "v"(a_) : "memory");
            }
            if (j == 2 && c < 2) glds16(Bg[ct] + (size_t)(c + 2) * 256 * DM * 2, lds0 + (40 * (c + 2) + 5 * wid + ct) * 1024);
            __builtin_amdgcn_sched_barrier(0);
        }
    }
#undef K2_A
#undef K2_B
    asm volatile("s_waitcnt lgkmcnt(0)\n\ts_barrier" ::: "memory");
#pragma unroll
    for (int ct = 0; ct < 5; ++ct)
        *(LAS f32x4*)(uintptr_t)(lds0 + (wid * 5 + ct) * 1024 + lane * 16) = kp ? acc[0][ct] : acc[1][ct];
    asm volatile("s_waitcnt lgkmcnt(0)\n\ts_barrier" ::: "memory");
    float* pp = part + ((size_t)(sk * 64 + 8 * mt + 2 * rb + kp) * 8 + nt) * 1152 + lane * 4;
#pragma unroll
    for (int ct = 0; ct < 5; ++ct) {
        const f32x4 v = *(const LAS f32x4*)(uintptr_t)(lds0 + ((wid ^ 4) * 5 + ct) * 1024 + lane * 16);
        const f32x4 r_ = (kp ? acc[1][ct] : acc[0][ct]) + v;
        if (ct < 4 || lane < 32)
            asm volatile("global_store_dwordx4 %0, %1, off sc1" :: "v"(pp + ct * 256), "v"(r_) : "memory");
    }
    asm volatile("s_waitcnt vmcnt(0)\n\ts_barrier" ::: "memory");
    if (wid == 0) {
        gu32* cw = (gu32*)((const char*)Og + CNT_OFF) + (mt * 8 + nt) * 32;
        unsigned todo = 1u << sk;
        unsigned old = 0;
        if (lane == 0) old = __hip_atomic_fetch_add(cw, 1u, RLX_AGENT);
        old = __builtin_amdgcn_readfirstlane(old);
        if ((old & 255u) == 3u) todo |= (old >> 8) & 15u;
        else {
            const unsigned long long t0 = __builtin_amdgcn_s_memrealtime();
            for (;;) {
                const unsigned v = __builtin_amdgcn_readfirstlane(__hip_atomic_load(cw, RLX_AGENT));
                if ((v & 255u) == 4u) break;
                if (__builtin_amdgcn_s_memrealtime() - t0 > 5000ull) {
                    unsigned o2 = 0;
                    if (lane == 0) o2 = __hip_atomic_fetch_or(cw, 256u << sk, RLX_AGENT);
                    if ((__builtin_amdgcn_readfirstlane(o2) & 255u) != 4u) todo = 0;
                    break;
                }
                __builtin_amdgcn_s_sleep(1);
            }
        }
        if (lane == 0) *(LAS unsigned*)(uintptr_t)lds0 = todo;
    }
    asm volatile("s_waitcnt lgkmcnt(0)\n\ts_barrier" ::: "memory");
    unsigned todo = __builtin_amdgcn_readfirstlane(*(const LAS unsigned*)(uintptr_t)lds0);
    while (todo) {
        const int q = __builtin_ctz(todo);
        todo &= todo - 1;
        if (tid < 288) {
            const float* p = part + ((size_t)(8 * mt + 2 * q) * 8 + nt) * 1152 + tid * 4;
            f32x4 v[2][4];
#pragma unroll
            for (int r = 0; r < 2; ++r)
#pragma unroll
                for (int s = 0; s < 4; ++s)
                    asm volatile("global_load_dwordx4 %0, %1, off sc1" : "=v"(v[r][s]) : "v"(p + (size_t)s * 64 * 8 * 1152 + r * 8 * 1152) : "memory");
            asm volatile("s_waitcnt vmcnt(0)" : "+v"(v[0][0]), "+v"(v[0][1]), "+v"(v[0][2]), "+v"(v[0][3]),
                                                "+v"(v[1][0]), "+v"(v[1][1]), "+v"(v[1][2]), "+v"(v[1][3]) :: "memory");
#pragma unroll
            for (int r = 0; r < 2; ++r) {
                const f32x4 t = ((v[r][0] + v[r][1]) + v[r][2]) + v[r][3];
                u32x2 w = {cvtpk(t[0], t[1]), cvtpk(t[2], t[3])};
                *(u32x2*)(out + (size_t)(16 * (8 * mt + 2 * q + r) + (tid & 15)) * DM + 72 * nt + 4 * (tid >> 4)) = w;
            }
        }
    }
}

extern "C" void kernel_launch(void* const* d_in, const int* in_sizes, int n_in, void* d_out, int out_size,
                              void* d_ws, size_t ws_size, hipStream_t stream) {
    const u16* x = (const u16*)d_in[0];
    const u16* kc = (const u16*)d_in[1];
    const u16* vc = (const u16*)d_in[2];
    const u16* wq = (const u16*)d_in[3];
    const u16* wo = (const u16*)d_in[4];
    u16* Og = (u16*)d_ws;
    float* part = (float*)((char*)d_ws + (size_t)NB * SQ * NQ * 2);
    (void)hipFuncSetAttribute((const void*)attn_kernel, hipFuncAttributeMaxDynamicSharedMemorySize, LDS1_BYTES);
    (void)hipFuncSetAttribute((const void*)oproj_kernel, hipFuncAttributeMaxDynamicSharedMemorySize, LDS2_BYTES);
    attn_kernel<<<NB * HKV, 512, LDS1_BYTES, stream>>>(x, kc, vc, wq, Og, wo);
    oproj_kernel<<<256, 512, LDS2_BYTES, stream>>>(Og, wo, part, (u16*)d_out);
}
```

```cpp
#define PACE 122
#define XS0 5
#define XS1 7
#define YS0 5
#define YS1 7
#include <hip/hip_runtime.h>
#include <hip/hip_bf16.h>
#include <stdint.h>

typedef short bf16x8 __attribute__((ext_vector_type(8)));
typedef short s16x4 __attribute__((ext_vector_type(4)));
typedef float f32x16 __attribute__((ext_vector_type(16)));
typedef float f32x4 __attribute__((ext_vector_type(4)));
typedef unsigned u32x4 __attribute__((ext_vector_type(4)));
typedef unsigned u32x2 __attribute__((ext_vector_type(2)));
typedef unsigned short u16;
#define LAS __attribute__((address_space(3)))
typedef LAS const char* lds_cptr;
typedef __attribute__((address_space(1))) unsigned gu32;
#define RLX_AGENT __ATOMIC_RELAXED, __HIP_MEMORY_SCOPE_AGENT

constexpr int NB = 16, SQ = 64, DM = 576, HQ = 32, HKV = 16, SKV = 1024, DH = 128;
constexpr int NQ = HQ * DH;
constexpr int NT = SKV / 64;
constexpr int SLOT = 32768;
constexpr int LDS_Q = 4 * SLOT;
constexpr int LDS1_BYTES = 5 * SLOT;
constexpr size_t CNT_OFF = (size_t)20 << 20;
constexpr float RTHR = 8.0f;
constexpr float C2 = 0.088388347648318447f * 1.4426950408889634f;

#define MFMA32(a, b, c) __builtin_amdgcn_mfma_f32_32x32x16_bf16(a, b, c, 0, 0, 0)

typedef __bf16 bf16x2_t __attribute__((ext_vector_type(2)));
typedef float f32x2 __attribute__((ext_vector_type(2)));
__device__ __forceinline__ unsigned cvtpk(float lo, float hi) {
    f32x2 v = {lo, hi};
    return __builtin_bit_cast(unsigned, __builtin_convertvector(v, bf16x2_t));
}
__device__ __forceinline__ void glds16(const void* g, unsigned lds_base) {
    unsigned sv;
    asm volatile("s_mov_b32 %0, m0\n\ts_mov_b32 m0, %2\n\ts_nop 0\n\tglobal_load_lds_dwordx4 %1, off\n\ts_mov_b32 m0, %0"
                 : "=&s"(sv) : "v"(g), "s"(lds_base) : "memory");
}
__device__ __forceinline__ void glds16nt(const void* g, unsigned lds_base) {
    unsigned sv;
    asm volatile("s_mov_b32 %0, m0\n\ts_mov_b32 m0, %2\n\ts_nop 0\n\tglobal_load_lds_dwordx4 %1, off nt\n\ts_mov_b32 m0, %0"
                 : "=&s"(sv) : "v"(g), "s"(lds_base) : "memory");
}
__device__ __forceinline__ s16x4 vtr(unsigned a) {
    return __builtin_amdgcn_ds_read_tr16_b64_v4i16((LAS s16x4*)(uintptr_t)a);
}
__device__ __forceinline__ bf16x8 ldsr128(unsigned a) { return *(const LAS bf16x8*)(uintptr_t)a; }
__device__ __forceinline__ float swap_max(float v) {
    auto rr = __builtin_amdgcn_permlane32_swap(__float_as_uint(v), __float_as_uint(v), false, false);
    return __builtin_fmaxf(__uint_as_float(rr[0]), __uint_as_float(rr[1]));
}
__device__ __forceinline__ float max3f(float a, float b, float c) { float r; asm("v_max3_f32 %0, %1, %2, %3" : "=v"(r) : "v"(a), "v"(b), "v"(c)); return r; }
__device__ __forceinline__ float swap_max3(float v) {
    auto rr = __builtin_amdgcn_permlane32_swap(__float_as_uint(v), __float_as_uint(v), false, false);
    return max3f(__uint_as_float(rr[0]), __uint_as_float(rr[1]), __uint_as_float(rr[1]));
}
__device__ __forceinline__ float swap_add(float v) {
    auto rr = __builtin_amdgcn_permlane32_swap(__float_as_uint(v), __float_as_uint(v), false, false);
    return __uint_as_float(rr[0]) + __uint_as_float(rr[1]);
}

constexpr int XIMG = 0;
constexpr int WRING = 73728;
__global__ __launch_bounds__(512, 2) void attn_kernel(const u16* __restrict__ x, const u16* __restrict__ Kc,
                                                      const u16* __restrict__ Vc, const u16* __restrict__ wq,
                                                      u16* __restrict__ Og, const u16* __restrict__ wo) {
    extern __shared__ __attribute__((aligned(16))) char lds[];
    const unsigned lds0 = (unsigned)(uintptr_t)lds;
    const int tid = threadIdx.x, lane = tid & 63, r32 = lane & 31, hi = lane >> 5;
    const int wid = __builtin_amdgcn_readfirstlane(tid >> 6);
    const int qb = wid & 3, kh = wid >> 2;
    const int bid = blockIdx.x, b = 8 * (bid & 1) + ((bid >> 3) & 7), kvh = 4 * ((bid >> 1) & 3) + (bid >> 6);
    const int head = 2 * kvh + (qb >> 1);
    if (tid == 0 && bid < 64) ((unsigned*)((char*)Og + CNT_OFF))[bid * 32] = 0u;
    const int gi = lane >> 4, vq = (lane & 15) >> 2, vp = lane & 3;

    const unsigned vsrc = (16 * (wid >> 1) + ((lane >> 2) & 7)) * 256 + (wid & 1) * 128 + hi * 64;
    const unsigned voffV = vsrc + vp * 16;
    const unsigned voffK0 = vsrc + ((vp ^ (wid & 2)) << 4), voffK1 = vsrc + ((vp ^ ((wid & 2) | 1)) << 4);
    const char* Kg = (const char*)Kc + (size_t)(b * HKV + kvh) * SKV * DH * 2;
    const char* Vg = (const char*)Vc + (size_t)(b * HKV + kvh) * SKV * DH * 2;
    const unsigned dwo = lds0 + (wid >> 1) * 4096 + (wid & 1) * 1024;
#define KVDMA(voff, gp, dst, OFF) asm volatile("s_mov_b32 m0, %2\n\ts_nop 0\n\tglobal_load_lds_dwordx4 %0, %1 offset:" #OFF " sc1 nt" \
            :: "v"(voff), "s"(gp), "s"(dst) : "memory")
#define VSO(tn) ((((tn) + 3) & 3) * 16384)
#define KSO(tn) (65536 + ((tn) & 3) * 16384)
#define DMA_V0(tn) KVDMA(voffV, Vg + (tn) * 16384, dwo + VSO(tn), 0)
#define DMA_V1(tn) KVDMA(voffV, Vg + (tn) * 16384, dwo + VSO(tn), 2048)
#define DMA_K0(tn) KVDMA(voffK0, Kg + (tn) * 16384, dwo + KSO(tn), 0)
#define DMA_K1(tn) KVDMA(voffK1, Kg + (tn) * 16384, dwo + KSO(tn), 2048)
#define DMA_V(tn) do { DMA_V0(tn); DMA_V1(tn); } while (0)
#define DMA_K(tn) do { DMA_K0(tn); DMA_K1(tn); } while (0)
    bf16x8 qf[8];
    {
        int kb = (b + 2 * (kvh & 3)) % 9;
        const unsigned xo = (8 * wid + (lane >> 3)) * DM * 2 + (((lane & 7) ^ ((4 * wid + (lane >> 4)) & 7)) << 4);
        const unsigned wo0 = (8 * wid + hi) * NQ * 2 + ((r32 ^ (hi << 2)) << 4);
        const unsigned wo1 = (8 * wid + 2 + hi) * NQ * 2 + ((r32 ^ ((2 + hi) << 2)) << 4);
        const unsigned wo2 = wo0 + 4 * NQ * 2, wo3 = wo1 + 4 * NQ * 2;
        const char* Xs = (const char*)x + (size_t)b * SQ * DM * 2;
        const char* Ws = (const char*)wq + kvh * 512;
#define QDMA(voff, gp, dst) asm volatile("s_mov_b32 m0, %2\n\ts_nop 0\n\tglobal_load_lds_dwordx4 %0, %1" :: "v"(voff), "s"(gp), "s"(dst) : "memory")
#define QS_PIECE(slot, p) do { const unsigned s_ = lds0 + (slot) * 40960 + wid * 4096; const char* w_ = Ws + (size_t)kb * 64 * NQ * 2; \
            if ((p) == 0) QDMA(wo0, w_, s_); \
            if ((p) == 1) QDMA(wo1, w_, s_ + 1024); \
            if ((p) == 2) QDMA(wo2, w_, s_ + 2048); \
            if ((p) == 3) QDMA(wo3, w_, s_ + 3072); \
            if ((p) == 4) { QDMA(xo, Xs + kb * 128, lds0 + (slot) * 40960 + 32768 + wid * 1024); kb = kb == 8 ? 0 : kb + 1; } } while (0)
#define QS_ISSUE(slot) do { QS_PIECE(slot, 0); QS_PIECE(slot, 1); QS_PIECE(slot, 2); QS_PIECE(slot, 3); QS_PIECE(slot, 4); } while (0)
#pragma unroll 1
        for (int j = 0; j < 2; ++j) { QS_ISSUE(j); asm volatile("s_barrier" ::: "memory"); }
        const int pp = ((vp & 1) << 1) | (vp >> 1);
        const unsigned abase = lds0 + (8 * hi + vq) * 512 + (((4 * wid + 2 * (gi & 1) + (pp >> 1)) ^ (vq << 2)) << 4) + 8 * (pp & 1);
        const unsigned xbase = lds0 + 32768 + r32 * 128 + ((hi ^ ((r32 >> 1) & 7)) << 4);
        f32x16 acc0 = {}, acc1 = {};
        s16x4 lo[2][4], h4[2][4]; bf16x8 x0[2][4], x1[2][4];
        asm volatile("s_waitcnt vmcnt(5)\n\ts_barrier" ::: "memory");
        QS_ISSUE(2);
#pragma unroll
        for (int i = 0; i < 4; ++i) {
            lo[0][i] = vtr(abase + i * 8192); h4[0][i] = vtr(abase + i * 8192 + 2048);
            x0[0][i] = ldsr128(xbase ^ (i << 5)); x1[0][i] = ldsr128((xbase ^ (i << 5)) + 4096);
        }
#pragma unroll
        for (int st = 0; st < 9; ++st) {
            if (st == 0) asm volatile("s_waitcnt vmcnt(5)" ::: "memory");
            else if (st < 6) asm volatile("s_waitcnt vmcnt(10)" ::: "memory");
            else if (st == 6) asm volatile("s_waitcnt vmcnt(9)" ::: "memory");
            else if (st == 7) asm volatile("s_waitcnt vmcnt(8)" ::: "memory");
            asm volatile("s_waitcnt lgkmcnt(0)\n\ts_barrier" ::: "memory");
            const unsigned so = ((st + 1) & 3) * 40960;
            const int cu = st & 1, nx = cu ^ 1;
            unsigned aa = abase + so;
            asm volatile("" : "+v"(aa));
            __builtin_amdgcn_sched_barrier(0);
            if (st == 0) QS_ISSUE(3);
#pragma unroll
            for (int i = 0; i < 4; ++i) {
                const bf16x8 a0 = {lo[cu][i][0], lo[cu][i][1], lo[cu][i][2], lo[cu][i][3], h4[cu][i][0], h4[cu][i][1], h4[cu][i][2], h4[cu][i][3]};
                acc0 = MFMA32(a0, x0[cu][i], acc0);
                if (st < 8) { lo[nx][i] = vtr(aa + i * 8192); h4[nx][i] = vtr(aa + i * 8192 + 2048); }
                if (st < 5) QS_PIECE(st & 3, i);
                __builtin_amdgcn_sched_barrier(0);
                acc1 = MFMA32(a0, x1[cu][i], acc1);
                if (st < 8) { unsigned xa = (xbase ^ (i << 5)) + so; asm volatile("" : "+v"(xa)); x0[nx][i] = ldsr128(xa); x1[nx][i] = ldsr128(xa + 4096); }
                if (st < 5 && i == 3) QS_PIECE(st & 3, 4);
                if (st == 5 && i == 3) { DMA_K(0); DMA_V(0); }
                if (st == 6 && i == 3) { DMA_K(1); DMA_K(2); }
                if (st == 7 && i == 3) DMA_K(3);
                __builtin_amdgcn_sched_barrier(0);
            }
        }
#undef QS_ISSUE
#undef QS_PIECE
#define QW(ACC, qblk) do { _Pragma("unroll") for (int s = 0; s < 2; ++s) { \
            u32x4 w_ = {cvtpk(ACC[8 * s] * C2, ACC[8 * s + 1] * C2), cvtpk(ACC[8 * s + 2] * C2, ACC[8 * s + 3] * C2), \
                        cvtpk(ACC[8 * s + 4] * C2, ACC[8 * s + 5] * C2), cvtpk(ACC[8 * s + 6] * C2, ACC[8 * s + 7] * C2)}; \
            *(LAS u32x4*)(uintptr_t)(lds0 + LDS_Q + (wid >> 2) * 16384 + (qblk) * 8192 + (2 * (wid & 3) + s) * 1024 + lane * 16) = w_; } } while (0)
        QW(acc0, 0); QW(acc1, 1);
#undef QW
        DMA_V(1); DMA_V(2);
    }
    asm volatile("s_waitcnt vmcnt(8) lgkmcnt(0)\n\ts_barrier" ::: "memory");
#pragma unroll
    for (int f = 0; f < 8; ++f) qf[f] = ldsr128(lds0 + LDS_Q + qb * 8192 + f * 1024 + lane * 16);

    const unsigned long long tstart = __builtin_amdgcn_s_memrealtime();
    const unsigned kb0 = lds0 + (4 * kh + (r32 >> 3)) * 2048 + (r32 & 7) * 64 + ((hi ^ (r32 >> 3)) << 4);
    const unsigned kb1 = kb0 ^ 32;
    const unsigned vb = lds0 + kh * 8192 + (4 * hi + vq) * 64 + (gi & 1) * 32 + vp * 8;
#define KRD(d0, so) ldsr128((((d0) & 1) ? kb1 : kb0) + (so) + ((d0) >> 1) * 512)

    float m_run, l_run = 0.f, tmax = 0.f;
    f32x16 o[4] = {};
    f32x16 sA, sB, negm;

    bf16x8 kf[8];
    {
        bf16x8 k0[8];
        unsigned ka0 = kb0 + KSO(0), ka1 = kb1 + KSO(0);
        asm volatile("" : "+v"(ka0), "+v"(ka1));
#pragma unroll
        for (int d0 = 0; d0 < 8; ++d0) k0[d0] = ldsr128(((d0 & 1) ? ka1 : ka0) + (d0 >> 1) * 512);
#pragma unroll
        for (int d0 = 0; d0 < 8; ++d0) kf[d0] = ldsr128(((d0 & 1) ? ka1 : ka0) + 16384 + (d0 >> 1) * 512);
        __builtin_amdgcn_sched_barrier(0);
        asm volatile("s_waitcnt lgkmcnt(0)\n\ts_barrier" ::: "memory");
        DMA_K(4);
        sA = f32x16{};
#pragma unroll
        for (int d0 = 0; d0 < 8; ++d0) sA = MFMA32(k0[d0], qf[d0], sA);
        m_run = sA[0];
#pragma unroll
        for (int r = 1; r < 16; ++r) m_run = __builtin_fmaxf(m_run, sA[r]);
        m_run = swap_max(m_run);
#pragma unroll
        for (int r = 0; r < 16; ++r) { sA[r] -= m_run; negm[r] = -m_run; }
    }
#define EXP1(r) do { sc[r] = __builtin_amdgcn_exp2f(sc[r]); } while (0)
#define SUM1(k) do { ps0 += sc[2 * (k)]; ps1 += sc[2 * (k) + 1]; pw[k] = cvtpk(sc[2 * (k)], sc[2 * (k) + 1]); asm volatile("" : "+v"(pw[k]), "+v"(ps0), "+v"(ps1)); } while (0)
#define STEP(t, SC_, SN_, TAIL) do { \
        f32x16& sc = SC_; f32x16& sn = SN_; \
        s16x4 vlo[8], vhi[8]; \
        if ((t) < 13) { while (__builtin_amdgcn_s_memrealtime() - tstart < (unsigned long long)((t) * PACE)) __builtin_amdgcn_s_sleep(1); } \
        if (!TAIL || (t) < 12) asm volatile("s_waitcnt vmcnt(8)" ::: "memory"); \
        else if ((t) == 12) asm volatile("s_waitcnt vmcnt(6)" ::: "memory"); \
        else if ((t) == 13) asm volatile("s_waitcnt vmcnt(4)" ::: "memory"); \
        else if ((t) == 14) asm volatile("s_waitcnt vmcnt(2)" ::: "memory"); \
        else asm volatile("s_waitcnt vmcnt(0)" ::: "memory"); \
        asm volatile("s_waitcnt lgkmcnt(0)\n\ts_barrier" ::: "memory"); \
        if (__builtin_expect(__any(tmax > RTHR), 0)) { \
            const float dl = __builtin_fmaxf(tmax, 0.f); \
            const float alpha = __builtin_amdgcn_exp2f(-dl); \
            m_run += dl; l_run *= alpha; \
            _Pragma("unroll") for (int r = 0; r < 16; ++r) { sc[r] -= dl; negm[r] = -m_run; } \
            _Pragma("unroll") for (int dt = 0; dt < 4; ++dt) \
                _Pragma("unroll") for (int r = 0; r < 16; ++r) o[dt][r] *= alpha; \
        } \
        const unsigned va = vb + VSO(t); \
        const unsigned ka0 = kb0 + KSO((t) + 2), ka1 = kb1 + KSO((t) + 2);       \
        unsigned pw[8]; \
        float ps0 = 0.f, ps1 = 0.f, t2; \
        __builtin_amdgcn_sched_barrier(0); \
          \
        _Pragma("unroll") for (int i = 0; i < 8; ++i) { \
            sn = MFMA32(kf[i], qf[i], i == 0 ? negm : sn);                   \
            const int ss = i >> 2, dt = i & 3; \
            vlo[2 * dt + ss] = vtr(va + ss * 4096 + dt * 512); \
            vhi[2 * dt + ss] = vtr(va + ss * 4096 + 2048 + dt * 512); \
            if (i < 4) { EXP1(2 * i); EXP1(2 * i + 1); } else EXP1(4 + i); \
            if (i >= 1 && i <= 4) SUM1(i - 1); \
            if (i == 6) SUM1(4); \
            asm volatile("" : "+v"(sn), "+v"(sc)); \
            if (i == XS0 && (!TAIL || (t) < 13)) DMA_V0((t) + 3); \
            if (i == XS1 && (!TAIL || (t) < 13)) DMA_V1((t) + 3); \
            __builtin_amdgcn_sched_barrier(0); \
        } \
          \
        _Pragma("unroll") for (int i = 0; i < 8; ++i) { \
            const int ss = i >> 2, dt = i & 3; \
            const s16x4 lo = vlo[2 * dt + ss], hi4 = vhi[2 * dt + ss]; \
            const bf16x8 vf = {lo[0], lo[1], lo[2], lo[3], hi4[0], hi4[1], hi4[2], hi4[3]}; \
            const bf16x8 pf = __builtin_bit_cast(bf16x8, u32x4{pw[4 * ss], pw[4 * ss + 1], pw[4 * ss + 2], pw[4 * ss + 3]}); \
            o[dt] = MFMA32(vf, pf, o[dt]); \
            if (i < 4) { kf[2 * i] = ldsr128(ka0 + i * 512); kf[2 * i + 1] = ldsr128(ka1 + i * 512); } \
            if (i < 2) { EXP1(12 + 2 * i); EXP1(13 + 2 * i); asm volatile("" : "+v"(sc)); } \
            if (i < 3) SUM1(5 + i); \
            if (i == 3) { l_run += ps0 + ps1; asm volatile("" : "+v"(sn), "+v"(l_run)); \
                          tmax = max3f(sn[0], sn[1], sn[2]); t2 = max3f(sn[3], sn[4], sn[5]); asm volatile("" : "+v"(tmax), "+v"(t2)); } \
            if (i == 4) { tmax = max3f(tmax, sn[6], sn[7]); t2 = max3f(t2, sn[8], sn[9]); tmax = max3f(tmax, sn[10], sn[11]); asm volatile("" : "+v"(tmax), "+v"(t2)); } \
            if (i == 5) { t2 = max3f(t2, sn[12], sn[13]); tmax = max3f(tmax, sn[14], sn[15]); tmax = max3f(tmax, t2, t2); asm volatile("" : "+v"(tmax)); } \
            if (i == 6) { tmax = swap_max3(tmax); asm volatile("" : "+v"(tmax)); } \
            if (i == YS0 && (!TAIL || (t) < 11)) DMA_K0((t) + 5); \
            if (i == YS1 && (!TAIL || (t) < 11)) DMA_K1((t) + 5); \
            __builtin_amdgcn_sched_barrier(0); \
        } } while (0)
#pragma unroll 1
    for (int t = 0; t < 10; t += 2) { STEP(t, sA, sB, false); STEP(t + 1, sB, sA, false); }
#pragma unroll 1
    for (int t = 10; t < NT; t += 2) { STEP(t, sA, sB, true); STEP(t + 1, sB, sA, true); }
#undef STEP
#undef EXP1
#undef SUM1
    {
        const char* wp = (const char*)wo + (size_t)(1024 * ((bid >> 1) & 3) + 32 * (bid >> 3)) * DM * 2 + wid * 1024 + lane * 16;
#pragma unroll
        for (int i = 0; i < 4; ++i) glds16(wp + i * 8192, lds0 + LDS_Q + i * 8192 + wid * 1024);
        if (wid < 4) glds16(wp + 32768, lds0 + LDS_Q + wid * 1024);
    }
    l_run = swap_add(l_run);
    asm volatile("s_waitcnt lgkmcnt(0)\n\ts_barrier" ::: "memory");
    {
        f32x16 keep0, keep1, send0, send1;
        if (kh == 0) { keep0 = o[0]; keep1 = o[1]; send0 = o[2]; send1 = o[3]; }
        else         { keep0 = o[2]; keep1 = o[3]; send0 = o[0]; send1 = o[1]; }
        const unsigned xb = lds0 + wid * 9216;
#pragma unroll
        for (int r4 = 0; r4 < 4; ++r4) {
            f32x4 v0 = {send0[4 * r4], send0[4 * r4 + 1], send0[4 * r4 + 2], send0[4 * r4 + 3]};
            f32x4 v1 = {send1[4 * r4], send1[4 * r4 + 1], send1[4 * r4 + 2], send1[4 * r4 + 3]};
            *(LAS f32x4*)(uintptr_t)(xb + r4 * 1024 + lane * 16) = v0;
            *(LAS f32x4*)(uintptr_t)(xb + (4 + r4) * 1024 + lane * 16) = v1;
        }
        *(LAS float*)(uintptr_t)(xb + 8192 + lane * 4) = m_run;
        *(LAS float*)(uintptr_t)(xb + 8192 + 256 + lane * 4) = l_run;
        asm volatile("s_waitcnt lgkmcnt(0)\n\ts_barrier" ::: "memory");
        const unsigned pb = lds0 + (wid ^ 4) * 9216;
        const float m2 = *(const LAS float*)(uintptr_t)(pb + 8192 + lane * 4);
        const float l2 = *(const LAS float*)(uintptr_t)(pb + 8192 + 256 + lane * 4);
        const float mm = __builtin_fmaxf(m_run, m2);
        const float a1 = __builtin_amdgcn_exp2f(m_run - mm), a2 = __builtin_amdgcn_exp2f(m2 - mm);
        const float inv = 1.0f / (l_run * a1 + l2 * a2);
        const float w1 = a1 * inv, w2 = a2 * inv;
        const int row = b * SQ + 32 * (qb & 1) + r32;
        char* ob = (char*)Og + ((size_t)((row >> 4) * 128 + head * 4 + 2 * kh) * 64 + hi * 16 + (row & 15)) * 16;
#define OSTORE(KEEP, i) do { u32x2 w_[4]; \
            _Pragma("unroll") for (int r4 = 0; r4 < 4; ++r4) { \
                const f32x4 v = *(const LAS f32x4*)(uintptr_t)(pb + (4 * (i) + r4) * 1024 + lane * 16); \
                const float e0 = KEEP[4 * r4] * w1 + v[0] * w2, e1 = KEEP[4 * r4 + 1] * w1 + v[1] * w2; \
                const float e2 = KEEP[4 * r4 + 2] * w1 + v[2] * w2, e3 = KEEP[4 * r4 + 3] * w1 + v[3] * w2; \
                w_[r4] = u32x2{cvtpk(e0, e1), cvtpk(e2, e3)}; } \
            _Pragma("unroll") for (int g = 0; g < 2; ++g) { \
                u32x4 o4 = {w_[g].x, w_[g].y, w_[g + 2].x, w_[g + 2].y}; \
                asm volatile("global_store_dwordx4 %0, %1, off sc1" :: "v"(ob + (i) * 1024 + g * 512), "v"(o4) : "memory"); } } while (0)
        OSTORE(keep0, 0); OSTORE(keep1, 1);
#undef OSTORE
    }
}

constexpr int LDS2_BYTES = 1024 * 160;
#define MFMA16(a, b, c) __builtin_amdgcn_mfma_f32_16x16x32_bf16(a, b, c, 0, 0, 0)
__global__ __launch_bounds__(512, 2) void oproj_kernel(const u16* __restrict__ Og, const u16* __restrict__ wo,
                                                       float* __restrict__ part, u16* __restrict__ out) {
    extern __shared__ __attribute__((aligned(16))) char lds[];
    const unsigned lds0 = (unsigned)(uintptr_t)lds;
    const int tid = threadIdx.x, lane = tid & 63;
    const int wid = __builtin_amdgcn_readfirstlane(tid >> 6);
    const int rb = wid & 3, kp = wid >> 2;
    const int bid = blockIdx.x;
    const int sk = (bid >> 1) & 3, mt = 4 * (bid & 1) + ((bid >> 3) & 3), nt = bid >> 5;
    const char* Bg[5];
#pragma unroll
    for (int i = 0; i < 5; ++i) {
        const int L = i * 1024 + lane * 16;
        const int row = L / 160;
        int c = (L % 160) >> 4;
        c = c < 9 ? c : 0;
        Bg[i] = (const char*)wo + ((size_t)(1024 * sk + 32 * wid + row) * DM + 72 * nt) * 2 + c * 16;
    }
    const char* Ag = (const char*)Og + ((size_t)((8 * mt + 2 * rb) * 128 + 32 * sk + kp) * 64 + lane) * 16;
    u32x4 an[4][8];
#define K2_B(c) do { _Pragma("unroll") for (int i = 0; i < 5; ++i) \
        glds16(Bg[i] + (size_t)(c) * 256 * DM * 2, lds0 + (40 * (c) + 5 * wid + i) * 1024); } while (0)
#define K2_A(c) do { _Pragma("unroll") for (int j = 0; j < 4; j += 2) { const char* a_ = Ag + (8 * (c) + 2 * j) * 1024; \
        asm volatile("global_load_dwordx4 %0, %1, off" : "=v"(an[c][2 * j]) : "v"(a_) : "memory"); \
        asm volatile("global_load_dwordx4 %0, %1, off" : "=v"(an[c][2 * j + 1]) : "v"(a_ + 131072) : "memory"); \
        asm volatile("global_load_dwordx4 %0, %1, off offset:2048" : "=v"(an[c][2 * j + 2]) : "v"(a_) : "memory"); \
        asm volatile("global_load_dwordx4 %0, %1, off offset:2048" : "=v"(an[c][2 * j + 3]) : "v"(a_ + 131072) : "memory"); } } while (0)
    K2_B(0);
    asm volatile("s_barrier" ::: "memory");
    K2_A(0); K2_B(1);
    const int gi = lane >> 4, vq = (lane & 15) >> 2, vp = lane & 3;
    const unsigned bb = lds0 + (32 * kp + 4 * gi + vq) * 160 + 8 * vp;
    f32x4 acc[2][5];
#pragma unroll
    for (int ct = 0; ct < 5; ++ct) { acc[0][ct] = f32x4{0.f, 0.f, 0.f, 0.f}; acc[1][ct] = f32x4{0.f, 0.f, 0.f, 0.f}; }
    s16x4 blo[2][5], bhi[2][5];
    asm volatile("s_waitcnt vmcnt(13)\n\ts_barrier" ::: "memory");
#pragma unroll
    for (int ct = 0; ct < 5; ++ct) { blo[0][ct] = vtr(bb + 32 * ct); bhi[0][ct] = vtr(bb + 32 * ct + 2560); }
#pragma unroll
    for (int s = 0; s < 16; ++s) {
        const int c = s >> 2, j = s & 3;
        const int young = (6 - 2 * j) + (c < 3 ? 5 + (j < 2 ? 4 * j : 8) + (j == 3 && c < 2 ? 5 : 0) : 0);
        asm volatile("s_waitcnt vmcnt(%2)" : "+v"(an[c][2 * j]), "+v"(an[c][2 * j + 1]) : "n"(young) : "memory");

        if (j == 3 && c < 3) asm volatile("s_waitcnt vmcnt(%0)\n\ts_barrier" :: "n"(c < 2 ? 13 : 8) : "memory");
        __builtin_amdgcn_sched_barrier(0);
#pragma unroll
        for (int ct = 0; ct < 5; ++ct) {
            if (s < 15) {
                const unsigned ad = bb + (s + 1) * 10240 + 32 * ct;
                blo[(s + 1) & 1][ct] = vtr(ad); bhi[(s + 1) & 1][ct] = vtr(ad + 2560);
            }
            const s16x4 lo = blo[s & 1][ct], h4 = bhi[s & 1][ct];
            const bf16x8 bf = {lo[0], lo[1], lo[2], lo[3], h4[0], h4[1], h4[2], h4[3]};
            acc[0][ct] = MFMA16(bf, __builtin_bit_cast(bf16x8, an[c][2 * j]), acc[0][ct]);
            acc[1][ct] = MFMA16(bf, __builtin_bit_cast(bf16x8, an[c][2 * j + 1]), acc[1][ct]);
            if (j < 2 && c < 3 && ct < 4) {
                const char* a_ = Ag + (8 * (c + 1) + 4 * j + (ct >> 1) * 2) * 1024 + (ct & 1) * 131072;
                asm volatile("global_load_dwordx4 %0, %1, off" : "=v"(an[(c + 1) & 3][4 * j + ct]) : "v"(a_) : "memory");
            }
            if (j == 2 && c < 2) glds16(Bg[ct] + (size_t)(c + 2) * 256 * DM * 2, lds0 + (40 * (c + 2) + 5 * wid + ct) * 1024);
            __builtin_amdgcn_sched_barrier(0);
        }
    }
#undef K2_A
#undef K2_B
    asm volatile("s_waitcnt lgkmcnt(0)\n\ts_barrier" ::: "memory");
#pragma unroll
    for (int ct = 0; ct < 5; ++ct)
        *(LAS f32x4*)(uintptr_t)(lds0 + (wid * 5 + ct) * 1024 + lane * 16) = kp ? acc[0][ct] : acc[1][ct];
    asm volatile("s_waitcnt lgkmcnt(0)\n\ts_barrier" ::: "memory");
    float* pp = part + ((size_t)(sk * 64 + 8 * mt + 2 * rb + kp) * 8 + nt) * 1152 + lane * 4;
#pragma unroll
    for (int ct = 0; ct < 5; ++ct) {
        const f32x4 v = *(const LAS f32x4*)(uintptr_t)(lds0 + ((wid ^ 4) * 5 + ct) * 1024 + lane * 16);
        const f32x4 r_ = (kp ? acc[1][ct] : acc[0][ct]) + v;
        if (ct < 4 || lane < 32)
            asm volatile("global_store_dwordx4 %0, %1, off sc1" :: "v"(pp + ct * 256), "v"(r_) : "memory");
    }
    asm volatile("s_waitcnt vmcnt(0)\n\ts_barrier" ::: "memory");
    if (wid == 0) {
        gu32* cw = (gu32*)((const char*)Og + CNT_OFF) + (mt * 8 + nt) * 32;
        unsigned todo = 1u << sk;
        unsigned old = 0;
        if (lane == 0) old = __hip_atomic_fetch_add(cw, 1u, RLX_AGENT);
        old = __builtin_amdgcn_readfirstlane(old);
        if ((old & 255u) == 3u) todo |= (old >> 8) & 15u;
        else {
            const unsigned long long t0 = __builtin_amdgcn_s_memrealtime();
            for (;;) {
                const unsigned v = __builtin_amdgcn_readfirstlane(__hip_atomic_load(cw, RLX_AGENT));
                if ((v & 255u) == 4u) break;
                if (__builtin_amdgcn_s_memrealtime() - t0 > 5000ull) {
                    unsigned o2 = 0;
                    if (lane == 0) o2 = __hip_atomic_fetch_or(cw, 256u << sk, RLX_AGENT);
                    if ((__builtin_amdgcn_readfirstlane(o2) & 255u) != 4u) todo = 0;
                    break;
                }
                __builtin_amdgcn_s_sleep(1);
            }
        }
        if (lane == 0) *(LAS unsigned*)(uintptr_t)lds0 = todo;
    }
    asm volatile("s_waitcnt lgkmcnt(0)\n\ts_barrier" ::: "memory");
    unsigned todo = __builtin_amdgcn_readfirstlane(*(const LAS unsigned*)(uintptr_t)lds0);
    while (todo) {
        const int q = __builtin_ctz(todo);
        todo &= todo - 1;
        if (tid < 288) {
            const float* p = part + ((size_t)(8 * mt + 2 * q) * 8 + nt) * 1152 + tid * 4;
            f32x4 v[2][4];
#pragma unroll
            for (int r = 0; r < 2; ++r)
#pragma unroll
                for (int s = 0; s < 4; ++s)
                    asm volatile("global_load_dwordx4 %0, %1, off sc1" : "=v"(v[r][s]) : "v"(p + (size_t)s * 64 * 8 * 1152 + r * 8 * 1152) : "memory");
            asm volatile("s_waitcnt vmcnt(0)" : "+v"(v[0][0]), "+v"(v[0][1]), "+v"(v[0][2]), "+v"(v[0][3]),
                                                "+v"(v[1][0]), "+v"(v[1][1]), "+v"(v[1][2]), "+v"(v[1][3]) :: "memory");
#pragma unroll
            for (int r = 0; r < 2; ++r) {
                const f32x4 t = ((v[r][0] + v[r][1]) + v[r][2]) + v[r][3];
                u32x2 w = {cvtpk(t[0], t[1]), cvtpk(t[2], t[3])};
                *(u32x2*)(out + (size_t)(16 * (8 * mt + 2 * q + r) + (tid & 15)) * DM + 72 * nt + 4 * (tid >> 4)) = w;
            }
        }
    }
}

extern "C" void kernel_launch(void* const* d_in, const int* in_sizes, int n_in, void* d_out, int out_size,
                              void* d_ws, size_t ws_size, hipStream_t stream) {
    const u16* x = (const u16*)d_in[0];
    const u16* kc = (const u16*)d_in[1];
    const u16* vc = (const u16*)d_in[2];
    const u16* wq = (const u16*)d_in[3];
    const u16* wo = (const u16*)d_in[4];
    u16* Og = (u16*)d_ws;
    float* part = (float*)((char*)d_ws + (size_t)NB * SQ * NQ * 2);
    (void)hipFuncSetAttribute((const void*)attn_kernel, hipFuncAttributeMaxDynamicSharedMemorySize, LDS1_BYTES);
    (void)hipFuncSetAttribute((const void*)oproj_kernel, hipFuncAttributeMaxDynamicSharedMemorySize, LDS2_BYTES);
    attn_kernel<<<NB * HKV, 512, LDS1_BYTES, stream>>>(x, kc, vc, wq, Og, wo);
    oproj_kernel<<<256, 512, LDS2_BYTES, stream>>>(Og, wo, part, (u16*)d_out);
}
```

```cpp
#define PACE 100
#define XS0 5
#define XS1 7
#define YS0 5
#define YS1 7
#include <hip/hip_runtime.h>
#include <hip/hip_bf16.h>
#include <stdint.h>

typedef short bf16x8 __attribute__((ext_vector_type(8)));
typedef short s16x4 __attribute__((ext_vector_type(4)));
typedef float f32x16 __attribute__((ext_vector_type(16)));
typedef float f32x4 __attribute__((ext_vector_type(4)));
typedef unsigned u32x4 __attribute__((ext_vector_type(4)));
typedef unsigned u32x2 __attribute__((ext_vector_type(2)));
typedef unsigned short u16;
#define LAS __attribute__((address_space(3)))
typedef LAS const char* lds_cptr;
typedef __attribute__((address_space(1))) unsigned gu32;
#define RLX_AGENT __ATOMIC_RELAXED, __HIP_MEMORY_SCOPE_AGENT

constexpr int NB = 16, SQ = 64, DM = 576, HQ = 32, HKV = 16, SKV = 1024, DH = 128;
constexpr int NQ = HQ * DH;
constexpr int NT = SKV / 64;
constexpr int SLOT = 32768;
constexpr int LDS_Q = 4 * SLOT;
constexpr int LDS1_BYTES = 5 * SLOT;
constexpr size_t CNT_OFF = (size_t)20 << 20;
constexpr float RTHR = 8.0f;
constexpr float C2 = 0.088388347648318447f * 1.4426950408889634f;

#define MFMA32(a, b, c) __builtin_amdgcn_mfma_f32_32x32x16_bf16(a, b, c, 0, 0, 0)

typedef __bf16 bf16x2_t __attribute__((ext_vector_type(2)));
typedef float f32x2 __attribute__((ext_vector_type(2)));
__device__ __forceinline__ unsigned cvtpk(float lo, float hi) {
    f32x2 v = {lo, hi};
    return __builtin_bit_cast(unsigned, __builtin_convertvector(v, bf16x2_t));
}
__device__ __forceinline__ void glds16(const void* g, unsigned lds_base) {
    unsigned sv;
    asm volatile("s_mov_b32 %0, m0\n\ts_mov_b32 m0, %2\n\ts_nop 0\n\tglobal_load_lds_dwordx4 %1, off\n\ts_mov_b32 m0, %0"
                 : "=&s"(sv) : "v"(g), "s"(lds_base) : "memory");
}
__device__ __forceinline__ void glds16nt(const void* g, unsigned lds_base) {
    unsigned sv;
    asm volatile("s_mov_b32 %0, m0\n\ts_mov_b32 m0, %2\n\ts_nop 0\n\tglobal_load_lds_dwordx4 %1, off nt\n\ts_mov_b32 m0, %0"
                 : "=&s"(sv) : "v"(g), "s"(lds_base) : "memory");
}
__device__ __forceinline__ s16x4 vtr(unsigned a) {
    return __builtin_amdgcn_ds_read_tr16_b64_v4i16((LAS s16x4*)(uintptr_t)a);
}
__device__ __forceinline__ bf16x8 ldsr128(unsigned a) { return *(const LAS bf16x8*)(uintptr_t)a; }
__device__ __forceinline__ float swap_max(float v) {
    auto rr = __builtin_amdgcn_permlane32_swap(__float_as_uint(v), __float_as_uint(v), false, false);
    return __builtin_fmaxf(__uint_as_float(rr[0]), __uint_as_float(rr[1]));
}
__device__ __forceinline__ float max3f(float a, float b, float c) { float r; asm("v_max3_f32 %0, %1, %2, %3" : "=v"(r) : "v"(a), "v"(b), "v"(c)); return r; }
__device__ __forceinline__ float swap_max3(float v) {
    auto rr = __builtin_amdgcn_permlane32_swap(__float_as_uint(v), __float_as_uint(v), false, false);
    return max3f(__uint_as_float(rr[0]), __uint_as_float(rr[1]), __uint_as_float(rr[1]));
}
__device__ __forceinline__ float swap_add(float v) {
    auto rr = __builtin_amdgcn_permlane32_swap(__float_as_uint(v), __float_as_uint(v), false, false);
    return __uint_as_float(rr[0]) + __uint_as_float(rr[1]);
}

constexpr int XIMG = 0;
constexpr int WRING = 73728;
__global__ __launch_bounds__(512, 2) void attn_kernel(const u16* __restrict__ x, const u16* __restrict__ Kc,
                                                      const u16* __restrict__ Vc, const u16* __restrict__ wq,
                                                      u16* __restrict__ Og, const u16* __restrict__ wo) {
    extern __shared__ __attribute__((aligned(16))) char lds[];
    const unsigned lds0 = (unsigned)(uintptr_t)lds;
    const int tid = threadIdx.x, lane = tid & 63, r32 = lane & 31, hi = lane >> 5;
    const int wid = __builtin_amdgcn_readfirstlane(tid >> 6);
    const int qb = wid & 3, kh = wid >> 2;
    const int bid = blockIdx.x, b = 8 * (bid & 1) + ((bid >> 3) & 7), kvh = 4 * ((bid >> 1) & 3) + (bid >> 6);
    const int head = 2 * kvh + (qb >> 1);
    if (tid == 0 && bid < 64) ((unsigned*)((char*)Og + CNT_OFF))[bid * 32] = 0u;
    const int gi = lane >> 4, vq = (lane & 15) >> 2, vp = lane & 3;

    const unsigned vsrc = (16 * (wid >> 1) + ((lane >> 2) & 7)) * 256 + (wid & 1) * 128 + hi * 64;
    const unsigned voffV = vsrc + vp * 16;
    const unsigned voffK0 = vsrc + ((vp ^ (wid & 2)) << 4), voffK1 = vsrc + ((vp ^ ((wid & 2) | 1)) << 4);
    const char* Kg = (const char*)Kc + (size_t)(b * HKV + kvh) * SKV * DH * 2;
    const char* Vg = (const char*)Vc + (size_t)(b * HKV + kvh) * SKV * DH * 2;
    const unsigned dwo = lds0 + (wid >> 1) * 4096 + (wid & 1) * 1024;
#define KVDMA(voff, gp, dst, OFF) asm volatile("s_mov_b32 m0, %2\n\ts_nop 0\n\tglobal_load_lds_dwordx4 %0, %1 offset:" #OFF " sc1 nt" \
            :: "v"(voff), "s"(gp), "s"(dst) : "memory")
#define VSO(tn) ((((tn) + 3) & 3) * 16384)
#define KSO(tn) (65536 + ((tn) & 3) * 16384)
#define DMA_V0(tn) KVDMA(voffV, Vg + (tn) * 16384, dwo + VSO(tn), 0)
#define DMA_V1(tn) KVDMA(voffV, Vg + (tn) * 16384, dwo + VSO(tn), 2048)
#define DMA_K0(tn) KVDMA(voffK0, Kg + (tn) * 16384, dwo + KSO(tn), 0)
#define DMA_K1(tn) KVDMA(voffK1, Kg + (tn) * 16384, dwo + KSO(tn), 2048)
#define DMA_V(tn) do { DMA_V0(tn); DMA_V1(tn); } while (0)
#define DMA_K(tn) do { DMA_K0(tn); DMA_K1(tn); } while (0)
    bf16x8 qf[8];
    {
        int kb = (b + 2 * (kvh & 3)) % 9;
        const unsigned xo = (8 * wid + (lane >> 3)) * DM * 2 + (((lane & 7) ^ ((4 * wid + (lane >> 4)) & 7)) << 4);
        const unsigned wo0 = (8 * wid + hi) * NQ * 2 + ((r32 ^ (hi << 2)) << 4);
        const unsigned wo1 = (8 * wid + 2 + hi) * NQ * 2 + ((r32 ^ ((2 + hi) << 2)) << 4);
        const unsigned wo2 = wo0 + 4 * NQ * 2, wo3 = wo1 + 4 * NQ * 2;
        const char* Xs = (const char*)x + (size_t)b * SQ * DM * 2;
        const char* Ws = (const char*)wq + kvh * 512;
#define QDMA(voff, gp, dst) asm volatile("s_mov_b32 m0, %2\n\ts_nop 0\n\tglobal_load_lds_dwordx4 %0, %1" :: "v"(voff), "s"(gp), "s"(dst) : "memory")
#define QS_PIECE(slot, p) do { const unsigned s_ = lds0 + (slot) * 40960 + wid * 4096; const char* w_ = Ws + (size_t)kb * 64 * NQ * 2; \
            if ((p) == 0) QDMA(wo0, w_, s_); \
            if ((p) == 1) QDMA(wo1, w_, s_ + 1024); \
            if ((p) == 2) QDMA(wo2, w_, s_ + 2048); \
            if ((p) == 3) QDMA(wo3, w_, s_ + 3072); \
            if ((p) == 4) { QDMA(xo, Xs + kb * 128, lds0 + (slot) * 40960 + 32768 + wid * 1024); kb = kb == 8 ? 0 : kb + 1; } } while (0)
#define QS_ISSUE(slot) do { QS_PIECE(slot, 0); QS_PIECE(slot, 1); QS_PIECE(slot, 2); QS_PIECE(slot, 3); QS_PIECE(slot, 4); } while (0)
#pragma unroll 1
        for (int j = 0; j < 2; ++j) { QS_ISSUE(j); asm volatile("s_barrier" ::: "memory"); }
        const int pp = ((vp & 1) << 1) | (vp >> 1);
        const unsigned abase = lds0 + (8 * hi + vq) * 512 + (((4 * wid + 2 * (gi & 1) + (pp >> 1)) ^ (vq << 2)) << 4) + 8 * (pp & 1);
        const unsigned xbase = lds0 + 32768 + r32 * 128 + ((hi ^ ((r32 >> 1) & 7)) << 4);
        f32x16 acc0 = {}, acc1 = {};
        s16x4 lo[2][4], h4[2][4]; bf16x8 x0[2][4], x1[2][4];
        asm volatile("s_waitcnt vmcnt(5)\n\ts_barrier" ::: "memory");
        QS_ISSUE(2);
#pragma unroll
        for (int i = 0; i < 4; ++i) {
            lo[0][i] = vtr(abase + i * 8192); h4[0][i] = vtr(abase + i * 8192 + 2048);
            x0[0][i] = ldsr128(xbase ^ (i << 5)); x1[0][i] = ldsr128((xbase ^ (i << 5)) + 4096);
        }
#pragma unroll
        for (int st = 0; st < 9; ++st) {
            if (st == 0) asm volatile("s_waitcnt vmcnt(5)" ::: "memory");
            else if (st < 6) asm volatile("s_waitcnt vmcnt(10)" ::: "memory");
            else if (st == 6) asm volatile("s_waitcnt vmcnt(9)" ::: "memory");
            else if (st == 7) asm volatile("s_waitcnt vmcnt(8)" ::: "memory");
            asm volatile("s_waitcnt lgkmcnt(0)\n\ts_barrier" ::: "memory");
            const unsigned so = ((st + 1) & 3) * 40960;
            const int cu = st & 1, nx = cu ^ 1;
            unsigned aa = abase + so;
            asm volatile("" : "+v"(aa));
            __builtin_amdgcn_sched_barrier(0);
            if (st == 0) QS_ISSUE(3);
#pragma unroll
            for (int i = 0; i < 4; ++i) {
                const bf16x8 a0 = {lo[cu][i][0], lo[cu][i][1], lo[cu][i][2], lo[cu][i][3], h4[cu][i][0], h4[cu][i][1], h4[cu][i][2], h4[cu][i][3]};
                acc0 = MFMA32(a0, x0[cu][i], acc0);
                if (st < 8) { lo[nx][i] = vtr(aa + i * 8192); h4[nx][i] = vtr(aa + i * 8192 + 2048); }
                if (st < 5) QS_PIECE(st & 3, i);
                __builtin_amdgcn_sched_barrier(0);
                acc1 = MFMA32(a0, x1[cu][i], acc1);
                if (st < 8) { unsigned xa = (xbase ^ (i << 5)) + so; asm volatile("" : "+v"(xa)); x0[nx][i] = ldsr128(xa); x1[nx][i] = ldsr128(xa + 4096); }
                if (st < 5 && i == 3) QS_PIECE(st & 3, 4);
                if (st == 5 && i == 3) { DMA_K(0); DMA_V(0); }
                if (st == 6 && i == 3) { DMA_K(1); DMA_K(2); }
                if (st == 7 && i == 3) DMA_K(3);
                __builtin_amdgcn_sched_barrier(0);
            }
        }
#undef QS_ISSUE
#undef QS_PIECE
#define QW(ACC, qblk) do { _Pragma("unroll") for (int s = 0; s < 2; ++s) { \
            u32x4 w_ = {cvtpk(ACC[8 * s] * C2, ACC[8 * s + 1] * C2), cvtpk(ACC[8 * s + 2] * C2, ACC[8 * s + 3] * C2), \
                        cvtpk(ACC[8 * s + 4] * C2, ACC[8 * s + 5] * C2), cvtpk(ACC[8 * s + 6] * C2, ACC[8 * s + 7] * C2)}; \
            *(LAS u32x4*)(uintptr_t)(lds0 + LDS_Q + (wid >> 2) * 16384 + (qblk) * 8192 + (2 * (wid & 3) + s) * 1024 + lane * 16) = w_; } } while (0)
        QW(acc0, 0); QW(acc1, 1);
#undef QW
        DMA_V(1); DMA_V(2);
    }
    asm volatile("s_waitcnt vmcnt(8) lgkmcnt(0)\n\ts_barrier" ::: "memory");
#pragma unroll
    for (int f = 0; f < 8; ++f) qf[f] = ldsr128(lds0 + LDS_Q + qb * 8192 + f * 1024 + lane * 16);

    const unsigned kb0 = lds0 + (4 * kh + (r32 >> 3)) * 2048 + (r32 & 7) * 64 + ((hi ^ (r32 >> 3)) << 4);
    const unsigned kb1 = kb0 ^ 32;
    const unsigned vb = lds0 + kh * 8192 + (4 * hi + vq) * 64 + (gi & 1) * 32 + vp * 8;
#define KRD(d0, so) ldsr128((((d0) & 1) ? kb1 : kb0) + (so) + ((d0) >> 1) * 512)

    float m_run, l_run = 0.f, tmax = 0.f;
    unsigned long long tprev = 0;
    f32x16 o[4] = {};
    f32x16 sA, sB, negm;

    bf16x8 kf[8];
    {
        bf16x8 k0[8];
        unsigned ka0 = kb0 + KSO(0), ka1 = kb1 + KSO(0);
        asm volatile("" : "+v"(ka0), "+v"(ka1));
#pragma unroll
        for (int d0 = 0; d0 < 8; ++d0) k0[d0] = ldsr128(((d0 & 1) ? ka1 : ka0) + (d0 >> 1) * 512);
#pragma unroll
        for (int d0 = 0; d0 < 8; ++d0) kf[d0] = ldsr128(((d0 & 1) ? ka1 : ka0) + 16384 + (d0 >> 1) * 512);
        __builtin_amdgcn_sched_barrier(0);
        asm volatile("s_waitcnt lgkmcnt(0)\n\ts_barrier" ::: "memory");
        DMA_K(4);
        sA = f32x16{};
#pragma unroll
        for (int d0 = 0; d0 < 8; ++d0) sA = MFMA32(k0[d0], qf[d0], sA);
        m_run = sA[0];
#pragma unroll
        for (int r = 1; r < 16; ++r) m_run = __builtin_fmaxf(m_run, sA[r]);
        m_run = swap_max(m_run);
#pragma unroll
        for (int r = 0; r < 16; ++r) { sA[r] -= m_run; negm[r] = -m_run; }
    }
#define EXP1(r) do { sc[r] = __builtin_amdgcn_exp2f(sc[r]); } while (0)
#define SUM1(k) do { ps0 += sc[2 * (k)]; ps1 += sc[2 * (k) + 1]; pw[k] = cvtpk(sc[2 * (k)], sc[2 * (k) + 1]); asm volatile("" : "+v"(pw[k]), "+v"(ps0), "+v"(ps1)); } while (0)
#define STEP(t, SC_, SN_, TAIL) do { \
        f32x16& sc = SC_; f32x16& sn = SN_; \
        s16x4 vlo[8], vhi[8]; \
        if (!TAIL || (t) < 12) asm volatile("s_waitcnt vmcnt(8)" ::: "memory"); \
        else if ((t) == 12) asm volatile("s_waitcnt vmcnt(6)" ::: "memory"); \
        else if ((t) == 13) asm volatile("s_waitcnt vmcnt(4)" ::: "memory"); \
        else if ((t) == 14) asm volatile("s_waitcnt vmcnt(2)" ::: "memory"); \
        else asm volatile("s_waitcnt vmcnt(0)" ::: "memory"); \
        { unsigned long long n_; while ((n_ = __builtin_amdgcn_s_memrealtime()) - tprev < (unsigned long long)PACE) __builtin_amdgcn_s_sleep(1); } \
        asm volatile("s_waitcnt lgkmcnt(0)\n\ts_barrier" ::: "memory"); \
        tprev = __builtin_amdgcn_s_memrealtime(); \
        if (__builtin_expect(__any(tmax > RTHR), 0)) { \
            const float dl = __builtin_fmaxf(tmax, 0.f); \
            const float alpha = __builtin_amdgcn_exp2f(-dl); \
            m_run += dl; l_run *= alpha; \
            _Pragma("unroll") for (int r = 0; r < 16; ++r) { sc[r] -= dl; negm[r] = -m_run; } \
            _Pragma("unroll") for (int dt = 0; dt < 4; ++dt) \
                _Pragma("unroll") for (int r = 0; r < 16; ++r) o[dt][r] *= alpha; \
        } \
        const unsigned va = vb + VSO(t); \
        const unsigned ka0 = kb0 + KSO((t) + 2), ka1 = kb1 + KSO((t) + 2);       \
        unsigned pw[8]; \
        float ps0 = 0.f, ps1 = 0.f, t2; \
        __builtin_amdgcn_sched_barrier(0); \
          \
        _Pragma("unroll") for (int i = 0; i < 8; ++i) { \
            sn = MFMA32(kf[i], qf[i], i == 0 ? negm : sn);                   \
            const int ss = i >> 2, dt = i & 3; \
            vlo[2 * dt + ss] = vtr(va + ss * 4096 + dt * 512); \
            vhi[2 * dt + ss] = vtr(va + ss * 4096 + 2048 + dt * 512); \
            if (i < 4) { EXP1(2 * i); EXP1(2 * i + 1); } else EXP1(4 + i); \
            if (i >= 1 && i <= 4) SUM1(i - 1); \
            if (i == 6) SUM1(4); \
            asm volatile("" : "+v"(sn), "+v"(sc)); \
            if (i == XS0 && (!TAIL || (t) < 13)) DMA_V0((t) + 3); \
            if (i == XS1 && (!TAIL || (t) < 13)) DMA_V1((t) + 3); \
            __builtin_amdgcn_sched_barrier(0); \
        } \
          \
        _Pragma("unroll") for (int i = 0; i < 8; ++i) { \
            const int ss = i >> 2, dt = i & 3; \
            const s16x4 lo = vlo[2 * dt + ss], hi4 = vhi[2 * dt + ss]; \
            const bf16x8 vf = {lo[0], lo[1], lo[2], lo[3], hi4[0], hi4[1], hi4[2], hi4[3]}; \
            const bf16x8 pf = __builtin_bit_cast(bf16x8, u32x4{pw[4 * ss], pw[4 * ss + 1], pw[4 * ss + 2], pw[4 * ss + 3]}); \
            o[dt] = MFMA32(vf, pf, o[dt]); \
            if (i < 4) { kf[2 * i] = ldsr128(ka0 + i * 512); kf[2 * i + 1] = ldsr128(ka1 + i * 512); } \
            if (i < 2) { EXP1(12 + 2 * i); EXP1(13 + 2 * i); asm volatile("" : "+v"(sc)); } \
            if (i < 3) SUM1(5 + i); \
            if (i == 3) { l_run += ps0 + ps1; asm volatile("" : "+v"(sn), "+v"(l_run)); \
                          tmax = max3f(sn[0], sn[1], sn[2]); t2 = max3f(sn[3], sn[4], sn[5]); asm volatile("" : "+v"(tmax), "+v"(t2)); } \
            if (i == 4) { tmax = max3f(tmax, sn[6], sn[7]); t2 = max3f(t2, sn[8], sn[9]); tmax = max3f(tmax, sn[10], sn[11]); asm volatile("" : "+v"(tmax), "+v"(t2)); } \
            if (i == 5) { t2 = max3f(t2, sn[12], sn[13]); tmax = max3f(tmax, sn[14], sn[15]); tmax = max3f(tmax, t2, t2); asm volatile("" : "+v"(tmax)); } \
            if (i == 6) { tmax = swap_max3(tmax); asm volatile("" : "+v"(tmax)); } \
            if (i == YS0 && (!TAIL || (t) < 11)) DMA_K0((t) + 5); \
            if (i == YS1 && (!TAIL || (t) < 11)) DMA_K1((t) + 5); \
            __builtin_amdgcn_sched_barrier(0); \
        } } while (0)
#pragma unroll 1
    for (int t = 0; t < 10; t += 2) { STEP(t, sA, sB, false); STEP(t + 1, sB, sA, false); }
#pragma unroll 1
    for (int t = 10; t < NT; t += 2) { STEP(t, sA, sB, true); STEP(t + 1, sB, sA, true); }
#undef STEP
#undef EXP1
#undef SUM1
    {
        const char* wp = (const char*)wo + (size_t)(1024 * ((bid >> 1) & 3) + 32 * (bid >> 3)) * DM * 2 + wid * 1024 + lane * 16;
#pragma unroll
        for (int i = 0; i < 4; ++i) glds16(wp + i * 8192, lds0 + LDS_Q + i * 8192 + wid * 1024);
        if (wid < 4) glds16(wp + 32768, lds0 + LDS_Q + wid * 1024);
    }
    l_run = swap_add(l_run);
    asm volatile("s_waitcnt lgkmcnt(0)\n\ts_barrier" ::: "memory");
    {
        f32x16 keep0, keep1, send0, send1;
        if (kh == 0) { keep0 = o[0]; keep1 = o[1]; send0 = o[2]; send1 = o[3]; }
        else         { keep0 = o[2]; keep1 = o[3]; send0 = o[0]; send1 = o[1]; }
        const unsigned xb = lds0 + wid * 9216;
#pragma unroll
        for (int r4 = 0; r4 < 4; ++r4) {
            f32x4 v0 = {send0[4 * r4], send0[4 * r4 + 1], send0[4 * r4 + 2], send0[4 * r4 + 3]};
            f32x4 v1 = {send1[4 * r4], send1[4 * r4 + 1], send1[4 * r4 + 2], send1[4 * r4 + 3]};
            *(LAS f32x4*)(uintptr_t)(xb + r4 * 1024 + lane * 16) = v0;
            *(LAS f32x4*)(uintptr_t)(xb + (4 + r4) * 1024 + lane * 16) = v1;
        }
        *(LAS float*)(uintptr_t)(xb + 8192 + lane * 4) = m_run;
        *(LAS float*)(uintptr_t)(xb + 8192 + 256 + lane * 4) = l_run;
        asm volatile("s_waitcnt lgkmcnt(0)\n\ts_barrier" ::: "memory");
        const unsigned pb = lds0 + (wid ^ 4) * 9216;
        const float m2 = *(const LAS float*)(uintptr_t)(pb + 8192 + lane * 4);
        const float l2 = *(const LAS float*)(uintptr_t)(pb + 8192 + 256 + lane * 4);
        const float mm = __builtin_fmaxf(m_run, m2);
        const float a1 = __builtin_amdgcn_exp2f(m_run - mm), a2 = __builtin_amdgcn_exp2f(m2 - mm);
        const float inv = 1.0f / (l_run * a1 + l2 * a2);
        const float w1 = a1 * inv, w2 = a2 * inv;
        const int row = b * SQ + 32 * (qb & 1) + r32;
        char* ob = (char*)Og + ((size_t)((row >> 4) * 128 + head * 4 + 2 * kh) * 64 + hi * 16 + (row & 15)) * 16;
#define OSTORE(KEEP, i) do { u32x2 w_[4]; \
            _Pragma("unroll") for (int r4 = 0; r4 < 4; ++r4) { \
                const f32x4 v = *(const LAS f32x4*)(uintptr_t)(pb + (4 * (i) + r4) * 1024 + lane * 16); \
                const float e0 = KEEP[4 * r4] * w1 + v[0] * w2, e1 = KEEP[4 * r4 + 1] * w1 + v[1] * w2; \
                const float e2 = KEEP[4 * r4 + 2] * w1 + v[2] * w2, e3 = KEEP[4 * r4 + 3] * w1 + v[3] * w2; \
                w_[r4] = u32x2{cvtpk(e0, e1), cvtpk(e2, e3)}; } \
            _Pragma("unroll") for (int g = 0; g < 2; ++g) { \
                u32x4 o4 = {w_[g].x, w_[g].y, w_[g + 2].x, w_[g + 2].y}; \
                asm volatile("global_store_dwordx4 %0, %1, off sc1" :: "v"(ob + (i) * 1024 + g * 512), "v"(o4) : "memory"); } } while (0)
        OSTORE(keep0, 0); OSTORE(keep1, 1);
#undef OSTORE
    }
}

constexpr int LDS2_BYTES = 1024 * 160;
#define MFMA16(a, b, c) __builtin_amdgcn_mfma_f32_16x16x32_bf16(a, b, c, 0, 0, 0)
__global__ __launch_bounds__(512, 2) void oproj_kernel(const u16* __restrict__ Og, const u16* __restrict__ wo,
                                                       float* __restrict__ part, u16* __restrict__ out) {
    extern __shared__ __attribute__((aligned(16))) char lds[];
    const unsigned lds0 = (unsigned)(uintptr_t)lds;
    const int tid = threadIdx.x, lane = tid & 63;
    const int wid = __builtin_amdgcn_readfirstlane(tid >> 6);
    const int rb = wid & 3, kp = wid >> 2;
    const int bid = blockIdx.x;
    const int sk = (bid >> 1) & 3, mt = 4 * (bid & 1) + ((bid >> 3) & 3), nt = bid >> 5;
    const char* Bg[5];
#pragma unroll
    for (int i = 0; i < 5; ++i) {
        const int L = i * 1024 + lane * 16;
        const int row = L / 160;
        int c = (L % 160) >> 4;
        c = c < 9 ? c : 0;
        Bg[i] = (const char*)wo + ((size_t)(1024 * sk + 32 * wid + row) * DM + 72 * nt) * 2 + c * 16;
    }
    const char* Ag = (const char*)Og + ((size_t)((8 * mt + 2 * rb) * 128 + 32 * sk + kp) * 64 + lane) * 16;
    u32x4 an[4][8];
#define K2_B(c) do { _Pragma("unroll") for (int i = 0; i < 5; ++i) \
        glds16(Bg[i] + (size_t)(c) * 256 * DM * 2, lds0 + (40 * (c) + 5 * wid + i) * 1024); } while (0)
#define K2_A(c) do { _Pragma("unroll") for (int j = 0; j < 4; j += 2) { const char* a_ = Ag + (8 * (c) + 2 * j) * 1024; \
        asm volatile("global_load_dwordx4 %0, %1, off" : "=v"(an[c][2 * j]) : "v"(a_) : "memory"); \
        asm volatile("global_load_dwordx4 %0, %1, off" : "=v"(an[c][2 * j + 1]) : "v"(a_ + 131072) : "memory"); \
        asm volatile("global_load_dwordx4 %0, %1, off offset:2048" : "=v"(an[c][2 * j + 2]) : "v"(a_) : "memory"); \
        asm volatile("global_load_dwordx4 %0, %1, off offset:2048" : "=v"(an[c][2 * j + 3]) : "v"(a_ + 131072) : "memory"); } } while (0)
    K2_B(0);
    asm volatile("s_barrier" ::: "memory");
    K2_A(0); K2_B(1);
    const int gi = lane >> 4, vq = (lane & 15) >> 2, vp = lane & 3;
    const unsigned bb = lds0 + (32 * kp + 4 * gi + vq) * 160 + 8 * vp;
    f32x4 acc[2][5];
#pragma unroll
    for (int ct = 0; ct < 5; ++ct) { acc[0][ct] = f32x4{0.f, 0.f, 0.f, 0.f}; acc[1][ct] = f32x4{0.f, 0.f, 0.f, 0.f}; }
    s16x4 blo[2][5], bhi[2][5];
    asm volatile("s_waitcnt vmcnt(13)\n\ts_barrier" ::: "memory");
#pragma unroll
    for (int ct = 0; ct < 5; ++ct) { blo[0][ct] = vtr(bb + 32 * ct); bhi[0][ct] = vtr(bb + 32 * ct + 2560); }
#pragma unroll
    for (int s = 0; s < 16; ++s) {
        const int c = s >> 2, j = s & 3;
        const int young = (6 - 2 * j) + (c < 3 ? 5 + (j < 2 ? 4 * j : 8) + (j == 3 && c < 2 ? 5 : 0) : 0);
        asm volatile("s_waitcnt vmcnt(%2)" : "+v"(an[c][2 * j]), "+v"(an[c][2 * j + 1]) : "n"(young) : "memory");

        if (j == 3 && c < 3) asm volatile("s_waitcnt vmcnt(%0)\n\ts_barrier" :: "n"(c < 2 ? 13 : 8) : "memory");
        __builtin_amdgcn_sched_barrier(0);
#pragma unroll
        for (int ct = 0; ct < 5; ++ct) {
            if (s < 15) {
                const unsigned ad = bb + (s + 1) * 10240 + 32 * ct;
                blo[(s + 1) & 1][ct] = vtr(ad); bhi[(s + 1) & 1][ct] = vtr(ad + 2560);
            }
            const s16x4 lo = blo[s & 1][ct], h4 = bhi[s & 1][ct];
            const bf16x8 bf = {lo[0], lo[1], lo[2], lo[3], h4[0], h4[1], h4[2], h4[3]};
            acc[0][ct] = MFMA16(bf, __builtin_bit_cast(bf16x8, an[c][2 * j]), acc[0][ct]);
            acc[1][ct] = MFMA16(bf, __builtin_bit_cast(bf16x8, an[c][2 * j + 1]), acc[1][ct]);
            if (j < 2 && c < 3 && ct < 4) {
                const char* a_ = Ag + (8 * (c + 1) + 4 * j + (ct >> 1) * 2) * 1024 + (ct & 1) * 131072;
                asm volatile("global_load_dwordx4 %0, %1, off" : "=v"(an[(c + 1) & 3][4 * j + ct]) : "v"(a_) : "memory");
            }
            if (j == 2 && c < 2) glds16(Bg[ct] + (size_t)(c + 2) * 256 * DM * 2, lds0 + (40 * (c + 2) + 5 * wid + ct) * 1024);
            __builtin_amdgcn_sched_barrier(0);
        }
    }
#undef K2_A
#undef K2_B
    asm volatile("s_waitcnt lgkmcnt(0)\n\ts_barrier" ::: "memory");
#pragma unroll
    for (int ct = 0; ct < 5; ++ct)
        *(LAS f32x4*)(uintptr_t)(lds0 + (wid * 5 + ct) * 1024 + lane * 16) = kp ? acc[0][ct] : acc[1][ct];
    asm volatile("s_waitcnt lgkmcnt(0)\n\ts_barrier" ::: "memory");
    float* pp = part + ((size_t)(sk * 64 + 8 * mt + 2 * rb + kp) * 8 + nt) * 1152 + lane * 4;
#pragma unroll
    for (int ct = 0; ct < 5; ++ct) {
        const f32x4 v = *(const LAS f32x4*)(uintptr_t)(lds0 + ((wid ^ 4) * 5 + ct) * 1024 + lane * 16);
        const f32x4 r_ = (kp ? acc[1][ct] : acc[0][ct]) + v;
        if (ct < 4 || lane < 32)
            asm volatile("global_store_dwordx4 %0, %1, off sc1" :: "v"(pp + ct * 256), "v"(r_) : "memory");
    }
    asm volatile("s_waitcnt vmcnt(0)\n\ts_barrier" ::: "memory");
    if (wid == 0) {
        gu32* cw = (gu32*)((const char*)Og + CNT_OFF) + (mt * 8 + nt) * 32;
        unsigned todo = 1u << sk;
        unsigned old = 0;
        if (lane == 0) old = __hip_atomic_fetch_add(cw, 1u, RLX_AGENT);
        old = __builtin_amdgcn_readfirstlane(old);
        if ((old & 255u) == 3u) todo |= (old >> 8) & 15u;
        else {
            const unsigned long long t0 = __builtin_amdgcn_s_memrealtime();
            for (;;) {
                const unsigned v = __builtin_amdgcn_readfirstlane(__hip_atomic_load(cw, RLX_AGENT));
                if ((v & 255u) == 4u) break;
                if (__builtin_amdgcn_s_memrealtime() - t0 > 5000ull) {
                    unsigned o2 = 0;
                    if (lane == 0) o2 = __hip_atomic_fetch_or(cw, 256u << sk, RLX_AGENT);
                    if ((__builtin_amdgcn_readfirstlane(o2) & 255u) != 4u) todo = 0;
                    break;
                }
                __builtin_amdgcn_s_sleep(1);
            }
        }
        if (lane == 0) *(LAS unsigned*)(uintptr_t)lds0 = todo;
    }
    asm volatile("s_waitcnt lgkmcnt(0)\n\ts_barrier" ::: "memory");
    unsigned todo = __builtin_amdgcn_readfirstlane(*(const LAS unsigned*)(uintptr_t)lds0);
    while (todo) {
        const int q = __builtin_ctz(todo);
        todo &= todo - 1;
        if (tid < 288) {
            const float* p = part + ((size_t)(8 * mt + 2 * q) * 8 + nt) * 1152 + tid * 4;
            f32x4 v[2][4];
#pragma unroll
            for (int r = 0; r < 2; ++r)
#pragma unroll
                for (int s = 0; s < 4; ++s)
                    asm volatile("global_load_dwordx4 %0, %1, off sc1" : "=v"(v[r][s]) : "v"(p + (size_t)s * 64 * 8 * 1152 + r * 8 * 1152) : "memory");
            asm volatile("s_waitcnt vmcnt(0)" : "+v"(v[0][0]), "+v"(v[0][1]), "+v"(v[0][2]), "+v"(v[0][3]),
                                                "+v"(v[1][0]), "+v"(v[1][1]), "+v"(v[1][2]), "+v"(v[1][3]) :: "memory");
#pragma unroll
            for (int r = 0; r < 2; ++r) {
                const f32x4 t = ((v[r][0] + v[r][1]) + v[r][2]) + v[r][3];
                u32x2 w = {cvtpk(t[0], t[1]), cvtpk(t[2], t[3])};
                *(u32x2*)(out + (size_t)(16 * (8 * mt + 2 * q + r) + (tid & 15)) * DM + 72 * nt + 4 * (tid >> 4)) = w;
            }
        }
    }
}

extern "C" void kernel_launch(void* const* d_in, const int* in_sizes, int n_in, void* d_out, int out_size,
                              void* d_ws, size_t ws_size, hipStream_t stream) {
    const u16* x = (const u16*)d_in[0];
    const u16* kc = (const u16*)d_in[1];
    const u16* vc = (const u16*)d_in[2];
    const u16* wq = (const u16*)d_in[3];
    const u16* wo = (const u16*)d_in[4];
    u16* Og = (u16*)d_ws;
    float* part = (float*)((char*)d_ws + (size_t)NB * SQ * NQ * 2);
    (void)hipFuncSetAttribute((const void*)attn_kernel, hipFuncAttributeMaxDynamicSharedMemorySize, LDS1_BYTES);
    (void)hipFuncSetAttribute((const void*)oproj_kernel, hipFuncAttributeMaxDynamicSharedMemorySize, LDS2_BYTES);
    attn_kernel<<<NB * HKV, 512, LDS1_BYTES, stream>>>(x, kc, vc, wq, Og, wo);
    oproj_kernel<<<256, 512, LDS2_BYTES, stream>>>(Og, wo, part, (u16*)d_out);
}
```

```cpp
#include <hip/hip_runtime.h>
#include <hip/hip_bf16.h>
#include <stdint.h>

typedef short bf16x8 __attribute__((ext_vector_type(8)));
typedef short s16x4 __attribute__((ext_vector_type(4)));
typedef float f32x16 __attribute__((ext_vector_type(16)));
typedef float f32x4 __attribute__((ext_vector_type(4)));
typedef unsigned u32x4 __attribute__((ext_vector_type(4)));
typedef unsigned u32x2 __attribute__((ext_vector_type(2)));
typedef unsigned short u16;
#define LAS __attribute__((address_space(3)))
typedef __attribute__((address_space(1))) unsigned gu32;
#define RLX_AGENT __ATOMIC_RELAXED, __HIP_MEMORY_SCOPE_AGENT

constexpr int NB = 16, SQ = 64, DM = 576, HQ = 32, HKV = 16, SKV = 1024, DH = 128;
constexpr int NQ = HQ * DH;
constexpr int NT = SKV / 64;
constexpr int SLOT = 32768;
constexpr int LDS_Q = 4 * SLOT;
constexpr int LDS1_BYTES = 5 * SLOT;
constexpr size_t CNT_OFF = (size_t)20 << 20;
constexpr int XS0 = 5, XS1 = 7, YS0 = 5, YS1 = 7;
constexpr int PACE = 108;
constexpr float RTHR = 8.0f;
constexpr float C2 = 0.088388347648318447f * 1.4426950408889634f;

#define MFMA32(a, b, c) __builtin_amdgcn_mfma_f32_32x32x16_bf16(a, b, c, 0, 0, 0)

typedef __bf16 bf16x2_t __attribute__((ext_vector_type(2)));
typedef float f32x2 __attribute__((ext_vector_type(2)));
__device__ __forceinline__ unsigned cvtpk(float lo, float hi) {
    f32x2 v = {lo, hi};
    return __builtin_bit_cast(unsigned, __builtin_convertvector(v, bf16x2_t));
}
__device__ __forceinline__ void glds16(const void* g, unsigned lds_base) {
    unsigned sv;
    asm volatile("s_mov_b32 %0, m0\n\ts_mov_b32 m0, %2\n\ts_nop 0\n\tglobal_load_lds_dwordx4 %1, off\n\ts_mov_b32 m0, %0"
                 : "=&s"(sv) : "v"(g), "s"(lds_base) : "memory");
}
__device__ __forceinline__ s16x4 vtr(unsigned a) {
    return __builtin_amdgcn_ds_read_tr16_b64_v4i16((LAS s16x4*)(uintptr_t)a);
}
__device__ __forceinline__ bf16x8 ldsr128(unsigned a) { return *(const LAS bf16x8*)(uintptr_t)a; }
__device__ __forceinline__ float swap_max(float v) {
    auto rr = __builtin_amdgcn_permlane32_swap(__float_as_uint(v), __float_as_uint(v), false, false);
    return __builtin_fmaxf(__uint_as_float(rr[0]), __uint_as_float(rr[1]));
}
__device__ __forceinline__ float max3f(float a, float b, float c) { float r; asm("v_max3_f32 %0, %1, %2, %3" : "=v"(r) : "v"(a), "v"(b), "v"(c)); return r; }
__device__ __forceinline__ float fmul1(float a, float b) { float r; asm("v_mul_f32_e32 %0, %1, %2" : "=v"(r) : "s"(b), "v"(a)); return r; }
__device__ __forceinline__ float swap_max3(float v) {
    auto rr = __builtin_amdgcn_permlane32_swap(__float_as_uint(v), __float_as_uint(v), false, false);
    return max3f(__uint_as_float(rr[0]), __uint_as_float(rr[1]), __uint_as_float(rr[1]));
}
__device__ __forceinline__ float swap_add(float v) {
    auto rr = __builtin_amdgcn_permlane32_swap(__float_as_uint(v), __float_as_uint(v), false, false);
    return __uint_as_float(rr[0]) + __uint_as_float(rr[1]);
}

__global__ __launch_bounds__(512, 2) void attn_kernel(const u16* __restrict__ x, const u16* __restrict__ Kc,
                                                      const u16* __restrict__ Vc, const u16* __restrict__ wq,
                                                      u16* __restrict__ Og, const u16* __restrict__ wo) {
    extern __shared__ __attribute__((aligned(16))) char lds[];
    const unsigned lds0 = (unsigned)(uintptr_t)lds;
    const int tid = threadIdx.x, lane = tid & 63, r32 = lane & 31, hi = lane >> 5;
    const int wid = __builtin_amdgcn_readfirstlane(tid >> 6);
    const int qb = wid & 3, kh = wid >> 2;
    const int bid = blockIdx.x, b = 8 * (bid & 1) + ((bid >> 3) & 7), kvh = 4 * ((bid >> 1) & 3) + (bid >> 6);
    const int head = 2 * kvh + (qb >> 1);
    if (tid == 0 && bid < 64) ((unsigned*)((char*)Og + CNT_OFF))[bid * 32] = 0u;
    const int gi = lane >> 4, vq = (lane & 15) >> 2, vp = lane & 3;

    const unsigned vsrc = (16 * (wid >> 1) + ((lane >> 2) & 7)) * 256 + (wid & 1) * 128 + hi * 64;
    const unsigned voffV = vsrc + vp * 16;
    const unsigned voffK0 = vsrc + ((vp ^ (wid & 2)) << 4), voffK1 = vsrc + ((vp ^ ((wid & 2) | 1)) << 4);
    const char* Kg = (const char*)Kc + (size_t)(b * HKV + kvh) * SKV * DH * 2;
    const char* Vg = (const char*)Vc + (size_t)(b * HKV + kvh) * SKV * DH * 2;
    const unsigned dwo = lds0 + (wid >> 1) * 4096 + (wid & 1) * 1024;
#define KVDMA(voff, gp, dst, OFF) asm volatile("s_mov_b32 m0, %2\n\ts_nop 0\n\tglobal_load_lds_dwordx4 %0, %1 offset:" #OFF " sc1 nt" \
            :: "v"(voff), "s"(gp), "s"(dst) : "memory")
#define VSO(tn) ((((tn) + 3) & 3) * 16384)
#define KSO(tn) (65536 + ((tn) & 3) * 16384)
#define DMA_V0(tn) KVDMA(voffV, Vg + (tn) * 16384, dwo + VSO(tn), 0)
#define DMA_V1(tn) KVDMA(voffV, Vg + (tn) * 16384, dwo + VSO(tn), 2048)
#define DMA_K0(tn) KVDMA(voffK0, Kg + (tn) * 16384, dwo + KSO(tn), 0)
#define DMA_K1(tn) KVDMA(voffK1, Kg + (tn) * 16384, dwo + KSO(tn), 2048)
#define DMA_V(tn) do { DMA_V0(tn); DMA_V1(tn); } while (0)
#define DMA_K(tn) do { DMA_K0(tn); DMA_K1(tn); } while (0)
    bf16x8 qf[8];
    {
        int kb = (b + 2 * (kvh & 3)) % 9;
        const unsigned xo = (8 * wid + (lane >> 3)) * DM * 2 + (((lane & 7) ^ ((4 * wid + (lane >> 4)) & 7)) << 4);
        const unsigned wo0 = (8 * wid + hi) * NQ * 2 + ((r32 ^ (hi << 2)) << 4);
        const unsigned wo1 = (8 * wid + 2 + hi) * NQ * 2 + ((r32 ^ ((2 + hi) << 2)) << 4);
        const unsigned wo2 = wo0 + 4 * NQ * 2, wo3 = wo1 + 4 * NQ * 2;
        const char* Xs = (const char*)x + (size_t)b * SQ * DM * 2;
        const char* Ws = (const char*)wq + kvh * 512;
#define QDMA(voff, gp, dst) asm volatile("s_mov_b32 m0, %2\n\ts_nop 0\n\tglobal_load_lds_dwordx4 %0, %1" :: "v"(voff), "s"(gp), "s"(dst) : "memory")
#define QS_PIECE(slot, p) do { const unsigned s_ = lds0 + (slot) * 40960 + wid * 4096; const char* w_ = Ws + (size_t)kb * 64 * NQ * 2; \
            if ((p) == 0) QDMA(wo0, w_, s_); \
            if ((p) == 1) QDMA(wo1, w_, s_ + 1024); \
            if ((p) == 2) QDMA(wo2, w_, s_ + 2048); \
            if ((p) == 3) QDMA(wo3, w_, s_ + 3072); \
            if ((p) == 4) { QDMA(xo, Xs + kb * 128, lds0 + (slot) * 40960 + 32768 + wid * 1024); kb = kb == 8 ? 0 : kb + 1; } } while (0)
#define QS_ISSUE(slot) do { QS_PIECE(slot, 0); QS_PIECE(slot, 1); QS_PIECE(slot, 2); QS_PIECE(slot, 3); QS_PIECE(slot, 4); } while (0)
#pragma unroll 1
        for (int j = 0; j < 2; ++j) { QS_ISSUE(j); asm volatile("s_barrier" ::: "memory"); }
        const int pp = ((vp & 1) << 1) | (vp >> 1);
        const unsigned abase = lds0 + (8 * hi + vq) * 512 + (((4 * wid + 2 * (gi & 1) + (pp >> 1)) ^ (vq << 2)) << 4) + 8 * (pp & 1);
        const unsigned xbase = lds0 + 32768 + r32 * 128 + ((hi ^ ((r32 >> 1) & 7)) << 4);
        f32x16 acc0 = {}, acc1 = {};
        s16x4 lo[2][4], h4[2][4]; bf16x8 x0[2][4], x1[2][4];
        asm volatile("s_waitcnt vmcnt(5)\n\ts_barrier" ::: "memory");
        QS_ISSUE(2);
#pragma unroll
        for (int i = 0; i < 4; ++i) {
            lo[0][i] = vtr(abase + i * 8192); h4[0][i] = vtr(abase + i * 8192 + 2048);
            x0[0][i] = ldsr128(xbase ^ (i << 5)); x1[0][i] = ldsr128((xbase ^ (i << 5)) + 4096);
        }
#pragma unroll
        for (int st = 0; st < 9; ++st) {
            if (st == 0) asm volatile("s_waitcnt vmcnt(5)" ::: "memory");
            else if (st < 6) asm volatile("s_waitcnt vmcnt(10)" ::: "memory");
            else if (st == 6) asm volatile("s_waitcnt vmcnt(9)" ::: "memory");
            else if (st == 7) asm volatile("s_waitcnt vmcnt(8)" ::: "memory");
            asm volatile("s_waitcnt lgkmcnt(0)\n\ts_barrier" ::: "memory");
            const unsigned so = ((st + 1) & 3) * 40960;
            const int cu = st & 1, nx = cu ^ 1;
            unsigned aa = abase + so;
            asm volatile("" : "+v"(aa));
            __builtin_amdgcn_sched_barrier(0);
            if (st == 0) QS_ISSUE(3);
#pragma unroll
            for (int i = 0; i < 4; ++i) {
                const bf16x8 a0 = {lo[cu][i][0], lo[cu][i][1], lo[cu][i][2], lo[cu][i][3], h4[cu][i][0], h4[cu][i][1], h4[cu][i][2], h4[cu][i][3]};
                acc0 = MFMA32(a0, x0[cu][i], acc0);
                if (st < 8) { lo[nx][i] = vtr(aa + i * 8192); h4[nx][i] = vtr(aa + i * 8192 + 2048); }
                if (st < 5) QS_PIECE(st & 3, i);
                __builtin_amdgcn_sched_barrier(0);
                acc1 = MFMA32(a0, x1[cu][i], acc1);
                if (st < 8) { unsigned xa = (xbase ^ (i << 5)) + so; asm volatile("" : "+v"(xa)); x0[nx][i] = ldsr128(xa); x1[nx][i] = ldsr128(xa + 4096); }
                if (st < 5 && i == 3) QS_PIECE(st & 3, 4);
                if (st == 5 && i == 3) { DMA_K(0); DMA_V(0); }
                if (st == 6 && i == 3) { DMA_K(1); DMA_K(2); }
                if (st == 7 && i == 3) DMA_K(3);
                __builtin_amdgcn_sched_barrier(0);
            }
        }
#undef QS_ISSUE
#undef QS_PIECE
#define QW(ACC, qblk) do { _Pragma("unroll") for (int s = 0; s < 2; ++s) { \
            u32x4 w_ = {cvtpk(ACC[8 * s], ACC[8 * s + 1]), cvtpk(ACC[8 * s + 2], ACC[8 * s + 3]), \
                        cvtpk(ACC[8 * s + 4], ACC[8 * s + 5]), cvtpk(ACC[8 * s + 6], ACC[8 * s + 7])}; \
            *(LAS u32x4*)(uintptr_t)(lds0 + LDS_Q + (wid >> 2) * 16384 + (qblk) * 8192 + (2 * (wid & 3) + s) * 1024 + lane * 16) = w_; } } while (0)
        QW(acc0, 0); QW(acc1, 1);
#undef QW
        DMA_V(1); DMA_V(2);
    }
    asm volatile("s_waitcnt vmcnt(8) lgkmcnt(0)\n\ts_barrier" ::: "memory");
#pragma unroll
    for (int f = 0; f < 8; ++f) qf[f] = ldsr128(lds0 + LDS_Q + qb * 8192 + f * 1024 + lane * 16);

    const unsigned kb0 = lds0 + (4 * kh + (r32 >> 3)) * 2048 + (r32 & 7) * 64 + ((hi ^ (r32 >> 3)) << 4);
    const unsigned kb1 = kb0 ^ 32;
    const unsigned vb = lds0 + kh * 8192 + (4 * hi + vq) * 64 + (gi & 1) * 32 + vp * 8;

    float m_run, l_run = 0.f, tmax = 0.f;
    unsigned long long tprev = 0;
    f32x16 o[4] = {};
    f32x16 sA, sB, negm;

    bf16x8 kf[8];
    {
        bf16x8 k0[8];
        unsigned ka0 = kb0 + KSO(0), ka1 = kb1 + KSO(0);
        asm volatile("" : "+v"(ka0), "+v"(ka1));
#pragma unroll
        for (int d0 = 0; d0 < 8; ++d0) k0[d0] = ldsr128(((d0 & 1) ? ka1 : ka0) + (d0 >> 1) * 512);
#pragma unroll
        for (int d0 = 0; d0 < 8; ++d0) kf[d0] = ldsr128(((d0 & 1) ? ka1 : ka0) + 16384 + (d0 >> 1) * 512);
        __builtin_amdgcn_sched_barrier(0);
        asm volatile("s_waitcnt lgkmcnt(0)\n\ts_barrier" ::: "memory");
        DMA_K(4);
        sA = f32x16{};
#pragma unroll
        for (int d0 = 0; d0 < 8; ++d0) sA = MFMA32(k0[d0], qf[d0], sA);
        m_run = sA[0];
#pragma unroll
        for (int r = 1; r < 16; ++r) m_run = __builtin_fmaxf(m_run, sA[r]);
        m_run = swap_max(m_run);
#pragma unroll
        for (int r = 0; r < 16; ++r) { sA[r] -= m_run; negm[r] = -m_run; }
    }
#define EXP1(r) do { sc[r] = __builtin_amdgcn_exp2f(fmul1(sc[r], C2)); } while (0)
#define SUM1(k) do { ps0 += sc[2 * (k)]; ps1 += sc[2 * (k) + 1]; pw[k] = cvtpk(sc[2 * (k)], sc[2 * (k) + 1]); asm volatile("" : "+v"(pw[k]), "+v"(ps0), "+v"(ps1)); } while (0)
#define STEP(t, SC_, SN_, TAIL) do { \
        f32x16& sc = SC_; f32x16& sn = SN_; \
        s16x4 vlo[8], vhi[8]; \
        if (!TAIL || (t) < 12) asm volatile("s_waitcnt vmcnt(8)" ::: "memory"); \
        else if ((t) == 12) asm volatile("s_waitcnt vmcnt(6)" ::: "memory"); \
        else if ((t) == 13) asm volatile("s_waitcnt vmcnt(4)" ::: "memory"); \
        else if ((t) == 14) asm volatile("s_waitcnt vmcnt(2)" ::: "memory"); \
        else asm volatile("s_waitcnt vmcnt(0)" ::: "memory"); \
        while (__builtin_amdgcn_s_memrealtime() - tprev < (unsigned long long)PACE) __builtin_amdgcn_s_sleep(1); \
        asm volatile("s_waitcnt lgkmcnt(0)\n\ts_barrier" ::: "memory"); \
        tprev = __builtin_amdgcn_s_memrealtime(); \
        if (__builtin_expect(__any(tmax > RTHR / C2), 0)) { \
            const float dl = __builtin_fmaxf(tmax, 0.f); \
            const float alpha = __builtin_amdgcn_exp2f(-dl * C2); \
            m_run += dl; l_run *= alpha; \
            _Pragma("unroll") for (int r = 0; r < 16; ++r) { sc[r] -= dl; negm[r] = -m_run; } \
            _Pragma("unroll") for (int dt = 0; dt < 4; ++dt) \
                _Pragma("unroll") for (int r = 0; r < 16; ++r) o[dt][r] *= alpha; \
        } \
        const unsigned va = vb + VSO(t); \
        const unsigned ka0 = kb0 + KSO((t) + 2), ka1 = kb1 + KSO((t) + 2);       \
        unsigned pw[8]; \
        float ps0 = 0.f, ps1 = 0.f, t2; \
        __builtin_amdgcn_sched_barrier(0); \
          \
        _Pragma("unroll") for (int i = 0; i < 8; ++i) { \
            sn = MFMA32(kf[i], qf[i], i == 0 ? negm : sn);                   \
            const int ss = i >> 2, dt = i & 3; \
            vlo[2 * dt + ss] = vtr(va + ss * 4096 + dt * 512); \
            vhi[2 * dt + ss] = vtr(va + ss * 4096 + 2048 + dt * 512); \
            if (i < 4) { EXP1(2 * i); EXP1(2 * i + 1); } else EXP1(4 + i); \
            if (i >= 1 && i <= 4) SUM1(i - 1); \
            if (i == 6) SUM1(4); \
            asm volatile("" : "+v"(sn), "+v"(sc)); \
            if (i == XS0 && (!TAIL || (t) < 13)) DMA_V0((t) + 3); \
            if (i == XS1 && (!TAIL || (t) < 13)) DMA_V1((t) + 3); \
            __builtin_amdgcn_sched_barrier(0); \
        } \
          \
        _Pragma("unroll") for (int i = 0; i < 8; ++i) { \
            const int ss = i >> 2, dt = i & 3; \
            const s16x4 lo = vlo[2 * dt + ss], hi4 = vhi[2 * dt + ss]; \
            const bf16x8 vf = {lo[0], lo[1], lo[2], lo[3], hi4[0], hi4[1], hi4[2], hi4[3]}; \
            const bf16x8 pf = __builtin_bit_cast(bf16x8, u32x4{pw[4 * ss], pw[4 * ss + 1], pw[4 * ss + 2], pw[4 * ss + 3]}); \
            o[dt] = MFMA32(vf, pf, o[dt]); \
            if (i < 4) { kf[2 * i] = ldsr128(ka0 + i * 512); kf[2 * i + 1] = ldsr128(ka1 + i * 512); } \
            if (i < 2) { EXP1(12 + 2 * i); EXP1(13 + 2 * i); asm volatile("" : "+v"(sc)); } \
            if (i < 3) SUM1(5 + i); \
            if (i == 3) { l_run += ps0 + ps1; asm volatile("" : "+v"(sn), "+v"(l_run)); \
                          tmax = max3f(sn[0], sn[1], sn[2]); t2 = max3f(sn[3], sn[4], sn[5]); asm volatile("" : "+v"(tmax), "+v"(t2)); } \
            if (i == 4) { tmax = max3f(tmax, sn[6], sn[7]); t2 = max3f(t2, sn[8], sn[9]); tmax = max3f(tmax, sn[10], sn[11]); asm volatile("" : "+v"(tmax), "+v"(t2)); } \
            if (i == 5) { t2 = max3f(t2, sn[12], sn[13]); tmax = max3f(tmax, sn[14], sn[15]); tmax = max3f(tmax, t2, t2); asm volatile("" : "+v"(tmax)); } \
            if (i == 6) { tmax = swap_max3(tmax); asm volatile("" : "+v"(tmax)); } \
            if (i == YS0 && (!TAIL || (t) < 11)) DMA_K0((t) + 5); \
            if (i == YS1 && (!TAIL || (t) < 11)) DMA_K1((t) + 5); \
            __builtin_amdgcn_sched_barrier(0); \
        } } while (0)
#pragma unroll 1
    for (int t = 0; t < 10; t += 2) { STEP(t, sA, sB, false); STEP(t + 1, sB, sA, false); }
#pragma unroll 1
    for (int t = 10; t < NT; t += 2) { STEP(t, sA, sB, true); STEP(t + 1, sB, sA, true); }
#undef STEP
#undef EXP1
#undef SUM1
    {
        const char* wp = (const char*)wo + (size_t)(1024 * ((bid >> 1) & 3) + 32 * (bid >> 3)) * DM * 2 + wid * 1024 + lane * 16;
#pragma unroll
        for (int i = 0; i < 4; ++i) glds16(wp + i * 8192, lds0 + LDS_Q + i * 8192 + wid * 1024);
        if (wid < 4) glds16(wp + 32768, lds0 + LDS_Q + wid * 1024);
    }
    l_run = swap_add(l_run);
    asm volatile("s_waitcnt lgkmcnt(0)\n\ts_barrier" ::: "memory");
    {
        f32x16 keep0, keep1, send0, send1;
        if (kh == 0) { keep0 = o[0]; keep1 = o[1]; send0 = o[2]; send1 = o[3]; }
        else         { keep0 = o[2]; keep1 = o[3]; send0 = o[0]; send1 = o[1]; }
        const unsigned xb = lds0 + wid * 9216;
#pragma unroll
        for (int r4 = 0; r4 < 4; ++r4) {
            f32x4 v0 = {send0[4 * r4], send0[4 * r4 + 1], send0[4 * r4 + 2], send0[4 * r4 + 3]};
            f32x4 v1 = {send1[4 * r4], send1[4 * r4 + 1], send1[4 * r4 + 2], send1[4 * r4 + 3]};
            *(LAS f32x4*)(uintptr_t)(xb + r4 * 1024 + lane * 16) = v0;
            *(LAS f32x4*)(uintptr_t)(xb + (4 + r4) * 1024 + lane * 16) = v1;
        }
        *(LAS float*)(uintptr_t)(xb + 8192 + lane * 4) = m_run;
        *(LAS float*)(uintptr_t)(xb + 8192 + 256 + lane * 4) = l_run;
        asm volatile("s_waitcnt lgkmcnt(0)\n\ts_barrier" ::: "memory");
        const unsigned pb = lds0 + (wid ^ 4) * 9216;
        const float m2 = *(const LAS float*)(uintptr_t)(pb + 8192 + lane * 4);
        const float l2 = *(const LAS float*)(uintptr_t)(pb + 8192 + 256 + lane * 4);
        const float mm = __builtin_fmaxf(m_run, m2);
        const float a1 = __builtin_amdgcn_exp2f((m_run - mm) * C2), a2 = __builtin_amdgcn_exp2f((m2 - mm) * C2);
        const float inv = 1.0f / (l_run * a1 + l2 * a2);
        const float w1 = a1 * inv, w2 = a2 * inv;
        const int row = b * SQ + 32 * (qb & 1) + r32;
        char* ob = (char*)Og + ((size_t)((row >> 4) * 128 + head * 4 + 2 * kh) * 64 + hi * 16 + (row & 15)) * 16;
#define OSTORE(KEEP, i) do { u32x2 w_[4]; \
            _Pragma("unroll") for (int r4 = 0; r4 < 4; ++r4) { \
                const f32x4 v = *(const LAS f32x4*)(uintptr_t)(pb + (4 * (i) + r4) * 1024 + lane * 16); \
                const float e0 = KEEP[4 * r4] * w1 + v[0] * w2, e1 = KEEP[4 * r4 + 1] * w1 + v[1] * w2; \
                const float e2 = KEEP[4 * r4 + 2] * w1 + v[2] * w2, e3 = KEEP[4 * r4 + 3] * w1 + v[3] * w2; \
                w_[r4] = u32x2{cvtpk(e0, e1), cvtpk(e2, e3)}; } \
            _Pragma("unroll") for (int g = 0; g < 2; ++g) { \
                u32x4 o4 = {w_[g].x, w_[g].y, w_[g + 2].x, w_[g + 2].y}; \
                asm volatile("global_store_dwordx4 %0, %1, off sc1" :: "v"(ob + (i) * 1024 + g * 512), "v"(o4) : "memory"); } } while (0)
        OSTORE(keep0, 0); OSTORE(keep1, 1);
#undef OSTORE
    }
}

constexpr int LDS2_BYTES = 1024 * 160;
#define MFMA16(a, b, c) __builtin_amdgcn_mfma_f32_16x16x32_bf16(a, b, c, 0, 0, 0)
__global__ __launch_bounds__(512, 2) void oproj_kernel(const u16* __restrict__ Og, const u16* __restrict__ wo,
                                                       float* __restrict__ part, u16* __restrict__ out) {
    extern __shared__ __attribute__((aligned(16))) char lds[];
    const unsigned lds0 = (unsigned)(uintptr_t)lds;
    const int tid = threadIdx.x, lane = tid & 63;
    const int wid = __builtin_amdgcn_readfirstlane(tid >> 6);
    const int rb = wid & 3, kp = wid >> 2;
    const int bid = blockIdx.x;
    const int sk = (bid >> 1) & 3, mt = 4 * (bid & 1) + ((bid >> 3) & 3), nt = bid >> 5;
    const char* Bg[5];
#pragma unroll
    for (int i = 0; i < 5; ++i) {
        const int L = i * 1024 + lane * 16;
        const int row = L / 160;
        int c = (L % 160) >> 4;
        c = c < 9 ? c : 0;
        Bg[i] = (const char*)wo + ((size_t)(1024 * sk + 32 * wid + row) * DM + 72 * nt) * 2 + c * 16;
    }
    const char* Ag = (const char*)Og + ((size_t)((8 * mt + 2 * rb) * 128 + 32 * sk + kp) * 64 + lane) * 16;
    u32x4 an[4][8];
#define K2_B(c) do { _Pragma("unroll") for (int i = 0; i < 5; ++i) \
        glds16(Bg[i] + (size_t)(c) * 256 * DM * 2, lds0 + (40 * (c) + 5 * wid + i) * 1024); } while (0)
#define K2_A(c) do { _Pragma("unroll") for (int j = 0; j < 4; j += 2) { const char* a_ = Ag + (8 * (c) + 2 * j) * 1024; \
        asm volatile("global_load_dwordx4 %0, %1, off" : "=v"(an[c][2 * j]) : "v"(a_) : "memory"); \
        asm volatile("global_load_dwordx4 %0, %1, off" : "=v"(an[c][2 * j + 1]) : "v"(a_ + 131072) : "memory"); \
        asm volatile("global_load_dwordx4 %0, %1, off offset:2048" : "=v"(an[c][2 * j + 2]) : "v"(a_) : "memory"); \
        asm volatile("global_load_dwordx4 %0, %1, off offset:2048" : "=v"(an[c][2 * j + 3]) : "v"(a_ + 131072) : "memory"); } } while (0)
    K2_B(0);
    asm volatile("s_barrier" ::: "memory");
    K2_A(0); K2_B(1);
    const int gi = lane >> 4, vq = (lane & 15) >> 2, vp = lane & 3;
    const unsigned bb = lds0 + (32 * kp + 4 * gi + vq) * 160 + 8 * vp;
    f32x4 acc[2][5];
#pragma unroll
    for (int ct = 0; ct < 5; ++ct) { acc[0][ct] = f32x4{0.f, 0.f, 0.f, 0.f}; acc[1][ct] = f32x4{0.f, 0.f, 0.f, 0.f}; }
    s16x4 blo[2][5], bhi[2][5];
    asm volatile("s_waitcnt vmcnt(13)\n\ts_barrier" ::: "memory");
#pragma unroll
    for (int ct = 0; ct < 5; ++ct) { blo[0][ct] = vtr(bb + 32 * ct); bhi[0][ct] = vtr(bb + 32 * ct + 2560); }
#pragma unroll
    for (int s = 0; s < 16; ++s) {
        const int c = s >> 2, j = s & 3;
        const int young = (6 - 2 * j) + (c < 3 ? 5 + (j < 2 ? 4 * j : 8) + (j == 3 && c < 2 ? 5 : 0) : 0);
        asm volatile("s_waitcnt vmcnt(%2)" : "+v"(an[c][2 * j]), "+v"(an[c][2 * j + 1]) : "n"(young) : "memory");

        if (j == 3 && c < 3) asm volatile("s_waitcnt vmcnt(%0)\n\ts_barrier" :: "n"(c < 2 ? 13 : 8) : "memory");
        __builtin_amdgcn_sched_barrier(0);
#pragma unroll
        for (int ct = 0; ct < 5; ++ct) {
            if (s < 15) {
                const unsigned ad = bb + (s + 1) * 10240 + 32 * ct;
                blo[(s + 1) & 1][ct] = vtr(ad); bhi[(s + 1) & 1][ct] = vtr(ad + 2560);
            }
            const s16x4 lo = blo[s & 1][ct], h4 = bhi[s & 1][ct];
            const bf16x8 bf = {lo[0], lo[1], lo[2], lo[3], h4[0], h4[1], h4[2], h4[3]};
            acc[0][ct] = MFMA16(bf, __builtin_bit_cast(bf16x8, an[c][2 * j]), acc[0][ct]);
            acc[1][ct] = MFMA16(bf, __builtin_bit_cast(bf16x8, an[c][2 * j + 1]), acc[1][ct]);
            if (j < 2 && c < 3 && ct < 4) {
                const char* a_ = Ag + (8 * (c + 1) + 4 * j + (ct >> 1) * 2) * 1024 + (ct & 1) * 131072;
                asm volatile("global_load_dwordx4 %0, %1, off" : "=v"(an[(c + 1) & 3][4 * j + ct]) : "v"(a_) : "memory");
            }
            if (j == 2 && c < 2) glds16(Bg[ct] + (size_t)(c + 2) * 256 * DM * 2, lds0 + (40 * (c + 2) + 5 * wid + ct) * 1024);
            __builtin_amdgcn_sched_barrier(0);
        }
    }
#undef K2_A
#undef K2_B
    asm volatile("s_waitcnt lgkmcnt(0)\n\ts_barrier" ::: "memory");
#pragma unroll
    for (int ct = 0; ct < 5; ++ct)
        *(LAS f32x4*)(uintptr_t)(lds0 + (wid * 5 + ct) * 1024 + lane * 16) = kp ? acc[0][ct] : acc[1][ct];
    asm volatile("s_waitcnt lgkmcnt(0)\n\ts_barrier" ::: "memory");
    float* pp = part + ((size_t)(sk * 64 + 8 * mt + 2 * rb + kp) * 8 + nt) * 1152 + lane * 4;
#pragma unroll
    for (int ct = 0; ct < 5; ++ct) {
        const f32x4 v = *(const LAS f32x4*)(uintptr_t)(lds0 + ((wid ^ 4) * 5 + ct) * 1024 + lane * 16);
        const f32x4 r_ = (kp ? acc[1][ct] : acc[0][ct]) + v;
        if (ct < 4 || lane < 32)
            asm volatile("global_store_dwordx4 %0, %1, off sc1" :: "v"(pp + ct * 256), "v"(r_) : "memory");
    }
    asm volatile("s_waitcnt vmcnt(0)\n\ts_barrier" ::: "memory");
    if (wid == 0) {
        gu32* cw = (gu32*)((const char*)Og + CNT_OFF) + (mt * 8 + nt) * 32;
        unsigned todo = 1u << sk;
        unsigned old = 0;
        if (lane == 0) old = __hip_atomic_fetch_add(cw, 1u, RLX_AGENT);
        old = __builtin_amdgcn_readfirstlane(old);
        if ((old & 255u) == 3u) todo |= (old >> 8) & 15u;
        else {
            const unsigned long long t0 = __builtin_amdgcn_s_memrealtime();
            for (;;) {
                const unsigned v = __builtin_amdgcn_readfirstlane(__hip_atomic_load(cw, RLX_AGENT));
                if ((v & 255u) == 4u) break;
                if (__builtin_amdgcn_s_memrealtime() - t0 > 5000ull) {
                    unsigned o2 = 0;
                    if (lane == 0) o2 = __hip_atomic_fetch_or(cw, 256u << sk, RLX_AGENT);
                    if ((__builtin_amdgcn_readfirstlane(o2) & 255u) != 4u) todo = 0;
                    break;
                }
                __builtin_amdgcn_s_sleep(1);
            }
        }
        if (lane == 0) *(LAS unsigned*)(uintptr_t)lds0 = todo;
    }
    asm volatile("s_waitcnt lgkmcnt(0)\n\ts_barrier" ::: "memory");
    unsigned todo = __builtin_amdgcn_readfirstlane(*(const LAS unsigned*)(uintptr_t)lds0);
    while (todo) {
        const int q = __builtin_ctz(todo);
        todo &= todo - 1;
        if (tid < 288) {
            const float* p = part + ((size_t)(8 * mt + 2 * q) * 8 + nt) * 1152 + tid * 4;
            f32x4 v[2][4];
#pragma unroll
            for (int r = 0; r < 2; ++r)
#pragma unroll
                for (int s = 0; s < 4; ++s)
                    asm volatile("global_load_dwordx4 %0, %1, off sc1" : "=v"(v[r][s]) : "v"(p + (size_t)s * 64 * 8 * 1152 + r * 8 * 1152) : "memory");
            asm volatile("s_waitcnt vmcnt(0)" : "+v"(v[0][0]), "+v"(v[0][1]), "+v"(v[0][2]), "+v"(v[0][3]),
                                                "+v"(v[1][0]), "+v"(v[1][1]), "+v"(v[1][2]), "+v"(v[1][3]) :: "memory");
#pragma unroll
            for (int r = 0; r < 2; ++r) {
                const f32x4 t = ((v[r][0] + v[r][1]) + v[r][2]) + v[r][3];
                u32x2 w = {cvtpk(t[0], t[1]), cvtpk(t[2], t[3])};
                *(u32x2*)(out + (size_t)(16 * (8 * mt + 2 * q + r) + (tid & 15)) * DM + 72 * nt + 4 * (tid >> 4)) = w;
            }
        }
    }
}

extern "C" void kernel_launch(void* const* d_in, const int* in_sizes, int n_in, void* d_out, int out_size,
                              void* d_ws, size_t ws_size, hipStream_t stream) {
    const u16* x = (const u16*)d_in[0];
    const u16* kc = (const u16*)d_in[1];
    const u16* vc = (const u16*)d_in[2];
    const u16* wq = (const u16*)d_in[3];
    const u16* wo = (const u16*)d_in[4];
    u16* Og = (u16*)d_ws;
    float* part = (float*)((char*)d_ws + (size_t)NB * SQ * NQ * 2);
    (void)hipFuncSetAttribute((const void*)attn_kernel, hipFuncAttributeMaxDynamicSharedMemorySize, LDS1_BYTES);
    (void)hipFuncSetAttribute((const void*)oproj_kernel, hipFuncAttributeMaxDynamicSharedMemorySize, LDS2_BYTES);
    attn_kernel<<<NB * HKV, 512, LDS1_BYTES, stream>>>(x, kc, vc, wq, Og, wo);
    oproj_kernel<<<256, 512, LDS2_BYTES, stream>>>(Og, wo, part, (u16*)d_out);
}
```

```cpp
#include <hip/hip_runtime.h>
#include <hip/hip_bf16.h>
#include <stdint.h>

typedef short bf16x8 __attribute__((ext_vector_type(8)));
typedef short s16x4 __attribute__((ext_vector_type(4)));
typedef float f32x16 __attribute__((ext_vector_type(16)));
typedef float f32x4 __attribute__((ext_vector_type(4)));
typedef unsigned u32x4 __attribute__((ext_vector_type(4)));
typedef unsigned u32x2 __attribute__((ext_vector_type(2)));
typedef unsigned short u16;
#define LAS __attribute__((address_space(3)))
typedef __attribute__((address_space(1))) unsigned gu32;
#define RLX_AGENT __ATOMIC_RELAXED, __HIP_MEMORY_SCOPE_AGENT

constexpr int NB = 16, SQ = 64, DM = 576, HQ = 32, HKV = 16, SKV = 1024, DH = 128;
constexpr int NQ = HQ * DH;
constexpr int NT = SKV / 64;
constexpr int SLOT = 32768;
constexpr int LDS_Q = 4 * SLOT;
constexpr int LDS1_BYTES = 5 * SLOT;
constexpr size_t CNT_OFF = (size_t)20 << 20;
constexpr int XS0 = 5, XS1 = 7, YS0 = 5, YS1 = 7;
constexpr int PACE = 108;
constexpr float RTHR = 8.0f;
constexpr float C2 = 0.088388347648318447f * 1.4426950408889634f;

#define MFMA32(a, b, c) __builtin_amdgcn_mfma_f32_32x32x16_bf16(a, b, c, 0, 0, 0)

typedef __bf16 bf16x2_t __attribute__((ext_vector_type(2)));
typedef float f32x2 __attribute__((ext_vector_type(2)));
__device__ __forceinline__ unsigned cvtpk(float lo, float hi) {
    f32x2 v = {lo, hi};
    return __builtin_bit_cast(unsigned, __builtin_convertvector(v, bf16x2_t));
}
__device__ __forceinline__ void glds16(const void* g, unsigned lds_base) {
    unsigned sv;
    asm volatile("s_mov_b32 %0, m0\n\ts_mov_b32 m0, %2\n\ts_nop 0\n\tglobal_load_lds_dwordx4 %1, off\n\ts_mov_b32 m0, %0"
                 : "=&s"(sv) : "v"(g), "s"(lds_base) : "memory");
}
__device__ __forceinline__ s16x4 vtr(unsigned a) {
    return __builtin_amdgcn_ds_read_tr16_b64_v4i16((LAS s16x4*)(uintptr_t)a);
}
__device__ __forceinline__ bf16x8 ldsr128(unsigned a) { return *(const LAS bf16x8*)(uintptr_t)a; }
__device__ __forceinline__ float swap_max(float v) {
    auto rr = __builtin_amdgcn_permlane32_swap(__float_as_uint(v), __float_as_uint(v), false, false);
    return __builtin_fmaxf(__uint_as_float(rr[0]), __uint_as_float(rr[1]));
}
__device__ __forceinline__ float max3f(float a, float b, float c) { float r; asm("v_max3_f32 %0, %1, %2, %3" : "=v"(r) : "v"(a), "v"(b), "v"(c)); return r; }
__device__ __forceinline__ float fmul1(float a, float b) { float r; asm("v_mul_f32_e32 %0, %1, %2" : "=v"(r) : "s"(b), "v"(a)); return r; }
__device__ __forceinline__ float swap_max3(float v) {
    auto rr = __builtin_amdgcn_permlane32_swap(__float_as_uint(v), __float_as_uint(v), false, false);
    return max3f(__uint_as_float(rr[0]), __uint_as_float(rr[1]), __uint_as_float(rr[1]));
}
__device__ __forceinline__ float swap_add(float v) {
    auto rr = __builtin_amdgcn_permlane32_swap(__float_as_uint(v), __float_as_uint(v), false, false);
    return __uint_as_float(rr[0]) + __uint_as_float(rr[1]);
}

__global__ __launch_bounds__(512, 2) void attn_kernel(const u16* __restrict__ x, const u16* __restrict__ Kc,
                                                      const u16* __restrict__ Vc, const u16* __restrict__ wq,
                                                      u16* __restrict__ Og, const u16* __restrict__ wo) {
    extern __shared__ __attribute__((aligned(16))) char lds[];
    const unsigned lds0 = (unsigned)(uintptr_t)lds;
    const int tid = threadIdx.x, lane = tid & 63, r32 = lane & 31, hi = lane >> 5;
    const int wid = __builtin_amdgcn_readfirstlane(tid >> 6);
    const int qb = wid & 3, kh = wid >> 2;
    const int bid = blockIdx.x, b = 8 * (bid & 1) + ((bid >> 3) & 7), kvh = 4 * ((bid >> 1) & 3) + (bid >> 6);
    const int head = 2 * kvh + (qb >> 1);
    if (tid == 0 && bid < 64) ((unsigned*)((char*)Og + CNT_OFF))[bid * 32] = 0u;
    const int gi = lane >> 4, vq = (lane & 15) >> 2, vp = lane & 3;

    const unsigned vsrc = (16 * (wid >> 1) + ((lane >> 2) & 7)) * 256 + (wid & 1) * 128 + hi * 64;
    const unsigned voffV = vsrc + vp * 16;
    const unsigned voffK0 = vsrc + ((vp ^ (wid & 2)) << 4), voffK1 = vsrc + ((vp ^ ((wid & 2) | 1)) << 4);
    const char* Kg = (const char*)Kc + (size_t)(b * HKV + kvh) * SKV * DH * 2;
    const char* Vg = (const char*)Vc + (size_t)(b * HKV + kvh) * SKV * DH * 2;
    const unsigned dwo = lds0 + (wid >> 1) * 4096 + (wid & 1) * 1024;
#define KVDMA(voff, gp, dst, OFF) asm volatile("s_mov_b32 m0, %2\n\ts_nop 0\n\tglobal_load_lds_dwordx4 %0, %1 offset:" #OFF " sc1 nt" \
            :: "v"(voff), "s"(gp), "s"(dst) : "memory")
#define VSO(tn) ((int)((0x2105u >> (4 * ((tn) & 3))) & 15) * 16384)
#define KSO(tn) ((int)((0x7643u >> (4 * ((tn) & 3))) & 15) * 16384)
#define DMA_V0(tn) KVDMA(voffV, Vg + (tn) * 16384, dwo + VSO(tn), 0)
#define DMA_V1(tn) KVDMA(voffV, Vg + (tn) * 16384, dwo + VSO(tn), 2048)
#define DMA_K0(tn) KVDMA(voffK0, Kg + (tn) * 16384, dwo + KSO(tn), 0)
#define DMA_K1(tn) KVDMA(voffK1, Kg + (tn) * 16384, dwo + KSO(tn), 2048)
#define DMA_V(tn) do { DMA_V0(tn); DMA_V1(tn); } while (0)
#define DMA_K(tn) do { DMA_K0(tn); DMA_K1(tn); } while (0)
    bf16x8 qf[8];
    {
        int kb = (b + 2 * (kvh & 3)) % 9;
        const unsigned xo = (8 * wid + (lane >> 3)) * DM * 2 + (((lane & 7) ^ ((4 * wid + (lane >> 4)) & 7)) << 4);
        const unsigned wo0 = (8 * wid + hi) * NQ * 2 + ((r32 ^ (hi << 2)) << 4);
        const unsigned wo1 = (8 * wid + 2 + hi) * NQ * 2 + ((r32 ^ ((2 + hi) << 2)) << 4);
        const unsigned wo2 = wo0 + 4 * NQ * 2, wo3 = wo1 + 4 * NQ * 2;
        const char* Xs = (const char*)x + (size_t)b * SQ * DM * 2;
        const char* Ws = (const char*)wq + kvh * 512;
#define QDMA(voff, gp, dst) asm volatile("s_mov_b32 m0, %2\n\ts_nop 0\n\tglobal_load_lds_dwordx4 %0, %1" :: "v"(voff), "s"(gp), "s"(dst) : "memory")
#define QS_PIECE(slot, p) do { const unsigned s_ = lds0 + (slot) * 40960 + wid * 4096; const char* w_ = Ws + (size_t)kb * 64 * NQ * 2; \
            if ((p) == 0) QDMA(wo0, w_, s_); \
            if ((p) == 1) QDMA(wo1, w_, s_ + 1024); \
            if ((p) == 2) QDMA(wo2, w_, s_ + 2048); \
            if ((p) == 3) QDMA(wo3, w_, s_ + 3072); \
            if ((p) == 4) { QDMA(xo, Xs + kb * 128, lds0 + (slot) * 40960 + 32768 + wid * 1024); kb = kb == 8 ? 0 : kb + 1; } } while (0)
#define QS_ISSUE(slot) do { QS_PIECE(slot, 0); QS_PIECE(slot, 1); QS_PIECE(slot, 2); QS_PIECE(slot, 3); QS_PIECE(slot, 4); } while (0)
#pragma unroll 1
        for (int j = 0; j < 2; ++j) { QS_ISSUE(j); asm volatile("s_barrier" ::: "memory"); }
        const int pp = ((vp & 1) << 1) | (vp >> 1);
        const unsigned abase = lds0 + (8 * hi + vq) * 512 + (((4 * wid + 2 * (gi & 1) + (pp >> 1)) ^ (vq << 2)) << 4) + 8 * (pp & 1);
        const unsigned xbase = lds0 + 32768 + r32 * 128 + ((hi ^ ((r32 >> 1) & 7)) << 4);
        f32x16 acc0 = {}, acc1 = {};
        s16x4 lo[2][4], h4[2][4]; bf16x8 x0[2][4], x1[2][4];
        asm volatile("s_waitcnt vmcnt(5)\n\ts_barrier" ::: "memory");
        QS_ISSUE(2);
#pragma unroll
        for (int i = 0; i < 4; ++i) {
            lo[0][i] = vtr(abase + i * 8192); h4[0][i] = vtr(abase + i * 8192 + 2048);
            x0[0][i] = ldsr128(xbase ^ (i << 5)); x1[0][i] = ldsr128((xbase ^ (i << 5)) + 4096);
        }
#pragma unroll
        for (int st = 0; st < 9; ++st) {
            if (st == 0) asm volatile("s_waitcnt vmcnt(5)" ::: "memory");
            else if (st < 6) asm volatile("s_waitcnt vmcnt(10)" ::: "memory");
            else if (st == 6) asm volatile("s_waitcnt vmcnt(9)" ::: "memory");
            else if (st == 7) asm volatile("s_waitcnt vmcnt(6)" ::: "memory");
            asm volatile("s_waitcnt lgkmcnt(0)\n\ts_barrier" ::: "memory");
            const unsigned so = ((st + 1) & 3) * 40960;
            const int cu = st & 1, nx = cu ^ 1;
            unsigned aa = abase + so;
            asm volatile("" : "+v"(aa));
            __builtin_amdgcn_sched_barrier(0);
            if (st == 0) QS_ISSUE(3);
#pragma unroll
            for (int i = 0; i < 4; ++i) {
                const bf16x8 a0 = {lo[cu][i][0], lo[cu][i][1], lo[cu][i][2], lo[cu][i][3], h4[cu][i][0], h4[cu][i][1], h4[cu][i][2], h4[cu][i][3]};
                acc0 = MFMA32(a0, x0[cu][i], acc0);
                if (st < 8) { lo[nx][i] = vtr(aa + i * 8192); h4[nx][i] = vtr(aa + i * 8192 + 2048); }
                if (st < 5) QS_PIECE(st & 3, i);
                __builtin_amdgcn_sched_barrier(0);
                acc1 = MFMA32(a0, x1[cu][i], acc1);
                if (st < 8) { unsigned xa = (xbase ^ (i << 5)) + so; asm volatile("" : "+v"(xa)); x0[nx][i] = ldsr128(xa); x1[nx][i] = ldsr128(xa + 4096); }
                if (st < 5 && i == 3) QS_PIECE(st & 3, 4);
                if (st == 5 && i == 3) { DMA_K(0); DMA_K(1); }
                if (st == 6 && i == 3) DMA_V(0);
                if (st == 7 && i == 3) DMA_K(2);
                if (st == 8 && i == 3) DMA_K(3);
                __builtin_amdgcn_sched_barrier(0);
            }
        }
#undef QS_ISSUE
#undef QS_PIECE
#define QW(ACC, qblk) do { _Pragma("unroll") for (int s = 0; s < 2; ++s) { \
            u32x4 w_ = {cvtpk(ACC[8 * s], ACC[8 * s + 1]), cvtpk(ACC[8 * s + 2], ACC[8 * s + 3]), \
                        cvtpk(ACC[8 * s + 4], ACC[8 * s + 5]), cvtpk(ACC[8 * s + 6], ACC[8 * s + 7])}; \
            *(LAS u32x4*)(uintptr_t)(lds0 + LDS_Q + (wid >> 2) * 16384 + (qblk) * 8192 + (2 * (wid & 3) + s) * 1024 + lane * 16) = w_; } } while (0)
        QW(acc0, 0); QW(acc1, 1);
#undef QW
        DMA_V(1);
    }
    asm volatile("s_waitcnt vmcnt(8) lgkmcnt(0)\n\ts_barrier" ::: "memory");
#pragma unroll
    for (int f = 0; f < 8; ++f) qf[f] = ldsr128(lds0 + LDS_Q + qb * 8192 + f * 1024 + lane * 16);

    const unsigned kb0 = lds0 + (4 * kh + (r32 >> 3)) * 2048 + (r32 & 7) * 64 + ((hi ^ (r32 >> 3)) << 4);
    const unsigned kb1 = kb0 ^ 32;
    const unsigned vb = lds0 + kh * 8192 + (4 * hi + vq) * 64 + (gi & 1) * 32 + vp * 8;

    float m_run, l_run = 0.f, tmax = 0.f;
    unsigned long long tprev = 0;
    f32x16 o[4] = {};
    f32x16 sA, sB, negm;

    bf16x8 kf[8];
    {
        bf16x8 k0[8];
        unsigned ka0 = kb0 + KSO(0), ka1 = kb1 + KSO(0);
        asm volatile("" : "+v"(ka0), "+v"(ka1));
#pragma unroll
        for (int d0 = 0; d0 < 8; ++d0) k0[d0] = ldsr128(((d0 & 1) ? ka1 : ka0) + (d0 >> 1) * 512);
#pragma unroll
        for (int d0 = 0; d0 < 8; ++d0) kf[d0] = ldsr128(((d0 & 1) ? ka1 : ka0) + 16384 + (d0 >> 1) * 512);
        __builtin_amdgcn_sched_barrier(0);
        asm volatile("s_waitcnt lgkmcnt(0)\n\ts_barrier" ::: "memory");
        sA = f32x16{};
#pragma unroll
        for (int d0 = 0; d0 < 8; ++d0) sA = MFMA32(k0[d0], qf[d0], sA);
        __builtin_amdgcn_sched_barrier(0);
        DMA_V(2); DMA_K(4);
        m_run = sA[0];
#pragma unroll
        for (int r = 1; r < 16; ++r) m_run = __builtin_fmaxf(m_run, sA[r]);
        m_run = swap_max(m_run);
#pragma unroll
        for (int r = 0; r < 16; ++r) { sA[r] -= m_run; negm[r] = -m_run; }
    }
#define EXP1(r) do { sc[r] = __builtin_amdgcn_exp2f(fmul1(sc[r], C2)); } while (0)
#define SUM1(k) do { ps0 += sc[2 * (k)]; ps1 += sc[2 * (k) + 1]; pw[k] = cvtpk(sc[2 * (k)], sc[2 * (k) + 1]); asm volatile("" : "+v"(pw[k]), "+v"(ps0), "+v"(ps1)); } while (0)
#define STEP(t, SC_, SN_, TAIL) do { \
        f32x16& sc = SC_; f32x16& sn = SN_; \
        s16x4 vlo[8], vhi[8]; \
        if (!TAIL || (t) < 12) asm volatile("s_waitcnt vmcnt(8)" ::: "memory"); \
        else if ((t) == 12) asm volatile("s_waitcnt vmcnt(6)" ::: "memory"); \
        else if ((t) == 13) asm volatile("s_waitcnt vmcnt(4)" ::: "memory"); \
        else if ((t) == 14) asm volatile("s_waitcnt vmcnt(2)" ::: "memory"); \
        else asm volatile("s_waitcnt vmcnt(0)" ::: "memory"); \
        while (__builtin_amdgcn_s_memrealtime() - tprev < (unsigned long long)PACE) __builtin_amdgcn_s_sleep(1); \
        asm volatile("s_waitcnt lgkmcnt(0)\n\ts_barrier" ::: "memory"); \
        tprev = __builtin_amdgcn_s_memrealtime(); \
        if (__builtin_expect(__any(tmax > RTHR / C2), 0)) { \
            const float dl = __builtin_fmaxf(tmax, 0.f); \
            const float alpha = __builtin_amdgcn_exp2f(-dl * C2); \
            m_run += dl; l_run *= alpha; \
            _Pragma("unroll") for (int r = 0; r < 16; ++r) { sc[r] -= dl; negm[r] = -m_run; } \
            _Pragma("unroll") for (int dt = 0; dt < 4; ++dt) \
                _Pragma("unroll") for (int r = 0; r < 16; ++r) o[dt][r] *= alpha; \
        } \
        const unsigned vwr = dwo + vso, kwr = dwo + kso;                         \
        vso = VSO(t); kso = KSO((t) + 2); \
        const unsigned va = vb + vso; \
        const unsigned ka0 = kb0 + kso, ka1 = kb1 + kso;                         \
        unsigned pw[8]; \
        float ps0 = 0.f, ps1 = 0.f, t2; \
        __builtin_amdgcn_sched_barrier(0); \
          \
        _Pragma("unroll") for (int i = 0; i < 8; ++i) { \
            sn = MFMA32(kf[i], qf[i], i == 0 ? negm : sn);                   \
            const int ss = i >> 2, dt = i & 3; \
            vlo[2 * dt + ss] = vtr(va + ss * 4096 + dt * 512); \
            vhi[2 * dt + ss] = vtr(va + ss * 4096 + 2048 + dt * 512); \
            if (i < 4) { EXP1(2 * i); EXP1(2 * i + 1); } else EXP1(4 + i); \
            if (i >= 1 && i <= 4) SUM1(i - 1); \
            if (i == 6) SUM1(4); \
            asm volatile("" : "+v"(sn), "+v"(sc)); \
            if (i == XS0 && (!TAIL || (t) < 13)) KVDMA(voffV, Vg + ((t) + 3) * 16384, vwr, 0); \
            if (i == XS1 && (!TAIL || (t) < 13)) KVDMA(voffV, Vg + ((t) + 3) * 16384, vwr, 2048); \
            __builtin_amdgcn_sched_barrier(0); \
        } \
          \
        _Pragma("unroll") for (int i = 0; i < 8; ++i) { \
            const int ss = i >> 2, dt = i & 3; \
            const s16x4 lo = vlo[2 * dt + ss], hi4 = vhi[2 * dt + ss]; \
            const bf16x8 vf = {lo[0], lo[1], lo[2], lo[3], hi4[0], hi4[1], hi4[2], hi4[3]}; \
            const bf16x8 pf = __builtin_bit_cast(bf16x8, u32x4{pw[4 * ss], pw[4 * ss + 1], pw[4 * ss + 2], pw[4 * ss + 3]}); \
            o[dt] = MFMA32(vf, pf, o[dt]); \
            if (i < 4) { kf[2 * i] = ldsr128(ka0 + i * 512); kf[2 * i + 1] = ldsr128(ka1 + i * 512); } \
            if (i < 2) { EXP1(12 + 2 * i); EXP1(13 + 2 * i); asm volatile("" : "+v"(sc)); } \
            if (i < 3) SUM1(5 + i); \
            if (i == 3) { l_run += ps0 + ps1; asm volatile("" : "+v"(sn), "+v"(l_run)); \
                          tmax = max3f(sn[0], sn[1], sn[2]); t2 = max3f(sn[3], sn[4], sn[5]); asm volatile("" : "+v"(tmax), "+v"(t2)); } \
            if (i == 4) { tmax = max3f(tmax, sn[6], sn[7]); t2 = max3f(t2, sn[8], sn[9]); tmax = max3f(tmax, sn[10], sn[11]); asm volatile("" : "+v"(tmax), "+v"(t2)); } \
            if (i == 5) { t2 = max3f(t2, sn[12], sn[13]); tmax = max3f(tmax, sn[14], sn[15]); tmax = max3f(tmax, t2, t2); asm volatile("" : "+v"(tmax)); } \
            if (i == 6) { tmax = swap_max3(tmax); asm volatile("" : "+v"(tmax)); } \
            if (i == YS0 && (!TAIL || (t) < 11)) KVDMA(voffK0, Kg + ((t) + 5) * 16384, kwr, 0); \
            if (i == YS1 && (!TAIL || (t) < 11)) KVDMA(voffK1, Kg + ((t) + 5) * 16384, kwr, 2048); \
            __builtin_amdgcn_sched_barrier(0); \
        } } while (0)
    unsigned vso = VSO(3), kso = KSO(1);
#pragma unroll 1
    for (int t = 0; t < 10; t += 2) { STEP(t, sA, sB, false); STEP(t + 1, sB, sA, false); }
#pragma unroll 1
    for (int t = 10; t < NT; t += 2) { STEP(t, sA, sB, true); STEP(t + 1, sB, sA, true); }
#undef STEP
#undef EXP1
#undef SUM1
    {
        const char* wp = (const char*)wo + (size_t)(1024 * ((bid >> 1) & 3) + 32 * (bid >> 3)) * DM * 2 + wid * 1024 + lane * 16;
#pragma unroll
        for (int i = 0; i < 4; ++i) glds16(wp + i * 8192, lds0 + LDS_Q + i * 8192 + wid * 1024);
        if (wid < 4) glds16(wp + 32768, lds0 + LDS_Q + wid * 1024);
    }
    l_run = swap_add(l_run);
    asm volatile("s_waitcnt lgkmcnt(0)\n\ts_barrier" ::: "memory");
    {
        f32x16 keep0, keep1, send0, send1;
        if (kh == 0) { keep0 = o[0]; keep1 = o[1]; send0 = o[2]; send1 = o[3]; }
        else         { keep0 = o[2]; keep1 = o[3]; send0 = o[0]; send1 = o[1]; }
        const unsigned xb = lds0 + wid * 9216;
#pragma unroll
        for (int r4 = 0; r4 < 4; ++r4) {
            f32x4 v0 = {send0[4 * r4], send0[4 * r4 + 1], send0[4 * r4 + 2], send0[4 * r4 + 3]};
            f32x4 v1 = {send1[4 * r4], send1[4 * r4 + 1], send1[4 * r4 + 2], send1[4 * r4 + 3]};
            *(LAS f32x4*)(uintptr_t)(xb + r4 * 1024 + lane * 16) = v0;
            *(LAS f32x4*)(uintptr_t)(xb + (4 + r4) * 1024 + lane * 16) = v1;
        }
        *(LAS float*)(uintptr_t)(xb + 8192 + lane * 4) = m_run;
        *(LAS float*)(uintptr_t)(xb + 8192 + 256 + lane * 4) = l_run;
        asm volatile("s_waitcnt lgkmcnt(0)\n\ts_barrier" ::: "memory");
        const unsigned pb = lds0 + (wid ^ 4) * 9216;
        const float m2 = *(const LAS float*)(uintptr_t)(pb + 8192 + lane * 4);
        const float l2 = *(const LAS float*)(uintptr_t)(pb + 8192 + 256 + lane * 4);
        const float mm = __builtin_fmaxf(m_run, m2);
        const float a1 = __builtin_amdgcn_exp2f((m_run - mm) * C2), a2 = __builtin_amdgcn_exp2f((m2 - mm) * C2);
        const float inv = 1.0f / (l_run * a1 + l2 * a2);
        const float w1 = a1 * inv, w2 = a2 * inv;
        const int row = b * SQ + 32 * (qb & 1) + r32;
        char* ob = (char*)Og + ((size_t)((row >> 4) * 128 + head * 4 + 2 * kh) * 64 + hi * 16 + (row & 15)) * 16;
#define OSTORE(KEEP, i) do { u32x2 w_[4]; \
            _Pragma("unroll") for (int r4 = 0; r4 < 4; ++r4) { \
                const f32x4 v = *(const LAS f32x4*)(uintptr_t)(pb + (4 * (i) + r4) * 1024 + lane * 16); \
                const float e0 = KEEP[4 * r4] * w1 + v[0] * w2, e1 = KEEP[4 * r4 + 1] * w1 + v[1] * w2; \
                const float e2 = KEEP[4 * r4 + 2] * w1 + v[2] * w2, e3 = KEEP[4 * r4 + 3] * w1 + v[3] * w2; \
                w_[r4] = u32x2{cvtpk(e0, e1), cvtpk(e2, e3)}; } \
            _Pragma("unroll") for (int g = 0; g < 2; ++g) { \
                u32x4 o4 = {w_[g].x, w_[g].y, w_[g + 2].x, w_[g + 2].y}; \
                asm volatile("global_store_dwordx4 %0, %1, off sc1" :: "v"(ob + (i) * 1024 + g * 512), "v"(o4) : "memory"); } } while (0)
        OSTORE(keep0, 0); OSTORE(keep1, 1);
#undef OSTORE
    }
}

constexpr int LDS2_BYTES = 1024 * 160;
#define MFMA16(a, b, c) __builtin_amdgcn_mfma_f32_16x16x32_bf16(a, b, c, 0, 0, 0)
__global__ __launch_bounds__(512, 2) void oproj_kernel(const u16* __restrict__ Og, const u16* __restrict__ wo,
                                                       float* __restrict__ part, u16* __restrict__ out) {
    extern __shared__ __attribute__((aligned(16))) char lds[];
    const unsigned lds0 = (unsigned)(uintptr_t)lds;
    const int tid = threadIdx.x, lane = tid & 63;
    const int wid = __builtin_amdgcn_readfirstlane(tid >> 6);
    const int rb = wid & 3, kp = wid >> 2;
    const int bid = blockIdx.x;
    const int sk = (bid >> 1) & 3, mt = 4 * (bid & 1) + ((bid >> 3) & 3), nt = bid >> 5;
    const char* Bg[5];
#pragma unroll
    for (int i = 0; i < 5; ++i) {
        const int L = i * 1024 + lane * 16;
        const int row = L / 160;
        int c = (L % 160) >> 4;
        c = c < 9 ? c : 0;
        Bg[i] = (const char*)wo + ((size_t)(1024 * sk + 32 * wid + row) * DM + 72 * nt) * 2 + c * 16;
    }
    const char* Ag = (const char*)Og + ((size_t)((8 * mt + 2 * rb) * 128 + 32 * sk + kp) * 64 + lane) * 16;
    u32x4 an[4][8];
#define K2_B(c) do { _Pragma("unroll") for (int i = 0; i < 5; ++i) \
        glds16(Bg[i] + (size_t)(c) * 256 * DM * 2, lds0 + (40 * (c) + 5 * wid + i) * 1024); } while (0)
#define K2_A(c) do { _Pragma("unroll") for (int j = 0; j < 4; j += 2) { const char* a_ = Ag + (8 * (c) + 2 * j) * 1024; \
        asm volatile("global_load_dwordx4 %0, %1, off" : "=v"(an[c][2 * j]) : "v"(a_) : "memory"); \
        asm volatile("global_load_dwordx4 %0, %1, off" : "=v"(an[c][2 * j + 1]) : "v"(a_ + 131072) : "memory"); \
        asm volatile("global_load_dwordx4 %0, %1, off offset:2048" : "=v"(an[c][2 * j + 2]) : "v"(a_) : "memory"); \
        asm volatile("global_load_dwordx4 %0, %1, off offset:2048" : "=v"(an[c][2 * j + 3]) : "v"(a_ + 131072) : "memory"); } } while (0)
    K2_B(0);
    asm volatile("s_barrier" ::: "memory");
    K2_A(0); K2_B(1);
    const int gi = lane >> 4, vq = (lane & 15) >> 2, vp = lane & 3;
    const unsigned bb = lds0 + (32 * kp + 4 * gi + vq) * 160 + 8 * vp;
    f32x4 acc[2][5];
#pragma unroll
    for (int ct = 0; ct < 5; ++ct) { acc[0][ct] = f32x4{0.f, 0.f, 0.f, 0.f}; acc[1][ct] = f32x4{0.f, 0.f, 0.f, 0.f}; }
    s16x4 blo[2][5], bhi[2][5];
    asm volatile("s_waitcnt vmcnt(13)\n\ts_barrier" ::: "memory");
#pragma unroll
    for (int ct = 0; ct < 5; ++ct) { blo[0][ct] = vtr(bb + 32 * ct); bhi[0][ct] = vtr(bb + 32 * ct + 2560); }
#pragma unroll
    for (int s = 0; s < 16; ++s) {
        const int c = s >> 2, j = s & 3;
        const int young = (6 - 2 * j) + (c < 3 ? 5 + (j < 2 ? 4 * j : 8) + (j == 3 && c < 2 ? 5 : 0) : 0);
        asm volatile("s_waitcnt vmcnt(%2)" : "+v"(an[c][2 * j]), "+v"(an[c][2 * j + 1]) : "n"(young) : "memory");

        if (j == 3 && c < 3) asm volatile("s_waitcnt vmcnt(%0)\n\ts_barrier" :: "n"(c < 2 ? 13 : 8) : "memory");
        __builtin_amdgcn_sched_barrier(0);
#pragma unroll
        for (int ct = 0; ct < 5; ++ct) {
            if (s < 15) {
                const unsigned ad = bb + (s + 1) * 10240 + 32 * ct;
                blo[(s + 1) & 1][ct] = vtr(ad); bhi[(s + 1) & 1][ct] = vtr(ad + 2560);
            }
            const s16x4 lo = blo[s & 1][ct], h4 = bhi[s & 1][ct];
            const bf16x8 bf = {lo[0], lo[1], lo[2], lo[3], h4[0], h4[1], h4[2], h4[3]};
            acc[0][ct] = MFMA16(bf, __builtin_bit_cast(bf16x8, an[c][2 * j]), acc[0][ct]);
            acc[1][ct] = MFMA16(bf, __builtin_bit_cast(bf16x8, an[c][2 * j + 1]), acc[1][ct]);
            if (j < 2 && c < 3 && ct < 4) {
                const char* a_ = Ag + (8 * (c + 1) + 4 * j + (ct >> 1) * 2) * 1024 + (ct & 1) * 131072;
                asm volatile("global_load_dwordx4 %0, %1, off" : "=v"(an[(c + 1) & 3][4 * j + ct]) : "v"(a_) : "memory");
            }
            if (j == 2 && c < 2) glds16(Bg[ct] + (size_t)(c + 2) * 256 * DM * 2, lds0 + (40 * (c + 2) + 5 * wid + ct) * 1024);
            __builtin_amdgcn_sched_barrier(0);
        }
    }
#undef K2_A
#undef K2_B
#pragma unroll
    for (int ct = 0; ct < 5; ++ct)
        *(LAS f32x4*)(uintptr_t)(lds0 + (wid * 5 + ct) * 1024 + lane * 16) = kp ? acc[0][ct] : acc[1][ct];
    asm volatile("s_waitcnt lgkmcnt(0)\n\ts_barrier" ::: "memory");
    float* pp = part + ((size_t)(sk * 64 + 8 * mt + 2 * rb + kp) * 8 + nt) * 1152 + lane * 4;
#pragma unroll
    for (int ct = 0; ct < 5; ++ct) {
        const f32x4 v = *(const LAS f32x4*)(uintptr_t)(lds0 + ((wid ^ 4) * 5 + ct) * 1024 + lane * 16);
        const f32x4 r_ = (kp ? acc[1][ct] : acc[0][ct]) + v;
        if (ct < 4 || lane < 32)
            asm volatile("global_store_dwordx4 %0, %1, off sc1" :: "v"(pp + ct * 256), "v"(r_) : "memory");
    }
    asm volatile("s_waitcnt vmcnt(0)\n\ts_barrier" ::: "memory");
    if (wid == 0) {
        gu32* cw = (gu32*)((const char*)Og + CNT_OFF) + (mt * 8 + nt) * 32;
        unsigned todo = 1u << sk;
        unsigned old = 0, v;
        if (lane == 0) old = __hip_atomic_fetch_add(cw, 1u, RLX_AGENT);
        v = __hip_atomic_load(cw, RLX_AGENT);
        old = __builtin_amdgcn_readfirstlane(old);
        if ((old & 255u) == 3u) todo |= (old >> 8) & 15u;
        else {
            const unsigned long long t0 = __builtin_amdgcn_s_memrealtime();
            while ((__builtin_amdgcn_readfirstlane(v) & 255u) != 4u) {
                if (__builtin_amdgcn_s_memrealtime() - t0 > 5000ull) {
                    unsigned o2 = 0;
                    if (lane == 0) o2 = __hip_atomic_fetch_or(cw, 256u << sk, RLX_AGENT);
                    if ((__builtin_amdgcn_readfirstlane(o2) & 255u) != 4u) todo = 0;
                    break;
                }
                v = __hip_atomic_load(cw, RLX_AGENT);
            }
        }
        if (lane == 0) *(LAS unsigned*)(uintptr_t)(lds0 + 65536) = todo;
    }
    asm volatile("s_waitcnt lgkmcnt(0)\n\ts_barrier" ::: "memory");
    unsigned todo = __builtin_amdgcn_readfirstlane(*(const LAS unsigned*)(uintptr_t)(lds0 + 65536));
    while (todo) {
        const int q = __builtin_ctz(todo);
        todo &= todo - 1;
        if (tid < 288) {
            const float* p = part + ((size_t)(8 * mt + 2 * q) * 8 + nt) * 1152 + tid * 4;
            f32x4 v[2][4];
#pragma unroll
            for (int r = 0; r < 2; ++r)
#pragma unroll
                for (int s = 0; s < 4; ++s)
                    asm volatile("global_load_dwordx4 %0, %1, off sc1" : "=v"(v[r][s]) : "v"(p + (size_t)s * 64 * 8 * 1152 + r * 8 * 1152) : "memory");
            asm volatile("s_waitcnt vmcnt(0)" : "+v"(v[0][0]), "+v"(v[0][1]), "+v"(v[0][2]), "+v"(v[0][3]),
                                                "+v"(v[1][0]), "+v"(v[1][1]), "+v"(v[1][2]), "+v"(v[1][3]) :: "memory");
#pragma unroll
            for (int r = 0; r < 2; ++r) {
                const f32x4 t = ((v[r][0] + v[r][1]) + v[r][2]) + v[r][3];
                u32x2 w = {cvtpk(t[0], t[1]), cvtpk(t[2], t[3])};
                *(u32x2*)(out + (size_t)(16 * (8 * mt + 2 * q + r) + (tid & 15)) * DM + 72 * nt + 4 * (tid >> 4)) = w;
            }
        }
    }
}

extern "C" void kernel_launch(void* const* d_in, const int* in_sizes, int n_in, void* d_out, int out_size,
                              void* d_ws, size_t ws_size, hipStream_t stream) {
    const u16* x = (const u16*)d_in[0];
    const u16* kc = (const u16*)d_in[1];
    const u16* vc = (const u16*)d_in[2];
    const u16* wq = (const u16*)d_in[3];
    const u16* wo = (const u16*)d_in[4];
    u16* Og = (u16*)d_ws;
    float* part = (float*)((char*)d_ws + (size_t)NB * SQ * NQ * 2);
    (void)hipFuncSetAttribute((const void*)attn_kernel, hipFuncAttributeMaxDynamicSharedMemorySize, LDS1_BYTES);
    (void)hipFuncSetAttribute((const void*)oproj_kernel, hipFuncAttributeMaxDynamicSharedMemorySize, LDS2_BYTES);
    attn_kernel<<<NB * HKV, 512, LDS1_BYTES, stream>>>(x, kc, vc, wq, Og, wo);
    oproj_kernel<<<256, 512, LDS2_BYTES, stream>>>(Og, wo, part, (u16*)d_out);
}
```

```cpp
#define PROBE_A 2
#define PROBE_B 5
#include <hip/hip_runtime.h>
#include <hip/hip_bf16.h>
#include <stdint.h>

typedef short bf16x8 __attribute__((ext_vector_type(8)));
typedef short s16x4 __attribute__((ext_vector_type(4)));
typedef float f32x16 __attribute__((ext_vector_type(16)));
typedef float f32x4 __attribute__((ext_vector_type(4)));
typedef unsigned u32x4 __attribute__((ext_vector_type(4)));
typedef unsigned u32x2 __attribute__((ext_vector_type(2)));
typedef unsigned short u16;
#define LAS __attribute__((address_space(3)))
typedef __attribute__((address_space(1))) unsigned gu32;
#define RLX_AGENT __ATOMIC_RELAXED, __HIP_MEMORY_SCOPE_AGENT

constexpr int NB = 16, SQ = 64, DM = 576, HQ = 32, HKV = 16, SKV = 1024, DH = 128;
constexpr int NQ = HQ * DH;
constexpr int NT = SKV / 64;
constexpr int REG = 40960;
constexpr int LDS_BYTES = 4 * REG;
constexpr size_t PART_OFF = (size_t)NB * SQ * NQ * 2;
constexpr size_t CNT_OFF = (size_t)20 << 20;
#define TMO 5000ull
#define TMO2 5000ull
constexpr int XS0 = 5, XS1 = 7, YS0 = 5, YS1 = 7;
constexpr int PACE = 108;
constexpr float RTHR = 8.0f;
constexpr float C2 = 0.088388347648318447f * 1.4426950408889634f;

#define MFMA32(a, b, c) __builtin_amdgcn_mfma_f32_32x32x16_bf16(a, b, c, 0, 0, 0)

typedef __bf16 bf16x2_t __attribute__((ext_vector_type(2)));
typedef float f32x2 __attribute__((ext_vector_type(2)));
__device__ __forceinline__ unsigned cvtpk(float lo, float hi) {
    f32x2 v = {lo, hi};
    return __builtin_bit_cast(unsigned, __builtin_convertvector(v, bf16x2_t));
}
__device__ __forceinline__ void glds16(const void* g, unsigned lds_base) {
    unsigned sv;
    asm volatile("s_mov_b32 %0, m0\n\ts_mov_b32 m0, %2\n\ts_nop 0\n\tglobal_load_lds_dwordx4 %1, off\n\ts_mov_b32 m0, %0"
                 : "=&s"(sv) : "v"(g), "s"(lds_base) : "memory");
}
__device__ __forceinline__ s16x4 vtr(unsigned a) {
    return __builtin_amdgcn_ds_read_tr16_b64_v4i16((LAS s16x4*)(uintptr_t)a);
}
__device__ __forceinline__ bf16x8 ldsr128(unsigned a) { return *(const LAS bf16x8*)(uintptr_t)a; }
__device__ __forceinline__ float swap_max(float v) {
    auto rr = __builtin_amdgcn_permlane32_swap(__float_as_uint(v), __float_as_uint(v), false, false);
    return __builtin_fmaxf(__uint_as_float(rr[0]), __uint_as_float(rr[1]));
}
__device__ __forceinline__ float max3f(float a, float b, float c) { float r; asm("v_max3_f32 %0, %1, %2, %3" : "=v"(r) : "v"(a), "v"(b), "v"(c)); return r; }
__device__ __forceinline__ float fmul1(float a, float b) { float r; asm("v_mul_f32_e32 %0, %1, %2" : "=v"(r) : "s"(b), "v"(a)); return r; }
__device__ __forceinline__ float swap_max3(float v) {
    auto rr = __builtin_amdgcn_permlane32_swap(__float_as_uint(v), __float_as_uint(v), false, false);
    return max3f(__uint_as_float(rr[0]), __uint_as_float(rr[1]), __uint_as_float(rr[1]));
}
__device__ __forceinline__ float swap_add(float v) {
    auto rr = __builtin_amdgcn_permlane32_swap(__float_as_uint(v), __float_as_uint(v), false, false);
    return __uint_as_float(rr[0]) + __uint_as_float(rr[1]);
}

#define TL(i) tl[i] = __builtin_amdgcn_s_memrealtime()
extern "C" __device__ unsigned long long amd_dispatch_id() __asm("llvm.amdgcn.dispatch.id");

template <unsigned N>
__device__ __forceinline__ unsigned rendezvous(const char* w, unsigned tag, unsigned own, int lane, unsigned long long tmo, unsigned& dep) {
    unsigned old = 0, todo = 1u << own;
    u32x2 v;
    if (lane == 0) asm volatile("global_atomic_add %0, %1, %2, off sc0" : "=v"(old) : "v"(w), "v"(1u) : "memory");
    asm volatile("global_load_dwordx2 %0, %1, off sc1" : "=v"(v) : "v"(w) : "memory");
    asm volatile("s_waitcnt vmcnt(0)" : "+v"(old), "+v"(v) :: "memory");
    old = __builtin_amdgcn_readfirstlane(old);
    if ((old & 255u) == N - 1) todo |= (old >> 8) & 255u;
    else {
        const unsigned long long t0 = __builtin_amdgcn_s_memrealtime();
        while ((__builtin_amdgcn_readfirstlane(v.x) & 255u) != N || __builtin_amdgcn_readfirstlane(v.y) != tag) {
            if (__builtin_amdgcn_s_memrealtime() - t0 > tmo) {
                unsigned o2 = 0;
                if (lane == 0) asm volatile("global_atomic_or %0, %1, %2, off sc0" : "=v"(o2) : "v"(w), "v"(256u << own) : "memory");
                asm volatile("s_waitcnt vmcnt(0)" : "+v"(o2) :: "memory");
                if ((__builtin_amdgcn_readfirstlane(o2) & 255u) != N) todo = 0;
                break;
            }
            asm volatile("global_load_dwordx2 %0, %1, off sc1\n\ts_waitcnt vmcnt(0)" : "=v"(v) : "v"(w) : "memory");
        }
    }
    dep = 0;
    if (lane == 0) asm volatile("global_atomic_add %0, %1, %2, off sc0" : "=v"(dep) : "v"(w), "v"(65536u) : "memory");
    return todo;
}
template <unsigned N>
__device__ __forceinline__ void depart(const char* w, unsigned dep, int lane) {
    asm volatile("" : "+v"(dep));
    if (lane == 0 && ((dep >> 16) & 255u) == N - 1)
        asm volatile("global_store_dwordx2 %0, %1, off sc1" :: "v"(w), "v"(u32x2{0u, 0u}) : "memory");
}

#define MFMA16(a, b, c) __builtin_amdgcn_mfma_f32_16x16x32_bf16(a, b, c, 0, 0, 0)
__global__ __launch_bounds__(512, 2) void gqa_kernel(const u16* __restrict__ x, const u16* __restrict__ Kc,
                                                     const u16* __restrict__ Vc, const u16* __restrict__ wq,
                                                     const u16* __restrict__ wo, char* ws, u16* __restrict__ out) {
    extern __shared__ __attribute__((aligned(16))) char lds[];
    unsigned long long tl[13];
    TL(0);
    const unsigned lds0 = (unsigned)(uintptr_t)lds;
    const int tid = threadIdx.x, lane = tid & 63, r32 = lane & 31, hi = lane >> 5;
    const int wid = __builtin_amdgcn_readfirstlane(tid >> 6);
    const int qb = wid & 3, kh = wid >> 2;
    const int bid = blockIdx.x, b = 8 * (bid & 1) + ((bid >> 3) & 7), kvh = 4 * ((bid >> 1) & 3) + (bid >> 6);
    const int head = 2 * kvh + (qb >> 1);
    const int mt = b >> 1, sk = kvh >> 2, nt = 4 * (b & 1) + (kvh & 3);
    const unsigned tag = 0x80000000u | ((unsigned)amd_dispatch_id() & 0xfffffu) |
                         ((((unsigned)((uintptr_t)__builtin_amdgcn_dispatch_ptr() >> 12) * 0x9e3779b1u) >> 21) << 20);
    const char* w1 = ws + CNT_OFF + (mt * 4 + sk) * 128;
    const char* w2 = ws + CNT_OFF + 4096 + mt * 8 * 128;
    const char* wcl = lane == 0 ? w1 : w2 + nt * 128;
    u32x2 clm = {0u, tag};
    if (wid == 1 && lane < 2)
        asm volatile("global_atomic_cmpswap_x2 %0, %1, %2, off sc0\n\ts_nop 1" : "=v"(clm) : "v"(wcl), "v"(u32x4{0u, tag, 0u, 0u}) : "memory");
    const int gi = lane >> 4, vq = (lane & 15) >> 2, vp = lane & 3;

    const unsigned vsrc = (16 * (wid >> 1) + ((lane >> 2) & 7)) * 256 + (wid & 1) * 128 + hi * 64;
    const unsigned voffV = vsrc + vp * 16;
    const unsigned voffK0 = vsrc + ((vp ^ (wid & 2)) << 4), voffK1 = vsrc + ((vp ^ ((wid & 2) | 1)) << 4);
    const char* Kg = (const char*)Kc + (size_t)(b * HKV + kvh) * SKV * DH * 2;
    const char* Vg = (const char*)Vc + (size_t)(b * HKV + kvh) * SKV * DH * 2;
    const unsigned dwo = lds0 + (wid >> 1) * 4096 + (wid & 1) * 1024;
#define KVDMA(voff, gp, dst, OFF) asm volatile("s_mov_b32 m0, %2\n\ts_nop 0\n\tglobal_load_lds_dwordx4 %0, %1 offset:" #OFF " sc1 nt" \
            :: "v"(voff), "s"(gp), "s"(dst) : "memory")
#define VSO(tn) ((int)((0x0200110au >> (8 * ((tn) & 3))) & 255) * 8192)
#define KSO(tn) ((int)((0x0f0c0705u >> (8 * ((tn) & 3))) & 255) * 8192)
#define DMA_V0(tn) KVDMA(voffV, Vg + (tn) * 16384, dwo + VSO(tn), 0)
#define DMA_V1(tn) KVDMA(voffV, Vg + (tn) * 16384, dwo + VSO(tn), 2048)
#define DMA_K0(tn) KVDMA(voffK0, Kg + (tn) * 16384, dwo + KSO(tn), 0)
#define DMA_K1(tn) KVDMA(voffK1, Kg + (tn) * 16384, dwo + KSO(tn), 2048)
#define DMA_V(tn) do { DMA_V0(tn); DMA_V1(tn); } while (0)
#define DMA_K(tn) do { DMA_K0(tn); DMA_K1(tn); } while (0)
    bf16x8 qf[8];
    {
        int kb = (b + 2 * (kvh & 3)) % 9;
        const unsigned xo = (8 * wid + (lane >> 3)) * DM * 2 + (((lane & 7) ^ ((4 * wid + (lane >> 4)) & 7)) << 4);
        const unsigned wo0 = (8 * wid + hi) * NQ * 2 + ((r32 ^ (hi << 2)) << 4);
        const unsigned wo1 = (8 * wid + 2 + hi) * NQ * 2 + ((r32 ^ ((2 + hi) << 2)) << 4);
        const unsigned wo2 = wo0 + 4 * NQ * 2, wo3 = wo1 + 4 * NQ * 2;
        const char* Xs = (const char*)x + (size_t)b * SQ * DM * 2;
        const char* Ws = (const char*)wq + kvh * 512;
#define QDMA(voff, gp, dst) asm volatile("s_mov_b32 m0, %2\n\ts_nop 0\n\tglobal_load_lds_dwordx4 %0, %1" :: "v"(voff), "s"(gp), "s"(dst) : "memory")
#define QS_PIECE(slot, p) do { const unsigned s_ = lds0 + (slot) * 40960 + wid * 4096; const char* w_ = Ws + (size_t)kb * 64 * NQ * 2; \
            if ((p) == 0) QDMA(wo0, w_, s_); \
            if ((p) == 1) QDMA(wo1, w_, s_ + 1024); \
            if ((p) == 2) QDMA(wo2, w_, s_ + 2048); \
            if ((p) == 3) QDMA(wo3, w_, s_ + 3072); \
            if ((p) == 4) { QDMA(xo, Xs + kb * 128, lds0 + (slot) * 40960 + 32768 + wid * 1024); kb = kb == 8 ? 0 : kb + 1; } } while (0)
#define QS_ISSUE(slot) do { QS_PIECE(slot, 0); QS_PIECE(slot, 1); QS_PIECE(slot, 2); QS_PIECE(slot, 3); QS_PIECE(slot, 4); } while (0)
#pragma unroll 1
        for (int j = 0; j < 2; ++j) { QS_ISSUE(j); asm volatile("s_barrier" ::: "memory"); }
        const int pp = ((vp & 1) << 1) | (vp >> 1);
        const unsigned abase = lds0 + (8 * hi + vq) * 512 + (((4 * wid + 2 * (gi & 1) + (pp >> 1)) ^ (vq << 2)) << 4) + 8 * (pp & 1);
        const unsigned xbase = lds0 + 32768 + r32 * 128 + ((hi ^ ((r32 >> 1) & 7)) << 4);
        f32x16 acc0 = {}, acc1 = {};
        s16x4 lo[2][4], h4[2][4]; bf16x8 x0[2][4], x1[2][4];
        asm volatile("s_waitcnt vmcnt(5)\n\ts_barrier" : "+v"(clm) :: "memory");
        QS_ISSUE(2);
        if (wid == 1 && lane < 2 && clm.y != tag && (clm.x | clm.y) != 0u)
            asm volatile("global_atomic_cmpswap_x2 %0, %1, off\n\ts_nop 1" :: "v"(wcl), "v"(u32x4{0u, tag, clm.x, clm.y}) : "memory");
#pragma unroll
        for (int i = 0; i < 4; ++i) {
            lo[0][i] = vtr(abase + i * 8192); h4[0][i] = vtr(abase + i * 8192 + 2048);
            x0[0][i] = ldsr128(xbase ^ (i << 5)); x1[0][i] = ldsr128((xbase ^ (i << 5)) + 4096);
        }
#pragma unroll
        for (int st = 0; st < 9; ++st) {
            if (st == 0) asm volatile("s_waitcnt vmcnt(5)" ::: "memory");
            else if (st < 6) asm volatile("s_waitcnt vmcnt(10)" ::: "memory");
            else if (st == 6) asm volatile("s_waitcnt vmcnt(9)" ::: "memory");
            else if (st == 7) asm volatile("s_waitcnt vmcnt(6)" ::: "memory");
            asm volatile("s_waitcnt lgkmcnt(0)\n\ts_barrier" ::: "memory");
            const unsigned so = ((st + 1) & 3) * 40960;
            const int cu = st & 1, nx = cu ^ 1;
            unsigned aa = abase + so;
            asm volatile("" : "+v"(aa));
            __builtin_amdgcn_sched_barrier(0);
            if (st == 0) QS_ISSUE(3);
#pragma unroll
            for (int i = 0; i < 4; ++i) {
                const bf16x8 a0 = {lo[cu][i][0], lo[cu][i][1], lo[cu][i][2], lo[cu][i][3], h4[cu][i][0], h4[cu][i][1], h4[cu][i][2], h4[cu][i][3]};
                acc0 = MFMA32(a0, x0[cu][i], acc0);
                if (st < 8) { lo[nx][i] = vtr(aa + i * 8192); h4[nx][i] = vtr(aa + i * 8192 + 2048); }
                if (st < 5) QS_PIECE(st & 3, i);
                __builtin_amdgcn_sched_barrier(0);
                acc1 = MFMA32(a0, x1[cu][i], acc1);
                if (st < 8) { unsigned xa = (xbase ^ (i << 5)) + so; asm volatile("" : "+v"(xa)); x0[nx][i] = ldsr128(xa); x1[nx][i] = ldsr128(xa + 4096); }
                if (st < 5 && i == 3) QS_PIECE(st & 3, 4);
                if (st == 5 && i == 3) { DMA_K(0); DMA_K(1); }
                if (st == 6 && i == 3) DMA_V(0);
                if (st == 7 && i == 3) DMA_K(2);
                if (st == 8 && i == 3) DMA_K(3);
                __builtin_amdgcn_sched_barrier(0);
            }
        }
#undef QS_ISSUE
#undef QS_PIECE
#define QW(ACC, qblk) do { _Pragma("unroll") for (int s = 0; s < 2; ++s) { \
            u32x4 w_ = {cvtpk(ACC[8 * s], ACC[8 * s + 1]), cvtpk(ACC[8 * s + 2], ACC[8 * s + 3]), \
                        cvtpk(ACC[8 * s + 4], ACC[8 * s + 5]), cvtpk(ACC[8 * s + 6], ACC[8 * s + 7])}; \
            *(LAS u32x4*)(uintptr_t)(lds0 + 32768 + (2 * (wid >> 2) + (qblk)) * REG + (2 * (wid & 3) + s) * 1024 + lane * 16) = w_; } } while (0)
        QW(acc0, 0); QW(acc1, 1);
#undef QW
        DMA_V(1);
    }
    asm volatile("s_waitcnt vmcnt(8) lgkmcnt(0)\n\ts_barrier" ::: "memory");
#pragma unroll
    for (int f = 0; f < 8; ++f) qf[f] = ldsr128(lds0 + 32768 + qb * REG + f * 1024 + lane * 16);
    TL(1);

    const unsigned kb0 = lds0 + (4 * kh + (r32 >> 3)) * 2048 + (r32 & 7) * 64 + ((hi ^ (r32 >> 3)) << 4);
    const unsigned kb1 = kb0 ^ 32;
    const unsigned vb = lds0 + kh * 8192 + (4 * hi + vq) * 64 + (gi & 1) * 32 + vp * 8;

    float m_run, l_run = 0.f, tmax = 0.f;
    unsigned long long tprev = 0;
    f32x16 o[4] = {};
    f32x16 sA, sB, negm;

    bf16x8 kf[8];
    {
        bf16x8 k0[8];
        unsigned ka0 = kb0 + KSO(0), ka1 = kb1 + KSO(0);
        asm volatile("" : "+v"(ka0), "+v"(ka1));
#pragma unroll
        for (int d0 = 0; d0 < 8; ++d0) k0[d0] = ldsr128(((d0 & 1) ? ka1 : ka0) + (d0 >> 1) * 512);
#pragma unroll
        for (int d0 = 0; d0 < 8; ++d0) kf[d0] = ldsr128(((d0 & 1) ? ka1 : ka0) + 16384 + (d0 >> 1) * 512);
        __builtin_amdgcn_sched_barrier(0);
        asm volatile("s_waitcnt lgkmcnt(0)\n\ts_barrier" ::: "memory");
        sA = f32x16{};
#pragma unroll
        for (int d0 = 0; d0 < 8; ++d0) sA = MFMA32(k0[d0], qf[d0], sA);
        __builtin_amdgcn_sched_barrier(0);
        DMA_V(2); DMA_K(4);
        m_run = sA[0];
#pragma unroll
        for (int r = 1; r < 16; ++r) m_run = __builtin_fmaxf(m_run, sA[r]);
        m_run = swap_max(m_run);
#pragma unroll
        for (int r = 0; r < 16; ++r) { sA[r] -= m_run; negm[r] = -m_run; }
    }
#define EXP1(r) do { sc[r] = __builtin_amdgcn_exp2f(fmul1(sc[r], C2)); } while (0)
#define SUM1(k) do { ps0 += sc[2 * (k)]; ps1 += sc[2 * (k) + 1]; pw[k] = cvtpk(sc[2 * (k)], sc[2 * (k) + 1]); asm volatile("" : "+v"(pw[k]), "+v"(ps0), "+v"(ps1)); } while (0)
#define STEP(t, SC_, SN_, TAIL) do { \
        f32x16& sc = SC_; f32x16& sn = SN_; \
        s16x4 vlo[8], vhi[8]; \
        if (!TAIL || (t) < 12 || (t) == 14) asm volatile("s_waitcnt vmcnt(8)" ::: "memory"); \
        else if ((t) < 14) asm volatile("s_waitcnt vmcnt(6)" ::: "memory"); \
        else asm volatile("s_waitcnt vmcnt(10)" ::: "memory"); \
        while (__builtin_amdgcn_s_memrealtime() - tprev < (unsigned long long)PACE) __builtin_amdgcn_s_sleep(1); \
        asm volatile("s_waitcnt lgkmcnt(0)\n\ts_barrier" ::: "memory"); \
        tprev = __builtin_amdgcn_s_memrealtime(); \
        if (__builtin_expect(__any(tmax > RTHR / C2), 0)) { \
            const float dl = __builtin_fmaxf(tmax, 0.f); \
            const float alpha = __builtin_amdgcn_exp2f(-dl * C2); \
            m_run += dl; l_run *= alpha; \
            _Pragma("unroll") for (int r = 0; r < 16; ++r) { sc[r] -= dl; negm[r] = -m_run; } \
            _Pragma("unroll") for (int dt = 0; dt < 4; ++dt) \
                _Pragma("unroll") for (int r = 0; r < 16; ++r) o[dt][r] *= alpha; \
        } \
        const unsigned vwr = dwo + vso, kwr = dwo + kso;                         \
        vso = VSO(t); kso = KSO((t) + 2); \
        const unsigned va = vb + vso; \
        const unsigned ka0 = kb0 + kso, ka1 = kb1 + kso;                         \
        unsigned pw[8]; \
        float ps0 = 0.f, ps1 = 0.f, t2; \
        __builtin_amdgcn_sched_barrier(0); \
          \
        _Pragma("unroll") for (int i = 0; i < 8; ++i) { \
            sn = MFMA32(kf[i], qf[i], i == 0 ? negm : sn);                   \
            const int ss = i >> 2, dt = i & 3; \
            vlo[2 * dt + ss] = vtr(va + ss * 4096 + dt * 512); \
            vhi[2 * dt + ss] = vtr(va + ss * 4096 + 2048 + dt * 512); \
            if (i < 4) { EXP1(2 * i); EXP1(2 * i + 1); } else EXP1(4 + i); \
            if (i >= 1 && i <= 4) SUM1(i - 1); \
            if (i == 6) SUM1(4); \
            asm volatile("" : "+v"(sn), "+v"(sc)); \
            if (i == XS0 && (!TAIL || (t) < 13)) KVDMA(voffV, Vg + ((t) + 3) * 16384, vwr, 0); \
            if (i == XS1 && (!TAIL || (t) < 13)) KVDMA(voffV, Vg + ((t) + 3) * 16384, vwr, 2048); \
            if (i == XS0 && TAIL && (t) == 13) BDMA(Bs, 0, 2); \
            if (i == XS1 && TAIL && (t) == 13) BDMA(Bs, 0, 3); \
            if (i == XS0 && TAIL && (t) == 14) BDMA(Bs, 1, 1); \
            if (i == XS1 && TAIL && (t) == 14) BDMA(Bs, 1, 2); \
            __builtin_amdgcn_sched_barrier(0); \
        } \
          \
        _Pragma("unroll") for (int i = 0; i < 8; ++i) { \
            const int ss = i >> 2, dt = i & 3; \
            const s16x4 lo = vlo[2 * dt + ss], hi4 = vhi[2 * dt + ss]; \
            const bf16x8 vf = {lo[0], lo[1], lo[2], lo[3], hi4[0], hi4[1], hi4[2], hi4[3]}; \
            const bf16x8 pf = __builtin_bit_cast(bf16x8, u32x4{pw[4 * ss], pw[4 * ss + 1], pw[4 * ss + 2], pw[4 * ss + 3]}); \
            o[dt] = MFMA32(vf, pf, o[dt]); \
            if (i < 4) { kf[2 * i] = ldsr128(ka0 + i * 512); kf[2 * i + 1] = ldsr128(ka1 + i * 512); } \
            if (i < 2) { EXP1(12 + 2 * i); EXP1(13 + 2 * i); asm volatile("" : "+v"(sc)); } \
            if (i < 3) SUM1(5 + i); \
            if (i == 3) { l_run += ps0 + ps1; asm volatile("" : "+v"(sn), "+v"(l_run)); \
                          tmax = max3f(sn[0], sn[1], sn[2]); t2 = max3f(sn[3], sn[4], sn[5]); asm volatile("" : "+v"(tmax), "+v"(t2)); } \
            if (i == 4) { tmax = max3f(tmax, sn[6], sn[7]); t2 = max3f(t2, sn[8], sn[9]); tmax = max3f(tmax, sn[10], sn[11]); asm volatile("" : "+v"(tmax), "+v"(t2)); } \
            if (i == 5) { t2 = max3f(t2, sn[12], sn[13]); tmax = max3f(tmax, sn[14], sn[15]); tmax = max3f(tmax, t2, t2); asm volatile("" : "+v"(tmax)); } \
            if (i == 6) { tmax = swap_max3(tmax); asm volatile("" : "+v"(tmax)); } \
            if (i == YS0 && (!TAIL || (t) < 11)) KVDMA(voffK0, Kg + ((t) + 5) * 16384, kwr, 0); \
            if (i == YS1 && (!TAIL || (t) < 11)) KVDMA(voffK1, Kg + ((t) + 5) * 16384, kwr, 2048); \
            if (i == YS0 && TAIL && (t) == 12) BDMA(Bs, 0, 0); \
            if (i == YS1 && TAIL && (t) == 12) BDMA(Bs, 0, 1); \
            if (i == YS0 && TAIL && (t) == 13) BDMA(Bs, 0, 4); \
            if (i == YS1 && TAIL && (t) == 13) BDMA(Bs, 1, 0); \
            if (i == YS0 && TAIL && (t) == 14) BDMA(Bs, 1, 3); \
            if (i == YS1 && TAIL && (t) == 14) BDMA(Bs, 1, 4); \
            __builtin_amdgcn_sched_barrier(0); \
        } } while (0)
    unsigned boff[5];
    const char* Bw = (const char*)wo + (size_t)(1024 * sk + 32 * wid) * DM * 2;
    const char* Bs = Bw + 144 * nt;
#define BDMA(bs, c, i) QDMA(boff[i], (bs) + (c) * (256 * DM * 2), lds0 + (((c) + 1) & 3) * REG + wid * 5120 + (i) * 1024)
    unsigned vso = VSO(3), kso = KSO(1);
#pragma unroll 1
    for (int t = 0; t < 10; t += 2) { STEP(t, sA, sB, false); STEP(t + 1, sB, sA, false); }
    {
        unsigned ln = lane;
        asm volatile("" : "+v"(ln));
#pragma unroll
        for (int i = 0; i < 5; ++i) {
            const unsigned n = 64 * i + ln, row = (n * 205u) >> 11, c = n - 10 * row;
            boff[i] = row * (DM * 2) + (c < 9 ? c : 0u) * 16;
        }
    }
#pragma unroll 1
    for (int t = 10; t < NT; t += 2) { STEP(t, sA, sB, true); STEP(t + 1, sB, sA, true); }
#undef STEP
#undef EXP1
#undef SUM1
    TL(2);
    l_run = swap_add(l_run);
    asm volatile("s_waitcnt lgkmcnt(0)\n\ts_barrier" ::: "memory");
    if (wid == 0) {
#pragma unroll
        for (int i = 0; i < 10; ++i) BDMA(Bs, 2 + i / 5, i % 5);
    }
    {
        f32x16 keep0, keep1, send0, send1;
        if (kh == 0) { keep0 = o[0]; keep1 = o[1]; send0 = o[2]; send1 = o[3]; }
        else         { keep0 = o[2]; keep1 = o[3]; send0 = o[0]; send1 = o[1]; }
        const unsigned xb = lds0 + (kh ? 0 : 3 * REG) + 5120 + qb * 8704;
#pragma unroll
        for (int r4 = 0; r4 < 4; ++r4) {
            f32x4 v0 = {send0[4 * r4], send0[4 * r4 + 1], send0[4 * r4 + 2], send0[4 * r4 + 3]};
            f32x4 v1 = {send1[4 * r4], send1[4 * r4 + 1], send1[4 * r4 + 2], send1[4 * r4 + 3]};
            *(LAS f32x4*)(uintptr_t)(xb + r4 * 1024 + lane * 16) = v0;
            *(LAS f32x4*)(uintptr_t)(xb + (4 + r4) * 1024 + lane * 16) = v1;
        }
        *(LAS float*)(uintptr_t)(xb + 8192 + lane * 4) = m_run;
        *(LAS float*)(uintptr_t)(xb + 8192 + 256 + lane * 4) = l_run;
        asm volatile("s_waitcnt lgkmcnt(0)\n\ts_barrier" ::: "memory");
        const unsigned pb = lds0 + (kh ? 3 * REG : 0) + 5120 + qb * 8704;
        const float m2 = *(const LAS float*)(uintptr_t)(pb + 8192 + lane * 4);
        const float l2 = *(const LAS float*)(uintptr_t)(pb + 8192 + 256 + lane * 4);
        const float mm = __builtin_fmaxf(m_run, m2);
        const float a1 = __builtin_amdgcn_exp2f((m_run - mm) * C2), a2 = __builtin_amdgcn_exp2f((m2 - mm) * C2);
        const float inv = 1.0f / (l_run * a1 + l2 * a2);
        const float w1 = a1 * inv, w2 = a2 * inv;
        const int row = b * SQ + 32 * (qb & 1) + r32;
        char* ob = ws + ((size_t)((row >> 4) * 128 + head * 4 + 2 * kh) * 64 + hi * 16 + (row & 15)) * 16;
#define OSTORE(KEEP, i) do { u32x2 w_[4]; \
            _Pragma("unroll") for (int r4 = 0; r4 < 4; ++r4) { \
                const f32x4 v = *(const LAS f32x4*)(uintptr_t)(pb + (4 * (i) + r4) * 1024 + lane * 16); \
                const float e0 = KEEP[4 * r4] * w1 + v[0] * w2, e1 = KEEP[4 * r4 + 1] * w1 + v[1] * w2; \
                const float e2 = KEEP[4 * r4 + 2] * w1 + v[2] * w2, e3 = KEEP[4 * r4 + 3] * w1 + v[3] * w2; \
                w_[r4] = u32x2{cvtpk(e0, e1), cvtpk(e2, e3)}; } \
            _Pragma("unroll") for (int g = 0; g < 2; ++g) { \
                u32x4 o4 = {w_[g].x, w_[g].y, w_[g + 2].x, w_[g + 2].y}; \
                asm volatile("global_store_dwordx4 %0, %1, off sc1\n\ts_nop 1" :: "v"(ob + (i) * 1024 + g * 512), "v"(o4) : "memory"); } } while (0)
        OSTORE(keep0, 0); OSTORE(keep1, 1);
#undef OSTORE
    }
    TL(3);
    const char* Ag = ws + (size_t)((8 * mt + 2 * qb) * 128 + 32 * sk + kh) * 1024;
    unsigned ao0 = lane * 16, ao1 = ao0 + 131072;
    unsigned bb[4];
#pragma unroll
    for (int c = 0; c < 4; ++c) { bb[c] = lds0 + ((c + 1) & 3) * REG + (32 * kh + 4 * gi + vq) * 160 + 8 * vp; asm volatile("" : "+v"(bb[c])); }
    asm volatile("" : "+v"(ao0), "+v"(ao1), "+s"(Ag));
    asm volatile("s_waitcnt vmcnt(0) lgkmcnt(0)\n\ts_barrier" ::: "memory");
    TL(4);
    unsigned dep1 = 0;
    if (wid == 0) {
        const unsigned td = rendezvous<8>(w1, tag, nt, lane, TMO, dep1);
        if (lane == 0) *(LAS unsigned*)(uintptr_t)(lds0 + REG + 144) = td;
    } else {
#pragma unroll
        for (int i = 0; i < 10; ++i) BDMA(Bs, 2 + i / 5, i % 5);
    }
    asm volatile("s_waitcnt lgkmcnt(0)\n\ts_barrier" ::: "memory");
    unsigned todo = __builtin_amdgcn_readfirstlane(*(const LAS unsigned*)(uintptr_t)(lds0 + REG + 144));
    TL(5);
    float* part = (float*)(ws + PART_OFF);
    while (todo) {
        const int ntc = ((todo >> nt) & 1u) ? nt : __builtin_ctz(todo);
        todo &= ~(1u << ntc);
        if (ntc != nt) {
            asm volatile("s_waitcnt vmcnt(0) lgkmcnt(0)\n\ts_barrier" ::: "memory");
#pragma unroll 1
            for (int c = 0; c < 4; ++c) {
#pragma unroll
                for (int i = 0; i < 5; ++i) BDMA(Bw + 144 * ntc, c, i);
            }
            asm volatile("s_waitcnt vmcnt(0)\n\ts_barrier" ::: "memory");
        }
        u32x4 an[32];
#pragma unroll
        for (int s2 = 0; s2 < 8; ++s2) {
            const char* ag = Ag;
            asm volatile("" : "+s"(ag));
            ag += s2 * 4096;
            asm volatile("global_load_dwordx4 %0, %1, %2 sc1" : "=v"(an[4 * s2]) : "v"(ao0), "s"(ag) : "memory");
            asm volatile("global_load_dwordx4 %0, %1, %2 sc1" : "=v"(an[4 * s2 + 1]) : "v"(ao1), "s"(ag) : "memory");
            asm volatile("global_load_dwordx4 %0, %1, %2 offset:2048 sc1" : "=v"(an[4 * s2 + 2]) : "v"(ao0), "s"(ag) : "memory");
            asm volatile("global_load_dwordx4 %0, %1, %2 offset:2048 sc1" : "=v"(an[4 * s2 + 3]) : "v"(ao1), "s"(ag) : "memory");
        }
        f32x4 acc[2][5];
#pragma unroll
        for (int ct = 0; ct < 5; ++ct) { acc[0][ct] = f32x4{0.f, 0.f, 0.f, 0.f}; acc[1][ct] = f32x4{0.f, 0.f, 0.f, 0.f}; }
        s16x4 blo[2][5], bhi[2][5];
#pragma unroll
        for (int ct = 0; ct < 5; ++ct) { blo[0][ct] = vtr(bb[0] + 32 * ct); bhi[0][ct] = vtr(bb[0] + 32 * ct + 2560); }
#pragma unroll
        for (int s = 0; s < 16; ++s) {
            asm volatile("s_waitcnt vmcnt(%2)" : "+v"(an[2 * s]), "+v"(an[2 * s + 1]) : "n"(30 - 2 * s) : "memory");
            if (s == 0) { TL(6); }
            if (s == 7) asm volatile("s_barrier" ::: "memory");
            __builtin_amdgcn_sched_barrier(0);
#pragma unroll
            for (int ct = 0; ct < 5; ++ct) {
                if (s < 15) {
                    const unsigned ad = bb[((s + 1) >> 2) & 3] + ((s + 1) & 3) * 10240 + 32 * ct;
                    blo[(s + 1) & 1][ct] = vtr(ad); bhi[(s + 1) & 1][ct] = vtr(ad + 2560);
                }
                const s16x4 lo = blo[s & 1][ct], h4 = bhi[s & 1][ct];
                const bf16x8 bf = {lo[0], lo[1], lo[2], lo[3], h4[0], h4[1], h4[2], h4[3]};
                acc[0][ct] = MFMA16(bf, __builtin_bit_cast(bf16x8, an[2 * s]), acc[0][ct]);
                acc[1][ct] = MFMA16(bf, __builtin_bit_cast(bf16x8, an[2 * s + 1]), acc[1][ct]);
                __builtin_amdgcn_sched_barrier(0);
            }
        }
        TL(7);
#pragma unroll
        for (int ct = 0; ct < 5; ++ct)
            *(LAS f32x4*)(uintptr_t)(lds0 + REG + (wid * 5 + ct) * 1024 + lane * 16) = kh ? acc[0][ct] : acc[1][ct];
        asm volatile("s_waitcnt lgkmcnt(0)\n\ts_barrier" ::: "memory");
        float* pp = part + ((size_t)(sk * 64 + 8 * mt + 2 * qb + kh) * 8 + ntc) * 1152 + lane * 4;
#pragma unroll
        for (int ct = 0; ct < 5; ++ct) {
            const f32x4 v = *(const LAS f32x4*)(uintptr_t)(lds0 + REG + ((wid ^ 4) * 5 + ct) * 1024 + lane * 16);
            const f32x4 r_ = (kh ? acc[1][ct] : acc[0][ct]) + v;
            if (ct < 4 || lane < 32)
                asm volatile("global_store_dwordx4 %0, %1, off sc1\n\ts_nop 1" :: "v"(pp + ct * 256), "v"(r_) : "memory");
        }
        TL(8);
        asm volatile("s_waitcnt vmcnt(0)\n\ts_barrier" ::: "memory");
        TL(9);
        unsigned dep2 = 0;
        if (wid == 0) {
            const unsigned td = rendezvous<4>(w2 + ntc * 128, tag, sk, lane, TMO2, dep2);
            if (lane == 0) *(LAS unsigned*)(uintptr_t)(lds0 + 2 * REG) = td;
        }
        asm volatile("s_waitcnt lgkmcnt(0)\n\ts_barrier" ::: "memory");
        unsigned qs = __builtin_amdgcn_readfirstlane(*(const LAS unsigned*)(uintptr_t)(lds0 + 2 * REG));
        TL(10);
        while (qs) {
            const int q = __builtin_ctz(qs);
            qs &= qs - 1;
            if (tid < 288) {
                const float* p = part + ((size_t)(8 * mt + 2 * q) * 8 + ntc) * 1152 + tid * 4;
                f32x4 v[2][4];
#pragma unroll
                for (int r = 0; r < 2; ++r)
#pragma unroll
                    for (int s = 0; s < 4; ++s)
                        asm volatile("global_load_dwordx4 %0, %1, off sc1" : "=v"(v[r][s]) : "v"(p + (size_t)s * 64 * 8 * 1152 + r * 8 * 1152) : "memory");
                asm volatile("s_waitcnt vmcnt(0)" : "+v"(v[0][0]), "+v"(v[0][1]), "+v"(v[0][2]), "+v"(v[0][3]),
                                                    "+v"(v[1][0]), "+v"(v[1][1]), "+v"(v[1][2]), "+v"(v[1][3]) :: "memory");
#pragma unroll
                for (int r = 0; r < 2; ++r) {
                    const f32x4 t = ((v[r][0] + v[r][1]) + v[r][2]) + v[r][3];
                    u32x2 w = {cvtpk(t[0], t[1]), cvtpk(t[2], t[3])};
                    *(u32x2*)(out + (size_t)(16 * (8 * mt + 2 * q + r) + (tid & 15)) * DM + 72 * ntc + 4 * (tid >> 4)) = w;
                }
            }
        }
        if (wid == 0) {
            asm volatile("s_waitcnt vmcnt(0)" ::: "memory");
            depart<4>(w2 + ntc * 128, dep2, lane);
        }
    }
    if (wid == 0) {
        asm volatile("s_waitcnt vmcnt(0)" ::: "memory");
        depart<8>(w1, dep1, lane);
    }
    TL(11);
    if (bid == 77 && wid == 0) { const unsigned long long d_ = (tl[PROBE_B] - tl[PROBE_A]) * 10; while (__builtin_amdgcn_s_memrealtime() - tl[11] < d_) __builtin_amdgcn_s_sleep(2); }
}

extern "C" void kernel_launch(void* const* d_in, const int* in_sizes, int n_in, void* d_out, int out_size,
                              void* d_ws, size_t ws_size, hipStream_t stream) {
    (void)hipFuncSetAttribute((const void*)gqa_kernel, hipFuncAttributeMaxDynamicSharedMemorySize, LDS_BYTES);
    gqa_kernel<<<NB * HKV, 512, LDS_BYTES, stream>>>((const u16*)d_in[0], (const u16*)d_in[1], (const u16*)d_in[2],
                                                     (const u16*)d_in[3], (const u16*)d_in[4], (char*)d_ws, (u16*)d_out);
}
```

```cpp
#define PROBE_A 0
#define PROBE_B 0
#include <hip/hip_runtime.h>
#include <hip/hip_bf16.h>
#include <stdint.h>

typedef short bf16x8 __attribute__((ext_vector_type(8)));
typedef short s16x4 __attribute__((ext_vector_type(4)));
typedef float f32x16 __attribute__((ext_vector_type(16)));
typedef float f32x4 __attribute__((ext_vector_type(4)));
typedef unsigned u32x4 __attribute__((ext_vector_type(4)));
typedef unsigned u32x2 __attribute__((ext_vector_type(2)));
typedef unsigned short u16;
#define LAS __attribute__((address_space(3)))
typedef __attribute__((address_space(1))) unsigned gu32;
#define RLX_AGENT __ATOMIC_RELAXED, __HIP_MEMORY_SCOPE_AGENT

constexpr int NB = 16, SQ = 64, DM = 576, HQ = 32, HKV = 16, SKV = 1024, DH = 128;
constexpr int NQ = HQ * DH;
constexpr int NT = SKV / 64;
constexpr int REG = 40960;
constexpr int LDS_BYTES = 4 * REG;
constexpr size_t PART_OFF = (size_t)NB * SQ * NQ * 2;
constexpr size_t CNT_OFF = (size_t)20 << 20;
#define TMO 5000ull
#define TMO2 5000ull
constexpr int XS0 = 5, XS1 = 7, YS0 = 5, YS1 = 7;
constexpr int PACE = 108;
constexpr float RTHR = 8.0f;
constexpr float C2 = 0.088388347648318447f * 1.4426950408889634f;

#define MFMA32(a, b, c) __builtin_amdgcn_mfma_f32_32x32x16_bf16(a, b, c, 0, 0, 0)

typedef __bf16 bf16x2_t __attribute__((ext_vector_type(2)));
typedef float f32x2 __attribute__((ext_vector_type(2)));
__device__ __forceinline__ unsigned cvtpk(float lo, float hi) {
    f32x2 v = {lo, hi};
    return __builtin_bit_cast(unsigned, __builtin_convertvector(v, bf16x2_t));
}
__device__ __forceinline__ void glds16(const void* g, unsigned lds_base) {
    unsigned sv;
    asm volatile("s_mov_b32 %0, m0\n\ts_mov_b32 m0, %2\n\ts_nop 0\n\tglobal_load_lds_dwordx4 %1, off\n\ts_mov_b32 m0, %0"
                 : "=&s"(sv) : "v"(g), "s"(lds_base) : "memory");
}
__device__ __forceinline__ s16x4 vtr(unsigned a) {
    return __builtin_amdgcn_ds_read_tr16_b64_v4i16((LAS s16x4*)(uintptr_t)a);
}
__device__ __forceinline__ bf16x8 ldsr128(unsigned a) { return *(const LAS bf16x8*)(uintptr_t)a; }
__device__ __forceinline__ float swap_max(float v) {
    auto rr = __builtin_amdgcn_permlane32_swap(__float_as_uint(v), __float_as_uint(v), false, false);
    return __builtin_fmaxf(__uint_as_float(rr[0]), __uint_as_float(rr[1]));
}
__device__ __forceinline__ float max3f(float a, float b, float c) { float r; asm("v_max3_f32 %0, %1, %2, %3" : "=v"(r) : "v"(a), "v"(b), "v"(c)); return r; }
__device__ __forceinline__ float fmul1(float a, float b) { float r; asm("v_mul_f32_e32 %0, %1, %2" : "=v"(r) : "s"(b), "v"(a)); return r; }
__device__ __forceinline__ float swap_max3(float v) {
    auto rr = __builtin_amdgcn_permlane32_swap(__float_as_uint(v), __float_as_uint(v), false, false);
    return max3f(__uint_as_float(rr[0]), __uint_as_float(rr[1]), __uint_as_float(rr[1]));
}
__device__ __forceinline__ float swap_add(float v) {
    auto rr = __builtin_amdgcn_permlane32_swap(__float_as_uint(v), __float_as_uint(v), false, false);
    return __uint_as_float(rr[0]) + __uint_as_float(rr[1]);
}

#define TL(i) tl[i] = __builtin_amdgcn_s_memrealtime()
extern "C" __device__ unsigned long long amd_dispatch_id() __asm("llvm.amdgcn.dispatch.id");

template <unsigned N>
__device__ __forceinline__ unsigned rendezvous(const char* w, unsigned tag, unsigned own, int lane, unsigned long long tmo, unsigned& dep) {
    unsigned old = 0, todo = 1u << own;
    u32x2 v;
    if (lane == 0) asm volatile("global_atomic_add %0, %1, %2, off sc0" : "=v"(old) : "v"(w), "v"(1u) : "memory");
    asm volatile("global_load_dwordx2 %0, %1, off sc1" : "=v"(v) : "v"(w) : "memory");
    asm volatile("s_waitcnt vmcnt(0)" : "+v"(old), "+v"(v) :: "memory");
    old = __builtin_amdgcn_readfirstlane(old);
    if ((old & 255u) == N - 1) todo |= (old >> 8) & 255u;
    else {
        const unsigned long long t0 = __builtin_amdgcn_s_memrealtime();
        while ((__builtin_amdgcn_readfirstlane(v.x) & 255u) != N || __builtin_amdgcn_readfirstlane(v.y) != tag) {
            if (__builtin_amdgcn_s_memrealtime() - t0 > tmo) {
                unsigned o2 = 0;
                if (lane == 0) asm volatile("global_atomic_or %0, %1, %2, off sc0" : "=v"(o2) : "v"(w), "v"(256u << own) : "memory");
                asm volatile("s_waitcnt vmcnt(0)" : "+v"(o2) :: "memory");
                if ((__builtin_amdgcn_readfirstlane(o2) & 255u) != N) todo = 0;
                break;
            }
            asm volatile("global_load_dwordx2 %0, %1, off sc1\n\ts_waitcnt vmcnt(0)" : "=v"(v) : "v"(w) : "memory");
        }
    }
    dep = 0;
    if (lane == 0) asm volatile("global_atomic_add %0, %1, %2, off sc0" : "=v"(dep) : "v"(w), "v"(65536u) : "memory");
    return todo;
}
template <unsigned N>
__device__ __forceinline__ void depart(const char* w, unsigned dep, int lane) {
    asm volatile("" : "+v"(dep));
    if (lane == 0 && ((dep >> 16) & 255u) == N - 1)
        asm volatile("global_store_dwordx2 %0, %1, off sc1" :: "v"(w), "v"(u32x2{0u, 0u}) : "memory");
}

#define MFMA16(a, b, c) __builtin_amdgcn_mfma_f32_16x16x32_bf16(a, b, c, 0, 0, 0)
__global__ __launch_bounds__(512, 2) void gqa_kernel(const u16* __restrict__ x, const u16* __restrict__ Kc,
                                                     const u16* __restrict__ Vc, const u16* __restrict__ wq,
                                                     const u16* __restrict__ wo, char* ws, u16* __restrict__ out) {
    extern __shared__ __attribute__((aligned(16))) char lds[];
    unsigned long long tl[13];
    TL(0);
    const unsigned lds0 = (unsigned)(uintptr_t)lds;
    const int tid = threadIdx.x, lane = tid & 63, r32 = lane & 31, hi = lane >> 5;
    const int wid = __builtin_amdgcn_readfirstlane(tid >> 6);
    const int qb = wid & 3, kh = wid >> 2;
    const int bid = blockIdx.x, b = 8 * (bid & 1) + ((bid >> 3) & 7), kvh = 4 * ((bid >> 1) & 3) + (bid >> 6);
    const int head = 2 * kvh + (qb >> 1);
    if (tid == 0 && bid < 8) asm volatile("global_store_dwordx2 %0, %1, off sc1" :: "v"(ws + CNT_OFF + 65536 + bid * 8), "v"(tl[0]) : "memory");
    const int mt = b >> 1, sk = kvh >> 2, nt = 4 * (b & 1) + (kvh & 3);
    const unsigned tag = 0x80000000u | ((unsigned)amd_dispatch_id() & 0xfffffu) |
                         ((((unsigned)((uintptr_t)__builtin_amdgcn_dispatch_ptr() >> 12) * 0x9e3779b1u) >> 21) << 20);
    const char* w1 = ws + CNT_OFF + (mt * 4 + sk) * 128;
    const char* w2 = ws + CNT_OFF + 4096 + mt * 8 * 128;
    const char* wcl = lane == 0 ? w1 : w2 + nt * 128;
    u32x2 clm = {0u, tag};
    if (wid == 1 && lane < 2)
        asm volatile("global_atomic_cmpswap_x2 %0, %1, %2, off sc0\n\ts_nop 1" : "=v"(clm) : "v"(wcl), "v"(u32x4{0u, tag, 0u, 0u}) : "memory");
    const int gi = lane >> 4, vq = (lane & 15) >> 2, vp = lane & 3;

    const unsigned vsrc = (16 * (wid >> 1) + ((lane >> 2) & 7)) * 256 + (wid & 1) * 128 + hi * 64;
    const unsigned voffV = vsrc + vp * 16;
    const unsigned voffK0 = vsrc + ((vp ^ (wid & 2)) << 4), voffK1 = vsrc + ((vp ^ ((wid & 2) | 1)) << 4);
    const char* Kg = (const char*)Kc + (size_t)(b * HKV + kvh) * SKV * DH * 2;
    const char* Vg = (const char*)Vc + (size_t)(b * HKV + kvh) * SKV * DH * 2;
    const unsigned dwo = lds0 + (wid >> 1) * 4096 + (wid & 1) * 1024;
#define KVDMA(voff, gp, dst, OFF) asm volatile("s_mov_b32 m0, %2\n\ts_nop 0\n\tglobal_load_lds_dwordx4 %0, %1 offset:" #OFF " sc1 nt" \
            :: "v"(voff), "s"(gp), "s"(dst) : "memory")
#define VSO(tn) ((int)((0x0200110au >> (8 * ((tn) & 3))) & 255) * 8192)
#define KSO(tn) ((int)((0x0f0c0705u >> (8 * ((tn) & 3))) & 255) * 8192)
#define DMA_V0(tn) KVDMA(voffV, Vg + (tn) * 16384, dwo + VSO(tn), 0)
#define DMA_V1(tn) KVDMA(voffV, Vg + (tn) * 16384, dwo + VSO(tn), 2048)
#define DMA_K0(tn) KVDMA(voffK0, Kg + (tn) * 16384, dwo + KSO(tn), 0)
#define DMA_K1(tn) KVDMA(voffK1, Kg + (tn) * 16384, dwo + KSO(tn), 2048)
#define DMA_V(tn) do { DMA_V0(tn); DMA_V1(tn); } while (0)
#define DMA_K(tn) do { DMA_K0(tn); DMA_K1(tn); } while (0)
    bf16x8 qf[8];
    {
        int kb = (b + 2 * (kvh & 3)) % 9;
        const unsigned xo = (8 * wid + (lane >> 3)) * DM * 2 + (((lane & 7) ^ ((4 * wid + (lane >> 4)) & 7)) << 4);
        const unsigned wo0 = (8 * wid + hi) * NQ * 2 + ((r32 ^ (hi << 2)) << 4);
        const unsigned wo1 = (8 * wid + 2 + hi) * NQ * 2 + ((r32 ^ ((2 + hi) << 2)) << 4);
        const unsigned wo2 = wo0 + 4 * NQ * 2, wo3 = wo1 + 4 * NQ * 2;
        const char* Xs = (const char*)x + (size_t)b * SQ * DM * 2;
        const char* Ws = (const char*)wq + kvh * 512;
#define QDMA(voff, gp, dst) asm volatile("s_mov_b32 m0, %2\n\ts_nop 0\n\tglobal_load_lds_dwordx4 %0, %1" :: "v"(voff), "s"(gp), "s"(dst) : "memory")
#define QS_PIECE(slot, p) do { const unsigned s_ = lds0 + (slot) * 40960 + wid * 4096; const char* w_ = Ws + (size_t)kb * 64 * NQ * 2; \
            if ((p) == 0) QDMA(wo0, w_, s_); \
            if ((p) == 1) QDMA(wo1, w_, s_ + 1024); \
            if ((p) == 2) QDMA(wo2, w_, s_ + 2048); \
            if ((p) == 3) QDMA(wo3, w_, s_ + 3072); \
            if ((p) == 4) { QDMA(xo, Xs + kb * 128, lds0 + (slot) * 40960 + 32768 + wid * 1024); kb = kb == 8 ? 0 : kb + 1; } } while (0)
#define QS_ISSUE(slot) do { QS_PIECE(slot, 0); QS_PIECE(slot, 1); QS_PIECE(slot, 2); QS_PIECE(slot, 3); QS_PIECE(slot, 4); } while (0)
#pragma unroll 1
        for (int j = 0; j < 2; ++j) { QS_ISSUE(j); asm volatile("s_barrier" ::: "memory"); }
        const int pp = ((vp & 1) << 1) | (vp >> 1);
        const unsigned abase = lds0 + (8 * hi + vq) * 512 + (((4 * wid + 2 * (gi & 1) + (pp >> 1)) ^ (vq << 2)) << 4) + 8 * (pp & 1);
        const unsigned xbase = lds0 + 32768 + r32 * 128 + ((hi ^ ((r32 >> 1) & 7)) << 4);
        f32x16 acc0 = {}, acc1 = {};
        s16x4 lo[2][4], h4[2][4]; bf16x8 x0[2][4], x1[2][4];
        asm volatile("s_waitcnt vmcnt(5)\n\ts_barrier" : "+v"(clm) :: "memory");
        QS_ISSUE(2);
        if (wid == 1 && lane < 2 && clm.y != tag && (clm.x | clm.y) != 0u)
            asm volatile("global_atomic_cmpswap_x2 %0, %1, off\n\ts_nop 1" :: "v"(wcl), "v"(u32x4{0u, tag, clm.x, clm.y}) : "memory");
#pragma unroll
        for (int i = 0; i < 4; ++i) {
            lo[0][i] = vtr(abase + i * 8192); h4[0][i] = vtr(abase + i * 8192 + 2048);
            x0[0][i] = ldsr128(xbase ^ (i << 5)); x1[0][i] = ldsr128((xbase ^ (i << 5)) + 4096);
        }
#pragma unroll
        for (int st = 0; st < 9; ++st) {
            if (st == 0) asm volatile("s_waitcnt vmcnt(5)" ::: "memory");
            else if (st < 6) asm volatile("s_waitcnt vmcnt(10)" ::: "memory");
            else if (st == 6) asm volatile("s_waitcnt vmcnt(9)" ::: "memory");
            else if (st == 7) asm volatile("s_waitcnt vmcnt(6)" ::: "memory");
            asm volatile("s_waitcnt lgkmcnt(0)\n\ts_barrier" ::: "memory");
            const unsigned so = ((st + 1) & 3) * 40960;
            const int cu = st & 1, nx = cu ^ 1;
            unsigned aa = abase + so;
            asm volatile("" : "+v"(aa));
            __builtin_amdgcn_sched_barrier(0);
            if (st == 0) QS_ISSUE(3);
#pragma unroll
            for (int i = 0; i < 4; ++i) {
                const bf16x8 a0 = {lo[cu][i][0], lo[cu][i][1], lo[cu][i][2], lo[cu][i][3], h4[cu][i][0], h4[cu][i][1], h4[cu][i][2], h4[cu][i][3]};
                acc0 = MFMA32(a0, x0[cu][i], acc0);
                if (st < 8) { lo[nx][i] = vtr(aa + i * 8192); h4[nx][i] = vtr(aa + i * 8192 + 2048); }
                if (st < 5) QS_PIECE(st & 3, i);
                __builtin_amdgcn_sched_barrier(0);
                acc1 = MFMA32(a0, x1[cu][i], acc1);
                if (st < 8) { unsigned xa = (xbase ^ (i << 5)) + so; asm volatile("" : "+v"(xa)); x0[nx][i] = ldsr128(xa); x1[nx][i] = ldsr128(xa + 4096); }
                if (st < 5 && i == 3) QS_PIECE(st & 3, 4);
                if (st == 5 && i == 3) { DMA_K(0); DMA_K(1); }
                if (st == 6 && i == 3) DMA_V(0);
                if (st == 7 && i == 3) DMA_K(2);
                if (st == 8 && i == 3) DMA_K(3);
                __builtin_amdgcn_sched_barrier(0);
            }
        }
#undef QS_ISSUE
#undef QS_PIECE
#define QW(ACC, qblk) do { _Pragma("unroll") for (int s = 0; s < 2; ++s) { \
            u32x4 w_ = {cvtpk(ACC[8 * s], ACC[8 * s + 1]), cvtpk(ACC[8 * s + 2], ACC[8 * s + 3]), \
                        cvtpk(ACC[8 * s + 4], ACC[8 * s + 5]), cvtpk(ACC[8 * s + 6], ACC[8 * s + 7])}; \
            *(LAS u32x4*)(uintptr_t)(lds0 + 32768 + (2 * (wid >> 2) + (qblk)) * REG + (2 * (wid & 3) + s) * 1024 + lane * 16) = w_; } } while (0)
        QW(acc0, 0); QW(acc1, 1);
#undef QW
        DMA_V(1);
    }
    asm volatile("s_waitcnt vmcnt(8) lgkmcnt(0)\n\ts_barrier" ::: "memory");
#pragma unroll
    for (int f = 0; f < 8; ++f) qf[f] = ldsr128(lds0 + 32768 + qb * REG + f * 1024 + lane * 16);
    TL(1);

    const unsigned kb0 = lds0 + (4 * kh + (r32 >> 3)) * 2048 + (r32 & 7) * 64 + ((hi ^ (r32 >> 3)) << 4);
    const unsigned kb1 = kb0 ^ 32;
    const unsigned vb = lds0 + kh * 8192 + (4 * hi + vq) * 64 + (gi & 1) * 32 + vp * 8;

    float m_run, l_run = 0.f, tmax = 0.f;
    unsigned long long tprev = 0;
    f32x16 o[4] = {};
    f32x16 sA, sB, negm;

    bf16x8 kf[8];
    {
        bf16x8 k0[8];
        unsigned ka0 = kb0 + KSO(0), ka1 = kb1 + KSO(0);
        asm volatile("" : "+v"(ka0), "+v"(ka1));
#pragma unroll
        for (int d0 = 0; d0 < 8; ++d0) k0[d0] = ldsr128(((d0 & 1) ? ka1 : ka0) + (d0 >> 1) * 512);
#pragma unroll
        for (int d0 = 0; d0 < 8; ++d0) kf[d0] = ldsr128(((d0 & 1) ? ka1 : ka0) + 16384 + (d0 >> 1) * 512);
        __builtin_amdgcn_sched_barrier(0);
        asm volatile("s_waitcnt lgkmcnt(0)\n\ts_barrier" ::: "memory");
        sA = f32x16{};
#pragma unroll
        for (int d0 = 0; d0 < 8; ++d0) sA = MFMA32(k0[d0], qf[d0], sA);
        __builtin_amdgcn_sched_barrier(0);
        DMA_V(2); DMA_K(4);
        m_run = sA[0];
#pragma unroll
        for (int r = 1; r < 16; ++r) m_run = __builtin_fmaxf(m_run, sA[r]);
        m_run = swap_max(m_run);
#pragma unroll
        for (int r = 0; r < 16; ++r) { sA[r] -= m_run; negm[r] = -m_run; }
    }
#define EXP1(r) do { sc[r] = __builtin_amdgcn_exp2f(fmul1(sc[r], C2)); } while (0)
#define SUM1(k) do { ps0 += sc[2 * (k)]; ps1 += sc[2 * (k) + 1]; pw[k] = cvtpk(sc[2 * (k)], sc[2 * (k) + 1]); asm volatile("" : "+v"(pw[k]), "+v"(ps0), "+v"(ps1)); } while (0)
#define STEP(t, SC_, SN_, TAIL) do { \
        f32x16& sc = SC_; f32x16& sn = SN_; \
        s16x4 vlo[8], vhi[8]; \
        if (!TAIL || (t) < 12 || (t) == 14) asm volatile("s_waitcnt vmcnt(8)" ::: "memory"); \
        else if ((t) < 14) asm volatile("s_waitcnt vmcnt(6)" ::: "memory"); \
        else asm volatile("s_waitcnt vmcnt(10)" ::: "memory"); \
        while (__builtin_amdgcn_s_memrealtime() - tprev < (unsigned long long)PACE) __builtin_amdgcn_s_sleep(1); \
        asm volatile("s_waitcnt lgkmcnt(0)\n\ts_barrier" ::: "memory"); \
        tprev = __builtin_amdgcn_s_memrealtime(); \
        if (__builtin_expect(__any(tmax > RTHR / C2), 0)) { \
            const float dl = __builtin_fmaxf(tmax, 0.f); \
            const float alpha = __builtin_amdgcn_exp2f(-dl * C2); \
            m_run += dl; l_run *= alpha; \
            _Pragma("unroll") for (int r = 0; r < 16; ++r) { sc[r] -= dl; negm[r] = -m_run; } \
            _Pragma("unroll") for (int dt = 0; dt < 4; ++dt) \
                _Pragma("unroll") for (int r = 0; r < 16; ++r) o[dt][r] *= alpha; \
        } \
        const unsigned vwr = dwo + vso, kwr = dwo + kso;                         \
        vso = VSO(t); kso = KSO((t) + 2); \
        const unsigned va = vb + vso; \
        const unsigned ka0 = kb0 + kso, ka1 = kb1 + kso;                         \
        unsigned pw[8]; \
        float ps0 = 0.f, ps1 = 0.f, t2; \
        __builtin_amdgcn_sched_barrier(0); \
          \
        _Pragma("unroll") for (int i = 0; i < 8; ++i) { \
            sn = MFMA32(kf[i], qf[i], i == 0 ? negm : sn);                   \
            const int ss = i >> 2, dt = i & 3; \
            vlo[2 * dt + ss] = vtr(va + ss * 4096 + dt * 512); \
            vhi[2 * dt + ss] = vtr(va + ss * 4096 + 2048 + dt * 512); \
            if (i < 4) { EXP1(2 * i); EXP1(2 * i + 1); } else EXP1(4 + i); \
            if (i >= 1 && i <= 4) SUM1(i - 1); \
            if (i == 6) SUM1(4); \
            asm volatile("" : "+v"(sn), "+v"(sc)); \
            if (i == XS0 && (!TAIL || (t) < 13)) KVDMA(voffV, Vg + ((t) + 3) * 16384, vwr, 0); \
            if (i == XS1 && (!TAIL || (t) < 13)) KVDMA(voffV, Vg + ((t) + 3) * 16384, vwr, 2048); \
            if (i == XS0 && TAIL && (t) == 13) BDMA(Bs, 0, 2); \
            if (i == XS1 && TAIL && (t) == 13) BDMA(Bs, 0, 3); \
            if (i == XS0 && TAIL && (t) == 14) BDMA(Bs, 1, 1); \
            if (i == XS1 && TAIL && (t) == 14) BDMA(Bs, 1, 2); \
            __builtin_amdgcn_sched_barrier(0); \
        } \
          \
        _Pragma("unroll") for (int i = 0; i < 8; ++i) { \
            const int ss = i >> 2, dt = i & 3; \
            const s16x4 lo = vlo[2 * dt + ss], hi4 = vhi[2 * dt + ss]; \
            const bf16x8 vf = {lo[0], lo[1], lo[2], lo[3], hi4[0], hi4[1], hi4[2], hi4[3]}; \
            const bf16x8 pf = __builtin_bit_cast(bf16x8, u32x4{pw[4 * ss], pw[4 * ss + 1], pw[4 * ss + 2], pw[4 * ss + 3]}); \
            o[dt] = MFMA32(vf, pf, o[dt]); \
            if (i < 4) { kf[2 * i] = ldsr128(ka0 + i * 512); kf[2 * i + 1] = ldsr128(ka1 + i * 512); } \
            if (i < 2) { EXP1(12 + 2 * i); EXP1(13 + 2 * i); asm volatile("" : "+v"(sc)); } \
            if (i < 3) SUM1(5 + i); \
            if (i == 3) { l_run += ps0 + ps1; asm volatile("" : "+v"(sn), "+v"(l_run)); \
                          tmax = max3f(sn[0], sn[1], sn[2]); t2 = max3f(sn[3], sn[4], sn[5]); asm volatile("" : "+v"(tmax), "+v"(t2)); } \
            if (i == 4) { tmax = max3f(tmax, sn[6], sn[7]); t2 = max3f(t2, sn[8], sn[9]); tmax = max3f(tmax, sn[10], sn[11]); asm volatile("" : "+v"(tmax), "+v"(t2)); } \
            if (i == 5) { t2 = max3f(t2, sn[12], sn[13]); tmax = max3f(tmax, sn[14], sn[15]); tmax = max3f(tmax, t2, t2); asm volatile("" : "+v"(tmax)); } \
            if (i == 6) { tmax = swap_max3(tmax); asm volatile("" : "+v"(tmax)); } \
            if (i == YS0 && (!TAIL || (t) < 11)) KVDMA(voffK0, Kg + ((t) + 5) * 16384, kwr, 0); \
            if (i == YS1 && (!TAIL || (t) < 11)) KVDMA(voffK1, Kg + ((t) + 5) * 16384, kwr, 2048); \
            if (i == YS0 && TAIL && (t) == 12) BDMA(Bs, 0, 0); \
            if (i == YS1 && TAIL && (t) == 12) BDMA(Bs, 0, 1); \
            if (i == YS0 && TAIL && (t) == 13) BDMA(Bs, 0, 4); \
            if (i == YS1 && TAIL && (t) == 13) BDMA(Bs, 1, 0); \
            if (i == YS0 && TAIL && (t) == 14) BDMA(Bs, 1, 3); \
            if (i == YS1 && TAIL && (t) == 14) BDMA(Bs, 1, 4); \
            __builtin_amdgcn_sched_barrier(0); \
        } } while (0)
    unsigned boff[5];
    const char* Bw = (const char*)wo + (size_t)(1024 * sk + 32 * wid) * DM * 2;
    const char* Bs = Bw + 144 * nt;
#define BDMA(bs, c, i) QDMA(boff[i], (bs) + (c) * (256 * DM * 2), lds0 + (((c) + 1) & 3) * REG + wid * 5120 + (i) * 1024)
    unsigned vso = VSO(3), kso = KSO(1);
#pragma unroll 1
    for (int t = 0; t < 10; t += 2) { STEP(t, sA, sB, false); STEP(t + 1, sB, sA, false); }
    {
        unsigned ln = lane;
        asm volatile("" : "+v"(ln));
#pragma unroll
        for (int i = 0; i < 5; ++i) {
            const unsigned n = 64 * i + ln, row = (n * 205u) >> 11, c = n - 10 * row;
            boff[i] = row * (DM * 2) + (c < 9 ? c : 0u) * 16;
        }
    }
#pragma unroll 1
    for (int t = 10; t < NT; t += 2) { STEP(t, sA, sB, true); STEP(t + 1, sB, sA, true); }
#undef STEP
#undef EXP1
#undef SUM1
    TL(2);
    l_run = swap_add(l_run);
    asm volatile("s_waitcnt lgkmcnt(0)\n\ts_barrier" ::: "memory");
    if (wid == 0) {
#pragma unroll
        for (int i = 0; i < 10; ++i) BDMA(Bs, 2 + i / 5, i % 5);
    }
    {
        f32x16 keep0, keep1, send0, send1;
        if (kh == 0) { keep0 = o[0]; keep1 = o[1]; send0 = o[2]; send1 = o[3]; }
        else         { keep0 = o[2]; keep1 = o[3]; send0 = o[0]; send1 = o[1]; }
        const unsigned xb = lds0 + (kh ? 0 : 3 * REG) + 5120 + qb * 8704;
#pragma unroll
        for (int r4 = 0; r4 < 4; ++r4) {
            f32x4 v0 = {send0[4 * r4], send0[4 * r4 + 1], send0[4 * r4 + 2], send0[4 * r4 + 3]};
            f32x4 v1 = {send1[4 * r4], send1[4 * r4 + 1], send1[4 * r4 + 2], send1[4 * r4 + 3]};
            *(LAS f32x4*)(uintptr_t)(xb + r4 * 1024 + lane * 16) = v0;
            *(LAS f32x4*)(uintptr_t)(xb + (4 + r4) * 1024 + lane * 16) = v1;
        }
        *(LAS float*)(uintptr_t)(xb + 8192 + lane * 4) = m_run;
        *(LAS float*)(uintptr_t)(xb + 8192 + 256 + lane * 4) = l_run;
        asm volatile("s_waitcnt lgkmcnt(0)\n\ts_barrier" ::: "memory");
        const unsigned pb = lds0 + (kh ? 3 * REG : 0) + 5120 + qb * 8704;
        const float m2 = *(const LAS float*)(uintptr_t)(pb + 8192 + lane * 4);
        const float l2 = *(const LAS float*)(uintptr_t)(pb + 8192 + 256 + lane * 4);
        const float mm = __builtin_fmaxf(m_run, m2);
        const float a1 = __builtin_amdgcn_exp2f((m_run - mm) * C2), a2 = __builtin_amdgcn_exp2f((m2 - mm) * C2);
        const float inv = 1.0f / (l_run * a1 + l2 * a2);
        const float w1 = a1 * inv, w2 = a2 * inv;
        const int row = b * SQ + 32 * (qb & 1) + r32;
        char* ob = ws + ((size_t)((row >> 4) * 128 + head * 4 + 2 * kh) * 64 + hi * 16 + (row & 15)) * 16;
#define OSTORE(KEEP, i) do { u32x2 w_[4]; \
            _Pragma("unroll") for (int r4 = 0; r4 < 4; ++r4) { \
                const f32x4 v = *(const LAS f32x4*)(uintptr_t)(pb + (4 * (i) + r4) * 1024 + lane * 16); \
                const float e0 = KEEP[4 * r4] * w1 + v[0] * w2, e1 = KEEP[4 * r4 + 1] * w1 + v[1] * w2; \
                const float e2 = KEEP[4 * r4 + 2] * w1 + v[2] * w2, e3 = KEEP[4 * r4 + 3] * w1 + v[3] * w2; \
                w_[r4] = u32x2{cvtpk(e0, e1), cvtpk(e2, e3)}; } \
            _Pragma("unroll") for (int g = 0; g < 2; ++g) { \
                u32x4 o4 = {w_[g].x, w_[g].y, w_[g + 2].x, w_[g + 2].y}; \
                asm volatile("global_store_dwordx4 %0, %1, off sc1\n\ts_nop 1" :: "v"(ob + (i) * 1024 + g * 512), "v"(o4) : "memory"); } } while (0)
        OSTORE(keep0, 0); OSTORE(keep1, 1);
#undef OSTORE
    }
    TL(3);
    const char* Ag = ws + (size_t)((8 * mt + 2 * qb) * 128 + 32 * sk + kh) * 1024;
    unsigned ao0 = lane * 16, ao1 = ao0 + 131072;
    unsigned bb[4];
#pragma unroll
    for (int c = 0; c < 4; ++c) { bb[c] = lds0 + ((c + 1) & 3) * REG + (32 * kh + 4 * gi + vq) * 160 + 8 * vp; asm volatile("" : "+v"(bb[c])); }
    asm volatile("" : "+v"(ao0), "+v"(ao1), "+s"(Ag));
    asm volatile("s_waitcnt vmcnt(0) lgkmcnt(0)\n\ts_barrier" ::: "memory");
    TL(4);
    unsigned dep1 = 0;
    if (wid == 0) {
        const unsigned td = rendezvous<8>(w1, tag, nt, lane, TMO, dep1);
        if (lane == 0) *(LAS unsigned*)(uintptr_t)(lds0 + REG + 144) = td;
    } else {
#pragma unroll
        for (int i = 0; i < 10; ++i) BDMA(Bs, 2 + i / 5, i % 5);
    }
    asm volatile("s_waitcnt lgkmcnt(0)\n\ts_barrier" ::: "memory");
    unsigned todo = __builtin_amdgcn_readfirstlane(*(const LAS unsigned*)(uintptr_t)(lds0 + REG + 144));
    TL(5);
    float* part = (float*)(ws + PART_OFF);
    while (todo) {
        const int ntc = ((todo >> nt) & 1u) ? nt : __builtin_ctz(todo);
        todo &= ~(1u << ntc);
        if (ntc != nt) {
            asm volatile("s_waitcnt vmcnt(0) lgkmcnt(0)\n\ts_barrier" ::: "memory");
#pragma unroll 1
            for (int c = 0; c < 4; ++c) {
#pragma unroll
                for (int i = 0; i < 5; ++i) BDMA(Bw + 144 * ntc, c, i);
            }
            asm volatile("s_waitcnt vmcnt(0)\n\ts_barrier" ::: "memory");
        }
        u32x4 an[32];
#pragma unroll
        for (int s2 = 0; s2 < 8; ++s2) {
            const char* ag = Ag;
            asm volatile("" : "+s"(ag));
            ag += s2 * 4096;
            asm volatile("global_load_dwordx4 %0, %1, %2 sc1" : "=v"(an[4 * s2]) : "v"(ao0), "s"(ag) : "memory");
            asm volatile("global_load_dwordx4 %0, %1, %2 sc1" : "=v"(an[4 * s2 + 1]) : "v"(ao1), "s"(ag) : "memory");
            asm volatile("global_load_dwordx4 %0, %1, %2 offset:2048 sc1" : "=v"(an[4 * s2 + 2]) : "v"(ao0), "s"(ag) : "memory");
            asm volatile("global_load_dwordx4 %0, %1, %2 offset:2048 sc1" : "=v"(an[4 * s2 + 3]) : "v"(ao1), "s"(ag) : "memory");
        }
        f32x4 acc[2][5];
#pragma unroll
        for (int ct = 0; ct < 5; ++ct) { acc[0][ct] = f32x4{0.f, 0.f, 0.f, 0.f}; acc[1][ct] = f32x4{0.f, 0.f, 0.f, 0.f}; }
        s16x4 blo[2][5], bhi[2][5];
#pragma unroll
        for (int ct = 0; ct < 5; ++ct) { blo[0][ct] = vtr(bb[0] + 32 * ct); bhi[0][ct] = vtr(bb[0] + 32 * ct + 2560); }
#pragma unroll
        for (int s = 0; s < 16; ++s) {
            asm volatile("s_waitcnt vmcnt(%2)" : "+v"(an[2 * s]), "+v"(an[2 * s + 1]) : "n"(30 - 2 * s) : "memory");
            if (s == 0) { TL(6); }
            if (s == 7) asm volatile("s_barrier" ::: "memory");
            __builtin_amdgcn_sched_barrier(0);
#pragma unroll
            for (int ct = 0; ct < 5; ++ct) {
                if (s < 15) {
                    const unsigned ad = bb[((s + 1) >> 2) & 3] + ((s + 1) & 3) * 10240 + 32 * ct;
                    blo[(s + 1) & 1][ct] = vtr(ad); bhi[(s + 1) & 1][ct] = vtr(ad + 2560);
                }
                const s16x4 lo = blo[s & 1][ct], h4 = bhi[s & 1][ct];
                const bf16x8 bf = {lo[0], lo[1], lo[2], lo[3], h4[0], h4[1], h4[2], h4[3]};
                acc[0][ct] = MFMA16(bf, __builtin_bit_cast(bf16x8, an[2 * s]), acc[0][ct]);
                acc[1][ct] = MFMA16(bf, __builtin_bit_cast(bf16x8, an[2 * s + 1]), acc[1][ct]);
                __builtin_amdgcn_sched_barrier(0);
            }
        }
        TL(7);
#pragma unroll
        for (int ct = 0; ct < 5; ++ct)
            *(LAS f32x4*)(uintptr_t)(lds0 + REG + (wid * 5 + ct) * 1024 + lane * 16) = kh ? acc[0][ct] : acc[1][ct];
        asm volatile("s_waitcnt lgkmcnt(0)\n\ts_barrier" ::: "memory");
        float* pp = part + ((size_t)(sk * 64 + 8 * mt + 2 * qb + kh) * 8 + ntc) * 1152 + lane * 4;
#pragma unroll
        for (int ct = 0; ct < 5; ++ct) {
            const f32x4 v = *(const LAS f32x4*)(uintptr_t)(lds0 + REG + ((wid ^ 4) * 5 + ct) * 1024 + lane * 16);
            const f32x4 r_ = (kh ? acc[1][ct] : acc[0][ct]) + v;
            if (ct < 4 || lane < 32)
                asm volatile("global_store_dwordx4 %0, %1, off sc1\n\ts_nop 1" :: "v"(pp + ct * 256), "v"(r_) : "memory");
        }
        TL(8);
        asm volatile("s_waitcnt vmcnt(0)\n\ts_barrier" ::: "memory");
        TL(9);
        unsigned dep2 = 0;
        if (wid == 0) {
            const unsigned td = rendezvous<4>(w2 + ntc * 128, tag, sk, lane, TMO2, dep2);
            if (lane == 0) *(LAS unsigned*)(uintptr_t)(lds0 + 2 * REG) = td;
        }
        asm volatile("s_waitcnt lgkmcnt(0)\n\ts_barrier" ::: "memory");
        unsigned qs = __builtin_amdgcn_readfirstlane(*(const LAS unsigned*)(uintptr_t)(lds0 + 2 * REG));
        TL(10);
        while (qs) {
            const int q = __builtin_ctz(qs);
            qs &= qs - 1;
            if (tid < 288) {
                const float* p = part + ((size_t)(8 * mt + 2 * q) * 8 + ntc) * 1152 + tid * 4;
                f32x4 v[2][4];
#pragma unroll
                for (int r = 0; r < 2; ++r)
#pragma unroll
                    for (int s = 0; s < 4; ++s)
                        asm volatile("global_load_dwordx4 %0, %1, off sc1" : "=v"(v[r][s]) : "v"(p + (size_t)s * 64 * 8 * 1152 + r * 8 * 1152) : "memory");
                asm volatile("s_waitcnt vmcnt(0)" : "+v"(v[0][0]), "+v"(v[0][1]), "+v"(v[0][2]), "+v"(v[0][3]),
                                                    "+v"(v[1][0]), "+v"(v[1][1]), "+v"(v[1][2]), "+v"(v[1][3]) :: "memory");
#pragma unroll
                for (int r = 0; r < 2; ++r) {
                    const f32x4 t = ((v[r][0] + v[r][1]) + v[r][2]) + v[r][3];
                    u32x2 w = {cvtpk(t[0], t[1]), cvtpk(t[2], t[3])};
                    *(u32x2*)(out + (size_t)(16 * (8 * mt + 2 * q + r) + (tid & 15)) * DM + 72 * ntc + 4 * (tid >> 4)) = w;
                }
            }
        }
        if (wid == 0) {
            asm volatile("s_waitcnt vmcnt(0)" ::: "memory");
            depart<4>(w2 + ntc * 128, dep2, lane);
        }
    }
    if (wid == 0) {
        asm volatile("s_waitcnt vmcnt(0)" ::: "memory");
        depart<8>(w1, dep1, lane);
    }
    TL(11);
    if (bid == 77 && wid == 0) { unsigned long long mn_ = ~0ull, mx_ = 0;
        for (int i_ = 0; i_ < 8; ++i_) { unsigned long long v_; asm volatile("global_load_dwordx2 %0, %1, off sc1\n\ts_waitcnt vmcnt(0)" : "=v"(v_) : "v"(ws + CNT_OFF + 65536 + i_ * 8) : "memory");
            mn_ = v_ < mn_ ? v_ : mn_; mx_ = v_ > mx_ ? v_ : mx_; }
        unsigned long long d_ = (PROBE_B == 0 ? mx_ - mn_ : tl[11] - mn_) * 10; d_ = d_ > 100000ull ? 100000ull : d_; while (__builtin_amdgcn_s_memrealtime() - tl[11] < d_) __builtin_amdgcn_s_sleep(2); }
}

extern "C" void kernel_launch(void* const* d_in, const int* in_sizes, int n_in, void* d_out, int out_size,
                              void* d_ws, size_t ws_size, hipStream_t stream) {
    (void)hipFuncSetAttribute((const void*)gqa_kernel, hipFuncAttributeMaxDynamicSharedMemorySize, LDS_BYTES);
    gqa_kernel<<<NB * HKV, 512, LDS_BYTES, stream>>>((const u16*)d_in[0], (const u16*)d_in[1], (const u16*)d_in[2],
                                                     (const u16*)d_in[3], (const u16*)d_in[4], (char*)d_ws, (u16*)d_out);
}
```

```cpp
#define PROBE_A 11
#define PROBE_B 11
#define PROBE_M 2
#include <hip/hip_runtime.h>
#include <hip/hip_bf16.h>
#include <stdint.h>

typedef short bf16x8 __attribute__((ext_vector_type(8)));
typedef short s16x4 __attribute__((ext_vector_type(4)));
typedef float f32x16 __attribute__((ext_vector_type(16)));
typedef float f32x4 __attribute__((ext_vector_type(4)));
typedef unsigned u32x4 __attribute__((ext_vector_type(4)));
typedef unsigned u32x2 __attribute__((ext_vector_type(2)));
typedef unsigned short u16;
#define LAS __attribute__((address_space(3)))
typedef __attribute__((address_space(1))) unsigned gu32;
#define RLX_AGENT __ATOMIC_RELAXED, __HIP_MEMORY_SCOPE_AGENT

constexpr int NB = 16, SQ = 64, DM = 576, HQ = 32, HKV = 16, SKV = 1024, DH = 128;
constexpr int NQ = HQ * DH;
constexpr int NT = SKV / 64;
constexpr int REG = 40960;
constexpr int LDS_BYTES = 4 * REG;
constexpr size_t PART_OFF = (size_t)NB * SQ * NQ * 2;
constexpr size_t CNT_OFF = (size_t)20 << 20;
#define TMO 5000ull
#define TMO2 5000ull
constexpr int XS0 = 5, XS1 = 7, YS0 = 5, YS1 = 7;
constexpr int PACE = 108;
constexpr float RTHR = 8.0f;
constexpr float C2 = 0.088388347648318447f * 1.4426950408889634f;

#define MFMA32(a, b, c) __builtin_amdgcn_mfma_f32_32x32x16_bf16(a, b, c, 0, 0, 0)

typedef __bf16 bf16x2_t __attribute__((ext_vector_type(2)));
typedef float f32x2 __attribute__((ext_vector_type(2)));
__device__ __forceinline__ unsigned cvtpk(float lo, float hi) {
    f32x2 v = {lo, hi};
    return __builtin_bit_cast(unsigned, __builtin_convertvector(v, bf16x2_t));
}
__device__ __forceinline__ void glds16(const void* g, unsigned lds_base) {
    unsigned sv;
    asm volatile("s_mov_b32 %0, m0\n\ts_mov_b32 m0, %2\n\ts_nop 0\n\tglobal_load_lds_dwordx4 %1, off\n\ts_mov_b32 m0, %0"
                 : "=&s"(sv) : "v"(g), "s"(lds_base) : "memory");
}
__device__ __forceinline__ s16x4 vtr(unsigned a) {
    return __builtin_amdgcn_ds_read_tr16_b64_v4i16((LAS s16x4*)(uintptr_t)a);
}
__device__ __forceinline__ bf16x8 ldsr128(unsigned a) { return *(const LAS bf16x8*)(uintptr_t)a; }
__device__ __forceinline__ float swap_max(float v) {
    auto rr = __builtin_amdgcn_permlane32_swap(__float_as_uint(v), __float_as_uint(v), false, false);
    return __builtin_fmaxf(__uint_as_float(rr[0]), __uint_as_float(rr[1]));
}
__device__ __forceinline__ float max3f(float a, float b, float c) { float r; asm("v_max3_f32 %0, %1, %2, %3" : "=v"(r) : "v"(a), "v"(b), "v"(c)); return r; }
__device__ __forceinline__ float fmul1(float a, float b) { float r; asm("v_mul_f32_e32 %0, %1, %2" : "=v"(r) : "s"(b), "v"(a)); return r; }
__device__ __forceinline__ float swap_max3(float v) {
    auto rr = __builtin_amdgcn_permlane32_swap(__float_as_uint(v), __float_as_uint(v), false, false);
    return max3f(__uint_as_float(rr[0]), __uint_as_float(rr[1]), __uint_as_float(rr[1]));
}
__device__ __forceinline__ float swap_add(float v) {
    auto rr = __builtin_amdgcn_permlane32_swap(__float_as_uint(v), __float_as_uint(v), false, false);
    return __uint_as_float(rr[0]) + __uint_as_float(rr[1]);
}

#define TL(i) tl[i] = __builtin_amdgcn_s_memrealtime()
extern "C" __device__ unsigned long long amd_dispatch_id() __asm("llvm.amdgcn.dispatch.id");

template <unsigned N>
__device__ __forceinline__ unsigned rendezvous(const char* w, unsigned tag, unsigned own, int lane, unsigned long long tmo, unsigned& dep) {
    unsigned old = 0, todo = 1u << own;
    u32x2 v;
    if (lane == 0) asm volatile("global_atomic_add %0, %1, %2, off sc0" : "=v"(old) : "v"(w), "v"(1u) : "memory");
    asm volatile("global_load_dwordx2 %0, %1, off sc1" : "=v"(v) : "v"(w) : "memory");
    asm volatile("s_waitcnt vmcnt(0)" : "+v"(old), "+v"(v) :: "memory");
    old = __builtin_amdgcn_readfirstlane(old);
    if ((old & 255u) == N - 1) todo |= (old >> 8) & 255u;
    else {
        const unsigned long long t0 = __builtin_amdgcn_s_memrealtime();
        while ((__builtin_amdgcn_readfirstlane(v.x) & 255u) != N || __builtin_amdgcn_readfirstlane(v.y) != tag) {
            if (__builtin_amdgcn_s_memrealtime() - t0 > tmo) {
                unsigned o2 = 0;
                if (lane == 0) asm volatile("global_atomic_or %0, %1, %2, off sc0" : "=v"(o2) : "v"(w), "v"(256u << own) : "memory");
                asm volatile("s_waitcnt vmcnt(0)" : "+v"(o2) :: "memory");
                if ((__builtin_amdgcn_readfirstlane(o2) & 255u) != N) todo = 0;
                break;
            }
            asm volatile("global_load_dwordx2 %0, %1, off sc1\n\ts_waitcnt vmcnt(0)" : "=v"(v) : "v"(w) : "memory");
        }
    }
    dep = 0;
    if (lane == 0) asm volatile("global_atomic_add %0, %1, %2, off sc0" : "=v"(dep) : "v"(w), "v"(65536u) : "memory");
    return todo;
}
template <unsigned N>
__device__ __forceinline__ void depart(const char* w, unsigned dep, int lane) {
    asm volatile("" : "+v"(dep));
    if (lane == 0 && ((dep >> 16) & 255u) == N - 1)
        asm volatile("global_store_dwordx2 %0, %1, off sc1" :: "v"(w), "v"(u32x2{0u, 0u}) : "memory");
}

#define MFMA16(a, b, c) __builtin_amdgcn_mfma_f32_16x16x32_bf16(a, b, c, 0, 0, 0)
__global__ __launch_bounds__(512, 2) void gqa_kernel(const u16* __restrict__ x, const u16* __restrict__ Kc,
                                                     const u16* __restrict__ Vc, const u16* __restrict__ wq,
                                                     const u16* __restrict__ wo, char* ws, u16* __restrict__ out) {
    extern __shared__ __attribute__((aligned(16))) char lds[];
    unsigned long long tl[13];
    TL(0);
    const unsigned lds0 = (unsigned)(uintptr_t)lds;
    const int tid = threadIdx.x, lane = tid & 63, r32 = lane & 31, hi = lane >> 5;
    const int wid = __builtin_amdgcn_readfirstlane(tid >> 6);
    const int qb = wid & 3, kh = wid >> 2;
    const int bid = blockIdx.x, b = 8 * (bid & 1) + ((bid >> 3) & 7), kvh = 4 * ((bid >> 1) & 3) + (bid >> 6);
    const int head = 2 * kvh + (qb >> 1);
    const int mt = b >> 1, sk = kvh >> 2, nt = 4 * (b & 1) + (kvh & 3);
    const unsigned tag = 0x80000000u | ((unsigned)amd_dispatch_id() & 0xfffffu) |
                         ((((unsigned)((uintptr_t)__builtin_amdgcn_dispatch_ptr() >> 12) * 0x9e3779b1u) >> 21) << 20);
    const char* w1 = ws + CNT_OFF + (mt * 4 + sk) * 128;
    const char* w2 = ws + CNT_OFF + 4096 + mt * 8 * 128;
    const char* wcl = lane == 0 ? w1 : w2 + nt * 128;
    u32x2 clm = {0u, tag};
    if (wid == 1 && lane < 2)
        asm volatile("global_atomic_cmpswap_x2 %0, %1, %2, off sc0\n\ts_nop 1" : "=v"(clm) : "v"(wcl), "v"(u32x4{0u, tag, 0u, 0u}) : "memory");
    const int gi = lane >> 4, vq = (lane & 15) >> 2, vp = lane & 3;

    const unsigned vsrc = (16 * (wid >> 1) + ((lane >> 2) & 7)) * 256 + (wid & 1) * 128 + hi * 64;
    const unsigned voffV = vsrc + vp * 16;
    const unsigned voffK0 = vsrc + ((vp ^ (wid & 2)) << 4), voffK1 = vsrc + ((vp ^ ((wid & 2) | 1)) << 4);
    const char* Kg = (const char*)Kc + (size_t)(b * HKV + kvh) * SKV * DH * 2;
    const char* Vg = (const char*)Vc + (size_t)(b * HKV + kvh) * SKV * DH * 2;
    const unsigned dwo = lds0 + (wid >> 1) * 4096 + (wid & 1) * 1024;
#define KVDMA(voff, gp, dst, OFF) asm volatile("s_mov_b32 m0, %2\n\ts_nop 0\n\tglobal_load_lds_dwordx4 %0, %1 offset:" #OFF " sc1 nt" \
            :: "v"(voff), "s"(gp), "s"(dst) : "memory")
#define VSO(tn) ((int)((0x0200110au >> (8 * ((tn) & 3))) & 255) * 8192)
#define KSO(tn) ((int)((0x0f0c0705u >> (8 * ((tn) & 3))) & 255) * 8192)
#define DMA_V0(tn) KVDMA(voffV, Vg + (tn) * 16384, dwo + VSO(tn), 0)
#define DMA_V1(tn) KVDMA(voffV, Vg + (tn) * 16384, dwo + VSO(tn), 2048)
#define DMA_K0(tn) KVDMA(voffK0, Kg + (tn) * 16384, dwo + KSO(tn), 0)
#define DMA_K1(tn) KVDMA(voffK1, Kg + (tn) * 16384, dwo + KSO(tn), 2048)
#define DMA_V(tn) do { DMA_V0(tn); DMA_V1(tn); } while (0)
#define DMA_K(tn) do { DMA_K0(tn); DMA_K1(tn); } while (0)
    bf16x8 qf[8];
    {
        int kb = (b + 2 * (kvh & 3)) % 9;
        const unsigned xo = (8 * wid + (lane >> 3)) * DM * 2 + (((lane & 7) ^ ((4 * wid + (lane >> 4)) & 7)) << 4);
        const unsigned wo0 = (8 * wid + hi) * NQ * 2 + ((r32 ^ (hi << 2)) << 4);
        const unsigned wo1 = (8 * wid + 2 + hi) * NQ * 2 + ((r32 ^ ((2 + hi) << 2)) << 4);
        const unsigned wo2 = wo0 + 4 * NQ * 2, wo3 = wo1 + 4 * NQ * 2;
        const char* Xs = (const char*)x + (size_t)b * SQ * DM * 2;
        const char* Ws = (const char*)wq + kvh * 512;
#define QDMA(voff, gp, dst) asm volatile("s_mov_b32 m0, %2\n\ts_nop 0\n\tglobal_load_lds_dwordx4 %0, %1" :: "v"(voff), "s"(gp), "s"(dst) : "memory")
#define QS_PIECE(slot, p) do { const unsigned s_ = lds0 + (slot) * 40960 + wid * 4096; const char* w_ = Ws + (size_t)kb * 64 * NQ * 2; \
            if ((p) == 0) QDMA(wo0, w_, s_); \
            if ((p) == 1) QDMA(wo1, w_, s_ + 1024); \
            if ((p) == 2) QDMA(wo2, w_, s_ + 2048); \
            if ((p) == 3) QDMA(wo3, w_, s_ + 3072); \
            if ((p) == 4) { QDMA(xo, Xs + kb * 128, lds0 + (slot) * 40960 + 32768 + wid * 1024); kb = kb == 8 ? 0 : kb + 1; } } while (0)
#define QS_ISSUE(slot) do { QS_PIECE(slot, 0); QS_PIECE(slot, 1); QS_PIECE(slot, 2); QS_PIECE(slot, 3); QS_PIECE(slot, 4); } while (0)
#pragma unroll 1
        for (int j = 0; j < 2; ++j) { QS_ISSUE(j); asm volatile("s_barrier" ::: "memory"); }
        const int pp = ((vp & 1) << 1) | (vp >> 1);
        const unsigned abase = lds0 + (8 * hi + vq) * 512 + (((4 * wid + 2 * (gi & 1) + (pp >> 1)) ^ (vq << 2)) << 4) + 8 * (pp & 1);
        const unsigned xbase = lds0 + 32768 + r32 * 128 + ((hi ^ ((r32 >> 1) & 7)) << 4);
        f32x16 acc0 = {}, acc1 = {};
        s16x4 lo[2][4], h4[2][4]; bf16x8 x0[2][4], x1[2][4];
        asm volatile("s_waitcnt vmcnt(5)\n\ts_barrier" : "+v"(clm) :: "memory");
        QS_ISSUE(2);
        if (wid == 1 && lane < 2 && clm.y != tag && (clm.x | clm.y) != 0u)
            asm volatile("global_atomic_cmpswap_x2 %0, %1, off\n\ts_nop 1" :: "v"(wcl), "v"(u32x4{0u, tag, clm.x, clm.y}) : "memory");
#pragma unroll
        for (int i = 0; i < 4; ++i) {
            lo[0][i] = vtr(abase + i * 8192); h4[0][i] = vtr(abase + i * 8192 + 2048);
            x0[0][i] = ldsr128(xbase ^ (i << 5)); x1[0][i] = ldsr128((xbase ^ (i << 5)) + 4096);
        }
#pragma unroll
        for (int st = 0; st < 9; ++st) {
            if (st == 0) asm volatile("s_waitcnt vmcnt(5)" ::: "memory");
            else if (st < 6) asm volatile("s_waitcnt vmcnt(10)" ::: "memory");
            else if (st == 6) asm volatile("s_waitcnt vmcnt(9)" ::: "memory");
            else if (st == 7) asm volatile("s_waitcnt vmcnt(6)" ::: "memory");
            asm volatile("s_waitcnt lgkmcnt(0)\n\ts_barrier" ::: "memory");
            const unsigned so = ((st + 1) & 3) * 40960;
            const int cu = st & 1, nx = cu ^ 1;
            unsigned aa = abase + so;
            asm volatile("" : "+v"(aa));
            __builtin_amdgcn_sched_barrier(0);
            if (st == 0) QS_ISSUE(3);
#pragma unroll
            for (int i = 0; i < 4; ++i) {
                const bf16x8 a0 = {lo[cu][i][0], lo[cu][i][1], lo[cu][i][2], lo[cu][i][3], h4[cu][i][0], h4[cu][i][1], h4[cu][i][2], h4[cu][i][3]};
                acc0 = MFMA32(a0, x0[cu][i], acc0);
                if (st < 8) { lo[nx][i] = vtr(aa + i * 8192); h4[nx][i] = vtr(aa + i * 8192 + 2048); }
                if (st < 5) QS_PIECE(st & 3, i);
                __builtin_amdgcn_sched_barrier(0);
                acc1 = MFMA32(a0, x1[cu][i], acc1);
                if (st < 8) { unsigned xa = (xbase ^ (i << 5)) + so; asm volatile("" : "+v"(xa)); x0[nx][i] = ldsr128(xa); x1[nx][i] = ldsr128(xa + 4096); }
                if (st < 5 && i == 3) QS_PIECE(st & 3, 4);
                if (st == 5 && i == 3) { DMA_K(0); DMA_K(1); }
                if (st == 6 && i == 3) DMA_V(0);
                if (st == 7 && i == 3) DMA_K(2);
                if (st == 8 && i == 3) DMA_K(3);
                __builtin_amdgcn_sched_barrier(0);
            }
        }
#undef QS_ISSUE
#undef QS_PIECE
#define QW(ACC, qblk) do { _Pragma("unroll") for (int s = 0; s < 2; ++s) { \
            u32x4 w_ = {cvtpk(ACC[8 * s], ACC[8 * s + 1]), cvtpk(ACC[8 * s + 2], ACC[8 * s + 3]), \
                        cvtpk(ACC[8 * s + 4], ACC[8 * s + 5]), cvtpk(ACC[8 * s + 6], ACC[8 * s + 7])}; \
            *(LAS u32x4*)(uintptr_t)(lds0 + 32768 + (2 * (wid >> 2) + (qblk)) * REG + (2 * (wid & 3) + s) * 1024 + lane * 16) = w_; } } while (0)
        QW(acc0, 0); QW(acc1, 1);
#undef QW
        DMA_V(1);
    }
    asm volatile("s_waitcnt vmcnt(8) lgkmcnt(0)\n\ts_barrier" ::: "memory");
#pragma unroll
    for (int f = 0; f < 8; ++f) qf[f] = ldsr128(lds0 + 32768 + qb * REG + f * 1024 + lane * 16);
    TL(1);

    const unsigned kb0 = lds0 + (4 * kh + (r32 >> 3)) * 2048 + (r32 & 7) * 64 + ((hi ^ (r32 >> 3)) << 4);
    const unsigned kb1 = kb0 ^ 32;
    const unsigned vb = lds0 + kh * 8192 + (4 * hi + vq) * 64 + (gi & 1) * 32 + vp * 8;

    float m_run, l_run = 0.f, tmax = 0.f;
    unsigned long long tprev = 0;
    f32x16 o[4] = {};
    f32x16 sA, sB, negm;

    bf16x8 kf[8];
    {
        bf16x8 k0[8];
        unsigned ka0 = kb0 + KSO(0), ka1 = kb1 + KSO(0);
        asm volatile("" : "+v"(ka0), "+v"(ka1));
#pragma unroll
        for (int d0 = 0; d0 < 8; ++d0) k0[d0] = ldsr128(((d0 & 1) ? ka1 : ka0) + (d0 >> 1) * 512);
#pragma unroll
        for (int d0 = 0; d0 < 8; ++d0) kf[d0] = ldsr128(((d0 & 1) ? ka1 : ka0) + 16384 + (d0 >> 1) * 512);
        __builtin_amdgcn_sched_barrier(0);
        asm volatile("s_waitcnt lgkmcnt(0)\n\ts_barrier" ::: "memory");
        sA = f32x16{};
#pragma unroll
        for (int d0 = 0; d0 < 8; ++d0) sA = MFMA32(k0[d0], qf[d0], sA);
        __builtin_amdgcn_sched_barrier(0);
        DMA_V(2); DMA_K(4);
        m_run = sA[0];
#pragma unroll
        for (int r = 1; r < 16; ++r) m_run = __builtin_fmaxf(m_run, sA[r]);
        m_run = swap_max(m_run);
#pragma unroll
        for (int r = 0; r < 16; ++r) { sA[r] -= m_run; negm[r] = -m_run; }
    }
#define EXP1(r) do { sc[r] = __builtin_amdgcn_exp2f(fmul1(sc[r], C2)); } while (0)
#define SUM1(k) do { ps0 += sc[2 * (k)]; ps1 += sc[2 * (k) + 1]; pw[k] = cvtpk(sc[2 * (k)], sc[2 * (k) + 1]); asm volatile("" : "+v"(pw[k]), "+v"(ps0), "+v"(ps1)); } while (0)
#define STEP(t, SC_, SN_, TAIL) do { \
        f32x16& sc = SC_; f32x16& sn = SN_; \
        s16x4 vlo[8], vhi[8]; \
        if (!TAIL || (t) < 12 || (t) == 14) asm volatile("s_waitcnt vmcnt(8)" ::: "memory"); \
        else if ((t) < 14) asm volatile("s_waitcnt vmcnt(6)" ::: "memory"); \
        else asm volatile("s_waitcnt vmcnt(10)" ::: "memory"); \
        while (__builtin_amdgcn_s_memrealtime() - tprev < (unsigned long long)PACE) __builtin_amdgcn_s_sleep(1); \
        asm volatile("s_waitcnt lgkmcnt(0)\n\ts_barrier" ::: "memory"); \
        tprev = __builtin_amdgcn_s_memrealtime(); \
        if (__builtin_expect(__any(tmax > RTHR / C2), 0)) { \
            const float dl = __builtin_fmaxf(tmax, 0.f); \
            const float alpha = __builtin_amdgcn_exp2f(-dl * C2); \
            m_run += dl; l_run *= alpha; \
            _Pragma("unroll") for (int r = 0; r < 16; ++r) { sc[r] -= dl; negm[r] = -m_run; } \
            _Pragma("unroll") for (int dt = 0; dt < 4; ++dt) \
                _Pragma("unroll") for (int r = 0; r < 16; ++r) o[dt][r] *= alpha; \
        } \
        const unsigned vwr = dwo + vso, kwr = dwo + kso;                         \
        vso = VSO(t); kso = KSO((t) + 2); \
        const unsigned va = vb + vso; \
        const unsigned ka0 = kb0 + kso, ka1 = kb1 + kso;                         \
        unsigned pw[8]; \
        float ps0 = 0.f, ps1 = 0.f, t2; \
        __builtin_amdgcn_sched_barrier(0); \
          \
        _Pragma("unroll") for (int i = 0; i < 8; ++i) { \
            sn = MFMA32(kf[i], qf[i], i == 0 ? negm : sn);                   \
            const int ss = i >> 2, dt = i & 3; \
            vlo[2 * dt + ss] = vtr(va + ss * 4096 + dt * 512); \
            vhi[2 * dt + ss] = vtr(va + ss * 4096 + 2048 + dt * 512); \
            if (i < 4) { EXP1(2 * i); EXP1(2 * i + 1); } else EXP1(4 + i); \
            if (i >= 1 && i <= 4) SUM1(i - 1); \
            if (i == 6) SUM1(4); \
            asm volatile("" : "+v"(sn), "+v"(sc)); \
            if (i == XS0 && (!TAIL || (t) < 13)) KVDMA(voffV, Vg + ((t) + 3) * 16384, vwr, 0); \
            if (i == XS1 && (!TAIL || (t) < 13)) KVDMA(voffV, Vg + ((t) + 3) * 16384, vwr, 2048); \
            if (i == XS0 && TAIL && (t) == 13) BDMA(Bs, 0, 2); \
            if (i == XS1 && TAIL && (t) == 13) BDMA(Bs, 0, 3); \
            if (i == XS0 && TAIL && (t) == 14) BDMA(Bs, 1, 1); \
            if (i == XS1 && TAIL && (t) == 14) BDMA(Bs, 1, 2); \
            __builtin_amdgcn_sched_barrier(0); \
        } \
          \
        _Pragma("unroll") for (int i = 0; i < 8; ++i) { \
            const int ss = i >> 2, dt = i & 3; \
            const s16x4 lo = vlo[2 * dt + ss], hi4 = vhi[2 * dt + ss]; \
            const bf16x8 vf = {lo[0], lo[1], lo[2], lo[3], hi4[0], hi4[1], hi4[2], hi4[3]}; \
            const bf16x8 pf = __builtin_bit_cast(bf16x8, u32x4{pw[4 * ss], pw[4 * ss + 1], pw[4 * ss + 2], pw[4 * ss + 3]}); \
            o[dt] = MFMA32(vf, pf, o[dt]); \
            if (i < 4) { kf[2 * i] = ldsr128(ka0 + i * 512); kf[2 * i + 1] = ldsr128(ka1 + i * 512); } \
            if (i < 2) { EXP1(12 + 2 * i); EXP1(13 + 2 * i); asm volatile("" : "+v"(sc)); } \
            if (i < 3) SUM1(5 + i); \
            if (i == 3) { l_run += ps0 + ps1; asm volatile("" : "+v"(sn), "+v"(l_run)); \
                          tmax = max3f(sn[0], sn[1], sn[2]); t2 = max3f(sn[3], sn[4], sn[5]); asm volatile("" : "+v"(tmax), "+v"(t2)); } \
            if (i == 4) { tmax = max3f(tmax, sn[6], sn[7]); t2 = max3f(t2, sn[8], sn[9]); tmax = max3f(tmax, sn[10], sn[11]); asm volatile("" : "+v"(tmax), "+v"(t2)); } \
            if (i == 5) { t2 = max3f(t2, sn[12], sn[13]); tmax = max3f(tmax, sn[14], sn[15]); tmax = max3f(tmax, t2, t2); asm volatile("" : "+v"(tmax)); } \
            if (i == 6) { tmax = swap_max3(tmax); asm volatile("" : "+v"(tmax)); } \
            if (i == YS0 && (!TAIL || (t) < 11)) KVDMA(voffK0, Kg + ((t) + 5) * 16384, kwr, 0); \
            if (i == YS1 && (!TAIL || (t) < 11)) KVDMA(voffK1, Kg + ((t) + 5) * 16384, kwr, 2048); \
            if (i == YS0 && TAIL && (t) == 12) BDMA(Bs, 0, 0); \
            if (i == YS1 && TAIL && (t) == 12) BDMA(Bs, 0, 1); \
            if (i == YS0 && TAIL && (t) == 13) BDMA(Bs, 0, 4); \
            if (i == YS1 && TAIL && (t) == 13) BDMA(Bs, 1, 0); \
            if (i == YS0 && TAIL && (t) == 14) BDMA(Bs, 1, 3); \
            if (i == YS1 && TAIL && (t) == 14) BDMA(Bs, 1, 4); \
            __builtin_amdgcn_sched_barrier(0); \
        } } while (0)
    unsigned boff[5];
    const char* Bw = (const char*)wo + (size_t)(1024 * sk + 32 * wid) * DM * 2;
    const char* Bs = Bw + 144 * nt;
#define BDMA(bs, c, i) QDMA(boff[i], (bs) + (c) * (256 * DM * 2), lds0 + (((c) + 1) & 3) * REG + wid * 5120 + (i) * 1024)
    unsigned vso = VSO(3), kso = KSO(1);
#pragma unroll 1
    for (int t = 0; t < 10; t += 2) { STEP(t, sA, sB, false); STEP(t + 1, sB, sA, false); }
    {
        unsigned ln = lane;
        asm volatile("" : "+v"(ln));
#pragma unroll
        for (int i = 0; i < 5; ++i) {
            const unsigned n = 64 * i + ln, row = (n * 205u) >> 11, c = n - 10 * row;
            boff[i] = row * (DM * 2) + (c < 9 ? c : 0u) * 16;
        }
    }
#pragma unroll 1
    for (int t = 10; t < NT; t += 2) { STEP(t, sA, sB, true); STEP(t + 1, sB, sA, true); }
#undef STEP
#undef EXP1
#undef SUM1
    TL(2);
    l_run = swap_add(l_run);
    asm volatile("s_waitcnt lgkmcnt(0)\n\ts_barrier" ::: "memory");
    if (wid == 0) {
#pragma unroll
        for (int i = 0; i < 10; ++i) BDMA(Bs, 2 + i / 5, i % 5);
    }
    {
        f32x16 keep0, keep1, send0, send1;
        if (kh == 0) { keep0 = o[0]; keep1 = o[1]; send0 = o[2]; send1 = o[3]; }
        else         { keep0 = o[2]; keep1 = o[3]; send0 = o[0]; send1 = o[1]; }
        const unsigned xb = lds0 + (kh ? 0 : 3 * REG) + 5120 + qb * 8704;
#pragma unroll
        for (int r4 = 0; r4 < 4; ++r4) {
            f32x4 v0 = {send0[4 * r4], send0[4 * r4 + 1], send0[4 * r4 + 2], send0[4 * r4 + 3]};
            f32x4 v1 = {send1[4 * r4], send1[4 * r4 + 1], send1[4 * r4 + 2], send1[4 * r4 + 3]};
            *(LAS f32x4*)(uintptr_t)(xb + r4 * 1024 + lane * 16) = v0;
            *(LAS f32x4*)(uintptr_t)(xb + (4 + r4) * 1024 + lane * 16) = v1;
        }
        *(LAS float*)(uintptr_t)(xb + 8192 + lane * 4) = m_run;
        *(LAS float*)(uintptr_t)(xb + 8192 + 256 + lane * 4) = l_run;
        asm volatile("s_waitcnt lgkmcnt(0)\n\ts_barrier" ::: "memory");
        const unsigned pb = lds0 + (kh ? 3 * REG : 0) + 5120 + qb * 8704;
        const float m2 = *(const LAS float*)(uintptr_t)(pb + 8192 + lane * 4);
        const float l2 = *(const LAS float*)(uintptr_t)(pb + 8192 + 256 + lane * 4);
        const float mm = __builtin_fmaxf(m_run, m2);
        const float a1 = __builtin_amdgcn_exp2f((m_run - mm) * C2), a2 = __builtin_amdgcn_exp2f((m2 - mm) * C2);
        const float inv = 1.0f / (l_run * a1 + l2 * a2);
        const float w1 = a1 * inv, w2 = a2 * inv;
        const int row = b * SQ + 32 * (qb & 1) + r32;
        char* ob = ws + ((size_t)((row >> 4) * 128 + head * 4 + 2 * kh) * 64 + hi * 16 + (row & 15)) * 16;
#define OSTORE(KEEP, i) do { u32x2 w_[4]; \
            _Pragma("unroll") for (int r4 = 0; r4 < 4; ++r4) { \
                const f32x4 v = *(const LAS f32x4*)(uintptr_t)(pb + (4 * (i) + r4) * 1024 + lane * 16); \
                const float e0 = KEEP[4 * r4] * w1 + v[0] * w2, e1 = KEEP[4 * r4 + 1] * w1 + v[1] * w2; \
                const float e2 = KEEP[4 * r4 + 2] * w1 + v[2] * w2, e3 = KEEP[4 * r4 + 3] * w1 + v[3] * w2; \
                w_[r4] = u32x2{cvtpk(e0, e1), cvtpk(e2, e3)}; } \
            _Pragma("unroll") for (int g = 0; g < 2; ++g) { \
                u32x4 o4 = {w_[g].x, w_[g].y, w_[g + 2].x, w_[g + 2].y}; \
                asm volatile("global_store_dwordx4 %0, %1, off sc1\n\ts_nop 1" :: "v"(ob + (i) * 1024 + g * 512), "v"(o4) : "memory"); } } while (0)
        OSTORE(keep0, 0); OSTORE(keep1, 1);
#undef OSTORE
    }
    TL(3);
    const char* Ag = ws + (size_t)((8 * mt + 2 * qb) * 128 + 32 * sk + kh) * 1024;
    unsigned ao0 = lane * 16, ao1 = ao0 + 131072;
    unsigned bb[4];
#pragma unroll
    for (int c = 0; c < 4; ++c) { bb[c] = lds0 + ((c + 1) & 3) * REG + (32 * kh + 4 * gi + vq) * 160 + 8 * vp; asm volatile("" : "+v"(bb[c])); }
    asm volatile("" : "+v"(ao0), "+v"(ao1), "+s"(Ag));
    asm volatile("s_waitcnt vmcnt(0) lgkmcnt(0)\n\ts_barrier" ::: "memory");
    TL(4);
    unsigned dep1 = 0;
    if (wid == 0) {
        const unsigned td = rendezvous<8>(w1, tag, nt, lane, TMO, dep1);
        if (lane == 0) *(LAS unsigned*)(uintptr_t)(lds0 + REG + 144) = td;
    } else {
#pragma unroll
        for (int i = 0; i < 10; ++i) BDMA(Bs, 2 + i / 5, i % 5);
    }
    asm volatile("s_waitcnt lgkmcnt(0)\n\ts_barrier" ::: "memory");
    unsigned todo = __builtin_amdgcn_readfirstlane(*(const LAS unsigned*)(uintptr_t)(lds0 + REG + 144));
    TL(5);
    float* part = (float*)(ws + PART_OFF);
    while (todo) {
        const int ntc = ((todo >> nt) & 1u) ? nt : __builtin_ctz(todo);
        todo &= ~(1u << ntc);
        if (ntc != nt) {
            asm volatile("s_waitcnt vmcnt(0) lgkmcnt(0)\n\ts_barrier" ::: "memory");
#pragma unroll 1
            for (int c = 0; c < 4; ++c) {
#pragma unroll
                for (int i = 0; i < 5; ++i) BDMA(Bw + 144 * ntc, c, i);
            }
            asm volatile("s_waitcnt vmcnt(0)\n\ts_barrier" ::: "memory");
        }
        u32x4 an[32];
#pragma unroll
        for (int s2 = 0; s2 < 8; ++s2) {
            const char* ag = Ag;
            asm volatile("" : "+s"(ag));
            ag += s2 * 4096;
            asm volatile("global_load_dwordx4 %0, %1, %2 sc1" : "=v"(an[4 * s2]) : "v"(ao0), "s"(ag) : "memory");
            asm volatile("global_load_dwordx4 %0, %1, %2 sc1" : "=v"(an[4 * s2 + 1]) : "v"(ao1), "s"(ag) : "memory");
            asm volatile("global_load_dwordx4 %0, %1, %2 offset:2048 sc1" : "=v"(an[4 * s2 + 2]) : "v"(ao0), "s"(ag) : "memory");
            asm volatile("global_load_dwordx4 %0, %1, %2 offset:2048 sc1" : "=v"(an[4 * s2 + 3]) : "v"(ao1), "s"(ag) : "memory");
        }
        f32x4 acc[2][5];
#pragma unroll
        for (int ct = 0; ct < 5; ++ct) { acc[0][ct] = f32x4{0.f, 0.f, 0.f, 0.f}; acc[1][ct] = f32x4{0.f, 0.f, 0.f, 0.f}; }
        s16x4 blo[2][5], bhi[2][5];
#pragma unroll
        for (int ct = 0; ct < 5; ++ct) { blo[0][ct] = vtr(bb[0] + 32 * ct); bhi[0][ct] = vtr(bb[0] + 32 * ct + 2560); }
#pragma unroll
        for (int s = 0; s < 16; ++s) {
            asm volatile("s_waitcnt vmcnt(%2)" : "+v"(an[2 * s]), "+v"(an[2 * s + 1]) : "n"(30 - 2 * s) : "memory");
            if (s == 0) { TL(6); }
            if (s == 7) asm volatile("s_barrier" ::: "memory");
            __builtin_amdgcn_sched_barrier(0);
#pragma unroll
            for (int ct = 0; ct < 5; ++ct) {
                if (s < 15) {
                    const unsigned ad = bb[((s + 1) >> 2) & 3] + ((s + 1) & 3) * 10240 + 32 * ct;
                    blo[(s + 1) & 1][ct] = vtr(ad); bhi[(s + 1) & 1][ct] = vtr(ad + 2560);
                }
                const s16x4 lo = blo[s & 1][ct], h4 = bhi[s & 1][ct];
                const bf16x8 bf = {lo[0], lo[1], lo[2], lo[3], h4[0], h4[1], h4[2], h4[3]};
                acc[0][ct] = MFMA16(bf, __builtin_bit_cast(bf16x8, an[2 * s]), acc[0][ct]);
                acc[1][ct] = MFMA16(bf, __builtin_bit_cast(bf16x8, an[2 * s + 1]), acc[1][ct]);
                __builtin_amdgcn_sched_barrier(0);
            }
        }
        TL(7);
#pragma unroll
        for (int ct = 0; ct < 5; ++ct)
            *(LAS f32x4*)(uintptr_t)(lds0 + REG + (wid * 5 + ct) * 1024 + lane * 16) = kh ? acc[0][ct] : acc[1][ct];
        asm volatile("s_waitcnt lgkmcnt(0)\n\ts_barrier" ::: "memory");
        float* pp = part + ((size_t)(sk * 64 + 8 * mt + 2 * qb + kh) * 8 + ntc) * 1152 + lane * 4;
#pragma unroll
        for (int ct = 0; ct < 5; ++ct) {
            const f32x4 v = *(const LAS f32x4*)(uintptr_t)(lds0 + REG + ((wid ^ 4) * 5 + ct) * 1024 + lane * 16);
            const f32x4 r_ = (kh ? acc[1][ct] : acc[0][ct]) + v;
            if (ct < 4 || lane < 32)
                asm volatile("global_store_dwordx4 %0, %1, off sc1\n\ts_nop 1" :: "v"(pp + ct * 256), "v"(r_) : "memory");
        }
        TL(8);
        asm volatile("s_waitcnt vmcnt(0)\n\ts_barrier" ::: "memory");
        TL(9);
        unsigned dep2 = 0;
        if (wid == 0) {
            const unsigned td = rendezvous<4>(w2 + ntc * 128, tag, sk, lane, TMO2, dep2);
            if (lane == 0) *(LAS unsigned*)(uintptr_t)(lds0 + 2 * REG) = td;
        }
        asm volatile("s_waitcnt lgkmcnt(0)\n\ts_barrier" ::: "memory");
        unsigned qs = __builtin_amdgcn_readfirstlane(*(const LAS unsigned*)(uintptr_t)(lds0 + 2 * REG));
        TL(10);
        while (qs) {
            const int q = __builtin_ctz(qs);
            qs &= qs - 1;
            if (tid < 288) {
                const float* p = part + ((size_t)(8 * mt + 2 * q) * 8 + ntc) * 1152 + tid * 4;
                f32x4 v[2][4];
#pragma unroll
                for (int r = 0; r < 2; ++r)
#pragma unroll
                    for (int s = 0; s < 4; ++s)
                        asm volatile("global_load_dwordx4 %0, %1, off sc1" : "=v"(v[r][s]) : "v"(p + (size_t)s * 64 * 8 * 1152 + r * 8 * 1152) : "memory");
                asm volatile("s_waitcnt vmcnt(0)" : "+v"(v[0][0]), "+v"(v[0][1]), "+v"(v[0][2]), "+v"(v[0][3]),
                                                    "+v"(v[1][0]), "+v"(v[1][1]), "+v"(v[1][2]), "+v"(v[1][3]) :: "memory");
#pragma unroll
                for (int r = 0; r < 2; ++r) {
                    const f32x4 t = ((v[r][0] + v[r][1]) + v[r][2]) + v[r][3];
                    u32x2 w = {cvtpk(t[0], t[1]), cvtpk(t[2], t[3])};
                    *(u32x2*)(out + (size_t)(16 * (8 * mt + 2 * q + r) + (tid & 15)) * DM + 72 * ntc + 4 * (tid >> 4)) = w;
                }
            }
        }
        if (wid == 0) {
            asm volatile("s_waitcnt vmcnt(0)" ::: "memory");
            depart<4>(w2 + ntc * 128, dep2, lane);
        }
    }
    if (wid == 0) {
        asm volatile("s_waitcnt vmcnt(0)" ::: "memory");
        depart<8>(w1, dep1, lane);
    }
    TL(11);
    if (tid == 0) { asm volatile("global_store_dwordx2 %0, %1, off sc1" :: "v"(ws + CNT_OFF + 65536 + bid * 16), "v"(tl[PROBE_A]) : "memory");
                    asm volatile("global_store_dwordx2 %0, %1, off sc1" :: "v"(ws + CNT_OFF + 65536 + bid * 16 + 8), "v"(tl[PROBE_B]) : "memory"); }
    if (bid == 77 && wid == 0) {
        while (__builtin_amdgcn_s_memrealtime() - tl[11] < 2000ull) __builtin_amdgcn_s_sleep(2);
        unsigned mn_ = ~0u, mx_ = 0;
        for (int i_ = 0; i_ < 4; ++i_) { u32x4 v_; asm volatile("global_load_dwordx4 %0, %1, off sc1\n\ts_waitcnt vmcnt(0)" : "=v"(v_) : "v"(ws + CNT_OFF + 65536 + (i_ * 64 + lane) * 16) : "memory");
            const unsigned a_ = v_.x - (unsigned)tl[0] + 100000u, b_ = v_.z - (unsigned)tl[0] + 100000u;
            mn_ = a_ < mn_ ? a_ : mn_; mx_ = b_ > mx_ ? b_ : mx_; }
        for (int o_ = 1; o_ < 64; o_ <<= 1) { unsigned a_ = __shfl_xor(mn_, o_), b_ = __shfl_xor(mx_, o_); mn_ = a_ < mn_ ? a_ : mn_; mx_ = b_ > mx_ ? b_ : mx_; }
        if (PROBE_M == 1) mn_ = (unsigned)tl[PROBE_A] - (unsigned)tl[0] + 100000u;
        unsigned long long d_ = (unsigned long long)__builtin_amdgcn_readfirstlane(mx_ - mn_) * (PROBE_M == 2 ? 0 : 10); d_ = d_ > 100000ull ? 100000ull : d_;
        while (__builtin_amdgcn_s_memrealtime() - tl[11] < 2500ull + d_) __builtin_amdgcn_s_sleep(2);
    }
}

extern "C" void kernel_launch(void* const* d_in, const int* in_sizes, int n_in, void* d_out, int out_size,
                              void* d_ws, size_t ws_size, hipStream_t stream) {
    (void)hipFuncSetAttribute((const void*)gqa_kernel, hipFuncAttributeMaxDynamicSharedMemorySize, LDS_BYTES);
    gqa_kernel<<<NB * HKV, 512, LDS_BYTES, stream>>>((const u16*)d_in[0], (const u16*)d_in[1], (const u16*)d_in[2],
                                                     (const u16*)d_in[3], (const u16*)d_in[4], (char*)d_ws, (u16*)d_out);
}
```

```cpp
#include <hip/hip_runtime.h>
#include <hip/hip_bf16.h>
#include <stdint.h>

typedef short bf16x8 __attribute__((ext_vector_type(8)));
typedef short s16x4 __attribute__((ext_vector_type(4)));
typedef float f32x16 __attribute__((ext_vector_type(16)));
typedef float f32x4 __attribute__((ext_vector_type(4)));
typedef unsigned u32x4 __attribute__((ext_vector_type(4)));
typedef unsigned u32x2 __attribute__((ext_vector_type(2)));
typedef unsigned short u16;
#define LAS __attribute__((address_space(3)))
typedef __attribute__((address_space(1))) unsigned gu32;
#define RLX_AGENT __ATOMIC_RELAXED, __HIP_MEMORY_SCOPE_AGENT

constexpr int NB = 16, SQ = 64, DM = 576, HQ = 32, HKV = 16, SKV = 1024, DH = 128;
constexpr int NQ = HQ * DH;
constexpr int NT = SKV / 64;
constexpr int REG = 40960;
constexpr int LDS_BYTES = 4 * REG;
constexpr size_t PART_OFF = (size_t)NB * SQ * NQ * 2;
constexpr size_t CNT_OFF = (size_t)20 << 20;
#define AHEAD 4
#define TMO 5000ull
#define TMO2 5000ull
constexpr int XS0 = 5, XS1 = 7, YS0 = 5, YS1 = 7;
constexpr int PACE = 108;
constexpr float RTHR = 8.0f;
constexpr float C2 = 0.088388347648318447f * 1.4426950408889634f;

#define MFMA32(a, b, c) __builtin_amdgcn_mfma_f32_32x32x16_bf16(a, b, c, 0, 0, 0)

typedef __bf16 bf16x2_t __attribute__((ext_vector_type(2)));
typedef float f32x2 __attribute__((ext_vector_type(2)));
__device__ __forceinline__ unsigned cvtpk(float lo, float hi) {
    f32x2 v = {lo, hi};
    return __builtin_bit_cast(unsigned, __builtin_convertvector(v, bf16x2_t));
}
__device__ __forceinline__ void glds16(const void* g, unsigned lds_base) {
    unsigned sv;
    asm volatile("s_mov_b32 %0, m0\n\ts_mov_b32 m0, %2\n\ts_nop 0\n\tglobal_load_lds_dwordx4 %1, off\n\ts_mov_b32 m0, %0"
                 : "=&s"(sv) : "v"(g), "s"(lds_base) : "memory");
}
__device__ __forceinline__ s16x4 vtr(unsigned a) {
    return __builtin_amdgcn_ds_read_tr16_b64_v4i16((LAS s16x4*)(uintptr_t)a);
}
__device__ __forceinline__ bf16x8 ldsr128(unsigned a) { return *(const LAS bf16x8*)(uintptr_t)a; }
__device__ __forceinline__ float swap_max(float v) {
    auto rr = __builtin_amdgcn_permlane32_swap(__float_as_uint(v), __float_as_uint(v), false, false);
    return __builtin_fmaxf(__uint_as_float(rr[0]), __uint_as_float(rr[1]));
}
__device__ __forceinline__ float max3f(float a, float b, float c) { float r; asm("v_max3_f32 %0, %1, %2, %3" : "=v"(r) : "v"(a), "v"(b), "v"(c)); return r; }
__device__ __forceinline__ float fmul1(float a, float b) { float r; asm("v_mul_f32_e32 %0, %1, %2" : "=v"(r) : "s"(b), "v"(a)); return r; }
__device__ __forceinline__ float swap_max3(float v) {
    auto rr = __builtin_amdgcn_permlane32_swap(__float_as_uint(v), __float_as_uint(v), false, false);
    return max3f(__uint_as_float(rr[0]), __uint_as_float(rr[1]), __uint_as_float(rr[1]));
}
__device__ __forceinline__ float swap_add(float v) {
    auto rr = __builtin_amdgcn_permlane32_swap(__float_as_uint(v), __float_as_uint(v), false, false);
    return __uint_as_float(rr[0]) + __uint_as_float(rr[1]);
}

#define TL(i)
extern "C" __device__ unsigned long long amd_dispatch_id() __asm("llvm.amdgcn.dispatch.id");

template <unsigned N>
__device__ __forceinline__ unsigned rendezvous(const char* w, unsigned tag, unsigned own, int lane, unsigned long long tmo, unsigned& dep) {
    unsigned old = 0, todo = 1u << own;
    u32x2 v;
    if (lane == 0) asm volatile("global_atomic_add %0, %1, %2, off sc0" : "=v"(old) : "v"(w), "v"(1u) : "memory");
    asm volatile("global_load_dwordx2 %0, %1, off sc1" : "=v"(v) : "v"(w) : "memory");
    asm volatile("s_waitcnt vmcnt(0)" : "+v"(old), "+v"(v) :: "memory");
    old = __builtin_amdgcn_readfirstlane(old);
    if ((old & 255u) == N - 1) todo |= (old >> 8) & 255u;
    else {
        const unsigned long long t0 = __builtin_amdgcn_s_memrealtime();
        while ((__builtin_amdgcn_readfirstlane(v.x) & 255u) != N || __builtin_amdgcn_readfirstlane(v.y) != tag) {
            if (__builtin_amdgcn_s_memrealtime() - t0 > tmo) {
                unsigned o2 = 0;
                if (lane == 0) asm volatile("global_atomic_or %0, %1, %2, off sc0" : "=v"(o2) : "v"(w), "v"(256u << own) : "memory");
                asm volatile("s_waitcnt vmcnt(0)" : "+v"(o2) :: "memory");
                if ((__builtin_amdgcn_readfirstlane(o2) & 255u) != N) todo = 0;
                break;
            }
            asm volatile("global_load_dwordx2 %0, %1, off sc1\n\ts_waitcnt vmcnt(0)" : "=v"(v) : "v"(w) : "memory");
        }
    }
    dep = 0;
    if (lane == 0) asm volatile("global_atomic_add %0, %1, %2, off sc0" : "=v"(dep) : "v"(w), "v"(65536u) : "memory");
    return todo;
}
template <unsigned N>
__device__ __forceinline__ void depart(const char* w, unsigned dep, int lane) {
    asm volatile("" : "+v"(dep));
    if (lane == 0 && ((dep >> 16) & 255u) == N - 1)
        asm volatile("global_store_dwordx2 %0, %1, off sc1" :: "v"(w), "v"(u32x2{0u, 0u}) : "memory");
}

#define MFMA16(a, b, c) __builtin_amdgcn_mfma_f32_16x16x32_bf16(a, b, c, 0, 0, 0)
__global__ __launch_bounds__(512, 2) void gqa_kernel(const u16* __restrict__ x, const u16* __restrict__ Kc,
                                                     const u16* __restrict__ Vc, const u16* __restrict__ wq,
                                                     const u16* __restrict__ wo, char* ws, u16* __restrict__ out) {
    extern __shared__ __attribute__((aligned(16))) char lds[];
    TL(0);
    const unsigned lds0 = (unsigned)(uintptr_t)lds;
    const int tid = threadIdx.x, lane = tid & 63, r32 = lane & 31, hi = lane >> 5;
    const int wid = __builtin_amdgcn_readfirstlane(tid >> 6);
    const int qb = wid & 3, kh = wid >> 2;
    const int bid = blockIdx.x, b = 8 * (bid & 1) + ((bid >> 3) & 7), kvh = 4 * ((bid >> 1) & 3) + (bid >> 6);
    const int head = 2 * kvh + (qb >> 1);
    const int mt = b >> 1, sk = kvh >> 2, nt = 4 * (b & 1) + (kvh & 3);
    const unsigned tag = 0x80000000u | ((unsigned)amd_dispatch_id() & 0xfffffu) |
                         ((((unsigned)((uintptr_t)__builtin_amdgcn_dispatch_ptr() >> 12) * 0x9e3779b1u) >> 21) << 20);
    const char* w1 = ws + CNT_OFF + (mt * 4 + sk) * 128;
    const char* w2 = ws + CNT_OFF + 4096 + mt * 8 * 128;
    const char* wcl = lane == 0 ? w1 : w2 + nt * 128;
    u32x2 clm = {0u, tag};
    if (wid == 1 && lane < 2)
        asm volatile("global_atomic_cmpswap_x2 %0, %1, %2, off sc0\n\ts_nop 1" : "=v"(clm) : "v"(wcl), "v"(u32x4{0u, tag, 0u, 0u}) : "memory");
    const int gi = lane >> 4, vq = (lane & 15) >> 2, vp = lane & 3;

    const unsigned vsrc = (16 * (wid >> 1) + ((lane >> 2) & 7)) * 256 + (wid & 1) * 128 + hi * 64;
    const unsigned voffV = vsrc + vp * 16;
    const unsigned voffK0 = vsrc + ((vp ^ (wid & 2)) << 4), voffK1 = vsrc + ((vp ^ ((wid & 2) | 1)) << 4);
    const char* Kg = (const char*)Kc + (size_t)(b * HKV + kvh) * SKV * DH * 2;
    const char* Vg = (const char*)Vc + (size_t)(b * HKV + kvh) * SKV * DH * 2;
    const unsigned dwo = lds0 + (wid >> 1) * 4096 + (wid & 1) * 1024;
#define KVDMA(voff, gp, dst, OFF) asm volatile("s_mov_b32 m0, %2\n\ts_nop 0\n\tglobal_load_lds_dwordx4 %0, %1 offset:" #OFF " sc1 nt" \
            :: "v"(voff), "s"(gp), "s"(dst) : "memory")
#define VSO(tn) ((int)((0x0200110au >> (8 * ((tn) & 3))) & 255) * 8192)
#define KSO(tn) ((int)((0x0f0c0705u >> (8 * ((tn) & 3))) & 255) * 8192)
#define DMA_V0(tn) KVDMA(voffV, Vg + (tn) * 16384, dwo + VSO(tn), 0)
#define DMA_V1(tn) KVDMA(voffV, Vg + (tn) * 16384, dwo + VSO(tn), 2048)
#define DMA_K0(tn) KVDMA(voffK0, Kg + (tn) * 16384, dwo + KSO(tn), 0)
#define DMA_K1(tn) KVDMA(voffK1, Kg + (tn) * 16384, dwo + KSO(tn), 2048)
#define DMA_V(tn) do { DMA_V0(tn); DMA_V1(tn); } while (0)
#define DMA_K(tn) do { DMA_K0(tn); DMA_K1(tn); } while (0)
    const char* Bw = (const char*)wo + (size_t)(1024 * sk + 32 * wid) * DM * 2;
    bf16x8 qf[8];
    {
        int kb = (b + 2 * (kvh & 3)) % 9;
        const unsigned xo = (8 * wid + (lane >> 3)) * DM * 2 + (((lane & 7) ^ ((4 * wid + (lane >> 4)) & 7)) << 4);
        const unsigned wo0 = (8 * wid + hi) * NQ * 2 + ((r32 ^ (hi << 2)) << 4);
        const unsigned wo1 = (8 * wid + 2 + hi) * NQ * 2 + ((r32 ^ ((2 + hi) << 2)) << 4);
        const unsigned wo2 = wo0 + 4 * NQ * 2, wo3 = wo1 + 4 * NQ * 2;
        const char* Xs = (const char*)x + (size_t)b * SQ * DM * 2;
        const char* Ws = (const char*)wq + kvh * 512;
#define QDMA(voff, gp, dst) asm volatile("s_mov_b32 m0, %2\n\ts_nop 0\n\tglobal_load_lds_dwordx4 %0, %1" :: "v"(voff), "s"(gp), "s"(dst) : "memory")
#define QS_PIECE(slot, p) do { const unsigned s_ = lds0 + (slot) * 40960 + wid * 4096; const char* w_ = Ws + (size_t)kb * 64 * NQ * 2; \
            if ((p) == 0) QDMA(wo0, w_, s_); \
            if ((p) == 1) QDMA(wo1, w_, s_ + 1024); \
            if ((p) == 2) QDMA(wo2, w_, s_ + 2048); \
            if ((p) == 3) QDMA(wo3, w_, s_ + 3072); \
            if ((p) == 4) { QDMA(xo, Xs + kb * 128, lds0 + (slot) * 40960 + 32768 + wid * 1024); kb = kb == 8 ? 0 : kb + 1; } } while (0)
#define QS_ISSUE(slot) do { QS_PIECE(slot, 0); QS_PIECE(slot, 1); QS_PIECE(slot, 2); QS_PIECE(slot, 3); QS_PIECE(slot, 4); } while (0)
#pragma unroll 1
        for (int j = 0; j < 2; ++j) { QS_ISSUE(j); asm volatile("s_barrier" ::: "memory"); }
        const int pp = ((vp & 1) << 1) | (vp >> 1);
        const unsigned abase = lds0 + (8 * hi + vq) * 512 + (((4 * wid + 2 * (gi & 1) + (pp >> 1)) ^ (vq << 2)) << 4) + 8 * (pp & 1);
        const unsigned xbase = lds0 + 32768 + r32 * 128 + ((hi ^ ((r32 >> 1) & 7)) << 4);
        f32x16 acc0 = {}, acc1 = {};
        s16x4 lo[2][4], h4[2][4]; bf16x8 x0[2][4], x1[2][4];
        asm volatile("s_waitcnt vmcnt(5)\n\ts_barrier" : "+v"(clm) :: "memory");
        QS_ISSUE(2);
        if (wid == 1 && lane < 2 && clm.y != tag && (clm.x | clm.y) != 0u)
            asm volatile("global_atomic_cmpswap_x2 %0, %1, off\n\ts_nop 1" :: "v"(wcl), "v"(u32x4{0u, tag, clm.x, clm.y}) : "memory");
#pragma unroll
        for (int i = 0; i < 4; ++i) {
            lo[0][i] = vtr(abase + i * 8192); h4[0][i] = vtr(abase + i * 8192 + 2048);
            x0[0][i] = ldsr128(xbase ^ (i << 5)); x1[0][i] = ldsr128((xbase ^ (i << 5)) + 4096);
        }
#pragma unroll
        for (int st = 0; st < 9; ++st) {
            if (st == 0) asm volatile("s_waitcnt vmcnt(5)" ::: "memory");
            else if (st < 6) asm volatile("s_waitcnt vmcnt(10)" ::: "memory");
            else if (st == 6) asm volatile("s_waitcnt vmcnt(9)" ::: "memory");
            else if (st == 7) asm volatile("s_waitcnt vmcnt(6)" ::: "memory");
            asm volatile("s_waitcnt lgkmcnt(0)\n\ts_barrier" ::: "memory");
            const unsigned so = ((st + 1) & 3) * 40960;
            const int cu = st & 1, nx = cu ^ 1;
            unsigned aa = abase + so;
            asm volatile("" : "+v"(aa));
            __builtin_amdgcn_sched_barrier(0);
            if (st == 0) QS_ISSUE(3);
#pragma unroll
            for (int i = 0; i < 4; ++i) {
                const bf16x8 a0 = {lo[cu][i][0], lo[cu][i][1], lo[cu][i][2], lo[cu][i][3], h4[cu][i][0], h4[cu][i][1], h4[cu][i][2], h4[cu][i][3]};
                acc0 = MFMA32(a0, x0[cu][i], acc0);
                if (st < 8) { lo[nx][i] = vtr(aa + i * 8192); h4[nx][i] = vtr(aa + i * 8192 + 2048); }
                if (st < 5) QS_PIECE(st & 3, i);
                __builtin_amdgcn_sched_barrier(0);
                acc1 = MFMA32(a0, x1[cu][i], acc1);
                if (st < 8) { unsigned xa = (xbase ^ (i << 5)) + so; asm volatile("" : "+v"(xa)); x0[nx][i] = ldsr128(xa); x1[nx][i] = ldsr128(xa + 4096); }
                if (st < 5 && i == 3) QS_PIECE(st & 3, 4);
                if (st == 5 && i == 3) { DMA_K(0); DMA_K(1); }
                if (st == 6 && i == 3) DMA_V(0);
                if (st == 7 && i == 3) DMA_K(2);
                if (st == 8 && i == 3) DMA_K(3);
                __builtin_amdgcn_sched_barrier(0);
            }
        }
#undef QS_ISSUE
#undef QS_PIECE
#define QW(ACC, qblk) do { _Pragma("unroll") for (int s = 0; s < 2; ++s) { \
            u32x4 w_ = {cvtpk(ACC[8 * s], ACC[8 * s + 1]), cvtpk(ACC[8 * s + 2], ACC[8 * s + 3]), \
                        cvtpk(ACC[8 * s + 4], ACC[8 * s + 5]), cvtpk(ACC[8 * s + 6], ACC[8 * s + 7])}; \
            *(LAS u32x4*)(uintptr_t)(lds0 + 32768 + (2 * (wid >> 2) + (qblk)) * REG + (2 * (wid & 3) + s) * 1024 + lane * 16) = w_; } } while (0)
        QW(acc0, 0); QW(acc1, 1);
#undef QW
        DMA_V(1);
    }
    asm volatile("s_waitcnt vmcnt(8) lgkmcnt(0)\n\ts_barrier" ::: "memory");
#pragma unroll
    for (int f = 0; f < 8; ++f) qf[f] = ldsr128(lds0 + 32768 + qb * REG + f * 1024 + lane * 16);
    TL(1);

    const unsigned kb0 = lds0 + (4 * kh + (r32 >> 3)) * 2048 + (r32 & 7) * 64 + ((hi ^ (r32 >> 3)) << 4);
    const unsigned kb1 = kb0 ^ 32;
    const unsigned vb = lds0 + kh * 8192 + (4 * hi + vq) * 64 + (gi & 1) * 32 + vp * 8;

    float m_run, l_run = 0.f, tmax = 0.f;
    unsigned long long tprev = 0;
    f32x16 o[4] = {};
    f32x16 sA, sB, negm;

    bf16x8 kf[8];
    {
        bf16x8 k0[8];
        unsigned ka0 = kb0 + KSO(0), ka1 = kb1 + KSO(0);
        asm volatile("" : "+v"(ka0), "+v"(ka1));
#pragma unroll
        for (int d0 = 0; d0 < 8; ++d0) k0[d0] = ldsr128(((d0 & 1) ? ka1 : ka0) + (d0 >> 1) * 512);
#pragma unroll
        for (int d0 = 0; d0 < 8; ++d0) kf[d0] = ldsr128(((d0 & 1) ? ka1 : ka0) + 16384 + (d0 >> 1) * 512);
        __builtin_amdgcn_sched_barrier(0);
        asm volatile("s_waitcnt lgkmcnt(0)\n\ts_barrier" ::: "memory");
        sA = f32x16{};
#pragma unroll
        for (int d0 = 0; d0 < 8; ++d0) sA = MFMA32(k0[d0], qf[d0], sA);
        __builtin_amdgcn_sched_barrier(0);
        DMA_V(2); DMA_K(4);
        m_run = sA[0];
#pragma unroll
        for (int r = 1; r < 16; ++r) m_run = __builtin_fmaxf(m_run, sA[r]);
        m_run = swap_max(m_run);
#pragma unroll
        for (int r = 0; r < 16; ++r) { sA[r] -= m_run; negm[r] = -m_run; }
    }
#define EXP1(r) do { sc[r] = __builtin_amdgcn_exp2f(fmul1(sc[r], C2)); } while (0)
#define SUM1(k) do { ps0 += sc[2 * (k)]; ps1 += sc[2 * (k) + 1]; pw[k] = cvtpk(sc[2 * (k)], sc[2 * (k) + 1]); asm volatile("" : "+v"(pw[k]), "+v"(ps0), "+v"(ps1)); } while (0)
#define STEP(t, SC_, SN_, TAIL) do { \
        f32x16& sc = SC_; f32x16& sn = SN_; \
        s16x4 vlo[8], vhi[8]; \
        if (!TAIL || (t) < 12) asm volatile("s_waitcnt vmcnt(8)" ::: "memory"); \
        else if ((t) < 14) asm volatile("s_waitcnt vmcnt(7)" ::: "memory"); \
        else if ((t) == 14) asm volatile("s_waitcnt vmcnt(9)" ::: "memory"); \
        else asm volatile("s_waitcnt vmcnt(10)" ::: "memory"); \
        while (__builtin_amdgcn_s_memrealtime() - tprev < (unsigned long long)PACE) __builtin_amdgcn_s_sleep(1); \
        asm volatile("s_waitcnt lgkmcnt(0)\n\ts_barrier" ::: "memory"); \
        tprev = __builtin_amdgcn_s_memrealtime(); \
        if (__builtin_expect(__any(tmax > RTHR / C2), 0)) { \
            const float dl = __builtin_fmaxf(tmax, 0.f); \
            const float alpha = __builtin_amdgcn_exp2f(-dl * C2); \
            m_run += dl; l_run *= alpha; \
            _Pragma("unroll") for (int r = 0; r < 16; ++r) { sc[r] -= dl; negm[r] = -m_run; } \
            _Pragma("unroll") for (int dt = 0; dt < 4; ++dt) \
                _Pragma("unroll") for (int r = 0; r < 16; ++r) o[dt][r] *= alpha; \
        } \
        const unsigned vwr = dwo + vso, kwr = dwo + kso;                         \
        vso = VSO(t); kso = KSO((t) + 2); \
        const unsigned va = vb + vso; \
        const unsigned ka0 = kb0 + kso, ka1 = kb1 + kso;                         \
        unsigned pw[8]; \
        float ps0 = 0.f, ps1 = 0.f, t2; \
        __builtin_amdgcn_sched_barrier(0); \
          \
        _Pragma("unroll") for (int i = 0; i < 8; ++i) { \
            sn = MFMA32(kf[i], qf[i], i == 0 ? negm : sn);                   \
            const int ss = i >> 2, dt = i & 3; \
            vlo[2 * dt + ss] = vtr(va + ss * 4096 + dt * 512); \
            vhi[2 * dt + ss] = vtr(va + ss * 4096 + 2048 + dt * 512); \
            if (i < 4) { EXP1(2 * i); EXP1(2 * i + 1); } else EXP1(4 + i); \
            if (i >= 1 && i <= 4) SUM1(i - 1); \
            if (i == 6) SUM1(4); \
            asm volatile("" : "+v"(sn), "+v"(sc)); \
            if (i == XS0 && (!TAIL || (t) < 13)) KVDMA(voffV, Vg + ((t) + 3) * 16384, vwr, 0); \
            if (i == XS1 && (!TAIL || (t) < 13)) KVDMA(voffV, Vg + ((t) + 3) * 16384, vwr, 2048); \
            if (i == XS0 && TAIL && (t) == 13) BDMA(Bs, 0, 2); \
            if (i == XS1 && TAIL && (t) == 13) BDMA(Bs, 0, 3); \
            if (i == XS0 && TAIL && (t) == 14) BDMA(Bs, 1, 1); \
            if (i == XS1 && TAIL && (t) == 14) BDMA(Bs, 1, 2); \
            __builtin_amdgcn_sched_barrier(0); \
        } \
          \
        _Pragma("unroll") for (int i = 0; i < 8; ++i) { \
            const int ss = i >> 2, dt = i & 3; \
            const s16x4 lo = vlo[2 * dt + ss], hi4 = vhi[2 * dt + ss]; \
            const bf16x8 vf = {lo[0], lo[1], lo[2], lo[3], hi4[0], hi4[1], hi4[2], hi4[3]}; \
            const bf16x8 pf = __builtin_bit_cast(bf16x8, u32x4{pw[4 * ss], pw[4 * ss + 1], pw[4 * ss + 2], pw[4 * ss + 3]}); \
            o[dt] = MFMA32(vf, pf, o[dt]); \
            if (i < 4) { kf[2 * i] = ldsr128(ka0 + i * 512); kf[2 * i + 1] = ldsr128(ka1 + i * 512); } \
            if (i < 2) { EXP1(12 + 2 * i); EXP1(13 + 2 * i); asm volatile("" : "+v"(sc)); } \
            if (i < 3) SUM1(5 + i); \
            if (i == 3) { l_run += ps0 + ps1; asm volatile("" : "+v"(sn), "+v"(l_run)); \
                          tmax = max3f(sn[0], sn[1], sn[2]); t2 = max3f(sn[3], sn[4], sn[5]); asm volatile("" : "+v"(tmax), "+v"(t2)); } \
            if (i == 4) { tmax = max3f(tmax, sn[6], sn[7]); t2 = max3f(t2, sn[8], sn[9]); tmax = max3f(tmax, sn[10], sn[11]); asm volatile("" : "+v"(tmax), "+v"(t2)); } \
            if (i == 5) { t2 = max3f(t2, sn[12], sn[13]); tmax = max3f(tmax, sn[14], sn[15]); tmax = max3f(tmax, t2, t2); asm volatile("" : "+v"(tmax)); } \
            if (i == 6) { tmax = swap_max3(tmax); asm volatile("" : "+v"(tmax)); } \
            if (i == YS0 && (!TAIL || (t) < 11)) KVDMA(voffK0, Kg + ((t) + 5) * 16384, kwr, 0); \
            if (i == YS1 && (!TAIL || (t) < 11)) KVDMA(voffK1, Kg + ((t) + 5) * 16384, kwr, 2048); \
            if (i == YS0 && TAIL && (t) == 11) BWARM(); \
            if (i == YS0 && TAIL && (t) == 12) BDMA(Bs, 0, 0); \
            if (i == YS1 && TAIL && (t) == 12) BDMA(Bs, 0, 1); \
            if (i == YS0 && TAIL && (t) == 13) BDMA(Bs, 0, 4); \
            if (i == YS1 && TAIL && (t) == 13) BDMA(Bs, 1, 0); \
            if (i == YS0 && TAIL && (t) == 14) BDMA(Bs, 1, 3); \
            if (i == YS1 && TAIL && (t) == 14) BDMA(Bs, 1, 4); \
            __builtin_amdgcn_sched_barrier(0); \
        } } while (0)
    unsigned boff[5];
    const char* Bs = Bw + 144 * nt;
#define BDMA(bs, c, i) QDMA(boff[i], (bs) + (c) * (256 * DM * 2), lds0 + (((c) + 1) & 3) * REG + wid * 5120 + (i) * 1024)
#define BWARM() do { unsigned ln_ = lane; asm volatile("" : "+v"(ln_)); const unsigned wo_ = (ln_ >> 1) * (DM * 2) + (ln_ & 1) * 128; \
        asm volatile("s_mov_b32 m0, %2\n\ts_nop 0\n\tglobal_load_lds_dword %0, %1" :: "v"(wo_), \
                     "s"(Bw + (2 + (mt & 1)) * (256 * DM * 2) + 128 * nt), "s"(lds0 + 32768 + wid * 256) : "memory"); } while (0)
    unsigned vso = VSO(3), kso = KSO(1);
#pragma unroll 1
    for (int t = 0; t < 10; t += 2) { STEP(t, sA, sB, false); STEP(t + 1, sB, sA, false); }
    {
        unsigned ln = lane;
        asm volatile("" : "+v"(ln));
#pragma unroll
        for (int i = 0; i < 5; ++i) {
            const unsigned n = 64 * i + ln, row = (n * 205u) >> 11, c = n - 10 * row;
            boff[i] = row * (DM * 2) + (c < 9 ? c : 0u) * 16;
        }
    }
#pragma unroll 1
    for (int t = 10; t < NT; t += 2) { STEP(t, sA, sB, true); STEP(t + 1, sB, sA, true); }
#undef STEP
#undef EXP1
#undef SUM1
    TL(2);
    l_run = swap_add(l_run);
    asm volatile("s_waitcnt lgkmcnt(0)\n\ts_barrier" ::: "memory");
    if (wid == 0) {
#pragma unroll
        for (int i = 0; i < 10; ++i) BDMA(Bs, 2 + i / 5, i % 5);
    }
    {
        f32x16 keep0, keep1, send0, send1;
        if (kh == 0) { keep0 = o[0]; keep1 = o[1]; send0 = o[2]; send1 = o[3]; }
        else         { keep0 = o[2]; keep1 = o[3]; send0 = o[0]; send1 = o[1]; }
        const unsigned xb = lds0 + (kh ? 0 : 3 * REG) + 5120 + qb * 8704;
#pragma unroll
        for (int r4 = 0; r4 < 4; ++r4) {
            f32x4 v0 = {send0[4 * r4], send0[4 * r4 + 1], send0[4 * r4 + 2], send0[4 * r4 + 3]};
            f32x4 v1 = {send1[4 * r4], send1[4 * r4 + 1], send1[4 * r4 + 2], send1[4 * r4 + 3]};
            *(LAS f32x4*)(uintptr_t)(xb + r4 * 1024 + lane * 16) = v0;
            *(LAS f32x4*)(uintptr_t)(xb + (4 + r4) * 1024 + lane * 16) = v1;
        }
        *(LAS float*)(uintptr_t)(xb + 8192 + lane * 4) = m_run;
        *(LAS float*)(uintptr_t)(xb + 8192 + 256 + lane * 4) = l_run;
        asm volatile("s_waitcnt lgkmcnt(0)\n\ts_barrier" ::: "memory");
        const unsigned pb = lds0 + (kh ? 3 * REG : 0) + 5120 + qb * 8704;
        const float m2 = *(const LAS float*)(uintptr_t)(pb + 8192 + lane * 4);
        const float l2 = *(const LAS float*)(uintptr_t)(pb + 8192 + 256 + lane * 4);
        const float mm = __builtin_fmaxf(m_run, m2);
        const float a1 = __builtin_amdgcn_exp2f((m_run - mm) * C2), a2 = __builtin_amdgcn_exp2f((m2 - mm) * C2);
        const float inv = 1.0f / (l_run * a1 + l2 * a2);
        const float w1 = a1 * inv, w2 = a2 * inv;
        const int row = b * SQ + 32 * (qb & 1) + r32;
        char* ob = ws + ((size_t)((row >> 4) * 128 + head * 4 + 2 * kh) * 64 + hi * 16 + (row & 15)) * 16;
#define OSTORE(KEEP, i) do { u32x2 w_[4]; \
            _Pragma("unroll") for (int r4 = 0; r4 < 4; ++r4) { \
                const f32x4 v = *(const LAS f32x4*)(uintptr_t)(pb + (4 * (i) + r4) * 1024 + lane * 16); \
                const float e0 = KEEP[4 * r4] * w1 + v[0] * w2, e1 = KEEP[4 * r4 + 1] * w1 + v[1] * w2; \
                const float e2 = KEEP[4 * r4 + 2] * w1 + v[2] * w2, e3 = KEEP[4 * r4 + 3] * w1 + v[3] * w2; \
                w_[r4] = u32x2{cvtpk(e0, e1), cvtpk(e2, e3)}; } \
            _Pragma("unroll") for (int g = 0; g < 2; ++g) { \
                u32x4 o4 = {w_[g].x, w_[g].y, w_[g + 2].x, w_[g + 2].y}; \
                asm volatile("global_store_dwordx4 %0, %1, off sc1\n\ts_nop 1" :: "v"(ob + (i) * 1024 + g * 512), "v"(o4) : "memory"); } } while (0)
        OSTORE(keep0, 0); OSTORE(keep1, 1);
#undef OSTORE
    }
    TL(3);
    const char* Ag = ws + (size_t)((8 * mt + 2 * qb) * 128 + 32 * sk + kh) * 1024;
    unsigned ao0 = lane * 16, ao1 = ao0 + 131072;
    unsigned bb[4];
#pragma unroll
    for (int c = 0; c < 4; ++c) { bb[c] = lds0 + ((c + 1) & 3) * REG + (32 * kh + 4 * gi + vq) * 160 + 8 * vp; asm volatile("" : "+v"(bb[c])); }
    asm volatile("" : "+v"(ao0), "+v"(ao1), "+s"(Ag));
    asm volatile("s_waitcnt vmcnt(0) lgkmcnt(0)\n\ts_barrier" ::: "memory");
    TL(4);
    unsigned dep1 = 0;
    if (wid == 0) {
        const unsigned td = rendezvous<8>(w1, tag, nt, lane, TMO, dep1);
        if (lane == 0) *(LAS unsigned*)(uintptr_t)(lds0 + REG + 144) = td;
    } else {
#pragma unroll
        for (int i = 0; i < 10; ++i) BDMA(Bs, 2 + i / 5, i % 5);
    }
    asm volatile("s_waitcnt lgkmcnt(0)\n\ts_barrier" ::: "memory");
    unsigned todo = __builtin_amdgcn_readfirstlane(*(const LAS unsigned*)(uintptr_t)(lds0 + REG + 144));
    TL(5);
    float* part = (float*)(ws + PART_OFF);
    while (todo) {
        const int ntc = ((todo >> nt) & 1u) ? nt : __builtin_ctz(todo);
        todo &= ~(1u << ntc);
        if (ntc != nt) {
            asm volatile("s_waitcnt vmcnt(0) lgkmcnt(0)\n\ts_barrier" ::: "memory");
#pragma unroll 1
            for (int c = 0; c < 4; ++c) {
#pragma unroll
                for (int i = 0; i < 5; ++i) BDMA(Bw + 144 * ntc, c, i);
            }
            asm volatile("s_waitcnt vmcnt(0)\n\ts_barrier" ::: "memory");
        }
        u32x4 an[32];
#define ALD(s_) do { const char* ag = Ag; asm volatile("" : "+s"(ag)); ag += ((s_) >> 1) * 4096; \
            if ((s_) & 1) { \
                asm volatile("global_load_dwordx4 %0, %1, %2 offset:2048 sc1" : "=v"(an[2 * (s_)]) : "v"(ao0), "s"(ag) : "memory"); \
                asm volatile("global_load_dwordx4 %0, %1, %2 offset:2048 sc1" : "=v"(an[2 * (s_) + 1]) : "v"(ao1), "s"(ag) : "memory"); \
            } else { \
                asm volatile("global_load_dwordx4 %0, %1, %2 sc1" : "=v"(an[2 * (s_)]) : "v"(ao0), "s"(ag) : "memory"); \
                asm volatile("global_load_dwordx4 %0, %1, %2 sc1" : "=v"(an[2 * (s_) + 1]) : "v"(ao1), "s"(ag) : "memory"); } } while (0)
#pragma unroll
        for (int s2 = 0; s2 < AHEAD; ++s2) ALD(s2);
        f32x4 acc[2][5];
#pragma unroll
        for (int ct = 0; ct < 5; ++ct) { acc[0][ct] = f32x4{0.f, 0.f, 0.f, 0.f}; acc[1][ct] = f32x4{0.f, 0.f, 0.f, 0.f}; }
        s16x4 blo[2][5], bhi[2][5];
#pragma unroll
        for (int ct = 0; ct < 5; ++ct) { blo[0][ct] = vtr(bb[0] + 32 * ct); bhi[0][ct] = vtr(bb[0] + 32 * ct + 2560); }
#pragma unroll
        for (int s = 0; s < 16; ++s) {
            asm volatile("s_waitcnt vmcnt(%2)" : "+v"(an[2 * s]), "+v"(an[2 * s + 1]) : "n"(s + AHEAD <= 16 ? 2 * AHEAD - 2 : 30 - 2 * s) : "memory");
            if (s == 0) { TL(6); }
            if (s == 7) asm volatile("s_barrier" ::: "memory");
            __builtin_amdgcn_sched_barrier(0);
#pragma unroll
            for (int ct = 0; ct < 5; ++ct) {
                if (s < 15) {
                    const unsigned ad = bb[((s + 1) >> 2) & 3] + ((s + 1) & 3) * 10240 + 32 * ct;
                    blo[(s + 1) & 1][ct] = vtr(ad); bhi[(s + 1) & 1][ct] = vtr(ad + 2560);
                }
                const s16x4 lo = blo[s & 1][ct], h4 = bhi[s & 1][ct];
                const bf16x8 bf = {lo[0], lo[1], lo[2], lo[3], h4[0], h4[1], h4[2], h4[3]};
                acc[0][ct] = MFMA16(bf, __builtin_bit_cast(bf16x8, an[2 * s]), acc[0][ct]);
                acc[1][ct] = MFMA16(bf, __builtin_bit_cast(bf16x8, an[2 * s + 1]), acc[1][ct]);
                if (ct == 1 && s + AHEAD < 16) ALD(s + AHEAD);
                __builtin_amdgcn_sched_barrier(0);
            }
        }
        TL(7);
#pragma unroll
        for (int ct = 0; ct < 5; ++ct)
            *(LAS f32x4*)(uintptr_t)(lds0 + REG + (wid * 5 + ct) * 1024 + lane * 16) = kh ? acc[0][ct] : acc[1][ct];
        asm volatile("s_waitcnt lgkmcnt(0)\n\ts_barrier" ::: "memory");
        float* pp = part + ((size_t)(sk * 64 + 8 * mt + 2 * qb + kh) * 8 + ntc) * 1152 + lane * 4;
#pragma unroll
        for (int ct = 0; ct < 5; ++ct) {
            const f32x4 v = *(const LAS f32x4*)(uintptr_t)(lds0 + REG + ((wid ^ 4) * 5 + ct) * 1024 + lane * 16);
            const f32x4 r_ = (kh ? acc[1][ct] : acc[0][ct]) + v;
            if (ct < 4 || lane < 32)
                asm volatile("global_store_dwordx4 %0, %1, off sc1\n\ts_nop 1" :: "v"(pp + ct * 256), "v"(r_) : "memory");
        }
        TL(8);
        asm volatile("s_waitcnt vmcnt(0)\n\ts_barrier" ::: "memory");
        TL(9);
        unsigned dep2 = 0;
        if (wid == 0) {
            const unsigned td = rendezvous<4>(w2 + ntc * 128, tag, sk, lane, TMO2, dep2);
            if (lane == 0) *(LAS unsigned*)(uintptr_t)(lds0 + 2 * REG) = td;
        }
        asm volatile("s_waitcnt lgkmcnt(0)\n\ts_barrier" ::: "memory");
        unsigned qs = __builtin_amdgcn_readfirstlane(*(const LAS unsigned*)(uintptr_t)(lds0 + 2 * REG));
        TL(10);
        while (qs) {
            const int q = __builtin_ctz(qs);
            qs &= qs - 1;
            if (tid < 288) {
                const float* p = part + ((size_t)(8 * mt + 2 * q) * 8 + ntc) * 1152 + tid * 4;
                f32x4 v[2][4];
#pragma unroll
                for (int r = 0; r < 2; ++r)
#pragma unroll
                    for (int s = 0; s < 4; ++s)
                        asm volatile("global_load_dwordx4 %0, %1, off sc1" : "=v"(v[r][s]) : "v"(p + (size_t)s * 64 * 8 * 1152 + r * 8 * 1152) : "memory");
                asm volatile("s_waitcnt vmcnt(0)" : "+v"(v[0][0]), "+v"(v[0][1]), "+v"(v[0][2]), "+v"(v[0][3]),
                                                    "+v"(v[1][0]), "+v"(v[1][1]), "+v"(v[1][2]), "+v"(v[1][3]) :: "memory");
#pragma unroll
                for (int r = 0; r < 2; ++r) {
                    const f32x4 t = ((v[r][0] + v[r][1]) + v[r][2]) + v[r][3];
                    u32x2 w = {cvtpk(t[0], t[1]), cvtpk(t[2], t[3])};
                    *(u32x2*)(out + (size_t)(16 * (8 * mt + 2 * q + r) + (tid & 15)) * DM + 72 * ntc + 4 * (tid >> 4)) = w;
                }
            }
        }
        if (wid == 0) {
            asm volatile("s_waitcnt vmcnt(0)" ::: "memory");
            depart<4>(w2 + ntc * 128, dep2, lane);
        }
    }
    if (wid == 0) {
        asm volatile("s_waitcnt vmcnt(0)" ::: "memory");
        depart<8>(w1, dep1, lane);
    }
    TL(11);
}

extern "C" void kernel_launch(void* const* d_in, const int* in_sizes, int n_in, void* d_out, int out_size,
                              void* d_ws, size_t ws_size, hipStream_t stream) {
    (void)hipFuncSetAttribute((const void*)gqa_kernel, hipFuncAttributeMaxDynamicSharedMemorySize, LDS_BYTES);
    gqa_kernel<<<NB * HKV, 512, LDS_BYTES, stream>>>((const u16*)d_in[0], (const u16*)d_in[1], (const u16*)d_in[2],
                                                     (const u16*)d_in[3], (const u16*)d_in[4], (char*)d_ws, (u16*)d_out);
}
```

```cpp
#include <hip/hip_runtime.h>
#include <hip/hip_bf16.h>
#include <stdint.h>

typedef short bf16x8 __attribute__((ext_vector_type(8)));
typedef short s16x4 __attribute__((ext_vector_type(4)));
typedef float f32x16 __attribute__((ext_vector_type(16)));
typedef float f32x4 __attribute__((ext_vector_type(4)));
typedef unsigned u32x4 __attribute__((ext_vector_type(4)));
typedef unsigned u32x2 __attribute__((ext_vector_type(2)));
typedef unsigned short u16;
#define LAS __attribute__((address_space(3)))
typedef __attribute__((address_space(1))) unsigned gu32;
#define RLX_AGENT __ATOMIC_RELAXED, __HIP_MEMORY_SCOPE_AGENT

constexpr int NB = 16, SQ = 64, DM = 576, HQ = 32, HKV = 16, SKV = 1024, DH = 128;
constexpr int NQ = HQ * DH;
constexpr int NT = SKV / 64;
constexpr int REG = 40960;
constexpr int LDS_BYTES = 4 * REG;
constexpr size_t PART_OFF = (size_t)NB * SQ * NQ * 2;
constexpr size_t CNT_OFF = (size_t)20 << 20;
#define AHEAD 4
#define TMO 5000ull
#define TMO2 5000ull
constexpr int XS0 = 5, XS1 = 7, YS0 = 5, YS1 = 7;
constexpr int PACE = 108;
constexpr float RTHR = 8.0f;
constexpr float C2 = 0.088388347648318447f * 1.4426950408889634f;

#define MFMA32(a, b, c) __builtin_amdgcn_mfma_f32_32x32x16_bf16(a, b, c, 0, 0, 0)

typedef __bf16 bf16x2_t __attribute__((ext_vector_type(2)));
typedef float f32x2 __attribute__((ext_vector_type(2)));
__device__ __forceinline__ unsigned cvtpk(float lo, float hi) {
    f32x2 v = {lo, hi};
    return __builtin_bit_cast(unsigned, __builtin_convertvector(v, bf16x2_t));
}
__device__ __forceinline__ void glds16(const void* g, unsigned lds_base) {
    unsigned sv;
    asm volatile("s_mov_b32 %0, m0\n\ts_mov_b32 m0, %2\n\ts_nop 0\n\tglobal_load_lds_dwordx4 %1, off\n\ts_mov_b32 m0, %0"
                 : "=&s"(sv) : "v"(g), "s"(lds_base) : "memory");
}
__device__ __forceinline__ s16x4 vtr(unsigned a) {
    return __builtin_amdgcn_ds_read_tr16_b64_v4i16((LAS s16x4*)(uintptr_t)a);
}
__device__ __forceinline__ bf16x8 ldsr128(unsigned a) { return *(const LAS bf16x8*)(uintptr_t)a; }
__device__ __forceinline__ float swap_max(float v) {
    auto rr = __builtin_amdgcn_permlane32_swap(__float_as_uint(v), __float_as_uint(v), false, false);
    return __builtin_fmaxf(__uint_as_float(rr[0]), __uint_as_float(rr[1]));
}
__device__ __forceinline__ float max3f(float a, float b, float c) { float r; asm("v_max3_f32 %0, %1, %2, %3" : "=v"(r) : "v"(a), "v"(b), "v"(c)); return r; }
__device__ __forceinline__ float fmul1(float a, float b) { float r; asm("v_mul_f32_e32 %0, %1, %2" : "=v"(r) : "s"(b), "v"(a)); return r; }
__device__ __forceinline__ float swap_max3(float v) {
    auto rr = __builtin_amdgcn_permlane32_swap(__float_as_uint(v), __float_as_uint(v), false, false);
    return max3f(__uint_as_float(rr[0]), __uint_as_float(rr[1]), __uint_as_float(rr[1]));
}
__device__ __forceinline__ float swap_add(float v) {
    auto rr = __builtin_amdgcn_permlane32_swap(__float_as_uint(v), __float_as_uint(v), false, false);
    return __uint_as_float(rr[0]) + __uint_as_float(rr[1]);
}

#define TL(i)
extern "C" __device__ unsigned long long amd_dispatch_id() __asm("llvm.amdgcn.dispatch.id");

template <unsigned N>
__device__ __forceinline__ unsigned rendezvous(const char* w, unsigned tag, unsigned own, int lane, unsigned long long tmo, unsigned& dep) {
    unsigned old = 0, todo = 1u << own;
    u32x2 v;
    if (lane == 0) asm volatile("global_atomic_add %0, %1, %2, off sc0" : "=v"(old) : "v"(w), "v"(1u) : "memory");
    asm volatile("global_load_dwordx2 %0, %1, off sc1" : "=v"(v) : "v"(w) : "memory");
    asm volatile("s_waitcnt vmcnt(0)" : "+v"(old), "+v"(v) :: "memory");
    old = __builtin_amdgcn_readfirstlane(old);
    if ((old & 255u) == N - 1) todo |= (old >> 8) & 255u;
    else {
        const unsigned long long t0 = __builtin_amdgcn_s_memrealtime();
        while ((__builtin_amdgcn_readfirstlane(v.x) & 255u) != N || __builtin_amdgcn_readfirstlane(v.y) != tag) {
            if (__builtin_amdgcn_s_memrealtime() - t0 > tmo) {
                unsigned o2 = 0;
                if (lane == 0) asm volatile("global_atomic_or %0, %1, %2, off sc0" : "=v"(o2) : "v"(w), "v"(256u << own) : "memory");
                asm volatile("s_waitcnt vmcnt(0)" : "+v"(o2) :: "memory");
                if ((__builtin_amdgcn_readfirstlane(o2) & 255u) != N) todo = 0;
                break;
            }
            asm volatile("global_load_dwordx2 %0, %1, off sc1\n\ts_waitcnt vmcnt(0)" : "=v"(v) : "v"(w) : "memory");
        }
    }
    dep = 0;
    if (lane == 0) asm volatile("global_atomic_add %0, %1, %2, off sc0" : "=v"(dep) : "v"(w), "v"(65536u) : "memory");
    return todo;
}
template <unsigned N>
__device__ __forceinline__ void depart(const char* w, unsigned dep, int lane) {
    asm volatile("" : "+v"(dep));
    if (lane == 0 && ((dep >> 16) & 255u) == N - 1)
        asm volatile("global_store_dwordx2 %0, %1, off sc1" :: "v"(w), "v"(u32x2{0u, 0u}) : "memory");
}

#define MFMA16(a, b, c) __builtin_amdgcn_mfma_f32_16x16x32_bf16(a, b, c, 0, 0, 0)
__global__ __launch_bounds__(512, 2) void gqa_kernel(const u16* __restrict__ x, const u16* __restrict__ Kc,
                                                     const u16* __restrict__ Vc, const u16* __restrict__ wq,
                                                     const u16* __restrict__ wo, char* ws, u16* __restrict__ out) {
    extern __shared__ __attribute__((aligned(16))) char lds[];
    TL(0);
    const unsigned lds0 = (unsigned)(uintptr_t)lds;
    const int tid = threadIdx.x, lane = tid & 63, r32 = lane & 31, hi = lane >> 5;
    const int wid = __builtin_amdgcn_readfirstlane(tid >> 6);
    const int qb = wid & 3, kh = wid >> 2;
    const int bid = blockIdx.x, b = 8 * (bid & 1) + ((bid >> 3) & 7), kvh = 4 * ((bid >> 1) & 3) + (bid >> 6);
    const int head = 2 * kvh + (qb >> 1);
    const int mt = b >> 1, sk = kvh >> 2, nt = 4 * (b & 1) + (kvh & 3);
    const unsigned tag = 0x80000000u | ((unsigned)amd_dispatch_id() & 0xfffffu) |
                         ((((unsigned)((uintptr_t)__builtin_amdgcn_dispatch_ptr() >> 12) * 0x9e3779b1u) >> 21) << 20);
    const char* w1 = ws + CNT_OFF + (mt * 4 + sk) * 128;
    const char* w2 = ws + CNT_OFF + 4096 + mt * 8 * 128;
    const char* wcl = lane == 0 ? w1 : w2 + nt * 128;
    u32x2 clm = {0u, tag};
    if (wid == 1 && lane < 2)
        asm volatile("global_atomic_cmpswap_x2 %0, %1, %2, off sc0\n\ts_nop 1" : "=v"(clm) : "v"(wcl), "v"(u32x4{0u, tag, 0u, 0u}) : "memory");
    const int gi = lane >> 4, vq = (lane & 15) >> 2, vp = lane & 3;

    const unsigned vsrc = (16 * (wid >> 1) + ((lane >> 2) & 7)) * 256 + (wid & 1) * 128 + hi * 64;
    const unsigned voffV = (vsrc ^ (kh * 128)) + vp * 16;
    const unsigned voffK0 = vsrc + ((vp ^ (wid & 2)) << 4), voffK1 = vsrc + ((vp ^ ((wid & 2) | 1)) << 4);
    const char* Kg = (const char*)Kc + (size_t)(b * HKV + kvh) * SKV * DH * 2;
    const char* Vg = (const char*)Vc + (size_t)(b * HKV + kvh) * SKV * DH * 2;
    const unsigned dwo = lds0 + (wid >> 1) * 4096 + (wid & 1) * 1024;
#define KVDMA(voff, gp, dst, OFF) asm volatile("s_mov_b32 m0, %2\n\ts_nop 0\n\tglobal_load_lds_dwordx4 %0, %1 offset:" #OFF " sc1 nt" \
            :: "v"(voff), "s"(gp), "s"(dst) : "memory")
#define VSO(tn) ((int)((0x0200110au >> (8 * ((tn) & 3))) & 255) * 8192)
#define KSO(tn) ((int)((0x0f0c0705u >> (8 * ((tn) & 3))) & 255) * 8192)
#define DMA_V0(tn) KVDMA(voffV, Vg + (tn) * 16384, dwo + VSO(tn), 0)
#define DMA_V1(tn) KVDMA(voffV, Vg + (tn) * 16384, dwo + VSO(tn), 2048)
#define DMA_K0(tn) KVDMA(voffK0, Kg + (tn) * 16384, dwo + KSO(tn), 0)
#define DMA_K1(tn) KVDMA(voffK1, Kg + (tn) * 16384, dwo + KSO(tn), 2048)
#define DMA_V(tn) do { DMA_V0(tn); DMA_V1(tn); } while (0)
#define DMA_K(tn) do { DMA_K0(tn); DMA_K1(tn); } while (0)
    const char* Bw = (const char*)wo + (size_t)(1024 * sk + 32 * wid) * DM * 2;
    bf16x8 qf[8];
    {
        int kb = (b + 2 * (kvh & 3)) % 9;
        const unsigned xo = (8 * wid + (lane >> 3)) * DM * 2 + (((lane & 7) ^ ((4 * wid + (lane >> 4)) & 7)) << 4);
        const unsigned wo0 = (8 * wid + hi) * NQ * 2 + ((r32 ^ (hi << 2)) << 4);
        const unsigned wo1 = (8 * wid + 2 + hi) * NQ * 2 + ((r32 ^ ((2 + hi) << 2)) << 4);
        const unsigned wo2 = wo0 + 4 * NQ * 2, wo3 = wo1 + 4 * NQ * 2;
        const char* Xs = (const char*)x + (size_t)b * SQ * DM * 2;
        const char* Ws = (const char*)wq + kvh * 512;
#define QDMA(voff, gp, dst) asm volatile("s_mov_b32 m0, %2\n\ts_nop 0\n\tglobal_load_lds_dwordx4 %0, %1" :: "v"(voff), "s"(gp), "s"(dst) : "memory")
#define QS_PIECE(slot, p) do { const unsigned s_ = lds0 + (slot) * 40960 + wid * 4096; const char* w_ = Ws + (size_t)kb * 64 * NQ * 2; \
            if ((p) == 0) QDMA(wo0, w_, s_); \
            if ((p) == 1) QDMA(wo1, w_, s_ + 1024); \
            if ((p) == 2) QDMA(wo2, w_, s_ + 2048); \
            if ((p) == 3) QDMA(wo3, w_, s_ + 3072); \
            if ((p) == 4) { QDMA(xo, Xs + kb * 128, lds0 + (slot) * 40960 + 32768 + wid * 1024); kb = kb == 8 ? 0 : kb + 1; } } while (0)
#define QS_ISSUE(slot) do { QS_PIECE(slot, 0); QS_PIECE(slot, 1); QS_PIECE(slot, 2); QS_PIECE(slot, 3); QS_PIECE(slot, 4); } while (0)
#pragma unroll 1
        for (int j = 0; j < 2; ++j) { QS_ISSUE(j); asm volatile("s_barrier" ::: "memory"); }
        const int pp = ((vp & 1) << 1) | (vp >> 1);
        const unsigned abase = lds0 + (8 * hi + vq) * 512 + (((4 * wid + 2 * (gi & 1) + (pp >> 1)) ^ (vq << 2)) << 4) + 8 * (pp & 1);
        const unsigned xbase = lds0 + 32768 + r32 * 128 + ((hi ^ ((r32 >> 1) & 7)) << 4);
        f32x16 acc0 = {}, acc1 = {};
        s16x4 lo[2][4], h4[2][4]; bf16x8 x0[2][4], x1[2][4];
        asm volatile("s_waitcnt vmcnt(5)\n\ts_barrier" : "+v"(clm) :: "memory");
        QS_ISSUE(2);
        if (wid == 1 && lane < 2 && clm.y != tag && (clm.x | clm.y) != 0u)
            asm volatile("global_atomic_cmpswap_x2 %0, %1, off\n\ts_nop 1" :: "v"(wcl), "v"(u32x4{0u, tag, clm.x, clm.y}) : "memory");
#pragma unroll
        for (int i = 0; i < 4; ++i) {
            lo[0][i] = vtr(abase + i * 8192); h4[0][i] = vtr(abase + i * 8192 + 2048);
            x0[0][i] = ldsr128(xbase ^ (i << 5)); x1[0][i] = ldsr128((xbase ^ (i << 5)) + 4096);
        }
#pragma unroll
        for (int st = 0; st < 9; ++st) {
            if (st == 0) asm volatile("s_waitcnt vmcnt(5)" ::: "memory");
            else if (st < 6) asm volatile("s_waitcnt vmcnt(10)" ::: "memory");
            else if (st == 6) asm volatile("s_waitcnt vmcnt(9)" ::: "memory");
            else if (st == 7) asm volatile("s_waitcnt vmcnt(6)" ::: "memory");
            asm volatile("s_waitcnt lgkmcnt(0)\n\ts_barrier" ::: "memory");
            const unsigned so = ((st + 1) & 3) * 40960;
            const int cu = st & 1, nx = cu ^ 1;
            unsigned aa = abase + so;
            asm volatile("" : "+v"(aa));
            __builtin_amdgcn_sched_barrier(0);
            if (st == 0) QS_ISSUE(3);
#pragma unroll
            for (int i = 0; i < 4; ++i) {
                const bf16x8 a0 = {lo[cu][i][0], lo[cu][i][1], lo[cu][i][2], lo[cu][i][3], h4[cu][i][0], h4[cu][i][1], h4[cu][i][2], h4[cu][i][3]};
                acc0 = MFMA32(a0, x0[cu][i], acc0);
                if (st < 8) { lo[nx][i] = vtr(aa + i * 8192); h4[nx][i] = vtr(aa + i * 8192 + 2048); }
                if (st < 5) QS_PIECE(st & 3, i);
                __builtin_amdgcn_sched_barrier(0);
                acc1 = MFMA32(a0, x1[cu][i], acc1);
                if (st < 8) { unsigned xa = (xbase ^ (i << 5)) + so; asm volatile("" : "+v"(xa)); x0[nx][i] = ldsr128(xa); x1[nx][i] = ldsr128(xa + 4096); }
                if (st < 5 && i == 3) QS_PIECE(st & 3, 4);
                if (st == 5 && i == 3) { DMA_K(0); DMA_K(1); }
                if (st == 6 && i == 3) DMA_V(0);
                if (st == 7 && i == 3) DMA_K(2);
                if (st == 8 && i == 3) DMA_K(3);
                __builtin_amdgcn_sched_barrier(0);
            }
        }
#undef QS_ISSUE
#undef QS_PIECE
#define QW(ACC, qblk) do { _Pragma("unroll") for (int s = 0; s < 2; ++s) { \
            u32x4 w_ = {cvtpk(ACC[8 * s], ACC[8 * s + 1]), cvtpk(ACC[8 * s + 2], ACC[8 * s + 3]), \
                        cvtpk(ACC[8 * s + 4], ACC[8 * s + 5]), cvtpk(ACC[8 * s + 6], ACC[8 * s + 7])}; \
            *(LAS u32x4*)(uintptr_t)(lds0 + 32768 + (2 * (wid >> 2) + (qblk)) * REG + (2 * (wid & 3) + s) * 1024 + lane * 16) = w_; } } while (0)
        QW(acc0, 0); QW(acc1, 1);
#undef QW
        DMA_V(1);
    }
    asm volatile("s_waitcnt vmcnt(8) lgkmcnt(0)\n\ts_barrier" ::: "memory");
#pragma unroll
    for (int f = 0; f < 8; ++f) qf[f] = ldsr128(lds0 + 32768 + qb * REG + f * 1024 + lane * 16);
    TL(1);

    const unsigned kb0 = lds0 + (4 * kh + (r32 >> 3)) * 2048 + (r32 & 7) * 64 + ((hi ^ (r32 >> 3)) << 4);
    const unsigned kb1 = kb0 ^ 32;
    const unsigned vb = lds0 + kh * 8192 + (4 * hi + vq) * 64 + (gi & 1) * 32 + vp * 8;

    float m_run, l_run = 0.f, tmax = 0.f;
    unsigned long long tprev = 0;
    f32x16 o[4] = {};
    f32x16 sA, sB, negm;

    bf16x8 kf[8];
    {
        bf16x8 k0[8];
        unsigned ka0 = kb0 + KSO(0), ka1 = kb1 + KSO(0);
        asm volatile("" : "+v"(ka0), "+v"(ka1));
#pragma unroll
        for (int d0 = 0; d0 < 8; ++d0) k0[d0] = ldsr128(((d0 & 1) ? ka1 : ka0) + (d0 >> 1) * 512);
#pragma unroll
        for (int d0 = 0; d0 < 8; ++d0) kf[d0] = ldsr128(((d0 & 1) ? ka1 : ka0) + 16384 + (d0 >> 1) * 512);
        __builtin_amdgcn_sched_barrier(0);
        asm volatile("s_waitcnt lgkmcnt(0)\n\ts_barrier" ::: "memory");
        sA = f32x16{};
#pragma unroll
        for (int d0 = 0; d0 < 8; ++d0) sA = MFMA32(k0[d0], qf[d0], sA);
        __builtin_amdgcn_sched_barrier(0);
        DMA_V(2); DMA_K(4);
        m_run = sA[0];
#pragma unroll
        for (int r = 1; r < 16; ++r) m_run = __builtin_fmaxf(m_run, sA[r]);
        m_run = swap_max(m_run);
#pragma unroll
        for (int r = 0; r < 16; ++r) { sA[r] -= m_run; negm[r] = -m_run; }
    }
#define EXP1(r) do { sc[r] = __builtin_amdgcn_exp2f(fmul1(sc[r], C2)); } while (0)
#define SUM1(k) do { ps0 += sc[2 * (k)]; ps1 += sc[2 * (k) + 1]; pw[k] = cvtpk(sc[2 * (k)], sc[2 * (k) + 1]); asm volatile("" : "+v"(pw[k]), "+v"(ps0), "+v"(ps1)); } while (0)
#define STEP(t, SC_, SN_, TAIL) do { \
        f32x16& sc = SC_; f32x16& sn = SN_; \
        s16x4 vlo[8], vhi[8]; \
        if (!TAIL || (t) < 12) asm volatile("s_waitcnt vmcnt(8)" ::: "memory"); \
        else if ((t) < 14) asm volatile("s_waitcnt vmcnt(7)" ::: "memory"); \
        else if ((t) == 14) asm volatile("s_waitcnt vmcnt(9)" ::: "memory"); \
        else asm volatile("s_waitcnt vmcnt(10)" ::: "memory"); \
        while (__builtin_amdgcn_s_memrealtime() - tprev < (unsigned long long)PACE) __builtin_amdgcn_s_sleep(1); \
        asm volatile("s_waitcnt lgkmcnt(0)\n\ts_barrier" ::: "memory"); \
        tprev = __builtin_amdgcn_s_memrealtime(); \
        if (__builtin_expect(__any(tmax > RTHR / C2), 0)) { \
            const float dl = __builtin_fmaxf(tmax, 0.f); \
            const float alpha = __builtin_amdgcn_exp2f(-dl * C2); \
            m_run += dl; l_run *= alpha; \
            _Pragma("unroll") for (int r = 0; r < 16; ++r) { sc[r] -= dl; negm[r] = -m_run; } \
            _Pragma("unroll") for (int dt = 0; dt < 4; ++dt) \
                _Pragma("unroll") for (int r = 0; r < 16; ++r) o[dt][r] *= alpha; \
        } \
        const unsigned vwr = dwo + vso, kwr = dwo + kso;                         \
        vso = VSO(t); kso = KSO((t) + 2); \
        const unsigned va = vb + vso; \
        const unsigned ka0 = kb0 + kso, ka1 = kb1 + kso;                         \
        unsigned pw[8]; \
        float ps0 = 0.f, ps1 = 0.f, t2; \
        __builtin_amdgcn_sched_barrier(0); \
          \
        _Pragma("unroll") for (int i = 0; i < 8; ++i) { \
            sn = MFMA32(kf[i], qf[i], i == 0 ? negm : sn);                   \
            const int ss = i >> 2, dt = i & 3; \
            vlo[2 * dt + ss] = vtr(va + ss * 4096 + dt * 512); \
            vhi[2 * dt + ss] = vtr(va + ss * 4096 + 2048 + dt * 512); \
            if (i < 4) { EXP1(2 * i); EXP1(2 * i + 1); } else EXP1(4 + i); \
            if (i >= 1 && i <= 4) SUM1(i - 1); \
            if (i == 6) SUM1(4); \
            asm volatile("" : "+v"(sn), "+v"(sc)); \
            if (i == XS0 && (!TAIL || (t) < 13)) KVDMA(voffV, Vg + ((t) + 3) * 16384, vwr, 0); \
            if (i == XS1 && (!TAIL || (t) < 13)) KVDMA(voffV, Vg + ((t) + 3) * 16384, vwr, 2048); \
            if (i == XS0 && TAIL && (t) == 13) BDMA(Bs, 0, 2); \
            if (i == XS1 && TAIL && (t) == 13) BDMA(Bs, 0, 3); \
            if (i == XS0 && TAIL && (t) == 14) BDMA(Bs, 1, 1); \
            if (i == XS1 && TAIL && (t) == 14) BDMA(Bs, 1, 2); \
            __builtin_amdgcn_sched_barrier(0); \
        } \
          \
        _Pragma("unroll") for (int i = 0; i < 8; ++i) { \
            const int ss = i >> 2, dt = i & 3; \
            const s16x4 lo = vlo[2 * dt + ss], hi4 = vhi[2 * dt + ss]; \
            const bf16x8 vf = {lo[0], lo[1], lo[2], lo[3], hi4[0], hi4[1], hi4[2], hi4[3]}; \
            const bf16x8 pf = __builtin_bit_cast(bf16x8, u32x4{pw[4 * ss], pw[4 * ss + 1], pw[4 * ss + 2], pw[4 * ss + 3]}); \
            o[dt] = MFMA32(vf, pf, o[dt]); \
            if (i < 4) { kf[2 * i] = ldsr128(ka0 + i * 512); kf[2 * i + 1] = ldsr128(ka1 + i * 512); } \
            if (i < 2) { EXP1(12 + 2 * i); EXP1(13 + 2 * i); asm volatile("" : "+v"(sc)); } \
            if (i < 3) SUM1(5 + i); \
            if (i == 3) { l_run += ps0 + ps1; asm volatile("" : "+v"(sn), "+v"(l_run)); \
                          tmax = max3f(sn[0], sn[1], sn[2]); t2 = max3f(sn[3], sn[4], sn[5]); asm volatile("" : "+v"(tmax), "+v"(t2)); } \
            if (i == 4) { tmax = max3f(tmax, sn[6], sn[7]); t2 = max3f(t2, sn[8], sn[9]); tmax = max3f(tmax, sn[10], sn[11]); asm volatile("" : "+v"(tmax), "+v"(t2)); } \
            if (i == 5) { t2 = max3f(t2, sn[12], sn[13]); tmax = max3f(tmax, sn[14], sn[15]); tmax = max3f(tmax, t2, t2); asm volatile("" : "+v"(tmax)); } \
            if (i == 6) { tmax = swap_max3(tmax); asm volatile("" : "+v"(tmax)); } \
            if (i == YS0 && (!TAIL || (t) < 11)) KVDMA(voffK0, Kg + ((t) + 5) * 16384, kwr, 0); \
            if (i == YS1 && (!TAIL || (t) < 11)) KVDMA(voffK1, Kg + ((t) + 5) * 16384, kwr, 2048); \
            if (i == YS0 && TAIL && (t) == 11) BWARM(); \
            if (i == YS0 && TAIL && (t) == 12) BDMA(Bs, 0, 0); \
            if (i == YS1 && TAIL && (t) == 12) BDMA(Bs, 0, 1); \
            if (i == YS0 && TAIL && (t) == 13) BDMA(Bs, 0, 4); \
            if (i == YS1 && TAIL && (t) == 13) BDMA(Bs, 1, 0); \
            if (i == YS0 && TAIL && (t) == 14) BDMA(Bs, 1, 3); \
            if (i == YS1 && TAIL && (t) == 14) BDMA(Bs, 1, 4); \
            __builtin_amdgcn_sched_barrier(0); \
        } } while (0)
    unsigned boff[5];
    const char* Bs = Bw + 144 * nt;
#define BDMA(bs, c, i) QDMA(boff[i], (bs) + (c) * (256 * DM * 2), lds0 + (((c) + 1) & 3) * REG + wid * 5120 + (i) * 1024)
#define BWARM() do { unsigned ln_ = lane; asm volatile("" : "+v"(ln_)); const unsigned wo_ = (ln_ >> 1) * (DM * 2) + (ln_ & 1) * 128; \
        asm volatile("s_mov_b32 m0, %2\n\ts_nop 0\n\tglobal_load_lds_dword %0, %1" :: "v"(wo_), \
                     "s"(Bw + (2 + (mt & 1)) * (256 * DM * 2) + 128 * nt), "s"(lds0 + 32768 + wid * 256) : "memory"); } while (0)
    unsigned vso = VSO(3), kso = KSO(1);
#pragma unroll 1
    for (int t = 0; t < 10; t += 2) { STEP(t, sA, sB, false); STEP(t + 1, sB, sA, false); }
    {
        unsigned ln = lane;
        asm volatile("" : "+v"(ln));
#pragma unroll
        for (int i = 0; i < 5; ++i) {
            const unsigned n = 64 * i + ln, row = (n * 205u) >> 11, c = n - 10 * row;
            boff[i] = row * (DM * 2) + (c < 9 ? c : 0u) * 16;
        }
    }
#pragma unroll 1
    for (int t = 10; t < NT; t += 2) { STEP(t, sA, sB, true); STEP(t + 1, sB, sA, true); }
#undef STEP
#undef EXP1
#undef SUM1
    TL(2);
    l_run = swap_add(l_run);
    asm volatile("s_waitcnt lgkmcnt(0)\n\ts_barrier" ::: "memory");
    {
        f32x16 &keep0 = o[0], &keep1 = o[1], &send0 = o[2], &send1 = o[3];
        const unsigned xb = lds0 + (kh ? 0 : 3 * REG) + qb * 8704;
#pragma unroll
        for (int r4 = 0; r4 < 4; ++r4) {
            f32x4 v0 = {send0[4 * r4], send0[4 * r4 + 1], send0[4 * r4 + 2], send0[4 * r4 + 3]};
            f32x4 v1 = {send1[4 * r4], send1[4 * r4 + 1], send1[4 * r4 + 2], send1[4 * r4 + 3]};
            *(LAS f32x4*)(uintptr_t)(xb + r4 * 1024 + lane * 16) = v0;
            *(LAS f32x4*)(uintptr_t)(xb + (4 + r4) * 1024 + lane * 16) = v1;
        }
        *(LAS float*)(uintptr_t)(xb + 8192 + lane * 4) = m_run;
        *(LAS float*)(uintptr_t)(xb + 8192 + 256 + lane * 4) = l_run;
        asm volatile("s_waitcnt lgkmcnt(0)\n\ts_barrier" ::: "memory");
        const unsigned pb = lds0 + (kh ? 3 * REG : 0) + qb * 8704;
        const float m2 = *(const LAS float*)(uintptr_t)(pb + 8192 + lane * 4);
        const float l2 = *(const LAS float*)(uintptr_t)(pb + 8192 + 256 + lane * 4);
        const float mm = __builtin_fmaxf(m_run, m2);
        const float a1 = __builtin_amdgcn_exp2f((m_run - mm) * C2), a2 = __builtin_amdgcn_exp2f((m2 - mm) * C2);
        const float inv = 1.0f / (l_run * a1 + l2 * a2);
        const float w1 = a1 * inv, w2 = a2 * inv;
        const int row = b * SQ + 32 * (qb & 1) + r32;
        char* ob = ws + ((size_t)((row >> 4) * 128 + head * 4 + 2 * kh) * 64 + hi * 16 + (row & 15)) * 16;
#define OSTORE(KEEP, i) do { u32x2 w_[4]; \
            _Pragma("unroll") for (int r4 = 0; r4 < 4; ++r4) { \
                const f32x4 v = *(const LAS f32x4*)(uintptr_t)(pb + (4 * (i) + r4) * 1024 + lane * 16); \
                const float e0 = KEEP[4 * r4] * w1 + v[0] * w2, e1 = KEEP[4 * r4 + 1] * w1 + v[1] * w2; \
                const float e2 = KEEP[4 * r4 + 2] * w1 + v[2] * w2, e3 = KEEP[4 * r4 + 3] * w1 + v[3] * w2; \
                w_[r4] = u32x2{cvtpk(e0, e1), cvtpk(e2, e3)}; } \
            _Pragma("unroll") for (int g = 0; g < 2; ++g) { \
                u32x4 o4 = {w_[g].x, w_[g].y, w_[g + 2].x, w_[g + 2].y}; \
                asm volatile("global_store_dwordx4 %0, %1, off sc1\n\ts_nop 1" :: "v"(ob + (i) * 1024 + g * 512), "v"(o4) : "memory"); } } while (0)
        OSTORE(keep0, 0); OSTORE(keep1, 1);
#undef OSTORE
    }
    TL(3);
    const char* Ag = ws + (size_t)((8 * mt + 2 * qb) * 128 + 32 * sk + kh) * 1024;
    unsigned ao0 = lane * 16 + kh * 131072, ao1 = ao0 ^ 131072;
    unsigned bb[4];
#pragma unroll
    for (int c = 0; c < 4; ++c) { bb[c] = lds0 + ((c + 1) & 3) * REG + (32 * kh + 4 * gi + vq) * 160 + 8 * vp; asm volatile("" : "+v"(bb[c])); }
    asm volatile("" : "+v"(ao0), "+v"(ao1), "+s"(Ag));
    asm volatile("s_waitcnt vmcnt(0) lgkmcnt(0)\n\ts_barrier" ::: "memory");
    TL(4);
    unsigned dep1 = 0;
    if (wid == 0) {
        const unsigned td = rendezvous<8>(w1, tag, nt, lane, TMO, dep1);
        if (lane == 0) *(LAS unsigned*)(uintptr_t)(lds0 + REG + 144) = td;
    } else {
#pragma unroll
        for (int i = 0; i < 10; ++i) BDMA(Bs, 2 + i / 5, i % 5);
        if (wid == 1) {
#pragma unroll
            for (int i = 0; i < 10; ++i)
                QDMA(boff[i % 5], Bs - 32 * DM * 2 + (2 + i / 5) * (256 * DM * 2), lds0 + ((3 + i / 5) & 3) * REG + (i % 5) * 1024);
        }
    }
    asm volatile("s_waitcnt lgkmcnt(0)\n\ts_barrier" ::: "memory");
    unsigned todo = __builtin_amdgcn_readfirstlane(*(const LAS unsigned*)(uintptr_t)(lds0 + REG + 144));
    TL(5);
    float* part = (float*)(ws + PART_OFF);
    bool left = false;
    while (todo) {
        const int ntc = ((todo >> nt) & 1u) ? nt : __builtin_ctz(todo);
        todo &= ~(1u << ntc);
        if (ntc != nt) {
            asm volatile("s_waitcnt vmcnt(0) lgkmcnt(0)\n\ts_barrier" ::: "memory");
#pragma unroll 1
            for (int c = 0; c < 4; ++c) {
#pragma unroll
                for (int i = 0; i < 5; ++i) BDMA(Bw + 144 * ntc, c, i);
            }
            asm volatile("s_waitcnt vmcnt(0)\n\ts_barrier" ::: "memory");
        }
        u32x4 an[32];
#define ALD(s_) do { const char* ag = Ag; asm volatile("" : "+s"(ag)); ag += ((s_) >> 1) * 4096; \
            if ((s_) & 1) { \
                asm volatile("global_load_dwordx4 %0, %1, %2 offset:2048 sc1" : "=v"(an[2 * (s_)]) : "v"(ao0), "s"(ag) : "memory"); \
                asm volatile("global_load_dwordx4 %0, %1, %2 offset:2048 sc1" : "=v"(an[2 * (s_) + 1]) : "v"(ao1), "s"(ag) : "memory"); \
            } else { \
                asm volatile("global_load_dwordx4 %0, %1, %2 sc1" : "=v"(an[2 * (s_)]) : "v"(ao0), "s"(ag) : "memory"); \
                asm volatile("global_load_dwordx4 %0, %1, %2 sc1" : "=v"(an[2 * (s_) + 1]) : "v"(ao1), "s"(ag) : "memory"); } } while (0)
#pragma unroll
        for (int s2 = 0; s2 < AHEAD; ++s2) ALD(s2);
        f32x4 acc[2][5];
#pragma unroll
        for (int ct = 0; ct < 5; ++ct) { acc[0][ct] = f32x4{0.f, 0.f, 0.f, 0.f}; acc[1][ct] = f32x4{0.f, 0.f, 0.f, 0.f}; }
        s16x4 blo[2][5], bhi[2][5];
#pragma unroll
        for (int ct = 0; ct < 5; ++ct) { blo[0][ct] = vtr(bb[0] + 32 * ct); bhi[0][ct] = vtr(bb[0] + 32 * ct + 2560); }
#pragma unroll
        for (int s = 0; s < 16; ++s) {
            asm volatile("s_waitcnt vmcnt(%2)" : "+v"(an[2 * s]), "+v"(an[2 * s + 1]) : "n"(s + AHEAD <= 16 ? 2 * AHEAD - 2 : 30 - 2 * s) : "memory");
            if (s == 0) { TL(6); }
            if (s == 7) asm volatile("s_barrier" ::: "memory");
            __builtin_amdgcn_sched_barrier(0);
#pragma unroll
            for (int ct = 0; ct < 5; ++ct) {
                if (s < 15) {
                    const unsigned ad = bb[((s + 1) >> 2) & 3] + ((s + 1) & 3) * 10240 + 32 * ct;
                    blo[(s + 1) & 1][ct] = vtr(ad); bhi[(s + 1) & 1][ct] = vtr(ad + 2560);
                }
                const s16x4 lo = blo[s & 1][ct], h4 = bhi[s & 1][ct];
                const bf16x8 bf = {lo[0], lo[1], lo[2], lo[3], h4[0], h4[1], h4[2], h4[3]};
                acc[0][ct] = MFMA16(bf, __builtin_bit_cast(bf16x8, an[2 * s]), acc[0][ct]);
                acc[1][ct] = MFMA16(bf, __builtin_bit_cast(bf16x8, an[2 * s + 1]), acc[1][ct]);
                if (ct == 1 && s + AHEAD < 16) ALD(s + AHEAD);
                __builtin_amdgcn_sched_barrier(0);
            }
        }
        TL(7);
#pragma unroll
        for (int ct = 0; ct < 5; ++ct)
            *(LAS f32x4*)(uintptr_t)(lds0 + REG + (wid * 5 + ct) * 1024 + lane * 16) = acc[1][ct];
        asm volatile("s_waitcnt lgkmcnt(0)\n\ts_barrier" ::: "memory");
        float* pp = part + ((size_t)(sk * 64 + 8 * mt + 2 * qb + kh) * 8 + ntc) * 1152 + lane * 4;
#pragma unroll
        for (int ct = 0; ct < 5; ++ct) acc[1][ct] = *(const LAS f32x4*)(uintptr_t)(lds0 + REG + ((wid ^ 4) * 5 + ct) * 1024 + lane * 16);
        if (wid == 0 && !left) { left = true; depart<8>(w1, dep1, lane); }
#pragma unroll
        for (int ct = 0; ct < 5; ++ct) {
            const f32x4 r_ = acc[0][ct] + acc[1][ct];
            if (ct < 4 || lane < 32)
                asm volatile("global_store_dwordx4 %0, %1, off sc1\n\ts_nop 1" :: "v"(pp + ct * 256), "v"(r_) : "memory");
        }
        TL(8);
        asm volatile("s_waitcnt vmcnt(0)\n\ts_barrier" ::: "memory");
        TL(9);
        unsigned dep2 = 0;
        if (wid == 0) {
            const unsigned td = rendezvous<4>(w2 + ntc * 128, tag, sk, lane, TMO2, dep2);
            if (lane == 0) *(LAS unsigned*)(uintptr_t)(lds0 + 2 * REG) = td;
        }
        asm volatile("s_waitcnt lgkmcnt(0)\n\ts_barrier" ::: "memory");
        unsigned qs = __builtin_amdgcn_readfirstlane(*(const LAS unsigned*)(uintptr_t)(lds0 + 2 * REG));
        TL(10);
        bool left2 = false;
        while (qs) {
            const int q = __builtin_ctz(qs);
            qs &= qs - 1;
            if (tid < 288) {
                const float* p = part + ((size_t)(8 * mt + 2 * q) * 8 + ntc) * 1152 + tid * 4;
                f32x4 v[2][4];
#pragma unroll
                for (int r = 0; r < 2; ++r)
#pragma unroll
                    for (int s = 0; s < 4; ++s)
                        asm volatile("global_load_dwordx4 %0, %1, off sc1" : "=v"(v[r][s]) : "v"(p + (size_t)s * 64 * 8 * 1152 + r * 8 * 1152) : "memory");
                asm volatile("s_waitcnt vmcnt(0)" : "+v"(v[0][0]), "+v"(v[0][1]), "+v"(v[0][2]), "+v"(v[0][3]),
                                                    "+v"(v[1][0]), "+v"(v[1][1]), "+v"(v[1][2]), "+v"(v[1][3]) :: "memory");
#pragma unroll
                for (int r = 0; r < 2; ++r) {
                    const f32x4 t = ((v[r][0] + v[r][1]) + v[r][2]) + v[r][3];
                    u32x2 w = {cvtpk(t[0], t[1]), cvtpk(t[2], t[3])};
                    *(u32x2*)(out + (size_t)(16 * (8 * mt + 2 * q + r) + (tid & 15)) * DM + 72 * ntc + 4 * (tid >> 4)) = w;
                }
            }
            if (wid == 0 && !left2) { left2 = true; depart<4>(w2 + ntc * 128, dep2, lane); }
        }
        if (wid == 0 && !left2) {
            asm volatile("s_waitcnt vmcnt(0)" ::: "memory");
            depart<4>(w2 + ntc * 128, dep2, lane);
        }
    }
    if (wid == 0 && !left) {
        asm volatile("s_waitcnt vmcnt(0)" ::: "memory");
        depart<8>(w1, dep1, lane);
    }
    TL(11);
}

extern "C" void kernel_launch(void* const* d_in, const int* in_sizes, int n_in, void* d_out, int out_size,
                              void* d_ws, size_t ws_size, hipStream_t stream) {
    (void)hipFuncSetAttribute((const void*)gqa_kernel, hipFuncAttributeMaxDynamicSharedMemorySize, LDS_BYTES);
    gqa_kernel<<<NB * HKV, 512, LDS_BYTES, stream>>>((const u16*)d_in[0], (const u16*)d_in[1], (const u16*)d_in[2],
                                                     (const u16*)d_in[3], (const u16*)d_in[4], (char*)d_ws, (u16*)d_out);
}
```

```cpp
#include <hip/hip_runtime.h>
#include <hip/hip_bf16.h>
#include <stdint.h>

typedef short bf16x8 __attribute__((ext_vector_type(8)));
typedef short s16x4 __attribute__((ext_vector_type(4)));
typedef float f32x16 __attribute__((ext_vector_type(16)));
typedef float f32x4 __attribute__((ext_vector_type(4)));
typedef unsigned u32x4 __attribute__((ext_vector_type(4)));
typedef unsigned u32x2 __attribute__((ext_vector_type(2)));
typedef unsigned short u16;
#define LAS __attribute__((address_space(3)))

constexpr int NB = 16, SQ = 64, DM = 576, HQ = 32, HKV = 16, SKV = 1024, DH = 128;
constexpr int NQ = HQ * DH;
constexpr int NT = SKV / 64;
constexpr int REG = 40960;
constexpr int LDS_BYTES = 4 * REG;
constexpr size_t PART_OFF = (size_t)NB * SQ * NQ * 2;
constexpr size_t CNT_OFF = (size_t)20 << 20;
constexpr int AHEAD = 4;
constexpr unsigned long long TMO = 5000ull;
constexpr int XS0 = 5, XS1 = 7, YS0 = 5, YS1 = 7;
constexpr int PACE = 108;
constexpr float RTHR = 8.0f;
constexpr float C2 = 0.088388347648318447f * 1.4426950408889634f;

#define MFMA32(a, b, c) __builtin_amdgcn_mfma_f32_32x32x16_bf16(a, b, c, 0, 0, 0)

typedef __bf16 bf16x2_t __attribute__((ext_vector_type(2)));
typedef float f32x2 __attribute__((ext_vector_type(2)));
__device__ __forceinline__ unsigned cvtpk(float lo, float hi) {
    f32x2 v = {lo, hi};
    return __builtin_bit_cast(unsigned, __builtin_convertvector(v, bf16x2_t));
}
__device__ __forceinline__ s16x4 vtr(unsigned a) {
    return __builtin_amdgcn_ds_read_tr16_b64_v4i16((LAS s16x4*)(uintptr_t)a);
}
__device__ __forceinline__ bf16x8 ldsr128(unsigned a) { return *(const LAS bf16x8*)(uintptr_t)a; }
__device__ __forceinline__ float swap_max(float v) {
    auto rr = __builtin_amdgcn_permlane32_swap(__float_as_uint(v), __float_as_uint(v), false, false);
    return __builtin_fmaxf(__uint_as_float(rr[0]), __uint_as_float(rr[1]));
}
__device__ __forceinline__ float max3f(float a, float b, float c) { float r; asm("v_max3_f32 %0, %1, %2, %3" : "=v"(r) : "v"(a), "v"(b), "v"(c)); return r; }
__device__ __forceinline__ float fmul1(float a, float b) { float r; asm("v_mul_f32_e32 %0, %1, %2" : "=v"(r) : "s"(b), "v"(a)); return r; }
__device__ __forceinline__ float swap_max3(float v) {
    auto rr = __builtin_amdgcn_permlane32_swap(__float_as_uint(v), __float_as_uint(v), false, false);
    return max3f(__uint_as_float(rr[0]), __uint_as_float(rr[1]), __uint_as_float(rr[1]));
}
__device__ __forceinline__ float swap_add(float v) {
    auto rr = __builtin_amdgcn_permlane32_swap(__float_as_uint(v), __float_as_uint(v), false, false);
    return __uint_as_float(rr[0]) + __uint_as_float(rr[1]);
}

extern "C" __device__ unsigned long long amd_dispatch_id() __asm("llvm.amdgcn.dispatch.id");

template <unsigned N>
__device__ __forceinline__ unsigned rendezvous(const char* w, unsigned tag, unsigned own, int lane, unsigned long long tmo, unsigned& dep) {
    unsigned old = 0, todo = 1u << own;
    u32x2 v;
    if (lane == 0) asm volatile("global_atomic_add %0, %1, %2, off sc0" : "=v"(old) : "v"(w), "v"(1u) : "memory");
    asm volatile("global_load_dwordx2 %0, %1, off sc1" : "=v"(v) : "v"(w) : "memory");
    asm volatile("s_waitcnt vmcnt(0)" : "+v"(old), "+v"(v) :: "memory");
    old = __builtin_amdgcn_readfirstlane(old);
    if ((old & 255u) == N - 1) todo |= (old >> 8) & 255u;
    else {
        const unsigned long long t0 = __builtin_amdgcn_s_memrealtime();
        while ((__builtin_amdgcn_readfirstlane(v.x) & 255u) != N || __builtin_amdgcn_readfirstlane(v.y) != tag) {
            if (__builtin_amdgcn_s_memrealtime() - t0 > tmo) {
                unsigned o2 = 0;
                if (lane == 0) asm volatile("global_atomic_or %0, %1, %2, off sc0" : "=v"(o2) : "v"(w), "v"(256u << own) : "memory");
                asm volatile("s_waitcnt vmcnt(0)" : "+v"(o2) :: "memory");
                if ((__builtin_amdgcn_readfirstlane(o2) & 255u) != N) todo = 0;
                break;
            }
            asm volatile("global_load_dwordx2 %0, %1, off sc1\n\ts_waitcnt vmcnt(0)" : "=v"(v) : "v"(w) : "memory");
        }
    }
    dep = 0;
    if (lane == 0) asm volatile("global_atomic_add %0, %1, %2, off sc0" : "=v"(dep) : "v"(w), "v"(65536u) : "memory");
    return todo;
}
template <unsigned N>
__device__ __forceinline__ void depart(const char* w, unsigned dep, int lane) {
    asm volatile("" : "+v"(dep));
    if (lane == 0 && ((dep >> 16) & 255u) == N - 1)
        asm volatile("global_store_dwordx2 %0, %1, off sc1" :: "v"(w), "v"(u32x2{0u, 0u}) : "memory");
}

#define MFMA16(a, b, c) __builtin_amdgcn_mfma_f32_16x16x32_bf16(a, b, c, 0, 0, 0)
__global__ __launch_bounds__(512, 2) void gqa_kernel(const u16* __restrict__ x, const u16* __restrict__ Kc,
                                                     const u16* __restrict__ Vc, const u16* __restrict__ wq,
                                                     const u16* __restrict__ wo, char* ws, u16* __restrict__ out) {
    extern __shared__ __attribute__((aligned(16))) char lds[];
    const unsigned lds0 = (unsigned)(uintptr_t)lds;
    const int tid = threadIdx.x, lane = tid & 63, r32 = lane & 31, hi = lane >> 5;
    const int wid = __builtin_amdgcn_readfirstlane(tid >> 6);
    const int qb = wid & 3, kh = wid >> 2;
    const int bid = blockIdx.x, b = 8 * (bid & 1) + ((bid >> 3) & 7), kvh = 4 * ((bid >> 1) & 3) + (bid >> 6);
    const int head = 2 * kvh + (qb >> 1);
    const int mt = b >> 1, sk = kvh >> 2, nt = 4 * (b & 1) + (kvh & 3);
    const unsigned tag = 0x80000000u | ((unsigned)amd_dispatch_id() & 0xfffffu) |
                         ((((unsigned)((uintptr_t)__builtin_amdgcn_dispatch_ptr() >> 12) * 0x9e3779b1u) >> 21) << 20);
    const char* w1 = ws + CNT_OFF + (mt * 4 + sk) * 128;
    const char* w2 = ws + CNT_OFF + 4096 + mt * 8 * 128;
    const char* wcl = lane == 0 ? w1 : w2 + nt * 128;
    u32x2 clm = {0u, tag};
    if (wid == 1 && lane < 2)
        asm volatile("global_atomic_cmpswap_x2 %0, %1, %2, off sc0\n\ts_nop 1" : "=v"(clm) : "v"(wcl), "v"(u32x4{0u, tag, 0u, 0u}) : "memory");
    const int gi = lane >> 4, vq = (lane & 15) >> 2, vp = lane & 3;

    const unsigned vsrc = (16 * (wid >> 1) + ((lane >> 2) & 7)) * 256 + (wid & 1) * 128 + hi * 64;
    const unsigned voffV = (vsrc ^ (kh * 128)) + vp * 16;
    const unsigned voffK0 = vsrc + ((vp ^ (wid & 2)) << 4), voffK1 = vsrc + ((vp ^ ((wid & 2) | 1)) << 4);
    const char* Kg = (const char*)Kc + (size_t)(b * HKV + kvh) * SKV * DH * 2;
    const char* Vg = (const char*)Vc + (size_t)(b * HKV + kvh) * SKV * DH * 2;
    const unsigned dwo = lds0 + (wid >> 1) * 4096 + (wid & 1) * 1024;
#define KVDMA(voff, gp, dst, OFF) asm volatile("s_mov_b32 m0, %2\n\ts_nop 0\n\tglobal_load_lds_dwordx4 %0, %1 offset:" #OFF " sc1 nt" \
            :: "v"(voff), "s"(gp), "s"(dst) : "memory")
#define VSO(tn) ((int)((0x0200110au >> (8 * ((tn) & 3))) & 255) * 8192)
#define KSO(tn) ((int)((0x0f0c0705u >> (8 * ((tn) & 3))) & 255) * 8192)
#define DMA_V0(tn) KVDMA(voffV, Vg + (tn) * 16384, dwo + VSO(tn), 0)
#define DMA_V1(tn) KVDMA(voffV, Vg + (tn) * 16384, dwo + VSO(tn), 2048)
#define DMA_K0(tn) KVDMA(voffK0, Kg + (tn) * 16384, dwo + KSO(tn), 0)
#define DMA_K1(tn) KVDMA(voffK1, Kg + (tn) * 16384, dwo + KSO(tn), 2048)
#define DMA_V(tn) do { DMA_V0(tn); DMA_V1(tn); } while (0)
#define DMA_K(tn) do { DMA_K0(tn); DMA_K1(tn); } while (0)
    const char* Bw = (const char*)wo + (size_t)(1024 * sk + 32 * wid) * DM * 2;
    bf16x8 qf[8];
    {
        int kb = (b + 2 * (kvh & 3)) % 9;
        const unsigned xo = (8 * wid + (lane >> 3)) * DM * 2 + (((lane & 7) ^ ((4 * wid + (lane >> 4)) & 7)) << 4);
        const unsigned wo0 = (8 * wid + hi) * NQ * 2 + ((r32 ^ (hi << 2)) << 4);
        const unsigned wo1 = (8 * wid + 2 + hi) * NQ * 2 + ((r32 ^ ((2 + hi) << 2)) << 4);
        const unsigned wo2 = wo0 + 4 * NQ * 2, wo3 = wo1 + 4 * NQ * 2;
        const char* Xs = (const char*)x + (size_t)b * SQ * DM * 2;
        const char* Ws = (const char*)wq + kvh * 512;
#define QDMA(voff, gp, dst) asm volatile("s_mov_b32 m0, %2\n\ts_nop 0\n\tglobal_load_lds_dwordx4 %0, %1" :: "v"(voff), "s"(gp), "s"(dst) : "memory")
#define QS_PIECE(slot, p) do { const unsigned s_ = lds0 + (slot) * 40960 + wid * 4096; const char* w_ = Ws + (size_t)kb * 64 * NQ * 2; \
            if ((p) == 0) QDMA(wo0, w_, s_); \
            if ((p) == 1) QDMA(wo1, w_, s_ + 1024); \
            if ((p) == 2) QDMA(wo2, w_, s_ + 2048); \
            if ((p) == 3) QDMA(wo3, w_, s_ + 3072); \
            if ((p) == 4) { QDMA(xo, Xs + kb * 128, lds0 + (slot) * 40960 + 32768 + wid * 1024); kb = kb == 8 ? 0 : kb + 1; } } while (0)
#define QS_ISSUE(slot) do { QS_PIECE(slot, 0); QS_PIECE(slot, 1); QS_PIECE(slot, 2); QS_PIECE(slot, 3); QS_PIECE(slot, 4); } while (0)
#pragma unroll 1
        for (int j = 0; j < 2; ++j) { QS_ISSUE(j); asm volatile("s_barrier" ::: "memory"); }
        const int pp = ((vp & 1) << 1) | (vp >> 1);
        const unsigned abase = lds0 + (8 * hi + vq) * 512 + (((4 * wid + 2 * (gi & 1) + (pp >> 1)) ^ (vq << 2)) << 4) + 8 * (pp & 1);
        const unsigned xbase = lds0 + 32768 + r32 * 128 + ((hi ^ ((r32 >> 1) & 7)) << 4);
        f32x16 acc0 = {}, acc1 = {};
        s16x4 lo[2][4], h4[2][4]; bf16x8 x0[2][4], x1[2][4];
        asm volatile("s_waitcnt vmcnt(5)\n\ts_barrier" : "+v"(clm) :: "memory");
        QS_ISSUE(2);
        if (wid == 1 && lane < 2 && clm.y != tag && (clm.x | clm.y) != 0u)
            asm volatile("global_atomic_cmpswap_x2 %0, %1, off\n\ts_nop 1" :: "v"(wcl), "v"(u32x4{0u, tag, clm.x, clm.y}) : "memory");
#pragma unroll
        for (int i = 0; i < 4; ++i) {
            lo[0][i] = vtr(abase + i * 8192); h4[0][i] = vtr(abase + i * 8192 + 2048);
            x0[0][i] = ldsr128(xbase ^ (i << 5)); x1[0][i] = ldsr128((xbase ^ (i << 5)) + 4096);
        }
#pragma unroll
        for (int st = 0; st < 9; ++st) {
            if (st == 0) asm volatile("s_waitcnt vmcnt(5)" ::: "memory");
            else if (st < 6) asm volatile("s_waitcnt vmcnt(10)" ::: "memory");
            else if (st == 6) asm volatile("s_waitcnt vmcnt(9)" ::: "memory");
            else if (st == 7) asm volatile("s_waitcnt vmcnt(6)" ::: "memory");
            asm volatile("s_waitcnt lgkmcnt(0)\n\ts_barrier" ::: "memory");
            const unsigned so = ((st + 1) & 3) * 40960;
            const int cu = st & 1, nx = cu ^ 1;
            unsigned aa = abase + so;
            asm volatile("" : "+v"(aa));
            __builtin_amdgcn_sched_barrier(0);
            if (st == 0) QS_ISSUE(3);
#pragma unroll
            for (int i = 0; i < 4; ++i) {
                const bf16x8 a0 = {lo[cu][i][0], lo[cu][i][1], lo[cu][i][2], lo[cu][i][3], h4[cu][i][0], h4[cu][i][1], h4[cu][i][2], h4[cu][i][3]};
                acc0 = MFMA32(a0, x0[cu][i], acc0);
                if (st < 8) { lo[nx][i] = vtr(aa + i * 8192); h4[nx][i] = vtr(aa + i * 8192 + 2048); }
                if (st < 5) QS_PIECE(st & 3, i);
                __builtin_amdgcn_sched_barrier(0);
                acc1 = MFMA32(a0, x1[cu][i], acc1);
                if (st < 8) { unsigned xa = (xbase ^ (i << 5)) + so; asm volatile("" : "+v"(xa)); x0[nx][i] = ldsr128(xa); x1[nx][i] = ldsr128(xa + 4096); }
                if (st < 5 && i == 3) QS_PIECE(st & 3, 4);
                if (st == 5 && i == 3) { DMA_K(0); DMA_K(1); }
                if (st == 6 && i == 3) DMA_V(0);
                if (st == 7 && i == 3) DMA_K(2);
                if (st == 8 && i == 3) DMA_K(3);
                __builtin_amdgcn_sched_barrier(0);
            }
        }
#undef QS_ISSUE
#undef QS_PIECE
#define QW(ACC, qblk) do { _Pragma("unroll") for (int s = 0; s < 2; ++s) { \
            u32x4 w_ = {cvtpk(ACC[8 * s], ACC[8 * s + 1]), cvtpk(ACC[8 * s + 2], ACC[8 * s + 3]), \
                        cvtpk(ACC[8 * s + 4], ACC[8 * s + 5]), cvtpk(ACC[8 * s + 6], ACC[8 * s + 7])}; \
            *(LAS u32x4*)(uintptr_t)(lds0 + 32768 + (2 * (wid >> 2) + (qblk)) * REG + (2 * (wid & 3) + s) * 1024 + lane * 16) = w_; } } while (0)
        QW(acc0, 0); QW(acc1, 1);
#undef QW
        DMA_V(1);
    }
    asm volatile("s_waitcnt vmcnt(8) lgkmcnt(0)\n\ts_barrier" ::: "memory");
#pragma unroll
    for (int f = 0; f < 8; ++f) qf[f] = ldsr128(lds0 + 32768 + qb * REG + f * 1024 + lane * 16);

    const unsigned kb0 = lds0 + (4 * kh + (r32 >> 3)) * 2048 + (r32 & 7) * 64 + ((hi ^ (r32 >> 3)) << 4);
    const unsigned kb1 = kb0 ^ 32;
    const unsigned vb = lds0 + kh * 8192 + (4 * hi + vq) * 64 + (gi & 1) * 32 + vp * 8;

    float m_run, l_run = 0.f, tmax = 0.f;
    unsigned long long tprev = 0;
    f32x16 o[4] = {};
    f32x16 sA, sB, negm;

    bf16x8 kf[8];
    {
        bf16x8 k0[8];
        unsigned ka0 = kb0 + KSO(0), ka1 = kb1 + KSO(0);
        asm volatile("" : "+v"(ka0), "+v"(ka1));
#pragma unroll
        for (int d0 = 0; d0 < 8; ++d0) k0[d0] = ldsr128(((d0 & 1) ? ka1 : ka0) + (d0 >> 1) * 512);
#pragma unroll
        for (int d0 = 0; d0 < 8; ++d0) kf[d0] = ldsr128(((d0 & 1) ? ka1 : ka0) + 16384 + (d0 >> 1) * 512);
        __builtin_amdgcn_sched_barrier(0);
        asm volatile("s_waitcnt lgkmcnt(0)\n\ts_barrier" ::: "memory");
        sA = f32x16{};
#pragma unroll
        for (int d0 = 0; d0 < 8; ++d0) sA = MFMA32(k0[d0], qf[d0], sA);
        __builtin_amdgcn_sched_barrier(0);
        DMA_V(2); DMA_K(4);
        m_run = sA[0];
#pragma unroll
        for (int r = 1; r < 16; ++r) m_run = __builtin_fmaxf(m_run, sA[r]);
        m_run = swap_max(m_run);
#pragma unroll
        for (int r = 0; r < 16; ++r) { sA[r] -= m_run; negm[r] = -m_run; }
    }
#define EXP1(r) do { sc[r] = __builtin_amdgcn_exp2f(fmul1(sc[r], C2)); } while (0)
#define SUM1(k) do { ps0 += sc[2 * (k)]; ps1 += sc[2 * (k) + 1]; pw[k] = cvtpk(sc[2 * (k)], sc[2 * (k) + 1]); asm volatile("" : "+v"(pw[k]), "+v"(ps0), "+v"(ps1)); } while (0)
#define STEP(t, SC_, SN_, TAIL) do { \
        f32x16& sc = SC_; f32x16& sn = SN_; \
        s16x4 vlo[8], vhi[8]; \
        if (!TAIL || (t) < 12) asm volatile("s_waitcnt vmcnt(8)" ::: "memory"); \
        else if ((t) < 14) asm volatile("s_waitcnt vmcnt(7)" ::: "memory"); \
        else if ((t) == 14) asm volatile("s_waitcnt vmcnt(9)" ::: "memory"); \
        else asm volatile("s_waitcnt vmcnt(10)" ::: "memory"); \
        while (__builtin_amdgcn_s_memrealtime() - tprev < (unsigned long long)PACE) __builtin_amdgcn_s_sleep(1); \
        asm volatile("s_waitcnt lgkmcnt(0)\n\ts_barrier" ::: "memory"); \
        tprev = __builtin_amdgcn_s_memrealtime(); \
        if (__builtin_expect(__any(tmax > RTHR / C2), 0)) { \
            const float dl = __builtin_fmaxf(tmax, 0.f); \
            const float alpha = __builtin_amdgcn_exp2f(-dl * C2); \
            m_run += dl; l_run *= alpha; \
            _Pragma("unroll") for (int r = 0; r < 16; ++r) { sc[r] -= dl; negm[r] = -m_run; } \
            _Pragma("unroll") for (int dt = 0; dt < 4; ++dt) \
                _Pragma("unroll") for (int r = 0; r < 16; ++r) o[dt][r] *= alpha; \
        } \
        const unsigned vwr = dwo + vso, kwr = dwo + kso;                         \
        vso = VSO(t); kso = KSO((t) + 2); \
        const unsigned va = vb + vso; \
        const unsigned ka0 = kb0 + kso, ka1 = kb1 + kso;                         \
        unsigned pw[8]; \
        float ps0 = 0.f, ps1 = 0.f, t2; \
        __builtin_amdgcn_sched_barrier(0); \
          \
        _Pragma("unroll") for (int i = 0; i < 8; ++i) { \
            sn = MFMA32(kf[i], qf[i], i == 0 ? negm : sn);                   \
            const int ss = i >> 2, dt = i & 3; \
            vlo[2 * dt + ss] = vtr(va + ss * 4096 + dt * 512); \
            vhi[2 * dt + ss] = vtr(va + ss * 4096 + 2048 + dt * 512); \
            if (i < 4) { EXP1(2 * i); EXP1(2 * i + 1); } else EXP1(4 + i); \
            if (i >= 1 && i <= 4) SUM1(i - 1); \
            if (i == 6) SUM1(4); \
            asm volatile("" : "+v"(sn), "+v"(sc)); \
            if (i == XS0 && (!TAIL || (t) < 13)) KVDMA(voffV, Vg + ((t) + 3) * 16384, vwr, 0); \
            if (i == XS1 && (!TAIL || (t) < 13)) KVDMA(voffV, Vg + ((t) + 3) * 16384, vwr, 2048); \
            if (i == XS0 && TAIL && (t) == 13) BDMA(Bs, 0, 2); \
            if (i == XS1 && TAIL && (t) == 13) BDMA(Bs, 0, 3); \
            if (i == XS0 && TAIL && (t) == 14) BDMA(Bs, 1, 1); \
            if (i == XS1 && TAIL && (t) == 14) BDMA(Bs, 1, 2); \
            __builtin_amdgcn_sched_barrier(0); \
        } \
          \
        _Pragma("unroll") for (int i = 0; i < 8; ++i) { \
            const int ss = i >> 2, dt = i & 3; \
            const s16x4 lo = vlo[2 * dt + ss], hi4 = vhi[2 * dt + ss]; \
            const bf16x8 vf = {lo[0], lo[1], lo[2], lo[3], hi4[0], hi4[1], hi4[2], hi4[3]}; \
            const bf16x8 pf = __builtin_bit_cast(bf16x8, u32x4{pw[4 * ss], pw[4 * ss + 1], pw[4 * ss + 2], pw[4 * ss + 3]}); \
            o[dt] = MFMA32(vf, pf, o[dt]); \
            if (i < 4) { kf[2 * i] = ldsr128(ka0 + i * 512); kf[2 * i + 1] = ldsr128(ka1 + i * 512); } \
            if (i < 2) { EXP1(12 + 2 * i); EXP1(13 + 2 * i); asm volatile("" : "+v"(sc)); } \
            if (i < 3) SUM1(5 + i); \
            if (i == 3) { l_run += ps0 + ps1; asm volatile("" : "+v"(sn), "+v"(l_run)); \
                          tmax = max3f(sn[0], sn[1], sn[2]); t2 = max3f(sn[3], sn[4], sn[5]); asm volatile("" : "+v"(tmax), "+v"(t2)); } \
            if (i == 4) { tmax = max3f(tmax, sn[6], sn[7]); t2 = max3f(t2, sn[8], sn[9]); tmax = max3f(tmax, sn[10], sn[11]); asm volatile("" : "+v"(tmax), "+v"(t2)); } \
            if (i == 5) { t2 = max3f(t2, sn[12], sn[13]); tmax = max3f(tmax, sn[14], sn[15]); tmax = max3f(tmax, t2, t2); asm volatile("" : "+v"(tmax)); } \
            if (i == 6) { tmax = swap_max3(tmax); asm volatile("" : "+v"(tmax)); } \
            if (i == YS0 && (!TAIL || (t) < 11)) KVDMA(voffK0, Kg + ((t) + 5) * 16384, kwr, 0); \
            if (i == YS1 && (!TAIL || (t) < 11)) KVDMA(voffK1, Kg + ((t) + 5) * 16384, kwr, 2048); \
            if (i == YS0 && TAIL && (t) == 11) BWARM(); \
            if (i == YS0 && TAIL && (t) == 12) BDMA(Bs, 0, 0); \
            if (i == YS1 && TAIL && (t) == 12) BDMA(Bs, 0, 1); \
            if (i == YS0 && TAIL && (t) == 13) BDMA(Bs, 0, 4); \
            if (i == YS1 && TAIL && (t) == 13) BDMA(Bs, 1, 0); \
            if (i == YS0 && TAIL && (t) == 14) BDMA(Bs, 1, 3); \
            if (i == YS1 && TAIL && (t) == 14) BDMA(Bs, 1, 4); \
            __builtin_amdgcn_sched_barrier(0); \
        } } while (0)
    unsigned boff[5];
    const char* Bs = Bw + 144 * nt;
#define BDMA(bs, c, i) QDMA(boff[i], (bs) + (c) * (256 * DM * 2), lds0 + (((c) + 1) & 3) * REG + wid * 5120 + (i) * 1024)
#define BWARM() do { unsigned ln_ = lane; asm volatile("" : "+v"(ln_)); const unsigned wo_ = (ln_ >> 1) * (DM * 2) + (ln_ & 1) * 128; \
        asm volatile("s_mov_b32 m0, %2\n\ts_nop 0\n\tglobal_load_lds_dword %0, %1" :: "v"(wo_), \
                     "s"(Bw + (2 + (mt & 1)) * (256 * DM * 2) + 128 * nt), "s"(lds0 + 32768 + wid * 256) : "memory"); } while (0)
    unsigned vso = VSO(3), kso = KSO(1);
#pragma unroll 1
    for (int t = 0; t < 10; t += 2) { STEP(t, sA, sB, false); STEP(t + 1, sB, sA, false); }
    {
        unsigned ln = lane;
        asm volatile("" : "+v"(ln));
#pragma unroll
        for (int i = 0; i < 5; ++i) {
            const unsigned n = 64 * i + ln, row = (n * 205u) >> 11, c = n - 10 * row;
            boff[i] = row * (DM * 2) + (c < 9 ? c : 0u) * 16;
        }
    }
#pragma unroll 1
    for (int t = 10; t < NT; t += 2) { STEP(t, sA, sB, true); STEP(t + 1, sB, sA, true); }
#undef STEP
#undef EXP1
#undef SUM1
    l_run = swap_add(l_run);
    asm volatile("s_waitcnt lgkmcnt(0)\n\ts_barrier" ::: "memory");
    {
        f32x16 &keep0 = o[0], &keep1 = o[1], &send0 = o[2], &send1 = o[3];
        const unsigned xb = lds0 + (kh ? 0 : 3 * REG) + qb * 8704;
#pragma unroll
        for (int r4 = 0; r4 < 4; ++r4) {
            f32x4 v0 = {send0[4 * r4], send0[4 * r4 + 1], send0[4 * r4 + 2], send0[4 * r4 + 3]};
            f32x4 v1 = {send1[4 * r4], send1[4 * r4 + 1], send1[4 * r4 + 2], send1[4 * r4 + 3]};
            *(LAS f32x4*)(uintptr_t)(xb + r4 * 1024 + lane * 16) = v0;
            *(LAS f32x4*)(uintptr_t)(xb + (4 + r4) * 1024 + lane * 16) = v1;
        }
        *(LAS float*)(uintptr_t)(xb + 8192 + lane * 4) = m_run;
        *(LAS float*)(uintptr_t)(xb + 8192 + 256 + lane * 4) = l_run;
        asm volatile("s_waitcnt lgkmcnt(0)\n\ts_barrier" ::: "memory");
        const unsigned pb = lds0 + (kh ? 3 * REG : 0) + qb * 8704;
        const float m2 = *(const LAS float*)(uintptr_t)(pb + 8192 + lane * 4);
        const float l2 = *(const LAS float*)(uintptr_t)(pb + 8192 + 256 + lane * 4);
        const float mm = __builtin_fmaxf(m_run, m2);
        const float a1 = __builtin_amdgcn_exp2f((m_run - mm) * C2), a2 = __builtin_amdgcn_exp2f((m2 - mm) * C2);
        const float inv = 1.0f / (l_run * a1 + l2 * a2);
        const float w1 = a1 * inv, w2 = a2 * inv;
        const int row = b * SQ + 32 * (qb & 1) + r32;
        char* ob = ws + ((size_t)((row >> 4) * 128 + head * 4 + 2 * kh) * 64 + hi * 16 + (row & 15)) * 16;
#define OSTORE(KEEP, i) do { u32x2 w_[4]; \
            _Pragma("unroll") for (int r4 = 0; r4 < 4; ++r4) { \
                const f32x4 v = *(const LAS f32x4*)(uintptr_t)(pb + (4 * (i) + r4) * 1024 + lane * 16); \
                const float e0 = KEEP[4 * r4] * w1 + v[0] * w2, e1 = KEEP[4 * r4 + 1] * w1 + v[1] * w2; \
                const float e2 = KEEP[4 * r4 + 2] * w1 + v[2] * w2, e3 = KEEP[4 * r4 + 3] * w1 + v[3] * w2; \
                w_[r4] = u32x2{cvtpk(e0, e1), cvtpk(e2, e3)}; } \
            _Pragma("unroll") for (int g = 0; g < 2; ++g) { \
                u32x4 o4 = {w_[g].x, w_[g].y, w_[g + 2].x, w_[g + 2].y}; \
                asm volatile("global_store_dwordx4 %0, %1, off sc1\n\ts_nop 1" :: "v"(ob + (i) * 1024 + g * 512), "v"(o4) : "memory"); } } while (0)
        OSTORE(keep0, 0); OSTORE(keep1, 1);
#undef OSTORE
    }
    const char* Ag = ws + (size_t)((8 * mt + 2 * qb) * 128 + 32 * sk + kh) * 1024;
    unsigned ao0 = lane * 16 + kh * 131072, ao1 = ao0 ^ 131072;
    unsigned bb[4];
#pragma unroll
    for (int c = 0; c < 4; ++c) { bb[c] = lds0 + ((c + 1) & 3) * REG + (32 * kh + 4 * gi + vq) * 160 + 8 * vp; asm volatile("" : "+v"(bb[c])); }
    asm volatile("" : "+v"(ao0), "+v"(ao1), "+s"(Ag));
    asm volatile("s_waitcnt vmcnt(0) lgkmcnt(0)\n\ts_barrier" ::: "memory");
    unsigned dep1 = 0;
    if (wid == 0) {
        const unsigned td = rendezvous<8>(w1, tag, nt, lane, TMO, dep1);
        if (lane == 0) *(LAS unsigned*)(uintptr_t)(lds0 + REG + 144) = td;
    } else {
#pragma unroll
        for (int i = 0; i < 10; ++i) BDMA(Bs, 2 + i / 5, i % 5);
        if (wid == 1) {
#pragma unroll
            for (int i = 0; i < 10; ++i)
                QDMA(boff[i % 5], Bs - 32 * DM * 2 + (2 + i / 5) * (256 * DM * 2), lds0 + ((3 + i / 5) & 3) * REG + (i % 5) * 1024);
        }
    }
    asm volatile("s_waitcnt lgkmcnt(0)\n\ts_barrier" ::: "memory");
    unsigned todo = __builtin_amdgcn_readfirstlane(*(const LAS unsigned*)(uintptr_t)(lds0 + REG + 144));
    float* part = (float*)(ws + PART_OFF);
    bool left = false;
    while (todo) {
        const int ntc = ((todo >> nt) & 1u) ? nt : __builtin_ctz(todo);
        todo &= ~(1u << ntc);
        if (ntc != nt) {
            asm volatile("s_waitcnt vmcnt(0) lgkmcnt(0)\n\ts_barrier" ::: "memory");
#pragma unroll 1
            for (int c = 0; c < 4; ++c) {
#pragma unroll
                for (int i = 0; i < 5; ++i) BDMA(Bw + 144 * ntc, c, i);
            }
            asm volatile("s_waitcnt vmcnt(0)\n\ts_barrier" ::: "memory");
        }
        u32x4 an[32];
#define ALD(s_) do { const char* ag = Ag; asm volatile("" : "+s"(ag)); ag += ((s_) >> 1) * 4096; \
            if ((s_) & 1) { \
                asm volatile("global_load_dwordx4 %0, %1, %2 offset:2048 sc1" : "=v"(an[2 * (s_)]) : "v"(ao0), "s"(ag) : "memory"); \
                asm volatile("global_load_dwordx4 %0, %1, %2 offset:2048 sc1" : "=v"(an[2 * (s_) + 1]) : "v"(ao1), "s"(ag) : "memory"); \
            } else { \
                asm volatile("global_load_dwordx4 %0, %1, %2 sc1" : "=v"(an[2 * (s_)]) : "v"(ao0), "s"(ag) : "memory"); \
                asm volatile("global_load_dwordx4 %0, %1, %2 sc1" : "=v"(an[2 * (s_) + 1]) : "v"(ao1), "s"(ag) : "memory"); } } while (0)
#pragma unroll
        for (int s2 = 0; s2 < AHEAD; ++s2) ALD(s2);
        f32x4 acc[2][5];
#pragma unroll
        for (int ct = 0; ct < 5; ++ct) { acc[0][ct] = f32x4{0.f, 0.f, 0.f, 0.f}; acc[1][ct] = f32x4{0.f, 0.f, 0.f, 0.f}; }
        s16x4 blo[2][5], bhi[2][5];
#pragma unroll
        for (int ct = 0; ct < 5; ++ct) { blo[0][ct] = vtr(bb[0] + 32 * ct); bhi[0][ct] = vtr(bb[0] + 32 * ct + 2560); }
#pragma unroll
        for (int s = 0; s < 16; ++s) {
            asm volatile("s_waitcnt vmcnt(%2)" : "+v"(an[2 * s]), "+v"(an[2 * s + 1]) : "n"(s + AHEAD <= 16 ? 2 * AHEAD - 2 : 30 - 2 * s) : "memory");
            if (s == 7) asm volatile("s_barrier" ::: "memory");
            __builtin_amdgcn_sched_barrier(0);
#pragma unroll
            for (int ct = 0; ct < 5; ++ct) {
                if (s < 15) {
                    const unsigned ad = bb[((s + 1) >> 2) & 3] + ((s + 1) & 3) * 10240 + 32 * ct;
                    blo[(s + 1) & 1][ct] = vtr(ad); bhi[(s + 1) & 1][ct] = vtr(ad + 2560);
                }
                const s16x4 lo = blo[s & 1][ct], h4 = bhi[s & 1][ct];
                const bf16x8 bf = {lo[0], lo[1], lo[2], lo[3], h4[0], h4[1], h4[2], h4[3]};
                acc[0][ct] = MFMA16(bf, __builtin_bit_cast(bf16x8, an[2 * s]), acc[0][ct]);
                acc[1][ct] = MFMA16(bf, __builtin_bit_cast(bf16x8, an[2 * s + 1]), acc[1][ct]);
                if (ct == 1 && s + AHEAD < 16) ALD(s + AHEAD);
                __builtin_amdgcn_sched_barrier(0);
            }
        }
#pragma unroll
        for (int ct = 0; ct < 5; ++ct)
            *(LAS f32x4*)(uintptr_t)(lds0 + REG + (wid * 5 + ct) * 1024 + lane * 16) = acc[1][ct];
        asm volatile("s_waitcnt lgkmcnt(0)\n\ts_barrier" ::: "memory");
        float* pp = part + ((size_t)(sk * 64 + 8 * mt + 2 * qb + kh) * 8 + ntc) * 1152 + lane * 4;
#pragma unroll
        for (int ct = 0; ct < 5; ++ct) acc[1][ct] = *(const LAS f32x4*)(uintptr_t)(lds0 + REG + ((wid ^ 4) * 5 + ct) * 1024 + lane * 16);
        if (wid == 0 && !left) { left = true; depart<8>(w1, dep1, lane); }
#pragma unroll
        for (int ct = 0; ct < 5; ++ct) {
            const f32x4 r_ = acc[0][ct] + acc[1][ct];
            if (ct < 4 || lane < 32)
                asm volatile("global_store_dwordx4 %0, %1, off sc1\n\ts_nop 1" :: "v"(pp + ct * 256), "v"(r_) : "memory");
        }
        asm volatile("s_waitcnt vmcnt(0)\n\ts_barrier" ::: "memory");
        unsigned dep2 = 0;
        if (wid == 0) {
            const unsigned td = rendezvous<4>(w2 + ntc * 128, tag, sk, lane, TMO, dep2);
            if (lane == 0) *(LAS unsigned*)(uintptr_t)(lds0 + 2 * REG) = td;
        }
        asm volatile("s_waitcnt lgkmcnt(0)\n\ts_barrier" ::: "memory");
        unsigned qs = __builtin_amdgcn_readfirstlane(*(const LAS unsigned*)(uintptr_t)(lds0 + 2 * REG));
        bool left2 = false;
        while (qs) {
            const int q = __builtin_ctz(qs);
            qs &= qs - 1;
            if (tid < 288) {
                const float* p = part + ((size_t)(8 * mt + 2 * q) * 8 + ntc) * 1152 + tid * 4;
                f32x4 v[2][4];
#pragma unroll
                for (int r = 0; r < 2; ++r)
#pragma unroll
                    for (int s = 0; s < 4; ++s)
                        asm volatile("global_load_dwordx4 %0, %1, off sc1" : "=v"(v[r][s]) : "v"(p + (size_t)s * 64 * 8 * 1152 + r * 8 * 1152) : "memory");
                asm volatile("s_waitcnt vmcnt(0)" : "+v"(v[0][0]), "+v"(v[0][1]), "+v"(v[0][2]), "+v"(v[0][3]),
                                                    "+v"(v[1][0]), "+v"(v[1][1]), "+v"(v[1][2]), "+v"(v[1][3]) :: "memory");
#pragma unroll
                for (int r = 0; r < 2; ++r) {
                    const f32x4 t = ((v[r][0] + v[r][1]) + v[r][2]) + v[r][3];
                    u32x2 w = {cvtpk(t[0], t[1]), cvtpk(t[2], t[3])};
                    *(u32x2*)(out + (size_t)(16 * (8 * mt + 2 * q + r) + (tid & 15)) * DM + 72 * ntc + 4 * (tid >> 4)) = w;
                }
            }
            if (wid == 0 && !left2) { left2 = true; depart<4>(w2 + ntc * 128, dep2, lane); }
        }
        if (wid == 0 && !left2) {
            asm volatile("s_waitcnt vmcnt(0)" ::: "memory");
            depart<4>(w2 + ntc * 128, dep2, lane);
        }
    }
    if (wid == 0 && !left) {
        asm volatile("s_waitcnt vmcnt(0)" ::: "memory");
        depart<8>(w1, dep1, lane);
    }
}

extern "C" void kernel_launch(void* const* d_in, const int* in_sizes, int n_in, void* d_out, int out_size,
                              void* d_ws, size_t ws_size, hipStream_t stream) {
    (void)hipFuncSetAttribute((const void*)gqa_kernel, hipFuncAttributeMaxDynamicSharedMemorySize, LDS_BYTES);
    gqa_kernel<<<NB * HKV, 512, LDS_BYTES, stream>>>((const u16*)d_in[0], (const u16*)d_in[1], (const u16*)d_in[2],
                                                     (const u16*)d_in[3], (const u16*)d_in[4], (char*)d_ws, (u16*)d_out);
}
```

```cpp
#include <hip/hip_runtime.h>
#include <hip/hip_bf16.h>
#include <stdint.h>

typedef short bf16x8 __attribute__((ext_vector_type(8)));
typedef short s16x4 __attribute__((ext_vector_type(4)));
typedef float f32x16 __attribute__((ext_vector_type(16)));
typedef float f32x4 __attribute__((ext_vector_type(4)));
typedef unsigned u32x4 __attribute__((ext_vector_type(4)));
typedef unsigned u32x2 __attribute__((ext_vector_type(2)));
typedef unsigned short u16;
#define LAS __attribute__((address_space(3)))
typedef __attribute__((address_space(1))) unsigned gu32;
#define RLX_AGENT __ATOMIC_RELAXED, __HIP_MEMORY_SCOPE_AGENT

constexpr int NB = 16, SQ = 64, DM = 576, HQ = 32, HKV = 16, SKV = 1024, DH = 128;
constexpr int NQ = HQ * DH;
constexpr int NT = SKV / 64;
constexpr int REG = 40960;
constexpr int LDS_BYTES = 4 * REG;
constexpr size_t PART_OFF = (size_t)NB * SQ * NQ * 2;
constexpr size_t CNT_OFF = (size_t)20 << 20;
#define AHEAD 4
#define TMO 5000ull
#define TMO2 5000ull
constexpr int XS0 = 5, XS1 = 7, YS0 = 5, YS1 = 7;
constexpr int PACE = 100;
constexpr float RTHR = 8.0f;
constexpr float C2 = 0.088388347648318447f * 1.4426950408889634f;

#define MFMA32(a, b, c) __builtin_amdgcn_mfma_f32_32x32x16_bf16(a, b, c, 0, 0, 0)

typedef __bf16 bf16x2_t __attribute__((ext_vector_type(2)));
typedef float f32x2 __attribute__((ext_vector_type(2)));
__device__ __forceinline__ unsigned cvtpk(float lo, float hi) {
    f32x2 v = {lo, hi};
    return __builtin_bit_cast(unsigned, __builtin_convertvector(v, bf16x2_t));
}
__device__ __forceinline__ void glds16(const void* g, unsigned lds_base) {
    unsigned sv;
    asm volatile("s_mov_b32 %0, m0\n\ts_mov_b32 m0, %2\n\ts_nop 0\n\tglobal_load_lds_dwordx4 %1, off\n\ts_mov_b32 m0, %0"
                 : "=&s"(sv) : "v"(g), "s"(lds_base) : "memory");
}
__device__ __forceinline__ s16x4 vtr(unsigned a) {
    return __builtin_amdgcn_ds_read_tr16_b64_v4i16((LAS s16x4*)(uintptr_t)a);
}
__device__ __forceinline__ bf16x8 ldsr128(unsigned a) { return *(const LAS bf16x8*)(uintptr_t)a; }
__device__ __forceinline__ float swap_max(float v) {
    auto rr = __builtin_amdgcn_permlane32_swap(__float_as_uint(v), __float_as_uint(v), false, false);
    return __builtin_fmaxf(__uint_as_float(rr[0]), __uint_as_float(rr[1]));
}
__device__ __forceinline__ float max3f(float a, float b, float c) { float r; asm("v_max3_f32 %0, %1, %2, %3" : "=v"(r) : "v"(a), "v"(b), "v"(c)); return r; }
__device__ __forceinline__ float fmul1(float a, float b) { float r; asm("v_mul_f32_e32 %0, %1, %2" : "=v"(r) : "s"(b), "v"(a)); return r; }
__device__ __forceinline__ float swap_max3(float v) {
    auto rr = __builtin_amdgcn_permlane32_swap(__float_as_uint(v), __float_as_uint(v), false, false);
    return max3f(__uint_as_float(rr[0]), __uint_as_float(rr[1]), __uint_as_float(rr[1]));
}
__device__ __forceinline__ float swap_add(float v) {
    auto rr = __builtin_amdgcn_permlane32_swap(__float_as_uint(v), __float_as_uint(v), false, false);
    return __uint_as_float(rr[0]) + __uint_as_float(rr[1]);
}

#define TL(i)
extern "C" __device__ unsigned long long amd_dispatch_id() __asm("llvm.amdgcn.dispatch.id");

template <unsigned N>
__device__ __forceinline__ unsigned rendezvous(const char* w, unsigned tag, unsigned own, int lane, unsigned long long tmo, unsigned& dep) {
    unsigned old = 0, todo = 1u << own;
    u32x2 v;
    if (lane == 0) asm volatile("global_atomic_add %0, %1, %2, off sc0" : "=v"(old) : "v"(w), "v"(1u) : "memory");
    asm volatile("global_load_dwordx2 %0, %1, off sc1" : "=v"(v) : "v"(w) : "memory");
    asm volatile("s_waitcnt vmcnt(0)" : "+v"(old), "+v"(v) :: "memory");
    old = __builtin_amdgcn_readfirstlane(old);
    if ((old & 255u) == N - 1) todo |= (old >> 8) & 255u;
    else {
        const unsigned long long t0 = __builtin_amdgcn_s_memrealtime();
        while ((__builtin_amdgcn_readfirstlane(v.x) & 255u) != N || __builtin_amdgcn_readfirstlane(v.y) != tag) {
            if (__builtin_amdgcn_s_memrealtime() - t0 > tmo) {
                unsigned o2 = 0;
                if (lane == 0) asm volatile("global_atomic_or %0, %1, %2, off sc0" : "=v"(o2) : "v"(w), "v"(256u << own) : "memory");
                asm volatile("s_waitcnt vmcnt(0)" : "+v"(o2) :: "memory");
                if ((__builtin_amdgcn_readfirstlane(o2) & 255u) != N) todo = 0;
                break;
            }
            asm volatile("global_load_dwordx2 %0, %1, off sc1\n\ts_waitcnt vmcnt(0)" : "=v"(v) : "v"(w) : "memory");
        }
    }
    dep = 0;
    if (lane == 0) asm volatile("global_atomic_add %0, %1, %2, off sc0" : "=v"(dep) : "v"(w), "v"(65536u) : "memory");
    return todo;
}
template <unsigned N>
__device__ __forceinline__ void depart(const char* w, unsigned dep, int lane) {
    asm volatile("" : "+v"(dep));
    if (lane == 0 && ((dep >> 16) & 255u) == N - 1)
        asm volatile("global_store_dwordx2 %0, %1, off sc1" :: "v"(w), "v"(u32x2{0u, 0u}) : "memory");
}

#define MFMA16(a, b, c) __builtin_amdgcn_mfma_f32_16x16x32_bf16(a, b, c, 0, 0, 0)
__global__ __launch_bounds__(512, 2) void gqa_kernel(const u16* __restrict__ x, const u16* __restrict__ Kc,
                                                     const u16* __restrict__ Vc, const u16* __restrict__ wq,
                                                     const u16* __restrict__ wo, char* ws, u16* __restrict__ out) {
    extern __shared__ __attribute__((aligned(16))) char lds[];
    TL(0);
    const unsigned lds0 = (unsigned)(uintptr_t)lds;
    const int tid = threadIdx.x, lane = tid & 63, r32 = lane & 31, hi = lane >> 5;
    const int wid = __builtin_amdgcn_readfirstlane(tid >> 6);
    const int qb = wid & 3, kh = wid >> 2;
    const int bid = blockIdx.x, b = 8 * (bid & 1) + ((bid >> 3) & 7), kvh = 4 * ((bid >> 1) & 3) + (bid >> 6);
    const int head = 2 * kvh + (qb >> 1);
    const int mt = b >> 1, sk = kvh >> 2, nt = 4 * (b & 1) + (kvh & 3);
    const unsigned tag = 0x80000000u | ((unsigned)amd_dispatch_id() & 0xfffffu) |
                         ((((unsigned)((uintptr_t)__builtin_amdgcn_dispatch_ptr() >> 12) * 0x9e3779b1u) >> 21) << 20);
    const char* w1 = ws + CNT_OFF + (mt * 4 + sk) * 128;
    const char* w2 = ws + CNT_OFF + 4096 + mt * 8 * 128;
    const char* wcl = lane == 0 ? w1 : w2 + nt * 128;
    u32x2 clm = {0u, tag};
    if (wid == 1 && lane < 2)
        asm volatile("global_atomic_cmpswap_x2 %0, %1, %2, off sc0\n\ts_nop 1" : "=v"(clm) : "v"(wcl), "v"(u32x4{0u, tag, 0u, 0u}) : "memory");
    const int gi = lane >> 4, vq = (lane & 15) >> 2, vp = lane & 3;

    const unsigned vsrc = (16 * (wid >> 1) + ((lane >> 2) & 7)) * 256 + (wid & 1) * 128 + hi * 64;
    const unsigned voffV = (vsrc ^ (kh * 128)) + vp * 16;
    const unsigned voffK0 = vsrc + ((vp ^ (wid & 2)) << 4), voffK1 = vsrc + ((vp ^ ((wid & 2) | 1)) << 4);
    const char* Kg = (const char*)Kc + (size_t)(b * HKV + kvh) * SKV * DH * 2;
    const char* Vg = (const char*)Vc + (size_t)(b * HKV + kvh) * SKV * DH * 2;
    const unsigned dwo = lds0 + (wid >> 1) * 4096 + (wid & 1) * 1024;
#define KVDMA(voff, gp, dst, OFF) asm volatile("s_mov_b32 m0, %2\n\ts_nop 0\n\tglobal_load_lds_dwordx4 %0, %1 offset:" #OFF " sc1 nt" \
            :: "v"(voff), "s"(gp), "s"(dst) : "memory")
#define VSO(tn) ((int)((0x0200110au >> (8 * ((tn) & 3))) & 255) * 8192)
#define KSO(tn) ((int)((0x0f0c0705u >> (8 * ((tn) & 3))) & 255) * 8192)
#define DMA_V0(tn) KVDMA(voffV, Vg + (tn) * 16384, dwo + VSO(tn), 0)
#define DMA_V1(tn) KVDMA(voffV, Vg + (tn) * 16384, dwo + VSO(tn), 2048)
#define DMA_K0(tn) KVDMA(voffK0, Kg + (tn) * 16384, dwo + KSO(tn), 0)
#define DMA_K1(tn) KVDMA(voffK1, Kg + (tn) * 16384, dwo + KSO(tn), 2048)
#define DMA_V(tn) do { DMA_V0(tn); DMA_V1(tn); } while (0)
#define DMA_K(tn) do { DMA_K0(tn); DMA_K1(tn); } while (0)
    const char* Bw = (const char*)wo + (size_t)(1024 * sk + 32 * wid) * DM * 2;
    unsigned boff[5];
    bf16x8 qf[8];
    {
        int kb = (b + 2 * (kvh & 3)) % 9;
        const unsigned xo = (8 * wid + (lane >> 3)) * DM * 2 + (((lane & 7) ^ ((4 * wid + (lane >> 4)) & 7)) << 4);
        const unsigned wo0 = (8 * wid + hi) * NQ * 2 + ((r32 ^ (hi << 2)) << 4);
        const unsigned wo1 = (8 * wid + 2 + hi) * NQ * 2 + ((r32 ^ ((2 + hi) << 2)) << 4);
        const unsigned wo2 = wo0 + 4 * NQ * 2, wo3 = wo1 + 4 * NQ * 2;
        const char* Xs = (const char*)x + (size_t)b * SQ * DM * 2;
        const char* Ws = (const char*)wq + kvh * 512;
#define QDMA(voff, gp, dst) asm volatile("s_mov_b32 m0, %2\n\ts_nop 0\n\tglobal_load_lds_dwordx4 %0, %1" :: "v"(voff), "s"(gp), "s"(dst) : "memory")
#define QS_PIECE(slot, p) do { const unsigned s_ = lds0 + (slot) * 40960 + wid * 4096; const char* w_ = Ws + (size_t)kb * 64 * NQ * 2; \
            if ((p) == 0) QDMA(wo0, w_, s_); \
            if ((p) == 1) QDMA(wo1, w_, s_ + 1024); \
            if ((p) == 2) QDMA(wo2, w_, s_ + 2048); \
            if ((p) == 3) QDMA(wo3, w_, s_ + 3072); \
            if ((p) == 4) { QDMA(xo, Xs + kb * 128, lds0 + (slot) * 40960 + 32768 + wid * 1024); kb = kb == 8 ? 0 : kb + 1; } } while (0)
#define QS_ISSUE(slot) do { QS_PIECE(slot, 0); QS_PIECE(slot, 1); QS_PIECE(slot, 2); QS_PIECE(slot, 3); QS_PIECE(slot, 4); } while (0)
#pragma unroll 1
        for (int j = 0; j < 2; ++j) { QS_ISSUE(j); asm volatile("s_barrier" ::: "memory"); }
#pragma unroll
        for (int i = 0; i < 5; ++i) {
            const unsigned n = 64 * i + lane, row = (n * 205u) >> 11, c = n - 10 * row;
            boff[i] = row * (DM * 2) + (c < 9 ? c : 0u) * 16;
            asm volatile("" : "+v"(boff[i]));
        }
        const int pp = ((vp & 1) << 1) | (vp >> 1);
        const unsigned abase = lds0 + (8 * hi + vq) * 512 + (((4 * wid + 2 * (gi & 1) + (pp >> 1)) ^ (vq << 2)) << 4) + 8 * (pp & 1);
        const unsigned xbase = lds0 + 32768 + r32 * 128 + ((hi ^ ((r32 >> 1) & 7)) << 4);
        f32x16 acc0 = {}, acc1 = {};
        s16x4 lo[2][4], h4[2][4]; bf16x8 x0[2][4], x1[2][4];
        asm volatile("s_waitcnt vmcnt(5)\n\ts_barrier" : "+v"(clm) :: "memory");
        QS_ISSUE(2);
        if (wid == 1 && lane < 2 && clm.y != tag && (clm.x | clm.y) != 0u)
            asm volatile("global_atomic_cmpswap_x2 %0, %1, off\n\ts_nop 1" :: "v"(wcl), "v"(u32x4{0u, tag, clm.x, clm.y}) : "memory");
#pragma unroll
        for (int i = 0; i < 4; ++i) {
            lo[0][i] = vtr(abase + i * 8192); h4[0][i] = vtr(abase + i * 8192 + 2048);
            x0[0][i] = ldsr128(xbase ^ (i << 5)); x1[0][i] = ldsr128((xbase ^ (i << 5)) + 4096);
        }
#pragma unroll
        for (int st = 0; st < 9; ++st) {
            if (st == 0) asm volatile("s_waitcnt vmcnt(5)" ::: "memory");
            else if (st < 6) asm volatile("s_waitcnt vmcnt(10)" ::: "memory");
            else if (st == 6) asm volatile("s_waitcnt vmcnt(9)" ::: "memory");
            else if (st == 7) asm volatile("s_waitcnt vmcnt(6)" ::: "memory");
            asm volatile("s_waitcnt lgkmcnt(0)\n\ts_barrier" ::: "memory");
            const unsigned so = ((st + 1) & 3) * 40960;
            const int cu = st & 1, nx = cu ^ 1;
            unsigned aa = abase + so;
            asm volatile("" : "+v"(aa));
            __builtin_amdgcn_sched_barrier(0);
            if (st == 0) QS_ISSUE(3);
#pragma unroll
            for (int i = 0; i < 4; ++i) {
                const bf16x8 a0 = {lo[cu][i][0], lo[cu][i][1], lo[cu][i][2], lo[cu][i][3], h4[cu][i][0], h4[cu][i][1], h4[cu][i][2], h4[cu][i][3]};
                acc0 = MFMA32(a0, x0[cu][i], acc0);
                if (st < 8) { lo[nx][i] = vtr(aa + i * 8192); h4[nx][i] = vtr(aa + i * 8192 + 2048); }
                if (st < 5) QS_PIECE(st & 3, i);
                __builtin_amdgcn_sched_barrier(0);
                acc1 = MFMA32(a0, x1[cu][i], acc1);
                if (st < 8) { unsigned xa = (xbase ^ (i << 5)) + so; asm volatile("" : "+v"(xa)); x0[nx][i] = ldsr128(xa); x1[nx][i] = ldsr128(xa + 4096); }
                if (st < 5 && i == 3) QS_PIECE(st & 3, 4);
                if (st == 5 && i == 3) { DMA_K(0); DMA_K(1); }
                if (st == 6 && i == 3) DMA_V(0);
                if (st == 7 && i == 3) DMA_K(2);
                if (st == 8 && i == 3) DMA_K(3);
                __builtin_amdgcn_sched_barrier(0);
            }
        }
#undef QS_ISSUE
#undef QS_PIECE
#define QW(ACC, qblk) do { _Pragma("unroll") for (int s = 0; s < 2; ++s) { \
            u32x4 w_ = {cvtpk(ACC[8 * s], ACC[8 * s + 1]), cvtpk(ACC[8 * s + 2], ACC[8 * s + 3]), \
                        cvtpk(ACC[8 * s + 4], ACC[8 * s + 5]), cvtpk(ACC[8 * s + 6], ACC[8 * s + 7])}; \
            *(LAS u32x4*)(uintptr_t)(lds0 + 32768 + (2 * (wid >> 2) + (qblk)) * REG + (2 * (wid & 3) + s) * 1024 + lane * 16) = w_; } } while (0)
        QW(acc0, 0); QW(acc1, 1);
#undef QW
        DMA_V(1);
    }
    asm volatile("s_waitcnt vmcnt(8) lgkmcnt(0)\n\ts_barrier" ::: "memory");
#pragma unroll
    for (int f = 0; f < 8; ++f) qf[f] = ldsr128(lds0 + 32768 + qb * REG + f * 1024 + lane * 16);
    TL(1);

    const unsigned kb0 = lds0 + (4 * kh + (r32 >> 3)) * 2048 + (r32 & 7) * 64 + ((hi ^ (r32 >> 3)) << 4);
    const unsigned kb1 = kb0 ^ 32;
    const unsigned vb = lds0 + kh * 8192 + (4 * hi + vq) * 64 + (gi & 1) * 32 + vp * 8;

    float m_run, l_run = 0.f, tmax = 0.f;
    unsigned long long tprev = 0;
    f32x16 o[4] = {};
    f32x16 sA, sB, negm;

    bf16x8 kf[8];
    {
        bf16x8 k0[8];
        unsigned ka0 = kb0 + KSO(0), ka1 = kb1 + KSO(0);
        asm volatile("" : "+v"(ka0), "+v"(ka1));
#pragma unroll
        for (int d0 = 0; d0 < 8; ++d0) k0[d0] = ldsr128(((d0 & 1) ? ka1 : ka0) + (d0 >> 1) * 512);
#pragma unroll
        for (int d0 = 0; d0 < 8; ++d0) kf[d0] = ldsr128(((d0 & 1) ? ka1 : ka0) + 16384 + (d0 >> 1) * 512);
        __builtin_amdgcn_sched_barrier(0);
        asm volatile("s_waitcnt lgkmcnt(0)\n\ts_barrier" ::: "memory");
        sA = f32x16{};
#pragma unroll
        for (int d0 = 0; d0 < 8; ++d0) sA = MFMA32(k0[d0], qf[d0], sA);
        __builtin_amdgcn_sched_barrier(0);
        DMA_V(2); DMA_K(4);
        m_run = sA[0];
#pragma unroll
        for (int r = 1; r < 16; ++r) m_run = __builtin_fmaxf(m_run, sA[r]);
        m_run = swap_max(m_run);
#pragma unroll
        for (int r = 0; r < 16; ++r) { sA[r] -= m_run; negm[r] = -m_run; }
    }
#define EXP1(r) do { sc[r] = __builtin_amdgcn_exp2f(fmul1(sc[r], C2)); } while (0)
#define SUM1(k) do { ps0 += sc[2 * (k)]; ps1 += sc[2 * (k) + 1]; pw[k] = cvtpk(sc[2 * (k)], sc[2 * (k) + 1]); asm volatile("" : "+v"(pw[k]), "+v"(ps0), "+v"(ps1)); } while (0)
#define STEP(t, SC_, SN_, TAIL) do { \
        f32x16& sc = SC_; f32x16& sn = SN_; \
        s16x4 vlo[8], vhi[8]; \
        if (!TAIL || (t) < 12) asm volatile("s_waitcnt vmcnt(8)" ::: "memory"); \
        else if ((t) < 14) asm volatile("s_waitcnt vmcnt(7)" ::: "memory"); \
        else if ((t) == 14) asm volatile("s_waitcnt vmcnt(9)" ::: "memory"); \
        else asm volatile("s_waitcnt vmcnt(10)" ::: "memory"); \
        while (__builtin_amdgcn_s_memrealtime() - tprev < (unsigned long long)PACE) __builtin_amdgcn_s_sleep(1); \
        asm volatile("s_waitcnt lgkmcnt(0)\n\ts_barrier" ::: "memory"); \
        tprev = __builtin_amdgcn_s_memrealtime(); \
        if (__builtin_expect(__any(tmax > RTHR / C2), 0)) { \
            const float dl = __builtin_fmaxf(tmax, 0.f); \
            const float alpha = __builtin_amdgcn_exp2f(-dl * C2); \
            m_run += dl; l_run *= alpha; \
            _Pragma("unroll") for (int r = 0; r < 16; ++r) { sc[r] -= dl; negm[r] = -m_run; } \
            _Pragma("unroll") for (int dt = 0; dt < 4; ++dt) \
                _Pragma("unroll") for (int r = 0; r < 16; ++r) o[dt][r] *= alpha; \
        } \
        const unsigned vwr = dwo + vso, kwr = dwo + kso;                         \
        vso = VSO(t); kso = KSO((t) + 2); \
        const unsigned va = vb + vso; \
        const unsigned ka0 = kb0 + kso, ka1 = kb1 + kso;                         \
        unsigned pw[8]; \
        float ps0 = 0.f, ps1 = 0.f, t2; \
        __builtin_amdgcn_sched_barrier(0); \
          \
        _Pragma("unroll") for (int i = 0; i < 8; ++i) { \
            sn = MFMA32(kf[i], qf[i], i == 0 ? negm : sn);                   \
            const int ss = i >> 2, dt = i & 3; \
            vlo[2 * dt + ss] = vtr(va + ss * 4096 + dt * 512); \
            vhi[2 * dt + ss] = vtr(va + ss * 4096 + 2048 + dt * 512); \
            if (i < 4) { EXP1(2 * i); EXP1(2 * i + 1); } else EXP1(4 + i); \
            if (i >= 1 && i <= 4) SUM1(i - 1); \
            if (i == 6) SUM1(4); \
            asm volatile("" : "+v"(sn), "+v"(sc)); \
            if (i == XS0 && (!TAIL || (t) < 13)) KVDMA(voffV, Vg + ((t) + 3) * 16384, vwr, 0); \
            if (i == XS1 && (!TAIL || (t) < 13)) KVDMA(voffV, Vg + ((t) + 3) * 16384, vwr, 2048); \
            if (i == XS0 && TAIL && (t) == 13) BDMA(Bs, 0, 2); \
            if (i == XS1 && TAIL && (t) == 13) BDMA(Bs, 0, 3); \
            if (i == XS0 && TAIL && (t) == 14) BDMA(Bs, 1, 1); \
            if (i == XS1 && TAIL && (t) == 14) BDMA(Bs, 1, 2); \
            __builtin_amdgcn_sched_barrier(0); \
        } \
          \
        _Pragma("unroll") for (int i = 0; i < 8; ++i) { \
            const int ss = i >> 2, dt = i & 3; \
            const s16x4 lo = vlo[2 * dt + ss], hi4 = vhi[2 * dt + ss]; \
            const bf16x8 vf = {lo[0], lo[1], lo[2], lo[3], hi4[0], hi4[1], hi4[2], hi4[3]}; \
            const bf16x8 pf = __builtin_bit_cast(bf16x8, u32x4{pw[4 * ss], pw[4 * ss + 1], pw[4 * ss + 2], pw[4 * ss + 3]}); \
            o[dt] = MFMA32(vf, pf, o[dt]); \
            if (i < 4) { kf[2 * i] = ldsr128(ka0 + i * 512); kf[2 * i + 1] = ldsr128(ka1 + i * 512); } \
            if (i < 2) { EXP1(12 + 2 * i); EXP1(13 + 2 * i); asm volatile("" : "+v"(sc)); } \
            if (i < 3) SUM1(5 + i); \
            if (i == 3) { l_run += ps0 + ps1; asm volatile("" : "+v"(sn), "+v"(l_run)); \
                          tmax = max3f(sn[0], sn[1], sn[2]); t2 = max3f(sn[3], sn[4], sn[5]); asm volatile("" : "+v"(tmax), "+v"(t2)); } \
            if (i == 4) { tmax = max3f(tmax, sn[6], sn[7]); t2 = max3f(t2, sn[8], sn[9]); tmax = max3f(tmax, sn[10], sn[11]); asm volatile("" : "+v"(tmax), "+v"(t2)); } \
            if (i == 5) { t2 = max3f(t2, sn[12], sn[13]); tmax = max3f(tmax, sn[14], sn[15]); tmax = max3f(tmax, t2, t2); asm volatile("" : "+v"(tmax)); } \
            if (i == 6) { tmax = swap_max3(tmax); asm volatile("" : "+v"(tmax)); } \
            if (i == YS0 && (!TAIL || (t) < 11)) KVDMA(voffK0, Kg + ((t) + 5) * 16384, kwr, 0); \
            if (i == YS1 && (!TAIL || (t) < 11)) KVDMA(voffK1, Kg + ((t) + 5) * 16384, kwr, 2048); \
            if (i == YS0 && TAIL && (t) == 11) BWARM(); \
            if (i == YS0 && TAIL && (t) == 12) BDMA(Bs, 0, 0); \
            if (i == YS1 && TAIL && (t) == 12) BDMA(Bs, 0, 1); \
            if (i == YS0 && TAIL && (t) == 13) BDMA(Bs, 0, 4); \
            if (i == YS1 && TAIL && (t) == 13) BDMA(Bs, 1, 0); \
            if (i == YS0 && TAIL && (t) == 14) BDMA(Bs, 1, 3); \
            if (i == YS1 && TAIL && (t) == 14) BDMA(Bs, 1, 4); \
            __builtin_amdgcn_sched_barrier(0); \
        } } while (0)
    const char* Bs = Bw + 144 * nt;
#define BDMA(bs, c, i) QDMA(boff[i], (bs) + (c) * (256 * DM * 2), lds0 + (((c) + 1) & 3) * REG + wid * 5120 + (i) * 1024)
#define BWARM() do { unsigned ln_ = lane; asm volatile("" : "+v"(ln_)); \
        if (ln_ < 18) asm volatile("s_mov_b32 m0, %2\n\ts_nop 0\n\tglobal_load_lds_dword %0, %1" :: "v"(ln_ * 128), \
                     "s"((const char*)wo + (size_t)(1024 * sk + 512) * DM * 2 + ((bid >> 3) * 8 + wid) * 2304), "s"(lds0 + 32768 + wid * 256) : "memory"); } while (0)
    unsigned vso = VSO(3), kso = KSO(1);
#pragma unroll 1
    for (int t = 0; t < 10; t += 2) { STEP(t, sA, sB, false); STEP(t + 1, sB, sA, false); }
#pragma unroll 1
    for (int t = 10; t < NT; t += 2) { STEP(t, sA, sB, true); STEP(t + 1, sB, sA, true); }
#undef STEP
#undef EXP1
#undef SUM1
    TL(2);
    l_run = swap_add(l_run);
    asm volatile("s_waitcnt lgkmcnt(0)\n\ts_barrier" ::: "memory");
    {
        f32x16 &keep0 = o[0], &keep1 = o[1], &send0 = o[2], &send1 = o[3];
        const unsigned xb = lds0 + (kh ? 0 : 3 * REG) + qb * 8704;
#pragma unroll
        for (int r4 = 0; r4 < 4; ++r4) {
            f32x4 v0 = {send0[4 * r4], send0[4 * r4 + 1], send0[4 * r4 + 2], send0[4 * r4 + 3]};
            f32x4 v1 = {send1[4 * r4], send1[4 * r4 + 1], send1[4 * r4 + 2], send1[4 * r4 + 3]};
            *(LAS f32x4*)(uintptr_t)(xb + r4 * 1024 + lane * 16) = v0;
            *(LAS f32x4*)(uintptr_t)(xb + (4 + r4) * 1024 + lane * 16) = v1;
        }
        *(LAS float*)(uintptr_t)(xb + 8192 + lane * 4) = m_run;
        *(LAS float*)(uintptr_t)(xb + 8192 + 256 + lane * 4) = l_run;
        asm volatile("s_waitcnt lgkmcnt(0)\n\ts_barrier" ::: "memory");
        const unsigned pb = lds0 + (kh ? 3 * REG : 0) + qb * 8704;
        const float m2 = *(const LAS float*)(uintptr_t)(pb + 8192 + lane * 4);
        const float l2 = *(const LAS float*)(uintptr_t)(pb + 8192 + 256 + lane * 4);
        const float mm = __builtin_fmaxf(m_run, m2);
        const float a1 = __builtin_amdgcn_exp2f((m_run - mm) * C2), a2 = __builtin_amdgcn_exp2f((m2 - mm) * C2);
        const float inv = 1.0f / (l_run * a1 + l2 * a2);
        const float w1 = a1 * inv, w2 = a2 * inv;
        const int row = b * SQ + 32 * (qb & 1) + r32;
        char* ob = ws + ((size_t)((row >> 4) * 128 + head * 4 + 2 * kh) * 64 + hi * 16 + (row & 15)) * 16;
#define OSTORE(KEEP, i) do { u32x2 w_[4]; \
            _Pragma("unroll") for (int r4 = 0; r4 < 4; ++r4) { \
                const f32x4 v = *(const LAS f32x4*)(uintptr_t)(pb + (4 * (i) + r4) * 1024 + lane * 16); \
                const float e0 = KEEP[4 * r4] * w1 + v[0] * w2, e1 = KEEP[4 * r4 + 1] * w1 + v[1] * w2; \
                const float e2 = KEEP[4 * r4 + 2] * w1 + v[2] * w2, e3 = KEEP[4 * r4 + 3] * w1 + v[3] * w2; \
                w_[r4] = u32x2{cvtpk(e0, e1), cvtpk(e2, e3)}; } \
            _Pragma("unroll") for (int g = 0; g < 2; ++g) { \
                u32x4 o4 = {w_[g].x, w_[g].y, w_[g + 2].x, w_[g + 2].y}; \
                asm volatile("global_store_dwordx4 %0, %1, off sc1\n\ts_nop 1" :: "v"(ob + (i) * 1024 + g * 512), "v"(o4) : "memory"); } } while (0)
        OSTORE(keep0, 0); OSTORE(keep1, 1);
#undef OSTORE
    }
    TL(3);
    const char* Ag = ws + (size_t)((8 * mt + 2 * qb) * 128 + 32 * sk + kh) * 1024;
    unsigned ao0 = lane * 16 + kh * 131072, ao1 = ao0 ^ 131072;
    unsigned bb[4];
#pragma unroll
    for (int c = 0; c < 4; ++c) { bb[c] = lds0 + ((c + 1) & 3) * REG + (32 * kh + 4 * gi + vq) * 160 + 8 * vp; asm volatile("" : "+v"(bb[c])); }
    asm volatile("" : "+v"(ao0), "+v"(ao1), "+s"(Ag));
    asm volatile("s_waitcnt vmcnt(0) lgkmcnt(0)\n\ts_barrier" ::: "memory");
    TL(4);
    unsigned dep1 = 0;
    if (wid == 0) {
        const unsigned td = rendezvous<8>(w1, tag, nt, lane, TMO, dep1);
        if (lane == 0) *(LAS unsigned*)(uintptr_t)(lds0 + REG + 144) = td;
    } else {
#pragma unroll
        for (int i = 0; i < 10; ++i) BDMA(Bs, 2 + i / 5, i % 5);
        if (wid == 1) {
#pragma unroll
            for (int i = 0; i < 10; ++i)
                QDMA(boff[i % 5], Bs - 32 * DM * 2 + (2 + i / 5) * (256 * DM * 2), lds0 + ((3 + i / 5) & 3) * REG + (i % 5) * 1024);
        }
    }
    asm volatile("s_waitcnt lgkmcnt(0)\n\ts_barrier" ::: "memory");
    unsigned todo = __builtin_amdgcn_readfirstlane(*(const LAS unsigned*)(uintptr_t)(lds0 + REG + 144));
    TL(5);
    float* part = (float*)(ws + PART_OFF);
    bool left = false;
    while (todo) {
        const int ntc = ((todo >> nt) & 1u) ? nt : __builtin_ctz(todo);
        todo &= ~(1u << ntc);
        if (ntc != nt) {
            asm volatile("s_waitcnt vmcnt(0) lgkmcnt(0)\n\ts_barrier" ::: "memory");
#pragma unroll 1
            for (int c = 0; c < 4; ++c) {
#pragma unroll
                for (int i = 0; i < 5; ++i) BDMA(Bw + 144 * ntc, c, i);
            }
            asm volatile("s_waitcnt vmcnt(0)\n\ts_barrier" ::: "memory");
        }
        u32x4 an[32];
#define ALD(s_) do { const char* ag = Ag; asm volatile("" : "+s"(ag)); ag += ((s_) >> 1) * 4096; \
            if ((s_) & 1) { \
                asm volatile("global_load_dwordx4 %0, %1, %2 offset:2048 sc1" : "=v"(an[2 * (s_)]) : "v"(ao0), "s"(ag) : "memory"); \
                asm volatile("global_load_dwordx4 %0, %1, %2 offset:2048 sc1" : "=v"(an[2 * (s_) + 1]) : "v"(ao1), "s"(ag) : "memory"); \
            } else { \
                asm volatile("global_load_dwordx4 %0, %1, %2 sc1" : "=v"(an[2 * (s_)]) : "v"(ao0), "s"(ag) : "memory"); \
                asm volatile("global_load_dwordx4 %0, %1, %2 sc1" : "=v"(an[2 * (s_) + 1]) : "v"(ao1), "s"(ag) : "memory"); } } while (0)
#pragma unroll
        for (int s2 = 0; s2 < AHEAD; ++s2) ALD(s2);
        f32x4 acc[2][5];
#pragma unroll
        for (int ct = 0; ct < 5; ++ct) { acc[0][ct] = f32x4{0.f, 0.f, 0.f, 0.f}; acc[1][ct] = f32x4{0.f, 0.f, 0.f, 0.f}; }
        s16x4 blo[2][5], bhi[2][5];
#pragma unroll
        for (int ct = 0; ct < 5; ++ct) { blo[0][ct] = vtr(bb[0] + 32 * ct); bhi[0][ct] = vtr(bb[0] + 32 * ct + 2560); }
#pragma unroll
        for (int s = 0; s < 16; ++s) {
            asm volatile("s_waitcnt vmcnt(%2)" : "+v"(an[2 * s]), "+v"(an[2 * s + 1]) : "n"(s + AHEAD <= 16 ? 2 * AHEAD - 2 : 30 - 2 * s) : "memory");
            if (s == 0) { TL(6); }
            if (s == 7) asm volatile("s_barrier" ::: "memory");
            __builtin_amdgcn_sched_barrier(0);
#pragma unroll
            for (int ct = 0; ct < 5; ++ct) {
                if (s < 15) {
                    const unsigned ad = bb[((s + 1) >> 2) & 3] + ((s + 1) & 3) * 10240 + 32 * ct;
                    blo[(s + 1) & 1][ct] = vtr(ad); bhi[(s + 1) & 1][ct] = vtr(ad + 2560);
                }
                const s16x4 lo = blo[s & 1][ct], h4 = bhi[s & 1][ct];
                const bf16x8 bf = {lo[0], lo[1], lo[2], lo[3], h4[0], h4[1], h4[2], h4[3]};
                acc[0][ct] = MFMA16(bf, __builtin_bit_cast(bf16x8, an[2 * s]), acc[0][ct]);
                acc[1][ct] = MFMA16(bf, __builtin_bit_cast(bf16x8, an[2 * s + 1]), acc[1][ct]);
                if (ct == 1 && s + AHEAD < 16) ALD(s + AHEAD);
                __builtin_amdgcn_sched_barrier(0);
            }
        }
        TL(7);
#pragma unroll
        for (int ct = 0; ct < 5; ++ct)
            *(LAS f32x4*)(uintptr_t)(lds0 + REG + (wid * 5 + ct) * 1024 + lane * 16) = acc[1][ct];
        asm volatile("s_waitcnt lgkmcnt(0)\n\ts_barrier" ::: "memory");
        float* pp = part + ((size_t)(sk * 64 + 8 * mt + 2 * qb + kh) * 8 + ntc) * 1152 + lane * 4;
#pragma unroll
        for (int ct = 0; ct < 5; ++ct) acc[1][ct] = *(const LAS f32x4*)(uintptr_t)(lds0 + REG + ((wid ^ 4) * 5 + ct) * 1024 + lane * 16);
        if (wid == 0 && !left) { left = true; depart<8>(w1, dep1, lane); }
#pragma unroll
        for (int ct = 0; ct < 5; ++ct) {
            const f32x4 r_ = acc[0][ct] + acc[1][ct];
            if (ct < 4 || lane < 32)
                asm volatile("global_store_dwordx4 %0, %1, off sc1\n\ts_nop 1" :: "v"(pp + ct * 256), "v"(r_) : "memory");
        }
        TL(8);
        asm volatile("s_waitcnt vmcnt(0)\n\ts_barrier" ::: "memory");
        TL(9);
        unsigned dep2 = 0;
        if (wid == 0) {
            const unsigned td = rendezvous<4>(w2 + ntc * 128, tag, sk, lane, TMO2, dep2);
            if (lane == 0) *(LAS unsigned*)(uintptr_t)(lds0 + 2 * REG) = td;
        }
        asm volatile("s_waitcnt lgkmcnt(0)\n\ts_barrier" ::: "memory");
        unsigned qs = __builtin_amdgcn_readfirstlane(*(const LAS unsigned*)(uintptr_t)(lds0 + 2 * REG));
        TL(10);
        bool left2 = false;
        while (qs) {
            const int q = __builtin_ctz(qs);
            qs &= qs - 1;
            if (tid < 288) {
                const float* p = part + ((size_t)(8 * mt + 2 * q) * 8 + ntc) * 1152 + tid * 4;
                f32x4 v[2][4];
#pragma unroll
                for (int r = 0; r < 2; ++r)
#pragma unroll
                    for (int s = 0; s < 4; ++s)
                        asm volatile("global_load_dwordx4 %0, %1, off sc1" : "=v"(v[r][s]) : "v"(p + (size_t)s * 64 * 8 * 1152 + r * 8 * 1152) : "memory");
                asm volatile("s_waitcnt vmcnt(0)" : "+v"(v[0][0]), "+v"(v[0][1]), "+v"(v[0][2]), "+v"(v[0][3]),
                                                    "+v"(v[1][0]), "+v"(v[1][1]), "+v"(v[1][2]), "+v"(v[1][3]) :: "memory");
#pragma unroll
                for (int r = 0; r < 2; ++r) {
                    const f32x4 t = ((v[r][0] + v[r][1]) + v[r][2]) + v[r][3];
                    u32x2 w = {cvtpk(t[0], t[1]), cvtpk(t[2], t[3])};
                    *(u32x2*)(out + (size_t)(16 * (8 * mt + 2 * q + r) + (tid & 15)) * DM + 72 * ntc + 4 * (tid >> 4)) = w;
                }
            }
            if (wid == 0 && !left2) { left2 = true; depart<4>(w2 + ntc * 128, dep2, lane); }
        }
        if (wid == 0 && !left2) {
            asm volatile("s_waitcnt vmcnt(0)" ::: "memory");
            depart<4>(w2 + ntc * 128, dep2, lane);
        }
    }
    if (wid == 0 && !left) {
        asm volatile("s_waitcnt vmcnt(0)" ::: "memory");
        depart<8>(w1, dep1, lane);
    }
    TL(11);
}

extern "C" void kernel_launch(void* const* d_in, const int* in_sizes, int n_in, void* d_out, int out_size,
                              void* d_ws, size_t ws_size, hipStream_t stream) {
    (void)hipFuncSetAttribute((const void*)gqa_kernel, hipFuncAttributeMaxDynamicSharedMemorySize, LDS_BYTES);
    gqa_kernel<<<NB * HKV, 512, LDS_BYTES, stream>>>((const u16*)d_in[0], (const u16*)d_in[1], (const u16*)d_in[2],
                                                     (const u16*)d_in[3], (const u16*)d_in[4], (char*)d_ws, (u16*)d_out);
}
```

```cpp
#include <hip/hip_runtime.h>
#include <hip/hip_bf16.h>
#include <stdint.h>

typedef short bf16x8 __attribute__((ext_vector_type(8)));
typedef short s16x4 __attribute__((ext_vector_type(4)));
typedef float f32x16 __attribute__((ext_vector_type(16)));
typedef float f32x4 __attribute__((ext_vector_type(4)));
typedef unsigned u32x4 __attribute__((ext_vector_type(4)));
typedef unsigned u32x2 __attribute__((ext_vector_type(2)));
typedef unsigned short u16;
#define LAS __attribute__((address_space(3)))

constexpr int NB = 16, SQ = 64, DM = 576, HQ = 32, HKV = 16, SKV = 1024, DH = 128;
constexpr int NQ = HQ * DH;
constexpr int NT = SKV / 64;
constexpr int REG = 40960;
constexpr int LDS_BYTES = 4 * REG;
constexpr size_t PART_OFF = (size_t)NB * SQ * NQ * 2;
constexpr size_t CNT_OFF = (size_t)20 << 20;
constexpr int AHEAD = 2;
constexpr unsigned long long TMO = 5000ull;
constexpr int XS0 = 5, XS1 = 7, YS0 = 5, YS1 = 7;
constexpr int PACE = 108;
constexpr float RTHR = 8.0f;
constexpr float C2 = 0.088388347648318447f * 1.4426950408889634f;

#define MFMA32(a, b, c) __builtin_amdgcn_mfma_f32_32x32x16_bf16(a, b, c, 0, 0, 0)

typedef __bf16 bf16x2_t __attribute__((ext_vector_type(2)));
typedef float f32x2 __attribute__((ext_vector_type(2)));
__device__ __forceinline__ unsigned cvtpk(float lo, float hi) {
    f32x2 v = {lo, hi};
    return __builtin_bit_cast(unsigned, __builtin_convertvector(v, bf16x2_t));
}
__device__ __forceinline__ s16x4 vtr(unsigned a) {
    return __builtin_amdgcn_ds_read_tr16_b64_v4i16((LAS s16x4*)(uintptr_t)a);
}
__device__ __forceinline__ bf16x8 ldsr128(unsigned a) { return *(const LAS bf16x8*)(uintptr_t)a; }
__device__ __forceinline__ float swap_max(float v) {
    auto rr = __builtin_amdgcn_permlane32_swap(__float_as_uint(v), __float_as_uint(v), false, false);
    return __builtin_fmaxf(__uint_as_float(rr[0]), __uint_as_float(rr[1]));
}
__device__ __forceinline__ float max3f(float a, float b, float c) { float r; asm("v_max3_f32 %0, %1, %2, %3" : "=v"(r) : "v"(a), "v"(b), "v"(c)); return r; }
__device__ __forceinline__ float fmul1(float a, float b) { float r; asm("v_mul_f32_e32 %0, %1, %2" : "=v"(r) : "s"(b), "v"(a)); return r; }
__device__ __forceinline__ float swap_max3(float v) {
    auto rr = __builtin_amdgcn_permlane32_swap(__float_as_uint(v), __float_as_uint(v), false, false);
    return max3f(__uint_as_float(rr[0]), __uint_as_float(rr[1]), __uint_as_float(rr[1]));
}
__device__ __forceinline__ float swap_add(float v) {
    auto rr = __builtin_amdgcn_permlane32_swap(__float_as_uint(v), __float_as_uint(v), false, false);
    return __uint_as_float(rr[0]) + __uint_as_float(rr[1]);
}

extern "C" __device__ unsigned long long amd_dispatch_id() __asm("llvm.amdgcn.dispatch.id");

template <unsigned N>
__device__ __forceinline__ unsigned rendezvous(const char* w, unsigned tag, unsigned own, int lane, unsigned long long tmo, unsigned& dep) {
    unsigned old = 0, todo = 1u << own;
    u32x2 v;
    if (lane == 0) asm volatile("global_atomic_add %0, %1, %2, off sc0" : "=v"(old) : "v"(w), "v"(1u) : "memory");
    asm volatile("global_load_dwordx2 %0, %1, off sc1" : "=v"(v) : "v"(w) : "memory");
    asm volatile("s_waitcnt vmcnt(0)" : "+v"(old), "+v"(v) :: "memory");
    old = __builtin_amdgcn_readfirstlane(old);
    if ((old & 255u) == N - 1) todo |= (old >> 8) & 255u;
    else {
        const unsigned long long t0 = __builtin_amdgcn_s_memrealtime();
        while ((__builtin_amdgcn_readfirstlane(v.x) & 255u) != N || __builtin_amdgcn_readfirstlane(v.y) != tag) {
            if (__builtin_amdgcn_s_memrealtime() - t0 > tmo) {
                unsigned o2 = 0;
                if (lane == 0) asm volatile("global_atomic_or %0, %1, %2, off sc0" : "=v"(o2) : "v"(w), "v"(256u << own) : "memory");
                asm volatile("s_waitcnt vmcnt(0)" : "+v"(o2) :: "memory");
                if ((__builtin_amdgcn_readfirstlane(o2) & 255u) != N) todo = 0;
                break;
            }
            asm volatile("global_load_dwordx2 %0, %1, off sc1\n\ts_waitcnt vmcnt(0)" : "=v"(v) : "v"(w) : "memory");
        }
    }
    dep = 0;
    if (lane == 0) asm volatile("global_atomic_add %0, %1, %2, off sc0" : "=v"(dep) : "v"(w), "v"(65536u) : "memory");
    return todo;
}
template <unsigned N>
__device__ __forceinline__ void depart(const char* w, unsigned dep, int lane) {
    asm volatile("" : "+v"(dep));
    if (lane == 0 && ((dep >> 16) & 255u) == N - 1)
        asm volatile("global_store_dwordx2 %0, %1, off sc1" :: "v"(w), "v"(u32x2{0u, 0u}) : "memory");
}

#define MFMA16(a, b, c) __builtin_amdgcn_mfma_f32_16x16x32_bf16(a, b, c, 0, 0, 0)
__global__ __launch_bounds__(512, 2) void gqa_kernel(const u16* __restrict__ x, const u16* __restrict__ Kc,
                                                     const u16* __restrict__ Vc, const u16* __restrict__ wq,
                                                     const u16* __restrict__ wo, char* ws, u16* __restrict__ out) {
    extern __shared__ __attribute__((aligned(16))) char lds[];
    const unsigned lds0 = (unsigned)(uintptr_t)lds;
    const int tid = threadIdx.x, lane = tid & 63, r32 = lane & 31, hi = lane >> 5;
    const int wid = __builtin_amdgcn_readfirstlane(tid >> 6);
    const int qb = wid & 3, kh = wid >> 2;
    const int bid = blockIdx.x, b = 8 * (bid & 1) + ((bid >> 3) & 7), kvh = 4 * ((bid >> 1) & 3) + (bid >> 6);
    const int head = 2 * kvh + (qb >> 1);
    const int mt = b >> 1, sk = kvh >> 2, nt = 4 * (b & 1) + (kvh & 3);
    const unsigned tag = 0x80000000u | ((unsigned)amd_dispatch_id() & 0xfffffu) |
                         ((((unsigned)((uintptr_t)__builtin_amdgcn_dispatch_ptr() >> 12) * 0x9e3779b1u) >> 21) << 20);
    const char* w1 = ws + CNT_OFF + (mt * 4 + sk) * 128;
    const char* w2 = ws + CNT_OFF + 4096 + mt * 8 * 128;
    const char* wcl = lane == 0 ? w1 : w2 + nt * 128;
    u32x2 clm = {0u, tag};
    if (wid == 1 && lane < 2)
        asm volatile("global_atomic_cmpswap_x2 %0, %1, %2, off sc0\n\ts_nop 1" : "=v"(clm) : "v"(wcl), "v"(u32x4{0u, tag, 0u, 0u}) : "memory");
    const int gi = lane >> 4, vq = (lane & 15) >> 2, vp = lane & 3;

    const unsigned vsrc = (16 * (wid >> 1) + ((lane >> 2) & 7)) * 256 + (wid & 1) * 128 + hi * 64;
    const unsigned voffV = (vsrc ^ (kh * 128)) + vp * 16;
    const unsigned voffK0 = vsrc + ((vp ^ (wid & 2)) << 4), voffK1 = vsrc + ((vp ^ ((wid & 2) | 1)) << 4);
    const char* Kg = (const char*)Kc + (size_t)(b * HKV + kvh) * SKV * DH * 2;
    const char* Vg = (const char*)Vc + (size_t)(b * HKV + kvh) * SKV * DH * 2;
    const unsigned dwo = lds0 + (wid >> 1) * 4096 + (wid & 1) * 1024;
#define KVDMA(voff, gp, dst, OFF) asm volatile("s_mov_b32 m0, %2\n\ts_nop 0\n\tglobal_load_lds_dwordx4 %0, %1 offset:" #OFF " sc1 nt" \
            :: "v"(voff), "s"(gp), "s"(dst) : "memory")
#define VSO(tn) ((int)((0x0200110au >> (8 * ((tn) & 3))) & 255) * 8192)
#define KSO(tn) ((int)((0x0f0c0705u >> (8 * ((tn) & 3))) & 255) * 8192)
#define DMA_V0(tn) KVDMA(voffV, Vg + (tn) * 16384, dwo + VSO(tn), 0)
#define DMA_V1(tn) KVDMA(voffV, Vg + (tn) * 16384, dwo + VSO(tn), 2048)
#define DMA_K0(tn) KVDMA(voffK0, Kg + (tn) * 16384, dwo + KSO(tn), 0)
#define DMA_K1(tn) KVDMA(voffK1, Kg + (tn) * 16384, dwo + KSO(tn), 2048)
#define DMA_V(tn) do { DMA_V0(tn); DMA_V1(tn); } while (0)
#define DMA_K(tn) do { DMA_K0(tn); DMA_K1(tn); } while (0)
    const char* Bw = (const char*)wo + (size_t)(1024 * sk + 32 * wid) * DM * 2;
    unsigned boff[5];
    bf16x8 qf[8];
    {
        int kb = (b + 2 * (kvh & 3)) % 9;
        const unsigned xo = (8 * wid + (lane >> 3)) * DM * 2 + (((lane & 7) ^ ((4 * wid + (lane >> 4)) & 7)) << 4);
        const unsigned wo0 = (8 * wid + hi) * NQ * 2 + ((r32 ^ (hi << 2)) << 4);
        const unsigned wo1 = (8 * wid + 2 + hi) * NQ * 2 + ((r32 ^ ((2 + hi) << 2)) << 4);
        const unsigned wo2 = wo0 + 4 * NQ * 2, wo3 = wo1 + 4 * NQ * 2;
        const char* Xs = (const char*)x + (size_t)b * SQ * DM * 2;
        const char* Ws = (const char*)wq + kvh * 512;
#define QDMA(voff, gp, dst) asm volatile("s_mov_b32 m0, %2\n\ts_nop 0\n\tglobal_load_lds_dwordx4 %0, %1" :: "v"(voff), "s"(gp), "s"(dst) : "memory")
#define QS_PIECE(slot, p) do { const unsigned s_ = lds0 + (slot) * 40960 + wid * 4096; const char* w_ = Ws + (size_t)kb * 64 * NQ * 2; \
            if ((p) == 0) QDMA(wo0, w_, s_); \
            if ((p) == 1) QDMA(wo1, w_, s_ + 1024); \
            if ((p) == 2) QDMA(wo2, w_, s_ + 2048); \
            if ((p) == 3) QDMA(wo3, w_, s_ + 3072); \
            if ((p) == 4) { QDMA(xo, Xs + kb * 128, lds0 + (slot) * 40960 + 32768 + wid * 1024); kb = kb == 8 ? 0 : kb + 1; } } while (0)
#define QS_ISSUE(slot) do { QS_PIECE(slot, 0); QS_PIECE(slot, 1); QS_PIECE(slot, 2); QS_PIECE(slot, 3); QS_PIECE(slot, 4); } while (0)
#pragma unroll 1
        for (int j = 0; j < 2; ++j) { QS_ISSUE(j); asm volatile("s_barrier" ::: "memory"); }
#define BOFF(odd) _Pragma("unroll") for (int i = 0; i < 5; ++i) { \
            unsigned ln_ = lane; asm volatile("" : "+v"(ln_)); \
            const unsigned n = 64 * i + ln_, row = (n * 205u) >> 11, c = n - 10 * row + 2 * (odd); \
            boff[i] = row * (DM * 2) + (c < 10 ? c : c - 10) * 16; \
            asm volatile("" : "+v"(boff[i])); }
        BOFF(nt & 1);
        const int pp = ((vp & 1) << 1) | (vp >> 1);
        const unsigned abase = lds0 + (8 * hi + vq) * 512 + (((4 * wid + 2 * (gi & 1) + (pp >> 1)) ^ (vq << 2)) << 4) + 8 * (pp & 1);
        const unsigned xbase = lds0 + 32768 + r32 * 128 + ((hi ^ ((r32 >> 1) & 7)) << 4);
        f32x16 acc0 = {}, acc1 = {};
        s16x4 lo[2][4], h4[2][4]; bf16x8 x0[2][4], x1[2][4];
        asm volatile("s_waitcnt vmcnt(5)\n\ts_barrier" : "+v"(clm) :: "memory");
        QS_ISSUE(2);
        if (wid == 1 && lane < 2 && clm.y != tag && (clm.x | clm.y) != 0u)
            asm volatile("global_atomic_cmpswap_x2 %0, %1, off\n\ts_nop 1" :: "v"(wcl), "v"(u32x4{0u, tag, clm.x, clm.y}) : "memory");
#pragma unroll
        for (int i = 0; i < 4; ++i) {
            lo[0][i] = vtr(abase + i * 8192); h4[0][i] = vtr(abase + i * 8192 + 2048);
            x0[0][i] = ldsr128(xbase ^ (i << 5)); x1[0][i] = ldsr128((xbase ^ (i << 5)) + 4096);
        }
#pragma unroll
        for (int st = 0; st < 9; ++st) {
            if (st == 0) asm volatile("s_waitcnt vmcnt(5)" ::: "memory");
            else if (st < 6) asm volatile("s_waitcnt vmcnt(10)" ::: "memory");
            else if (st == 6) asm volatile("s_waitcnt vmcnt(9)" ::: "memory");
            else if (st == 7) asm volatile("s_waitcnt vmcnt(6)" ::: "memory");
            asm volatile("s_waitcnt lgkmcnt(0)\n\ts_barrier" ::: "memory");
            const unsigned so = ((st + 1) & 3) * 40960;
            const int cu = st & 1, nx = cu ^ 1;
            unsigned aa = abase + so;
            asm volatile("" : "+v"(aa));
            __builtin_amdgcn_sched_barrier(0);
            if (st == 0) QS_ISSUE(3);
#pragma unroll
            for (int i = 0; i < 4; ++i) {
                const bf16x8 a0 = {lo[cu][i][0], lo[cu][i][1], lo[cu][i][2], lo[cu][i][3], h4[cu][i][0], h4[cu][i][1], h4[cu][i][2], h4[cu][i][3]};
                acc0 = MFMA32(a0, x0[cu][i], acc0);
                if (st < 8) { lo[nx][i] = vtr(aa + i * 8192); h4[nx][i] = vtr(aa + i * 8192 + 2048); }
                if (st < 5) QS_PIECE(st & 3, i);
                __builtin_amdgcn_sched_barrier(0);
                acc1 = MFMA32(a0, x1[cu][i], acc1);
                if (st < 8) { unsigned xa = (xbase ^ (i << 5)) + so; asm volatile("" : "+v"(xa)); x0[nx][i] = ldsr128(xa); x1[nx][i] = ldsr128(xa + 4096); }
                if (st < 5 && i == 3) QS_PIECE(st & 3, 4);
                if (st == 5 && i == 3) { DMA_K(0); DMA_K(1); }
                if (st == 6 && i == 3) DMA_V(0);
                if (st == 7 && i == 3) DMA_K(2);
                if (st == 8 && i == 3) DMA_K(3);
                __builtin_amdgcn_sched_barrier(0);
            }
        }
#undef QS_ISSUE
#undef QS_PIECE
#define QW(ACC, qblk) do { _Pragma("unroll") for (int s = 0; s < 2; ++s) { \
            u32x4 w_ = {cvtpk(ACC[8 * s], ACC[8 * s + 1]), cvtpk(ACC[8 * s + 2], ACC[8 * s + 3]), \
                        cvtpk(ACC[8 * s + 4], ACC[8 * s + 5]), cvtpk(ACC[8 * s + 6], ACC[8 * s + 7])}; \
            *(LAS u32x4*)(uintptr_t)(lds0 + 32768 + (2 * (wid >> 2) + (qblk)) * REG + (2 * (wid & 3) + s) * 1024 + lane * 16) = w_; } } while (0)
        QW(acc0, 0); QW(acc1, 1);
#undef QW
        DMA_V(1);
    }
    asm volatile("s_waitcnt vmcnt(8) lgkmcnt(0)\n\ts_barrier" ::: "memory");
#pragma unroll
    for (int f = 0; f < 8; ++f) qf[f] = ldsr128(lds0 + 32768 + qb * REG + f * 1024 + lane * 16);

    const unsigned kb0 = lds0 + (4 * kh + (r32 >> 3)) * 2048 + (r32 & 7) * 64 + ((hi ^ (r32 >> 3)) << 4);
    const unsigned kb1 = kb0 ^ 32;
    const unsigned vb = lds0 + kh * 8192 + (4 * hi + vq) * 64 + (gi & 1) * 32 + vp * 8;

    float m_run, l_run = 0.f, tmax = 0.f;
    unsigned tprev = (unsigned)__builtin_amdgcn_s_memrealtime() - (unsigned)PACE;
    f32x16 o[4] = {};
    f32x16 sA, sB, negm;

    bf16x8 kf[8];
    {
        bf16x8 k0[8];
        unsigned ka0 = kb0 + KSO(0), ka1 = kb1 + KSO(0);
        asm volatile("" : "+v"(ka0), "+v"(ka1));
#pragma unroll
        for (int d0 = 0; d0 < 8; ++d0) k0[d0] = ldsr128(((d0 & 1) ? ka1 : ka0) + (d0 >> 1) * 512);
#pragma unroll
        for (int d0 = 0; d0 < 8; ++d0) kf[d0] = ldsr128(((d0 & 1) ? ka1 : ka0) + 16384 + (d0 >> 1) * 512);
        __builtin_amdgcn_sched_barrier(0);
        asm volatile("s_waitcnt lgkmcnt(0)\n\ts_barrier" ::: "memory");
        sA = f32x16{};
#pragma unroll
        for (int d0 = 0; d0 < 8; ++d0) sA = MFMA32(k0[d0], qf[d0], sA);
        __builtin_amdgcn_sched_barrier(0);
        DMA_V(2); DMA_K(4);
        m_run = sA[0];
#pragma unroll
        for (int r = 1; r < 16; ++r) m_run = __builtin_fmaxf(m_run, sA[r]);
        m_run = swap_max(m_run);
#pragma unroll
        for (int r = 0; r < 16; ++r) { sA[r] -= m_run; negm[r] = -m_run; }
    }
#define EXP1(r) do { sc[r] = __builtin_amdgcn_exp2f(fmul1(sc[r], C2)); } while (0)
#define SUM1(k) do { ps0 += sc[2 * (k)]; ps1 += sc[2 * (k) + 1]; pw[k] = cvtpk(sc[2 * (k)], sc[2 * (k) + 1]); asm volatile("" : "+v"(pw[k]), "+v"(ps0), "+v"(ps1)); } while (0)
#define STEP(t, SC_, SN_, TAIL, LAST) do { \
        f32x16& sc = SC_; f32x16& sn = SN_; \
        s16x4 vlo[8], vhi[8]; \
        if (!TAIL || (t) < 12) asm volatile("s_waitcnt vmcnt(8)" ::: "memory"); \
        else if ((t) < 14) asm volatile("s_waitcnt vmcnt(7)" ::: "memory"); \
        else if ((t) == 14) asm volatile("s_waitcnt vmcnt(9)" ::: "memory"); \
        else asm volatile("s_waitcnt vmcnt(10)" ::: "memory"); \
        if (!(TAIL && (t) >= (NT - 2))) while ((unsigned)__builtin_amdgcn_s_memrealtime() - tprev < (unsigned)PACE) __builtin_amdgcn_s_sleep(1); \
        asm volatile("s_waitcnt lgkmcnt(0)\n\ts_barrier" ::: "memory"); \
        if (!(TAIL && (t) + 1 >= (NT - 2))) tprev = (unsigned)__builtin_amdgcn_s_memrealtime(); \
        if (__builtin_expect(__any(tmax > RTHR / C2), 0)) { \
            const float dl = __builtin_fmaxf(tmax, 0.f); \
            const float alpha = __builtin_amdgcn_exp2f(-dl * C2); \
            m_run += dl; l_run *= alpha; \
            _Pragma("unroll") for (int r = 0; r < 16; ++r) { sc[r] -= dl; negm[r] = -m_run; } \
            _Pragma("unroll") for (int dt = 0; dt < 4; ++dt) \
                _Pragma("unroll") for (int r = 0; r < 16; ++r) o[dt][r] *= alpha; \
        } \
        const unsigned vwr = dwo + vso, kwr = dwo + kso;                         \
        vso = VSO(t); kso = KSO((t) + 2); \
        const unsigned va = vb + vso; \
        const unsigned ka0 = kb0 + kso, ka1 = kb1 + kso;                         \
        unsigned pw[8]; \
        float ps0 = 0.f, ps1 = 0.f, t2; \
        __builtin_amdgcn_sched_barrier(0); \
          \
        _Pragma("unroll") for (int i = 0; i < 8; ++i) { \
            if (!LAST) sn = MFMA32(kf[i], qf[i], i == 0 ? negm : sn); \
            const int ss = i >> 2, dt = i & 3; \
            vlo[2 * dt + ss] = vtr(va + ss * 4096 + dt * 512); \
            vhi[2 * dt + ss] = vtr(va + ss * 4096 + 2048 + dt * 512); \
            if (i < 4) { EXP1(2 * i); EXP1(2 * i + 1); } else EXP1(4 + i); \
            if (i >= 1 && i <= 4) SUM1(i - 1); \
            if (i == 6) SUM1(4); \
            if (!LAST) asm volatile("" : "+v"(sn)); \
            asm volatile("" : "+v"(sc)); \
            if (i == XS0 && (!TAIL || (t) < 13)) KVDMA(voffV, Vg + ((t) + 3) * 16384, vwr, 0); \
            if (i == XS1 && (!TAIL || (t) < 13)) KVDMA(voffV, Vg + ((t) + 3) * 16384, vwr, 2048); \
            if (i == XS0 && TAIL && (t) == 13) BDMA(Bs, 0, 2); \
            if (i == XS1 && TAIL && (t) == 13) BDMA(Bs, 0, 3); \
            if (i == XS0 && TAIL && (t) == 14) BDMA(Bs, 1, 1); \
            if (i == XS1 && TAIL && (t) == 14) BDMA(Bs, 1, 2); \
            __builtin_amdgcn_sched_barrier(0); \
        } \
          \
        _Pragma("unroll") for (int i = 0; i < 8; ++i) { \
            const int ss = i >> 2, dt = i & 3; \
            const s16x4 lo = vlo[2 * dt + ss], hi4 = vhi[2 * dt + ss]; \
            const bf16x8 vf = {lo[0], lo[1], lo[2], lo[3], hi4[0], hi4[1], hi4[2], hi4[3]}; \
            const bf16x8 pf = __builtin_bit_cast(bf16x8, u32x4{pw[4 * ss], pw[4 * ss + 1], pw[4 * ss + 2], pw[4 * ss + 3]}); \
            o[dt] = MFMA32(vf, pf, o[dt]); \
            if (i < 4 && !LAST) { kf[2 * i] = ldsr128(ka0 + i * 512); kf[2 * i + 1] = ldsr128(ka1 + i * 512); } \
            if (i < 2) { EXP1(12 + 2 * i); EXP1(13 + 2 * i); asm volatile("" : "+v"(sc)); } \
            if (i < 3) SUM1(5 + i); \
            if (i == 3 && LAST) l_run += ps0 + ps1; \
            if (i == 3 && !LAST) { l_run += ps0 + ps1; asm volatile("" : "+v"(sn), "+v"(l_run)); \
                          tmax = max3f(sn[0], sn[1], sn[2]); t2 = max3f(sn[3], sn[4], sn[5]); asm volatile("" : "+v"(tmax), "+v"(t2)); } \
            if (i == 4 && !LAST) { tmax = max3f(tmax, sn[6], sn[7]); t2 = max3f(t2, sn[8], sn[9]); tmax = max3f(tmax, sn[10], sn[11]); asm volatile("" : "+v"(tmax), "+v"(t2)); } \
            if (i == 5 && !LAST) { t2 = max3f(t2, sn[12], sn[13]); tmax = max3f(tmax, sn[14], sn[15]); tmax = max3f(tmax, t2, t2); asm volatile("" : "+v"(tmax)); } \
            if (i == 6 && !LAST) { tmax = swap_max3(tmax); asm volatile("" : "+v"(tmax)); } \
            if (i == YS0 && (!TAIL || (t) < 11)) KVDMA(voffK0, Kg + ((t) + 5) * 16384, kwr, 0); \
            if (i == YS1 && (!TAIL || (t) < 11)) KVDMA(voffK1, Kg + ((t) + 5) * 16384, kwr, 2048); \
            if (i == YS0 && TAIL && (t) == 11) BWARM(); \
            if (i == YS0 && TAIL && (t) == 12) BDMA(Bs, 0, 0); \
            if (i == YS1 && TAIL && (t) == 12) BDMA(Bs, 0, 1); \
            if (i == YS0 && TAIL && (t) == 13) BDMA(Bs, 0, 4); \
            if (i == YS1 && TAIL && (t) == 13) BDMA(Bs, 1, 0); \
            if (i == YS0 && TAIL && (t) == 14) BDMA(Bs, 1, 3); \
            if (i == YS1 && TAIL && (t) == 14) BDMA(Bs, 1, 4); \
            __builtin_amdgcn_sched_barrier(0); \
        } } while (0)
    const char* Bs = Bw + 288 * (nt >> 1) + 128 * (nt & 1);
#define BDMA(bs, c, i) QDMA(boff[i], (bs) + (c) * (256 * DM * 2), lds0 + (((c) + 1) & 3) * REG + wid * 5120 + (i) * 1024)
#define BWARM() do { unsigned ln_ = lane; asm volatile("" : "+v"(ln_)); \
        if (ln_ < 18) asm volatile("s_mov_b32 m0, %2\n\ts_nop 0\n\tglobal_load_lds_dword %0, %1" :: "v"(ln_ * 128), \
                     "s"((const char*)wo + (size_t)(1024 * sk + 512) * DM * 2 + ((bid >> 3) * 8 + wid) * 2304), "s"(lds0 + 32768 + wid * 256) : "memory"); } while (0)
    unsigned vso = VSO(3), kso = KSO(1);
#pragma unroll 1
    for (int t = 0; t < 10; t += 2) { STEP(t, sA, sB, false, false); STEP(t + 1, sB, sA, false, false); }
    STEP(10, sA, sB, true, false); STEP(11, sB, sA, true, false); STEP(12, sA, sB, true, false); STEP(13, sB, sA, true, false);
    STEP(NT - 2, sA, sB, true, false);
    STEP(NT - 1, sB, sA, true, true);
#undef STEP
#undef EXP1
#undef SUM1
    l_run = swap_add(l_run);
    asm volatile("s_waitcnt lgkmcnt(0)\n\ts_barrier" ::: "memory");
    {
        f32x16 &keep0 = o[0], &keep1 = o[1], &send0 = o[2], &send1 = o[3];
        const unsigned xb = lds0 + (kh ? 0 : 3 * REG) + qb * 8704;
#pragma unroll
        for (int r4 = 0; r4 < 4; ++r4) {
            f32x4 v0 = {send0[4 * r4], send0[4 * r4 + 1], send0[4 * r4 + 2], send0[4 * r4 + 3]};
            f32x4 v1 = {send1[4 * r4], send1[4 * r4 + 1], send1[4 * r4 + 2], send1[4 * r4 + 3]};
            *(LAS f32x4*)(uintptr_t)(xb + r4 * 1024 + lane * 16) = v0;
            *(LAS f32x4*)(uintptr_t)(xb + (4 + r4) * 1024 + lane * 16) = v1;
        }
        *(LAS float*)(uintptr_t)(xb + 8192 + lane * 4) = m_run;
        *(LAS float*)(uintptr_t)(xb + 8192 + 256 + lane * 4) = l_run;
        asm volatile("s_waitcnt lgkmcnt(0)\n\ts_barrier" ::: "memory");
        const unsigned pb = lds0 + (kh ? 3 * REG : 0) + qb * 8704;
        const float m2 = *(const LAS float*)(uintptr_t)(pb + 8192 + lane * 4);
        const float l2 = *(const LAS float*)(uintptr_t)(pb + 8192 + 256 + lane * 4);
        const float mm = __builtin_fmaxf(m_run, m2);
        const float a1 = __builtin_amdgcn_exp2f((m_run - mm) * C2), a2 = __builtin_amdgcn_exp2f((m2 - mm) * C2);
        const float inv = 1.0f / (l_run * a1 + l2 * a2);
        const float w1 = a1 * inv, w2 = a2 * inv;
        const int row = b * SQ + 32 * (qb & 1) + r32;
        char* ob = ws + ((size_t)((row >> 4) * 128 + head * 4 + 2 * kh) * 64 + hi * 16 + (row & 15)) * 16;
#define OSTORE(KEEP, i) do { u32x2 w_[4]; \
            _Pragma("unroll") for (int r4 = 0; r4 < 4; ++r4) { \
                const f32x4 v = *(const LAS f32x4*)(uintptr_t)(pb + (4 * (i) + r4) * 1024 + lane * 16); \
                const float e0 = KEEP[4 * r4] * w1 + v[0] * w2, e1 = KEEP[4 * r4 + 1] * w1 + v[1] * w2; \
                const float e2 = KEEP[4 * r4 + 2] * w1 + v[2] * w2, e3 = KEEP[4 * r4 + 3] * w1 + v[3] * w2; \
                w_[r4] = u32x2{cvtpk(e0, e1), cvtpk(e2, e3)}; } \
            _Pragma("unroll") for (int g = 0; g < 2; ++g) { \
                u32x4 o4 = {w_[g].x, w_[g].y, w_[g + 2].x, w_[g + 2].y}; \
                asm volatile("global_store_dwordx4 %0, %1, off sc1\n\ts_nop 1" :: "v"(ob + (i) * 1024 + g * 512), "v"(o4) : "memory"); } } while (0)
        OSTORE(keep0, 0); OSTORE(keep1, 1);
#undef OSTORE
    }
    const char* Ag = ws + (size_t)((8 * mt + 2 * qb) * 128 + 32 * sk + kh) * 1024;
    unsigned ao0 = lane * 16 + (nt & 1) * 131072, ao1 = ao0 ^ 131072;
    unsigned bb[4];
#pragma unroll
    for (int c = 0; c < 4; ++c) { bb[c] = lds0 + ((c + 1) & 3) * REG + (32 * kh + 4 * gi + vq) * 160 + 8 * vp; asm volatile("" : "+v"(bb[c])); }
    asm volatile("" : "+v"(ao0), "+v"(ao1), "+s"(Ag));
    asm volatile("s_waitcnt vmcnt(0) lgkmcnt(0)\n\ts_barrier" ::: "memory");
    unsigned dep1 = 0;
    if (wid == 0) {
        const unsigned td = rendezvous<8>(w1, tag, nt, lane, TMO, dep1);
        if (lane == 0) *(LAS unsigned*)(uintptr_t)(lds0 + 4096) = td;
    } else {
#pragma unroll
        for (int i = 0; i < 10; ++i) BDMA(Bs, 2 + i / 5, i % 5);
        if (wid == 1) {
#pragma unroll
            for (int i = 0; i < 9; ++i)
                QDMA(boff[i % 5], Bs - 32 * DM * 2 + (2 + i / 5) * (256 * DM * 2), lds0 + ((3 + i / 5) & 3) * REG + (i % 5) * 1024);
        }
    }
    asm volatile("s_waitcnt lgkmcnt(0)\n\ts_barrier" ::: "memory");
    unsigned todo = __builtin_amdgcn_readfirstlane(*(const LAS unsigned*)(uintptr_t)(lds0 + 4096));
    float* part = (float*)(ws + PART_OFF);
    bool left = false;
    while (todo) {
        const int ntc = ((todo >> nt) & 1u) ? nt : __builtin_ctz(todo);
        todo &= ~(1u << ntc);
        const char* Bc = Bw + 288 * (ntc >> 1) + 128 * (ntc & 1);
        if (ntc != nt) {
            asm volatile("s_waitcnt vmcnt(0) lgkmcnt(0)\n\ts_barrier" ::: "memory");
            BOFF(ntc & 1);
            ao0 = lane * 16 + (ntc & 1) * 131072; ao1 = ao0 ^ 131072;
#pragma unroll 1
            for (int c = 0; c < 4; ++c) {
#pragma unroll
                for (int i = 0; i < 5; ++i) BDMA(Bc, c, i);
            }
            asm volatile("s_waitcnt vmcnt(0)\n\ts_barrier" ::: "memory");
        }
        u32x4 an[32];
#define ALD(s_) do { const char* ag = Ag; asm volatile("" : "+s"(ag)); ag += ((s_) >> 1) * 4096; \
            if ((s_) & 1) { \
                asm volatile("global_load_dwordx4 %0, %1, %2 offset:2048 sc1" : "=v"(an[2 * (s_)]) : "v"(ao0), "s"(ag) : "memory"); \
                asm volatile("global_load_dwordx4 %0, %1, %2 offset:2048 sc1" : "=v"(an[2 * (s_) + 1]) : "v"(ao1), "s"(ag) : "memory"); \
            } else { \
                asm volatile("global_load_dwordx4 %0, %1, %2 sc1" : "=v"(an[2 * (s_)]) : "v"(ao0), "s"(ag) : "memory"); \
                asm volatile("global_load_dwordx4 %0, %1, %2 sc1" : "=v"(an[2 * (s_) + 1]) : "v"(ao1), "s"(ag) : "memory"); } } while (0)
#pragma unroll
        for (int s2 = 0; s2 < AHEAD; ++s2) ALD(s2);
        f32x4 acc[2][5];
#pragma unroll
        for (int ct = 0; ct < 5; ++ct) { acc[0][ct] = f32x4{0.f, 0.f, 0.f, 0.f}; acc[1][ct] = f32x4{0.f, 0.f, 0.f, 0.f}; }
        s16x4 blo[2][5], bhi[2][5];
#pragma unroll
        for (int ct = 0; ct < 5; ++ct) { blo[0][ct] = vtr(bb[0] + 32 * ct); bhi[0][ct] = vtr(bb[0] + 32 * ct + 2560); }
#pragma unroll
        for (int s = 0; s < 16; ++s) {
            asm volatile("s_waitcnt vmcnt(%2)" : "+v"(an[2 * s]), "+v"(an[2 * s + 1]) : "n"(s + AHEAD <= 16 ? 2 * AHEAD - 2 : 30 - 2 * s) : "memory");
            if (s == 7) asm volatile("s_barrier" ::: "memory");
            if (s == 7 && wid == 0) BDMA(Bc, 3, 4);
            if (s == 11) asm volatile("s_barrier" ::: "memory");
            __builtin_amdgcn_sched_barrier(0);
#pragma unroll
            for (int ct = 0; ct < 5; ++ct) {
                if (s < 15) {
                    const unsigned ad = bb[((s + 1) >> 2) & 3] + ((s + 1) & 3) * 10240 + 32 * ct;
                    blo[(s + 1) & 1][ct] = vtr(ad); bhi[(s + 1) & 1][ct] = vtr(ad + 2560);
                }
                const s16x4 lo = blo[s & 1][ct], h4 = bhi[s & 1][ct];
                const bf16x8 bf = {lo[0], lo[1], lo[2], lo[3], h4[0], h4[1], h4[2], h4[3]};
                acc[0][ct] = MFMA16(bf, __builtin_bit_cast(bf16x8, an[2 * s]), acc[0][ct]);
                if (ct < 4) acc[1][ct] = MFMA16(bf, __builtin_bit_cast(bf16x8, an[2 * s + 1]), acc[1][ct]);
                if (ct == 3 && s + AHEAD < 16) ALD(s + AHEAD);
                __builtin_amdgcn_sched_barrier(0);
            }
        }
        float* pp = part + ((((size_t)(sk * 8 + mt) * 8 + ntc) * 4 + qb) * 9 + 4 * kh) * 256 + lane * 4;
#define PRED(K, NK, NS) do { \
            _Pragma("unroll") for (int ct = 0; ct < NS; ++ct) \
                *(LAS f32x4*)(uintptr_t)(lds0 + REG + (wid * 5 + ct) * 1024 + lane * 16) = acc[1 - K][ct]; \
            asm volatile("s_waitcnt lgkmcnt(0)\n\ts_barrier" ::: "memory"); \
            f32x4 v_[NK]; \
            _Pragma("unroll") for (int ct = 0; ct < NK; ++ct) v_[ct] = *(const LAS f32x4*)(uintptr_t)(lds0 + REG + ((wid ^ 4) * 5 + ct) * 1024 + lane * 16); \
            _Pragma("unroll") for (int ct = 0; ct < NK; ++ct) { \
                const f32x4 r_ = acc[K][ct] + v_[ct]; \
                asm volatile("global_store_dwordx4 %0, %1, off sc1\n\ts_nop 1" :: "v"(pp + (ct < 4 ? ct : 8) * 256), "v"(r_) : "memory"); } } while (0)
        if (kh == 0) PRED(0, 5, 4); else PRED(1, 4, 5);
#undef PRED
        if (wid == 0 && !left) { left = true; depart<8>(w1, dep1, lane); }
        asm volatile("s_waitcnt vmcnt(0)\n\ts_barrier" ::: "memory");
        unsigned dep2 = 0;
        if (wid == 0) {
            const unsigned td = rendezvous<4>(w2 + ntc * 128, tag, sk, lane, TMO, dep2);
            if (lane == 0) *(LAS unsigned*)(uintptr_t)(lds0 + 2 * REG) = td;
        }
        asm volatile("s_waitcnt lgkmcnt(0)\n\ts_barrier" ::: "memory");
        unsigned qs = __builtin_amdgcn_readfirstlane(*(const LAS unsigned*)(uintptr_t)(lds0 + 2 * REG));
        bool left2 = false;
        while (qs) {
            const int q = __builtin_ctz(qs);
            qs &= qs - 1;
            if (tid < 288) {
                const float* p = part + (((size_t)mt * 8 + ntc) * 4 + q) * 2304 + tid * 4;
                f32x4 v[2][4];
#pragma unroll
                for (int r = 0; r < 2; ++r)
#pragma unroll
                    for (int s = 0; s < 4; ++s)
                        asm volatile("global_load_dwordx4 %0, %1, off sc1" : "=v"(v[r][s]) : "v"(p + (size_t)s * 64 * 9216 + r * 1152) : "memory");
                asm volatile("s_waitcnt vmcnt(0)" : "+v"(v[0][0]), "+v"(v[0][1]), "+v"(v[0][2]), "+v"(v[0][3]),
                                                    "+v"(v[1][0]), "+v"(v[1][1]), "+v"(v[1][2]), "+v"(v[1][3]) :: "memory");
#pragma unroll
                for (int r = 0; r < 2; ++r) {
                    const f32x4 t = ((v[r][0] + v[r][1]) + v[r][2]) + v[r][3];
                    u32x2 w = {cvtpk(t[0], t[1]), cvtpk(t[2], t[3])};
                    const int pc = tid + 288 * r, tl_ = pc >> 6;
                    const int rt = (ntc ^ (tl_ >> 2)) & 1;
                    const int col = 144 * (ntc >> 1) + (tl_ == 8 ? 64 : 80 * (ntc & 1) + 16 * (tl_ & 3)) + ((pc >> 2) & 12);
                    *(u32x2*)(out + (size_t)(16 * (8 * mt + 2 * q + rt) + (pc & 15)) * DM + col) = w;
                }
            }
            if (wid == 0 && !left2) { left2 = true; depart<4>(w2 + ntc * 128, dep2, lane); }
        }
        if (wid == 0 && !left2) {
            asm volatile("s_waitcnt vmcnt(0)" ::: "memory");
            depart<4>(w2 + ntc * 128, dep2, lane);
        }
    }
    if (wid == 0 && !left) {
        asm volatile("s_waitcnt vmcnt(0)" ::: "memory");
        depart<8>(w1, dep1, lane);
    }
}

extern "C" void kernel_launch(void* const* d_in, const int* in_sizes, int n_in, void* d_out, int out_size,
                              void* d_ws, size_t ws_size, hipStream_t stream) {
    (void)hipFuncSetAttribute((const void*)gqa_kernel, hipFuncAttributeMaxDynamicSharedMemorySize, LDS_BYTES);
    gqa_kernel<<<NB * HKV, 512, LDS_BYTES, stream>>>((const u16*)d_in[0], (const u16*)d_in[1], (const u16*)d_in[2],
                                                     (const u16*)d_in[3], (const u16*)d_in[4], (char*)d_ws, (u16*)d_out);
}
```

```cpp
#include <hip/hip_runtime.h>
#include <hip/hip_bf16.h>
#include <stdint.h>

typedef short bf16x8 __attribute__((ext_vector_type(8)));
typedef short s16x4 __attribute__((ext_vector_type(4)));
typedef float f32x16 __attribute__((ext_vector_type(16)));
typedef float f32x4 __attribute__((ext_vector_type(4)));
typedef unsigned u32x4 __attribute__((ext_vector_type(4)));
typedef unsigned u32x2 __attribute__((ext_vector_type(2)));
typedef unsigned short u16;
#define LAS __attribute__((address_space(3)))

constexpr int NB = 16, SQ = 64, DM = 576, HQ = 32, HKV = 16, SKV = 1024, DH = 128;
constexpr int NQ = HQ * DH;
constexpr int NT = SKV / 64;
constexpr int REG = 40960;
constexpr int LDS_BYTES = 4 * REG;
constexpr size_t PART_OFF = (size_t)NB * SQ * NQ * 2;
constexpr size_t CNT_OFF = (size_t)20 << 20;
constexpr int AHEAD = 2;
constexpr unsigned long long TMO = 5000ull;
constexpr int XS0 = 5, XS1 = 7, YS0 = 5, YS1 = 7;
constexpr int PACE = 108;
constexpr float RTHR = 8.0f;
constexpr float C2 = 0.088388347648318447f * 1.4426950408889634f;

#define MFMA32(a, b, c) __builtin_amdgcn_mfma_f32_32x32x16_bf16(a, b, c, 0, 0, 0)

typedef __bf16 bf16x2_t __attribute__((ext_vector_type(2)));
typedef float f32x2 __attribute__((ext_vector_type(2)));
__device__ __forceinline__ unsigned cvtpk(float lo, float hi) {
    f32x2 v = {lo, hi};
    return __builtin_bit_cast(unsigned, __builtin_convertvector(v, bf16x2_t));
}
__device__ __forceinline__ s16x4 vtr(unsigned a) {
    return __builtin_amdgcn_ds_read_tr16_b64_v4i16((LAS s16x4*)(uintptr_t)a);
}
__device__ __forceinline__ bf16x8 ldsr128(unsigned a) { return *(const LAS bf16x8*)(uintptr_t)a; }
__device__ __forceinline__ float swap_max(float v) {
    auto rr = __builtin_amdgcn_permlane32_swap(__float_as_uint(v), __float_as_uint(v), false, false);
    return __builtin_fmaxf(__uint_as_float(rr[0]), __uint_as_float(rr[1]));
}
__device__ __forceinline__ float max3f(float a, float b, float c) { float r; asm("v_max3_f32 %0, %1, %2, %3" : "=v"(r) : "v"(a), "v"(b), "v"(c)); return r; }
__device__ __forceinline__ float fmul1(float a, float b) { float r; asm("v_mul_f32_e32 %0, %1, %2" : "=v"(r) : "s"(b), "v"(a)); return r; }
__device__ __forceinline__ float swap_max3(float v) {
    auto rr = __builtin_amdgcn_permlane32_swap(__float_as_uint(v), __float_as_uint(v), false, false);
    return max3f(__uint_as_float(rr[0]), __uint_as_float(rr[1]), __uint_as_float(rr[1]));
}
__device__ __forceinline__ float swap_add(float v) {
    auto rr = __builtin_amdgcn_permlane32_swap(__float_as_uint(v), __float_as_uint(v), false, false);
    return __uint_as_float(rr[0]) + __uint_as_float(rr[1]);
}

extern "C" __device__ unsigned long long amd_dispatch_id() __asm("llvm.amdgcn.dispatch.id");

template <unsigned N>
__device__ __forceinline__ unsigned rendezvous(const char* w, unsigned tag, unsigned own, int lane, unsigned long long tmo, unsigned& dep) {
    unsigned old = 0, todo = 1u << own;
    u32x2 v;
    if (lane == 0) asm volatile("global_atomic_add %0, %1, %2, off sc0" : "=v"(old) : "v"(w), "v"(1u) : "memory");
    asm volatile("global_load_dwordx2 %0, %1, off sc1" : "=v"(v) : "v"(w) : "memory");
    asm volatile("s_waitcnt vmcnt(0)" : "+v"(old), "+v"(v) :: "memory");
    old = __builtin_amdgcn_readfirstlane(old);
    if ((old & 255u) == N - 1) todo |= (old >> 8) & 255u;
    else {
        const unsigned long long t0 = __builtin_amdgcn_s_memrealtime();
        while ((__builtin_amdgcn_readfirstlane(v.x) & 255u) != N || __builtin_amdgcn_readfirstlane(v.y) != tag) {
            if (__builtin_amdgcn_s_memrealtime() - t0 > tmo) {
                unsigned o2 = 0;
                if (lane == 0) asm volatile("global_atomic_or %0, %1, %2, off sc0" : "=v"(o2) : "v"(w), "v"(256u << own) : "memory");
                asm volatile("s_waitcnt vmcnt(0)" : "+v"(o2) :: "memory");
                if ((__builtin_amdgcn_readfirstlane(o2) & 255u) != N) todo = 0;
                break;
            }
            asm volatile("global_load_dwordx2 %0, %1, off sc1\n\ts_waitcnt vmcnt(0)" : "=v"(v) : "v"(w) : "memory");
        }
    }
    dep = 0;
    if (lane == 0) asm volatile("global_atomic_add %0, %1, %2, off sc0" : "=v"(dep) : "v"(w), "v"(65536u) : "memory");
    return todo;
}
template <unsigned N>
__device__ __forceinline__ void depart(const char* w, unsigned dep, int lane) {
    asm volatile("" : "+v"(dep));
    if (lane == 0 && ((dep >> 16) & 255u) == N - 1)
        asm volatile("global_store_dwordx2 %0, %1, off sc1" :: "v"(w), "v"(u32x2{0u, 0u}) : "memory");
}

#define MFMA16(a, b, c) __builtin_amdgcn_mfma_f32_16x16x32_bf16(a, b, c, 0, 0, 0)
__global__ __launch_bounds__(512, 2) void gqa_kernel(const u16* __restrict__ x, const u16* __restrict__ Kc,
                                                     const u16* __restrict__ Vc, const u16* __restrict__ wq,
                                                     const u16* __restrict__ wo, char* ws, u16* __restrict__ out) {
    extern __shared__ __attribute__((aligned(16))) char lds[];
    const unsigned lds0 = (unsigned)(uintptr_t)lds;
    const int tid = threadIdx.x, lane = tid & 63, r32 = lane & 31, hi = lane >> 5;
    const int wid = __builtin_amdgcn_readfirstlane(tid >> 6);
    const int qb = wid & 3, kh = wid >> 2;
    const int bid = blockIdx.x, b = 8 * (bid & 1) + ((bid >> 3) & 7), kvh = 4 * ((bid >> 1) & 3) + (bid >> 6);
    const int head = 2 * kvh + (qb >> 1);
    const int mt = b >> 1, sk = kvh >> 2, nt = 4 * (b & 1) + (kvh & 3);
    const unsigned tag = 0x80000000u | ((unsigned)amd_dispatch_id() & 0xfffffu) |
                         ((((unsigned)((uintptr_t)__builtin_amdgcn_dispatch_ptr() >> 12) * 0x9e3779b1u) >> 21) << 20);
    const char* w1 = ws + CNT_OFF + (mt * 4 + sk) * 128;
    const char* w2 = ws + CNT_OFF + 4096 + mt * 8 * 128;
    const char* wcl = lane == 0 ? w1 : w2 + nt * 128;
    u32x2 clm = {0u, tag};
    if (wid == 1 && lane < 2)
        asm volatile("global_atomic_cmpswap_x2 %0, %1, %2, off sc0\n\ts_nop 1" : "=v"(clm) : "v"(wcl), "v"(u32x4{0u, tag, 0u, 0u}) : "memory");
    const int gi = lane >> 4, vq = (lane & 15) >> 2, vp = lane & 3;

    const unsigned vsrc = (16 * (wid >> 1) + ((lane >> 2) & 7)) * 256 + (wid & 1) * 128 + hi * 64;
    const unsigned voffV = (vsrc ^ (kh * 128)) + vp * 16;
    const unsigned voffK0 = vsrc + ((vp ^ (wid & 2)) << 4), voffK1 = vsrc + ((vp ^ ((wid & 2) | 1)) << 4);
    const char* Kg = (const char*)Kc + (size_t)(b * HKV + kvh) * SKV * DH * 2;
    const char* Vg = (const char*)Vc + (size_t)(b * HKV + kvh) * SKV * DH * 2;
    const unsigned dwo = lds0 + (wid >> 1) * 4096 + (wid & 1) * 1024;
#define KVDMA(voff, gp, dst, OFF) asm volatile("s_mov_b32 m0, %2\n\ts_nop 0\n\tglobal_load_lds_dwordx4 %0, %1 offset:" #OFF " sc1 nt" \
            :: "v"(voff), "s"(gp), "s"(dst) : "memory")
#define VSO(tn) ((int)((0x0200110au >> (8 * ((tn) & 3))) & 255) * 8192)
#define KSO(tn) ((int)((0x0f0c0705u >> (8 * ((tn) & 3))) & 255) * 8192)
#define DMA_V0(tn) KVDMA(voffV, Vg + (tn) * 16384, dwo + VSO(tn), 0)
#define DMA_V1(tn) KVDMA(voffV, Vg + (tn) * 16384, dwo + VSO(tn), 2048)
#define DMA_K0(tn) KVDMA(voffK0, Kg + (tn) * 16384, dwo + KSO(tn), 0)
#define DMA_K1(tn) KVDMA(voffK1, Kg + (tn) * 16384, dwo + KSO(tn), 2048)
#define DMA_V(tn) do { DMA_V0(tn); DMA_V1(tn); } while (0)
#define DMA_K(tn) do { DMA_K0(tn); DMA_K1(tn); } while (0)
    const char* Bw = (const char*)wo + (size_t)(1024 * sk + 32 * wid) * DM * 2;
    unsigned boff[5];
    bf16x8 qf[8];
    {
        int kb = (b + 2 * (kvh & 3)) % 9;
        const unsigned xo = (8 * wid + (lane >> 3)) * DM * 2 + (((lane & 7) ^ ((4 * wid + (lane >> 4)) & 7)) << 4);
        const unsigned wo0 = (8 * wid + hi) * NQ * 2 + ((r32 ^ (hi << 2)) << 4);
        const unsigned wo1 = (8 * wid + 2 + hi) * NQ * 2 + ((r32 ^ ((2 + hi) << 2)) << 4);
        const unsigned wo2 = wo0 + 4 * NQ * 2, wo3 = wo1 + 4 * NQ * 2;
        const char* Xs = (const char*)x + (size_t)b * SQ * DM * 2;
        const char* Ws = (const char*)wq + kvh * 512;
#define QDMA(voff, gp, dst) asm volatile("s_mov_b32 m0, %2\n\ts_nop 0\n\tglobal_load_lds_dwordx4 %0, %1" :: "v"(voff), "s"(gp), "s"(dst) : "memory")
#define QS_PIECE(slot, p) do { const unsigned s_ = lds0 + (slot) * 40960 + wid * 4096; const char* w_ = Ws + (size_t)kb * 64 * NQ * 2; \
            if ((p) == 0) QDMA(wo0, w_, s_); \
            if ((p) == 1) QDMA(wo1, w_, s_ + 1024); \
            if ((p) == 2) QDMA(wo2, w_, s_ + 2048); \
            if ((p) == 3) QDMA(wo3, w_, s_ + 3072); \
            if ((p) == 4) { QDMA(xo, Xs + kb * 128, lds0 + (slot) * 40960 + 32768 + wid * 1024); kb = kb == 8 ? 0 : kb + 1; } } while (0)
#define QS_ISSUE(slot) do { QS_PIECE(slot, 0); QS_PIECE(slot, 1); QS_PIECE(slot, 2); QS_PIECE(slot, 3); QS_PIECE(slot, 4); } while (0)
#pragma unroll 1
        for (int j = 0; j < 2; ++j) { QS_ISSUE(j); asm volatile("s_barrier" ::: "memory"); }
#define BOFF(odd) _Pragma("unroll") for (int i = 0; i < 5; ++i) { \
            unsigned ln_ = lane; asm volatile("" : "+v"(ln_)); \
            const unsigned n = 64 * i + ln_, row = (n * 205u) >> 11, c = n - 10 * row + 2 * (odd); \
            boff[i] = row * (DM * 2) + (c < 10 ? c : c - 10) * 16; \
            asm volatile("" : "+v"(boff[i])); }
        BOFF(nt & 1);
        const int pp = ((vp & 1) << 1) | (vp >> 1);
        const unsigned abase = lds0 + (8 * hi + vq) * 512 + (((4 * wid + 2 * (gi & 1) + (pp >> 1)) ^ (vq << 2)) << 4) + 8 * (pp & 1);
        const unsigned xbase = lds0 + 32768 + r32 * 128 + ((hi ^ ((r32 >> 1) & 7)) << 4);
        f32x16 acc0 = {}, acc1 = {};
        s16x4 lo[2][4], h4[2][4]; bf16x8 x0[2][4], x1[2][4];
        asm volatile("s_waitcnt vmcnt(5)\n\ts_barrier" : "+v"(clm) :: "memory");
        QS_ISSUE(2);
        if (wid == 1 && lane < 2 && clm.y != tag && (clm.x | clm.y) != 0u)
            asm volatile("global_atomic_cmpswap_x2 %0, %1, off\n\ts_nop 1" :: "v"(wcl), "v"(u32x4{0u, tag, clm.x, clm.y}) : "memory");
#pragma unroll
        for (int i = 0; i < 4; ++i) {
            lo[0][i] = vtr(abase + i * 8192); h4[0][i] = vtr(abase + i * 8192 + 2048);
            x0[0][i] = ldsr128(xbase ^ (i << 5)); x1[0][i] = ldsr128((xbase ^ (i << 5)) + 4096);
        }
#pragma unroll
        for (int st = 0; st < 9; ++st) {
            if (st == 0) asm volatile("s_waitcnt vmcnt(5)" ::: "memory");
            else if (st < 6) asm volatile("s_waitcnt vmcnt(10)" ::: "memory");
            else if (st == 6) asm volatile("s_waitcnt vmcnt(9)" ::: "memory");
            else if (st == 7) asm volatile("s_waitcnt vmcnt(6)" ::: "memory");
            asm volatile("s_waitcnt lgkmcnt(0)\n\ts_barrier" ::: "memory");
            const unsigned so = ((st + 1) & 3) * 40960;
            const int cu = st & 1, nx = cu ^ 1;
            unsigned aa = abase + so;
            asm volatile("" : "+v"(aa));
            __builtin_amdgcn_sched_barrier(0);
            if (st == 0) QS_ISSUE(3);
#pragma unroll
            for (int i = 0; i < 4; ++i) {
                const bf16x8 a0 = {lo[cu][i][0], lo[cu][i][1], lo[cu][i][2], lo[cu][i][3], h4[cu][i][0], h4[cu][i][1], h4[cu][i][2], h4[cu][i][3]};
                acc0 = MFMA32(a0, x0[cu][i], acc0);
                if (st < 8) { lo[nx][i] = vtr(aa + i * 8192); h4[nx][i] = vtr(aa + i * 8192 + 2048); }
                if (st < 5) QS_PIECE(st & 3, i);
                __builtin_amdgcn_sched_barrier(0);
                acc1 = MFMA32(a0, x1[cu][i], acc1);
                if (st < 8) { unsigned xa = (xbase ^ (i << 5)) + so; asm volatile("" : "+v"(xa)); x0[nx][i] = ldsr128(xa); x1[nx][i] = ldsr128(xa + 4096); }
                if (st < 5 && i == 3) QS_PIECE(st & 3, 4);
                if (st == 5 && i == 3) { DMA_K(0); DMA_K(1); }
                if (st == 6 && i == 3) DMA_V(0);
                if (st == 7 && i == 3) DMA_K(2);
                if (st == 8 && i == 3) DMA_K(3);
                __builtin_amdgcn_sched_barrier(0);
            }
        }
#undef QS_ISSUE
#undef QS_PIECE
#define QW(ACC, qblk) do { _Pragma("unroll") for (int s = 0; s < 2; ++s) { \
            u32x4 w_ = {cvtpk(ACC[8 * s], ACC[8 * s + 1]), cvtpk(ACC[8 * s + 2], ACC[8 * s + 3]), \
                        cvtpk(ACC[8 * s + 4], ACC[8 * s + 5]), cvtpk(ACC[8 * s + 6], ACC[8 * s + 7])}; \
            *(LAS u32x4*)(uintptr_t)(lds0 + 32768 + (2 * (wid >> 2) + (qblk)) * REG + (2 * (wid & 3) + s) * 1024 + lane * 16) = w_; } } while (0)
        QW(acc0, 0); QW(acc1, 1);
#undef QW
        DMA_V(1);
    }
    asm volatile("s_waitcnt vmcnt(8) lgkmcnt(0)\n\ts_barrier" ::: "memory");
#pragma unroll
    for (int f = 0; f < 8; ++f) qf[f] = ldsr128(lds0 + 32768 + qb * REG + f * 1024 + lane * 16);

    const unsigned kb0 = lds0 + (4 * kh + (r32 >> 3)) * 2048 + (r32 & 7) * 64 + ((hi ^ (r32 >> 3)) << 4);
    const unsigned kb1 = kb0 ^ 32;
    const unsigned vb = lds0 + kh * 8192 + (4 * hi + vq) * 64 + (gi & 1) * 32 + vp * 8;

    float m_run, l_run = 0.f, tmax = 0.f;
    unsigned tprev = (unsigned)__builtin_amdgcn_s_memrealtime() - (unsigned)PACE;
    f32x16 o[4] = {};
    f32x16 sA, sB, negm;

    bf16x8 kf[8];
    {
        bf16x8 k0[8];
        unsigned ka0 = kb0 + KSO(0), ka1 = kb1 + KSO(0);
        asm volatile("" : "+v"(ka0), "+v"(ka1));
#pragma unroll
        for (int d0 = 0; d0 < 8; ++d0) k0[d0] = ldsr128(((d0 & 1) ? ka1 : ka0) + (d0 >> 1) * 512);
#pragma unroll
        for (int d0 = 0; d0 < 8; ++d0) kf[d0] = ldsr128(((d0 & 1) ? ka1 : ka0) + 16384 + (d0 >> 1) * 512);
        __builtin_amdgcn_sched_barrier(0);
        asm volatile("s_waitcnt lgkmcnt(0)\n\ts_barrier" ::: "memory");
        sA = f32x16{};
#pragma unroll
        for (int d0 = 0; d0 < 8; ++d0) sA = MFMA32(k0[d0], qf[d0], sA);
        __builtin_amdgcn_sched_barrier(0);
        DMA_V(2); DMA_K(4);
        m_run = sA[0];
#pragma unroll
        for (int r = 1; r < 16; ++r) m_run = __builtin_fmaxf(m_run, sA[r]);
        m_run = swap_max(m_run);
#pragma unroll
        for (int r = 0; r < 16; ++r) { sA[r] -= m_run; negm[r] = -m_run; }
    }
#define EXP1(r) do { sc[r] = __builtin_amdgcn_exp2f(fmul1(sc[r], C2)); } while (0)
#define SUM1(k) do { ps0 += sc[2 * (k)]; ps1 += sc[2 * (k) + 1]; pw[k] = cvtpk(sc[2 * (k)], sc[2 * (k) + 1]); asm volatile("" : "+v"(pw[k]), "+v"(ps0), "+v"(ps1)); } while (0)
#define STEP(t, SC_, SN_, TAIL) do { \
        f32x16& sc = SC_; f32x16& sn = SN_; \
        s16x4 vlo[8], vhi[8]; \
        if (!TAIL || (t) < 12) asm volatile("s_waitcnt vmcnt(8)" ::: "memory"); \
        else if ((t) < 14) asm volatile("s_waitcnt vmcnt(7)" ::: "memory"); \
        else asm volatile("s_waitcnt vmcnt(9)" ::: "memory"); \
        if (!(TAIL && (t) >= (NT - 2))) while ((unsigned)__builtin_amdgcn_s_memrealtime() - tprev < (unsigned)PACE) __builtin_amdgcn_s_sleep(1); \
        asm volatile("s_waitcnt lgkmcnt(0)\n\ts_barrier" ::: "memory"); \
        if (!(TAIL && (t) + 1 >= (NT - 2))) tprev = (unsigned)__builtin_amdgcn_s_memrealtime(); \
        if (__builtin_expect(__any(tmax > RTHR / C2), 0)) { \
            const float dl = __builtin_fmaxf(tmax, 0.f); \
            const float alpha = __builtin_amdgcn_exp2f(-dl * C2); \
            m_run += dl; l_run *= alpha; \
            _Pragma("unroll") for (int r = 0; r < 16; ++r) { sc[r] -= dl; negm[r] = -m_run; } \
            _Pragma("unroll") for (int dt = 0; dt < 4; ++dt) \
                _Pragma("unroll") for (int r = 0; r < 16; ++r) o[dt][r] *= alpha; \
        } \
        const unsigned vwr = dwo + vso, kwr = dwo + kso;                         \
        vso = VSO(t); kso = KSO((t) + 2); \
        const unsigned va = vb + vso; \
        const unsigned ka0 = kb0 + kso, ka1 = kb1 + kso;                         \
        unsigned pw[8]; \
        float ps0 = 0.f, ps1 = 0.f, t2; \
        __builtin_amdgcn_sched_barrier(0); \
          \
        _Pragma("unroll") for (int i = 0; i < 8; ++i) { \
            sn = MFMA32(kf[i], qf[i], i == 0 ? negm : sn); \
            const int ss = i >> 2, dt = i & 3; \
            vlo[2 * dt + ss] = vtr(va + ss * 4096 + dt * 512); \
            vhi[2 * dt + ss] = vtr(va + ss * 4096 + 2048 + dt * 512); \
            if (i < 4) { EXP1(2 * i); EXP1(2 * i + 1); } else EXP1(4 + i); \
            if (i >= 1 && i <= 4) SUM1(i - 1); \
            if (i == 6) SUM1(4); \
            asm volatile("" : "+v"(sn)); \
            asm volatile("" : "+v"(sc)); \
            if (i == XS0 && (!TAIL || (t) < 13)) KVDMA(voffV, Vg + ((t) + 3) * 16384, vwr, 0); \
            if (i == XS1 && (!TAIL || (t) < 13)) KVDMA(voffV, Vg + ((t) + 3) * 16384, vwr, 2048); \
            if (i == XS0 && TAIL && (t) == 13) BDMA(Bs, 0, 2); \
            if (i == XS1 && TAIL && (t) == 13) BDMA(Bs, 0, 3); \
            if (i == XS0 && TAIL && (t) == 14) BDMA(Bs, 1, 1); \
            if (i == XS1 && TAIL && (t) == 14) BDMA(Bs, 1, 2); \
            __builtin_amdgcn_sched_barrier(0); \
        } \
          \
        _Pragma("unroll") for (int i = 0; i < 8; ++i) { \
            const int ss = i >> 2, dt = i & 3; \
            const s16x4 lo = vlo[2 * dt + ss], hi4 = vhi[2 * dt + ss]; \
            const bf16x8 vf = {lo[0], lo[1], lo[2], lo[3], hi4[0], hi4[1], hi4[2], hi4[3]}; \
            const bf16x8 pf = __builtin_bit_cast(bf16x8, u32x4{pw[4 * ss], pw[4 * ss + 1], pw[4 * ss + 2], pw[4 * ss + 3]}); \
            o[dt] = MFMA32(vf, pf, o[dt]); \
            if (i < 4) { kf[2 * i] = ldsr128(ka0 + i * 512); kf[2 * i + 1] = ldsr128(ka1 + i * 512); } \
            if (i < 2) { EXP1(12 + 2 * i); EXP1(13 + 2 * i); asm volatile("" : "+v"(sc)); } \
            if (i < 3) SUM1(5 + i); \
            if (i == 3) { l_run += ps0 + ps1; asm volatile("" : "+v"(sn), "+v"(l_run)); \
                          tmax = max3f(sn[0], sn[1], sn[2]); t2 = max3f(sn[3], sn[4], sn[5]); asm volatile("" : "+v"(tmax), "+v"(t2)); } \
            if (i == 4) { tmax = max3f(tmax, sn[6], sn[7]); t2 = max3f(t2, sn[8], sn[9]); tmax = max3f(tmax, sn[10], sn[11]); asm volatile("" : "+v"(tmax), "+v"(t2)); } \
            if (i == 5) { t2 = max3f(t2, sn[12], sn[13]); tmax = max3f(tmax, sn[14], sn[15]); tmax = max3f(tmax, t2, t2); asm volatile("" : "+v"(tmax)); } \
            if (i == 6) { tmax = swap_max3(tmax); asm volatile("" : "+v"(tmax)); } \
            if (i == YS0 && (!TAIL || (t) < 11)) KVDMA(voffK0, Kg + ((t) + 5) * 16384, kwr, 0); \
            if (i == YS1 && (!TAIL || (t) < 11)) KVDMA(voffK1, Kg + ((t) + 5) * 16384, kwr, 2048); \
            if (i == YS0 && TAIL && (t) == 11) BWARM(); \
            if (i == YS0 && TAIL && (t) == 12) BDMA(Bs, 0, 0); \
            if (i == YS1 && TAIL && (t) == 12) BDMA(Bs, 0, 1); \
            if (i == YS0 && TAIL && (t) == 13) BDMA(Bs, 0, 4); \
            if (i == YS1 && TAIL && (t) == 13) BDMA(Bs, 1, 0); \
            if (i == YS0 && TAIL && (t) == 14) BDMA(Bs, 1, 3); \
            if (i == YS1 && TAIL && (t) == 14) BDMA(Bs, 1, 4); \
            __builtin_amdgcn_sched_barrier(0); \
        } } while (0)
    const char* Bs = Bw + 288 * (nt >> 1) + 128 * (nt & 1);
#define BDMA(bs, c, i) QDMA(boff[i], (bs) + (c) * (256 * DM * 2), lds0 + (((c) + 1) & 3) * REG + wid * 5120 + (i) * 1024)
#define BWARM() do { unsigned ln_ = lane; asm volatile("" : "+v"(ln_)); \
        if (ln_ < 18) asm volatile("s_mov_b32 m0, %2\n\ts_nop 0\n\tglobal_load_lds_dword %0, %1" :: "v"(ln_ * 128), \
                     "s"((const char*)wo + (size_t)(1024 * sk + 512) * DM * 2 + ((bid >> 3) * 8 + wid) * 2304), "s"(lds0 + 32768 + wid * 256) : "memory"); } while (0)
    unsigned vso = VSO(3), kso = KSO(1);
#pragma unroll 1
    for (int t = 0; t < 10; t += 2) { STEP(t, sA, sB, false); STEP(t + 1, sB, sA, false); }
    STEP(10, sA, sB, true); STEP(11, sB, sA, true); STEP(12, sA, sB, true); STEP(13, sB, sA, true);
    STEP(NT - 2, sA, sB, true);
    const unsigned xb = lds0 + (kh ? 0 : 3 * REG) + qb * 8704;
    {
        f32x16& sc = sB;
        asm volatile("s_waitcnt vmcnt(10) lgkmcnt(0)\n\ts_barrier" ::: "memory");
        if (__builtin_expect(__any(tmax > RTHR / C2), 0)) {
            const float dl = __builtin_fmaxf(tmax, 0.f);
            const float alpha = __builtin_amdgcn_exp2f(-dl * C2);
            m_run += dl; l_run *= alpha;
#pragma unroll
            for (int r = 0; r < 16; ++r) sc[r] -= dl;
#pragma unroll
            for (int dt = 0; dt < 4; ++dt)
#pragma unroll
                for (int r = 0; r < 16; ++r) o[dt][r] *= alpha;
        }
        const unsigned va = vb + VSO(NT - 1);
        s16x4 vlo[8], vhi[8];
        unsigned pw[8];
        float ps0 = 0.f, ps1 = 0.f;
#pragma unroll
        for (int i = 0; i < 8; ++i) {
            const int ss = (i >> 1) & 1, dt = (i & 1) | ((~i >> 1) & 2);
            vlo[i] = vtr(va + ss * 4096 + dt * 512);
            vhi[i] = vtr(va + ss * 4096 + 2048 + dt * 512);
            EXP1(2 * i); EXP1(2 * i + 1);
            if (i) SUM1(i - 1);
        }
        SUM1(7);
        l_run += ps0 + ps1;
        l_run = swap_add(l_run);
        asm volatile("s_waitcnt lgkmcnt(0)\n\ts_barrier" ::: "memory");
        *(LAS float*)(uintptr_t)(xb + 8192 + lane * 4) = m_run;
        *(LAS float*)(uintptr_t)(xb + 8192 + 256 + lane * 4) = l_run;
        __builtin_amdgcn_sched_barrier(0);
#pragma unroll
        for (int i = 0; i < 8; ++i) {
            const int ss = (i >> 1) & 1, dt = (i & 1) | ((~i >> 1) & 2);
            const bf16x8 vf = {vlo[i][0], vlo[i][1], vlo[i][2], vlo[i][3], vhi[i][0], vhi[i][1], vhi[i][2], vhi[i][3]};
            const bf16x8 pf = __builtin_bit_cast(bf16x8, u32x4{pw[4 * ss], pw[4 * ss + 1], pw[4 * ss + 2], pw[4 * ss + 3]});
            o[dt] = MFMA32(vf, pf, o[dt]);
            if (i >= 4) {
#pragma unroll
                for (int j = 0; j < 2; ++j) {
                    const int r4 = 2 * (i & 1) + j, sd = 2 + ((i >> 1) & 1);
                    *(LAS f32x4*)(uintptr_t)(xb + (4 * (sd - 2) + r4) * 1024 + lane * 16) =
                        f32x4{o[sd][4 * r4], o[sd][4 * r4 + 1], o[sd][4 * r4 + 2], o[sd][4 * r4 + 3]};
                }
            }
            __builtin_amdgcn_sched_barrier(0);
        }
    }
#undef STEP
#undef EXP1
#undef SUM1
    {
        f32x16 &keep0 = o[0], &keep1 = o[1];
        asm volatile("s_waitcnt lgkmcnt(0)\n\ts_barrier" ::: "memory");
        const unsigned pb = lds0 + (kh ? 3 * REG : 0) + qb * 8704;
        const float m2 = *(const LAS float*)(uintptr_t)(pb + 8192 + lane * 4);
        const float l2 = *(const LAS float*)(uintptr_t)(pb + 8192 + 256 + lane * 4);
        const float mm = __builtin_fmaxf(m_run, m2);
        const float a1 = __builtin_amdgcn_exp2f((m_run - mm) * C2), a2 = __builtin_amdgcn_exp2f((m2 - mm) * C2);
        const float inv = 1.0f / (l_run * a1 + l2 * a2);
        const float w1 = a1 * inv, w2 = a2 * inv;
        const int row = b * SQ + 32 * (qb & 1) + r32;
        char* ob = ws + ((size_t)((row >> 4) * 128 + head * 4 + 2 * kh) * 64 + hi * 16 + (row & 15)) * 16;
#define OSTORE(KEEP, i) do { u32x2 w_[4]; \
            _Pragma("unroll") for (int r4 = 0; r4 < 4; ++r4) { \
                const f32x4 v = *(const LAS f32x4*)(uintptr_t)(pb + (4 * (i) + r4) * 1024 + lane * 16); \
                const float e0 = KEEP[4 * r4] * w1 + v[0] * w2, e1 = KEEP[4 * r4 + 1] * w1 + v[1] * w2; \
                const float e2 = KEEP[4 * r4 + 2] * w1 + v[2] * w2, e3 = KEEP[4 * r4 + 3] * w1 + v[3] * w2; \
                w_[r4] = u32x2{cvtpk(e0, e1), cvtpk(e2, e3)}; } \
            _Pragma("unroll") for (int g = 0; g < 2; ++g) { \
                u32x4 o4 = {w_[g].x, w_[g].y, w_[g + 2].x, w_[g + 2].y}; \
                asm volatile("global_store_dwordx4 %0, %1, off sc1\n\ts_nop 1" :: "v"(ob + (i) * 1024 + g * 512), "v"(o4) : "memory"); } } while (0)
        OSTORE(keep0, 0); OSTORE(keep1, 1);
#undef OSTORE
    }
    const char* Ag = ws + (size_t)((8 * mt + 2 * qb) * 128 + 32 * sk + kh) * 1024;
    unsigned ao0 = lane * 16 + (nt & 1) * 131072, ao1 = ao0 ^ 131072;
    unsigned bb[4];
#pragma unroll
    for (int c = 0; c < 4; ++c) { bb[c] = lds0 + ((c + 1) & 3) * REG + (32 * kh + 4 * gi + vq) * 160 + 8 * vp; asm volatile("" : "+v"(bb[c])); }
    asm volatile("" : "+v"(ao0), "+v"(ao1), "+s"(Ag));
    asm volatile("s_waitcnt vmcnt(0) lgkmcnt(0)\n\ts_barrier" ::: "memory");
    unsigned dep1 = 0;
    if (wid == 0) {
        const unsigned td = rendezvous<8>(w1, tag, nt, lane, TMO, dep1);
        if (lane == 0) *(LAS unsigned*)(uintptr_t)(lds0 + 4096) = td;
    } else {
#pragma unroll
        for (int i = 0; i < 10; ++i) BDMA(Bs, 2 + i / 5, i % 5);
        if (wid == 1) {
#pragma unroll
            for (int i = 0; i < 9; ++i)
                QDMA(boff[i % 5], Bs - 32 * DM * 2 + (2 + i / 5) * (256 * DM * 2), lds0 + ((3 + i / 5) & 3) * REG + (i % 5) * 1024);
        }
    }
    asm volatile("s_waitcnt lgkmcnt(0)\n\ts_barrier" ::: "memory");
    unsigned todo = __builtin_amdgcn_readfirstlane(*(const LAS unsigned*)(uintptr_t)(lds0 + 4096));
    float* part = (float*)(ws + PART_OFF);
    bool left = false;
    while (todo) {
        const int ntc = ((todo >> nt) & 1u) ? nt : __builtin_ctz(todo);
        todo &= ~(1u << ntc);
        const char* Bc = Bw + 288 * (ntc >> 1) + 128 * (ntc & 1);
        if (ntc != nt) {
            asm volatile("s_waitcnt vmcnt(0) lgkmcnt(0)\n\ts_barrier" ::: "memory");
            BOFF(ntc & 1);
            ao0 = lane * 16 + (ntc & 1) * 131072; ao1 = ao0 ^ 131072;
#pragma unroll 1
            for (int c = 0; c < 4; ++c) {
#pragma unroll
                for (int i = 0; i < 5; ++i) BDMA(Bc, c, i);
            }
            asm volatile("s_waitcnt vmcnt(0)\n\ts_barrier" ::: "memory");
        }
        u32x4 an[32];
#define ALD(s_) do { const char* ag = Ag; asm volatile("" : "+s"(ag)); ag += ((s_) >> 1) * 4096; \
            if ((s_) & 1) { \
                asm volatile("global_load_dwordx4 %0, %1, %2 offset:2048 sc1" : "=v"(an[2 * (s_)]) : "v"(ao0), "s"(ag) : "memory"); \
                asm volatile("global_load_dwordx4 %0, %1, %2 offset:2048 sc1" : "=v"(an[2 * (s_) + 1]) : "v"(ao1), "s"(ag) : "memory"); \
            } else { \
                asm volatile("global_load_dwordx4 %0, %1, %2 sc1" : "=v"(an[2 * (s_)]) : "v"(ao0), "s"(ag) : "memory"); \
                asm volatile("global_load_dwordx4 %0, %1, %2 sc1" : "=v"(an[2 * (s_) + 1]) : "v"(ao1), "s"(ag) : "memory"); } } while (0)
#pragma unroll
        for (int s2 = 0; s2 < AHEAD; ++s2) ALD(s2);
        f32x4 acc[2][5];
#pragma unroll
        for (int ct = 0; ct < 5; ++ct) { acc[0][ct] = f32x4{0.f, 0.f, 0.f, 0.f}; acc[1][ct] = f32x4{0.f, 0.f, 0.f, 0.f}; }
        s16x4 blo[2][5], bhi[2][5];
#pragma unroll
        for (int ct = 0; ct < 5; ++ct) { blo[0][ct] = vtr(bb[0] + 32 * ct); bhi[0][ct] = vtr(bb[0] + 32 * ct + 2560); }
#pragma unroll
        for (int s = 0; s < 16; ++s) {
            asm volatile("s_waitcnt vmcnt(%2)" : "+v"(an[2 * s]), "+v"(an[2 * s + 1]) : "n"(s + AHEAD <= 16 ? 2 * AHEAD - 2 : 30 - 2 * s) : "memory");
            if (s == 7) asm volatile("s_barrier" ::: "memory");
            if (s == 7 && wid == 0) BDMA(Bc, 3, 4);
            if (s == 11) asm volatile("s_barrier" ::: "memory");
            __builtin_amdgcn_sched_barrier(0);
#pragma unroll
            for (int ct = 0; ct < 5; ++ct) {
                if (s < 15) {
                    const unsigned ad = bb[((s + 1) >> 2) & 3] + ((s + 1) & 3) * 10240 + 32 * ct;
                    blo[(s + 1) & 1][ct] = vtr(ad); bhi[(s + 1) & 1][ct] = vtr(ad + 2560);
                }
                const s16x4 lo = blo[s & 1][ct], h4 = bhi[s & 1][ct];
                const bf16x8 bf = {lo[0], lo[1], lo[2], lo[3], h4[0], h4[1], h4[2], h4[3]};
                acc[0][ct] = MFMA16(bf, __builtin_bit_cast(bf16x8, an[2 * s]), acc[0][ct]);
                if (ct < 4) acc[1][ct] = MFMA16(bf, __builtin_bit_cast(bf16x8, an[2 * s + 1]), acc[1][ct]);
                if (ct == 3 && s + AHEAD < 16) ALD(s + AHEAD);
                __builtin_amdgcn_sched_barrier(0);
            }
        }
        float* pp = part + ((((size_t)(sk * 8 + mt) * 8 + ntc) * 4 + qb) * 9 + 4 * kh) * 256 + lane * 4;
#define PRED(K, NK, NS) do { \
            _Pragma("unroll") for (int ct = 0; ct < NS; ++ct) \
                *(LAS f32x4*)(uintptr_t)(lds0 + REG + (wid * 5 + ct) * 1024 + lane * 16) = acc[1 - K][ct]; \
            asm volatile("s_waitcnt lgkmcnt(0)\n\ts_barrier" ::: "memory"); \
            f32x4 v_[NK]; \
            _Pragma("unroll") for (int ct = 0; ct < NK; ++ct) v_[ct] = *(const LAS f32x4*)(uintptr_t)(lds0 + REG + ((wid ^ 4) * 5 + ct) * 1024 + lane * 16); \
            _Pragma("unroll") for (int ct = 0; ct < NK; ++ct) { \
                const f32x4 r_ = acc[K][ct] + v_[ct]; \
                asm volatile("global_store_dwordx4 %0, %1, off sc1\n\ts_nop 1" :: "v"(pp + (ct < 4 ? ct : 8) * 256), "v"(r_) : "memory"); } } while (0)
        if (kh == 0) PRED(0, 5, 4); else PRED(1, 4, 5);
#undef PRED
        if (wid == 0 && !left) { left = true; depart<8>(w1, dep1, lane); }
        asm volatile("s_waitcnt vmcnt(0)\n\ts_barrier" ::: "memory");
        unsigned dep2 = 0;
        if (wid == 0) {
            const unsigned td = rendezvous<4>(w2 + ntc * 128, tag, sk, lane, TMO, dep2);
            if (lane == 0) *(LAS unsigned*)(uintptr_t)(lds0 + 2 * REG) = td;
        }
        asm volatile("s_waitcnt lgkmcnt(0)\n\ts_barrier" ::: "memory");
        unsigned qs = __builtin_amdgcn_readfirstlane(*(const LAS unsigned*)(uintptr_t)(lds0 + 2 * REG));
        bool left2 = false;
        while (qs) {
            const int q = __builtin_ctz(qs);
            qs &= qs - 1;
            if (tid < 288) {
                const float* p = part + (((size_t)mt * 8 + ntc) * 4 + q) * 2304 + tid * 4;
                f32x4 v[2][4];
#pragma unroll
                for (int r = 0; r < 2; ++r)
#pragma unroll
                    for (int s = 0; s < 4; ++s)
                        asm volatile("global_load_dwordx4 %0, %1, off sc1" : "=v"(v[r][s]) : "v"(p + (size_t)s * 64 * 9216 + r * 1152) : "memory");
                asm volatile("s_waitcnt vmcnt(0)" : "+v"(v[0][0]), "+v"(v[0][1]), "+v"(v[0][2]), "+v"(v[0][3]),
                                                    "+v"(v[1][0]), "+v"(v[1][1]), "+v"(v[1][2]), "+v"(v[1][3]) :: "memory");
#pragma unroll
                for (int r = 0; r < 2; ++r) {
                    const f32x4 t = ((v[r][0] + v[r][1]) + v[r][2]) + v[r][3];
                    u32x2 w = {cvtpk(t[0], t[1]), cvtpk(t[2], t[3])};
                    const int pc = tid + 288 * r, tl_ = pc >> 6;
                    const int rt = (ntc ^ (tl_ >> 2)) & 1;
                    const int col = 144 * (ntc >> 1) + (tl_ == 8 ? 64 : 80 * (ntc & 1) + 16 * (tl_ & 3)) + ((pc >> 2) & 12);
                    *(u32x2*)(out + (size_t)(16 * (8 * mt + 2 * q + rt) + (pc & 15)) * DM + col) = w;
                }
            }
            if (wid == 0 && !left2) { left2 = true; depart<4>(w2 + ntc * 128, dep2, lane); }
        }
        if (wid == 0 && !left2) {
            asm volatile("s_waitcnt vmcnt(0)" ::: "memory");
            depart<4>(w2 + ntc * 128, dep2, lane);
        }
    }
    if (wid == 0 && !left) {
        asm volatile("s_waitcnt vmcnt(0)" ::: "memory");
        depart<8>(w1, dep1, lane);
    }
}

extern "C" void kernel_launch(void* const* d_in, const int* in_sizes, int n_in, void* d_out, int out_size,
                              void* d_ws, size_t ws_size, hipStream_t stream) {
    (void)hipFuncSetAttribute((const void*)gqa_kernel, hipFuncAttributeMaxDynamicSharedMemorySize, LDS_BYTES);
    gqa_kernel<<<NB * HKV, 512, LDS_BYTES, stream>>>((const u16*)d_in[0], (const u16*)d_in[1], (const u16*)d_in[2],
                                                     (const u16*)d_in[3], (const u16*)d_in[4], (char*)d_ws, (u16*)d_out);
}
```

```cpp
#define PROBE_A 0
#define PROBE_B 0
#define PROBE_OFF 0
#include <hip/hip_runtime.h>
#include <hip/hip_bf16.h>
#include <stdint.h>

typedef short bf16x8 __attribute__((ext_vector_type(8)));
typedef short s16x4 __attribute__((ext_vector_type(4)));
typedef float f32x16 __attribute__((ext_vector_type(16)));
typedef float f32x4 __attribute__((ext_vector_type(4)));
typedef unsigned u32x4 __attribute__((ext_vector_type(4)));
typedef unsigned u32x2 __attribute__((ext_vector_type(2)));
typedef unsigned short u16;
#define LAS __attribute__((address_space(3)))
typedef __attribute__((address_space(1))) unsigned gu32;
#define RLX_AGENT __ATOMIC_RELAXED, __HIP_MEMORY_SCOPE_AGENT

constexpr int NB = 16, SQ = 64, DM = 576, HQ = 32, HKV = 16, SKV = 1024, DH = 128;
constexpr int NQ = HQ * DH;
constexpr int NT = SKV / 64;
constexpr int REG = 40960;
constexpr int LDS_BYTES = 4 * REG;
constexpr size_t PART_OFF = (size_t)NB * SQ * NQ * 2;
constexpr size_t CNT_OFF = (size_t)20 << 20;
#define PT0 14
#define ALDPOS 3
#define AHEAD 2
#define TMO 5000ull
#define TMO2 5000ull
constexpr int XS0 = 5, XS1 = 7, YS0 = 5, YS1 = 7;
constexpr int PACE = 108;
constexpr float RTHR = 8.0f;
constexpr float C2 = 0.088388347648318447f * 1.4426950408889634f;

#define MFMA32(a, b, c) __builtin_amdgcn_mfma_f32_32x32x16_bf16(a, b, c, 0, 0, 0)

typedef __bf16 bf16x2_t __attribute__((ext_vector_type(2)));
typedef float f32x2 __attribute__((ext_vector_type(2)));
__device__ __forceinline__ unsigned cvtpk(float lo, float hi) {
    f32x2 v = {lo, hi};
    return __builtin_bit_cast(unsigned, __builtin_convertvector(v, bf16x2_t));
}
__device__ __forceinline__ void glds16(const void* g, unsigned lds_base) {
    unsigned sv;
    asm volatile("s_mov_b32 %0, m0\n\ts_mov_b32 m0, %2\n\ts_nop 0\n\tglobal_load_lds_dwordx4 %1, off\n\ts_mov_b32 m0, %0"
                 : "=&s"(sv) : "v"(g), "s"(lds_base) : "memory");
}
__device__ __forceinline__ s16x4 vtr(unsigned a) {
    return __builtin_amdgcn_ds_read_tr16_b64_v4i16((LAS s16x4*)(uintptr_t)a);
}
__device__ __forceinline__ bf16x8 ldsr128(unsigned a) { return *(const LAS bf16x8*)(uintptr_t)a; }
__device__ __forceinline__ float swap_max(float v) {
    auto rr = __builtin_amdgcn_permlane32_swap(__float_as_uint(v), __float_as_uint(v), false, false);
    return __builtin_fmaxf(__uint_as_float(rr[0]), __uint_as_float(rr[1]));
}
__device__ __forceinline__ float max3f(float a, float b, float c) { float r; asm("v_max3_f32 %0, %1, %2, %3" : "=v"(r) : "v"(a), "v"(b), "v"(c)); return r; }
__device__ __forceinline__ float fmul1(float a, float b) { float r; asm("v_mul_f32_e32 %0, %1, %2" : "=v"(r) : "s"(b), "v"(a)); return r; }
__device__ __forceinline__ float swap_max3(float v) {
    auto rr = __builtin_amdgcn_permlane32_swap(__float_as_uint(v), __float_as_uint(v), false, false);
    return max3f(__uint_as_float(rr[0]), __uint_as_float(rr[1]), __uint_as_float(rr[1]));
}
__device__ __forceinline__ float swap_add(float v) {
    auto rr = __builtin_amdgcn_permlane32_swap(__float_as_uint(v), __float_as_uint(v), false, false);
    return __uint_as_float(rr[0]) + __uint_as_float(rr[1]);
}

#define TL(i) tl[i] = __builtin_amdgcn_s_memrealtime()
extern "C" __device__ unsigned long long amd_dispatch_id() __asm("llvm.amdgcn.dispatch.id");

template <unsigned N>
__device__ __forceinline__ unsigned rendezvous(const char* w, unsigned tag, unsigned own, int lane, unsigned long long tmo, unsigned& dep) {
    unsigned old = 0, todo = 1u << own;
    u32x2 v;
    if (lane == 0) asm volatile("global_atomic_add %0, %1, %2, off sc0" : "=v"(old) : "v"(w), "v"(1u) : "memory");
    asm volatile("global_load_dwordx2 %0, %1, off sc1" : "=v"(v) : "v"(w) : "memory");
    asm volatile("s_waitcnt vmcnt(0)" : "+v"(old), "+v"(v) :: "memory");
    old = __builtin_amdgcn_readfirstlane(old);
    if ((old & 255u) == N - 1) todo |= (old >> 8) & 255u;
    else {
        const unsigned long long t0 = __builtin_amdgcn_s_memrealtime();
        while ((__builtin_amdgcn_readfirstlane(v.x) & 255u) != N || __builtin_amdgcn_readfirstlane(v.y) != tag) {
            if (__builtin_amdgcn_s_memrealtime() - t0 > tmo) {
                unsigned o2 = 0;
                if (lane == 0) asm volatile("global_atomic_or %0, %1, %2, off sc0" : "=v"(o2) : "v"(w), "v"(256u << own) : "memory");
                asm volatile("s_waitcnt vmcnt(0)" : "+v"(o2) :: "memory");
                if ((__builtin_amdgcn_readfirstlane(o2) & 255u) != N) todo = 0;
                break;
            }
            asm volatile("global_load_dwordx2 %0, %1, off sc1\n\ts_waitcnt vmcnt(0)" : "=v"(v) : "v"(w) : "memory");
        }
    }
    dep = 0;
    if (lane == 0) asm volatile("global_atomic_add %0, %1, %2, off sc0" : "=v"(dep) : "v"(w), "v"(65536u) : "memory");
    return todo;
}
template <unsigned N>
__device__ __forceinline__ void depart(const char* w, unsigned dep, int lane) {
    asm volatile("" : "+v"(dep));
    if (lane == 0 && ((dep >> 16) & 255u) == N - 1)
        asm volatile("global_store_dwordx2 %0, %1, off sc1" :: "v"(w), "v"(u32x2{0u, 0u}) : "memory");
}

#define MFMA16(a, b, c) __builtin_amdgcn_mfma_f32_16x16x32_bf16(a, b, c, 0, 0, 0)
__global__ __launch_bounds__(512, 2) void gqa_kernel(const u16* __restrict__ x, const u16* __restrict__ Kc,
                                                     const u16* __restrict__ Vc, const u16* __restrict__ wq,
                                                     const u16* __restrict__ wo, char* ws, u16* __restrict__ out) {
    extern __shared__ __attribute__((aligned(16))) char lds[];
    unsigned long long tl[13];
    TL(0);
    const unsigned lds0 = (unsigned)(uintptr_t)lds;
    const int tid = threadIdx.x, lane = tid & 63, r32 = lane & 31, hi = lane >> 5;
    const int wid = __builtin_amdgcn_readfirstlane(tid >> 6);
    const int qb = wid & 3, kh = wid >> 2;
    const int bid = blockIdx.x, b = 8 * (bid & 1) + ((bid >> 3) & 7), kvh = 4 * ((bid >> 1) & 3) + (bid >> 6);
    const int head = 2 * kvh + (qb >> 1);
    const int mt = b >> 1, sk = kvh >> 2, nt = 4 * (b & 1) + (kvh & 3);
    const unsigned tag = 0x80000000u | ((unsigned)amd_dispatch_id() & 0xfffffu) |
                         ((((unsigned)((uintptr_t)__builtin_amdgcn_dispatch_ptr() >> 12) * 0x9e3779b1u) >> 21) << 20);
    const char* w1 = ws + CNT_OFF + (mt * 4 + sk) * 128;
    const char* w2 = ws + CNT_OFF + 4096 + mt * 8 * 128;
    const char* wcl = lane == 0 ? w1 : w2 + nt * 128;
    u32x2 clm = {0u, tag};
    if (wid == 1 && lane < 2)
        asm volatile("global_atomic_cmpswap_x2 %0, %1, %2, off sc0\n\ts_nop 1" : "=v"(clm) : "v"(wcl), "v"(u32x4{0u, tag, 0u, 0u}) : "memory");
    const int gi = lane >> 4, vq = (lane & 15) >> 2, vp = lane & 3;

    const unsigned vsrc = (16 * (wid >> 1) + ((lane >> 2) & 7)) * 256 + (wid & 1) * 128 + hi * 64;
    const unsigned voffV = (vsrc ^ (kh * 128)) + vp * 16;
    const unsigned voffK0 = vsrc + ((vp ^ (wid & 2)) << 4), voffK1 = vsrc + ((vp ^ ((wid & 2) | 1)) << 4);
    const char* Kg = (const char*)Kc + (size_t)(b * HKV + kvh) * SKV * DH * 2;
    const char* Vg = (const char*)Vc + (size_t)(b * HKV + kvh) * SKV * DH * 2;
    const unsigned dwo = lds0 + (wid >> 1) * 4096 + (wid & 1) * 1024;
#define KVDMA(voff, gp, dst, OFF) asm volatile("s_mov_b32 m0, %2\n\ts_nop 0\n\tglobal_load_lds_dwordx4 %0, %1 offset:" #OFF " sc1 nt" \
            :: "v"(voff), "s"(gp), "s"(dst) : "memory")
#define VSO(tn) ((int)((0x0200110au >> (8 * ((tn) & 3))) & 255) * 8192)
#define KSO(tn) ((int)((0x0f0c0705u >> (8 * ((tn) & 3))) & 255) * 8192)
#define DMA_V0(tn) KVDMA(voffV, Vg + (tn) * 16384, dwo + VSO(tn), 0)
#define DMA_V1(tn) KVDMA(voffV, Vg + (tn) * 16384, dwo + VSO(tn), 2048)
#define DMA_K0(tn) KVDMA(voffK0, Kg + (tn) * 16384, dwo + KSO(tn), 0)
#define DMA_K1(tn) KVDMA(voffK1, Kg + (tn) * 16384, dwo + KSO(tn), 2048)
#define DMA_V(tn) do { DMA_V0(tn); DMA_V1(tn); } while (0)
#define DMA_K(tn) do { DMA_K0(tn); DMA_K1(tn); } while (0)
    const char* Bw = (const char*)wo + (size_t)(1024 * sk + 32 * wid) * DM * 2;
    unsigned boff[5];
    bf16x8 qf[8];
    {
        int kb = 3 * ((b + kvh) % 3);
        const unsigned xo = (8 * wid + (lane >> 3)) * DM * 2 + (((lane & 7) ^ ((4 * wid + (lane >> 4)) & 7)) << 4);
        const unsigned wo0 = (8 * wid + hi) * NQ * 2 + ((r32 ^ (hi << 2)) << 4);
        const unsigned wo1 = (8 * wid + 2 + hi) * NQ * 2 + ((r32 ^ ((2 + hi) << 2)) << 4);
        const unsigned wo2 = wo0 + 4 * NQ * 2, wo3 = wo1 + 4 * NQ * 2;
        const char* Xs = (const char*)x + (size_t)b * SQ * DM * 2;
        const char* Ws = (const char*)wq + kvh * 512;
#define QDMA(voff, gp, dst) asm volatile("s_mov_b32 m0, %2\n\ts_nop 0\n\tglobal_load_lds_dwordx4 %0, %1" :: "v"(voff), "s"(gp), "s"(dst) : "memory")
#define QS_PIECE(slot, p) do { const unsigned s_ = lds0 + (slot) * 40960 + wid * 4096; const char* w_ = Ws + (size_t)kb * 64 * NQ * 2; \
            if ((p) == 0) QDMA(wo0, w_, s_); \
            if ((p) == 1) QDMA(wo1, w_, s_ + 1024); \
            if ((p) == 2) QDMA(wo2, w_, s_ + 2048); \
            if ((p) == 3) QDMA(wo3, w_, s_ + 3072); \
            if ((p) == 4) { QDMA(xo, Xs + kb * 128, lds0 + (slot) * 40960 + 32768 + wid * 1024); kb = kb == 8 ? 0 : kb + 1; } } while (0)
#define QS_ISSUE(slot) do { QS_PIECE(slot, 0); QS_PIECE(slot, 1); QS_PIECE(slot, 2); QS_PIECE(slot, 3); QS_PIECE(slot, 4); } while (0)
#pragma unroll 1
        for (int j = 0; j < 2; ++j) { QS_ISSUE(j); asm volatile("s_barrier" ::: "memory"); }
#define BOFF(odd) _Pragma("unroll") for (int i = 0; i < 5; ++i) { \
            unsigned ln_ = lane; asm volatile("" : "+v"(ln_)); \
            const unsigned n = 64 * i + ln_, row = (n * 205u) >> 11, c = n - 10 * row + 2 * (odd); \
            boff[i] = row * (DM * 2) + (c < 10 ? c : c - 10) * 16; \
            asm volatile("" : "+v"(boff[i])); }
        BOFF(nt & 1);
        const int pp = ((vp & 1) << 1) | (vp >> 1);
        const unsigned abase = lds0 + (8 * hi + vq) * 512 + (((4 * wid + 2 * (gi & 1) + (pp >> 1)) ^ (vq << 2)) << 4) + 8 * (pp & 1);
        const unsigned xbase = lds0 + 32768 + r32 * 128 + ((hi ^ ((r32 >> 1) & 7)) << 4);
        f32x16 acc0 = {}, acc1 = {};
        s16x4 lo[2][4], h4[2][4]; bf16x8 x0[2][4], x1[2][4];
        asm volatile("s_waitcnt vmcnt(5)\n\ts_barrier" : "+v"(clm) :: "memory");
        QS_ISSUE(2);
        if (wid == 1 && lane < 2 && clm.y != tag && (clm.x | clm.y) != 0u)
            asm volatile("global_atomic_cmpswap_x2 %0, %1, off\n\ts_nop 1" :: "v"(wcl), "v"(u32x4{0u, tag, clm.x, clm.y}) : "memory");
#pragma unroll
        for (int i = 0; i < 4; ++i) {
            lo[0][i] = vtr(abase + i * 8192); h4[0][i] = vtr(abase + i * 8192 + 2048);
            x0[0][i] = ldsr128(xbase ^ (i << 5)); x1[0][i] = ldsr128((xbase ^ (i << 5)) + 4096);
        }
#pragma unroll
        for (int st = 0; st < 9; ++st) {
            if (st == 0) asm volatile("s_waitcnt vmcnt(5)" ::: "memory");
            else if (st < 6) asm volatile("s_waitcnt vmcnt(10)" ::: "memory");
            else if (st == 6) asm volatile("s_waitcnt vmcnt(9)" ::: "memory");
            else if (st == 7) asm volatile("s_waitcnt vmcnt(6)" ::: "memory");
            asm volatile("s_waitcnt lgkmcnt(0)\n\ts_barrier" ::: "memory");
            const unsigned so = ((st + 1) & 3) * 40960;
            const int cu = st & 1, nx = cu ^ 1;
            unsigned aa = abase + so;
            asm volatile("" : "+v"(aa));
            __builtin_amdgcn_sched_barrier(0);
            if (st == 0) QS_ISSUE(3);
#pragma unroll
            for (int i = 0; i < 4; ++i) {
                const bf16x8 a0 = {lo[cu][i][0], lo[cu][i][1], lo[cu][i][2], lo[cu][i][3], h4[cu][i][0], h4[cu][i][1], h4[cu][i][2], h4[cu][i][3]};
                acc0 = MFMA32(a0, x0[cu][i], acc0);
                if (st < 8) { lo[nx][i] = vtr(aa + i * 8192); h4[nx][i] = vtr(aa + i * 8192 + 2048); }
                if (st < 5) QS_PIECE(st & 3, i);
                __builtin_amdgcn_sched_barrier(0);
                acc1 = MFMA32(a0, x1[cu][i], acc1);
                if (st < 8) { unsigned xa = (xbase ^ (i << 5)) + so; asm volatile("" : "+v"(xa)); x0[nx][i] = ldsr128(xa); x1[nx][i] = ldsr128(xa + 4096); }
                if (st < 5 && i == 3) QS_PIECE(st & 3, 4);
                if (st == 5 && i == 3) { DMA_K(0); DMA_K(1); }
                if (st == 6 && i == 3) DMA_V(0);
                if (st == 7 && i == 3) DMA_K(2);
                if (st == 8 && i == 3) DMA_K(3);
                __builtin_amdgcn_sched_barrier(0);
            }
        }
#undef QS_ISSUE
#undef QS_PIECE
#define QW(ACC, qblk) do { _Pragma("unroll") for (int s = 0; s < 2; ++s) { \
            u32x4 w_ = {cvtpk(ACC[8 * s], ACC[8 * s + 1]), cvtpk(ACC[8 * s + 2], ACC[8 * s + 3]), \
                        cvtpk(ACC[8 * s + 4], ACC[8 * s + 5]), cvtpk(ACC[8 * s + 6], ACC[8 * s + 7])}; \
            *(LAS u32x4*)(uintptr_t)(lds0 + 32768 + (2 * (wid >> 2) + (qblk)) * REG + (2 * (wid & 3) + s) * 1024 + lane * 16) = w_; } } while (0)
        QW(acc0, 0); QW(acc1, 1);
#undef QW
        DMA_V(1);
    }
    asm volatile("s_waitcnt vmcnt(8) lgkmcnt(0)\n\ts_barrier" ::: "memory");
#pragma unroll
    for (int f = 0; f < 8; ++f) qf[f] = ldsr128(lds0 + 32768 + qb * REG + f * 1024 + lane * 16);
    TL(1);

    const unsigned kb0 = lds0 + (4 * kh + (r32 >> 3)) * 2048 + (r32 & 7) * 64 + ((hi ^ (r32 >> 3)) << 4);
    const unsigned kb1 = kb0 ^ 32;
    const unsigned vb = lds0 + kh * 8192 + (4 * hi + vq) * 64 + (gi & 1) * 32 + vp * 8;

    float m_run, l_run = 0.f, tmax = 0.f;
    unsigned tprev = (unsigned)__builtin_amdgcn_s_memrealtime() - (unsigned)PACE;
    f32x16 o[4] = {};
    f32x16 sA, sB, negm;

    bf16x8 kf[8];
    {
        bf16x8 k0[8];
        unsigned ka0 = kb0 + KSO(0), ka1 = kb1 + KSO(0);
        asm volatile("" : "+v"(ka0), "+v"(ka1));
#pragma unroll
        for (int d0 = 0; d0 < 8; ++d0) k0[d0] = ldsr128(((d0 & 1) ? ka1 : ka0) + (d0 >> 1) * 512);
#pragma unroll
        for (int d0 = 0; d0 < 8; ++d0) kf[d0] = ldsr128(((d0 & 1) ? ka1 : ka0) + 16384 + (d0 >> 1) * 512);
        __builtin_amdgcn_sched_barrier(0);
        asm volatile("s_waitcnt lgkmcnt(0)\n\ts_barrier" ::: "memory");
        sA = f32x16{};
#pragma unroll
        for (int d0 = 0; d0 < 8; ++d0) sA = MFMA32(k0[d0], qf[d0], sA);
        __builtin_amdgcn_sched_barrier(0);
        DMA_V(2); DMA_K(4);
        m_run = sA[0];
#pragma unroll
        for (int r = 1; r < 16; ++r) m_run = __builtin_fmaxf(m_run, sA[r]);
        m_run = swap_max(m_run);
#pragma unroll
        for (int r = 0; r < 16; ++r) { sA[r] -= m_run; negm[r] = -m_run; }
    }
#define EXP1(r) do { sc[r] = __builtin_amdgcn_exp2f(fmul1(sc[r], C2)); } while (0)
#define SUM1(k) do { ps0 += sc[2 * (k)]; ps1 += sc[2 * (k) + 1]; pw[k] = cvtpk(sc[2 * (k)], sc[2 * (k) + 1]); asm volatile("" : "+v"(pw[k]), "+v"(ps0), "+v"(ps1)); } while (0)
#define STEP(t, SC_, SN_, TAIL, LAST) do { \
        f32x16& sc = SC_; f32x16& sn = SN_; \
        s16x4 vlo[8], vhi[8]; \
        if (!TAIL || (t) < 12) asm volatile("s_waitcnt vmcnt(8)" ::: "memory"); \
        else if ((t) < 14) asm volatile("s_waitcnt vmcnt(7)" ::: "memory"); \
        else if ((t) == 14) asm volatile("s_waitcnt vmcnt(9)" ::: "memory"); \
        else asm volatile("s_waitcnt vmcnt(10)" ::: "memory"); \
        if (!(TAIL && (t) >= PT0)) while ((unsigned)__builtin_amdgcn_s_memrealtime() - tprev < (unsigned)PACE) __builtin_amdgcn_s_sleep(1); \
        asm volatile("s_waitcnt lgkmcnt(0)\n\ts_barrier" ::: "memory"); \
        if (!(TAIL && (t) + 1 >= PT0)) tprev = (unsigned)__builtin_amdgcn_s_memrealtime(); \
        if (__builtin_expect(__any(tmax > RTHR / C2), 0)) { \
            const float dl = __builtin_fmaxf(tmax, 0.f); \
            const float alpha = __builtin_amdgcn_exp2f(-dl * C2); \
            m_run += dl; l_run *= alpha; \
            _Pragma("unroll") for (int r = 0; r < 16; ++r) { sc[r] -= dl; negm[r] = -m_run; } \
            _Pragma("unroll") for (int dt = 0; dt < 4; ++dt) \
                _Pragma("unroll") for (int r = 0; r < 16; ++r) o[dt][r] *= alpha; \
        } \
        const unsigned vwr = dwo + vso, kwr = dwo + kso;                         \
        vso = VSO(t); kso = KSO((t) + 2); \
        const unsigned va = vb + vso; \
        const unsigned ka0 = kb0 + kso, ka1 = kb1 + kso;                         \
        unsigned pw[8]; \
        float ps0 = 0.f, ps1 = 0.f, t2; \
        __builtin_amdgcn_sched_barrier(0); \
          \
        _Pragma("unroll") for (int i = 0; i < 8; ++i) { \
            if (!LAST) sn = MFMA32(kf[i], qf[i], i == 0 ? negm : sn); \
            const int ss = i >> 2, dt = i & 3; \
            vlo[2 * dt + ss] = vtr(va + ss * 4096 + dt * 512); \
            vhi[2 * dt + ss] = vtr(va + ss * 4096 + 2048 + dt * 512); \
            if (i < 4) { EXP1(2 * i); EXP1(2 * i + 1); } else EXP1(4 + i); \
            if (i >= 1 && i <= 4) SUM1(i - 1); \
            if (i == 6) SUM1(4); \
            if (!LAST) asm volatile("" : "+v"(sn)); \
            asm volatile("" : "+v"(sc)); \
            if (i == XS0 && (!TAIL || (t) < 13)) KVDMA(voffV, Vg + ((t) + 3) * 16384, vwr, 0); \
            if (i == XS1 && (!TAIL || (t) < 13)) KVDMA(voffV, Vg + ((t) + 3) * 16384, vwr, 2048); \
            if (i == XS0 && TAIL && (t) == 13) BDMA(Bs, 0, 2); \
            if (i == XS1 && TAIL && (t) == 13) BDMA(Bs, 0, 3); \
            if (i == XS0 && TAIL && (t) == 14) BDMA(Bs, 1, 1); \
            if (i == XS1 && TAIL && (t) == 14) BDMA(Bs, 1, 2); \
            __builtin_amdgcn_sched_barrier(0); \
        } \
          \
        _Pragma("unroll") for (int i = 0; i < 8; ++i) { \
            const int ss = i >> 2, dt = i & 3; \
            const s16x4 lo = vlo[2 * dt + ss], hi4 = vhi[2 * dt + ss]; \
            const bf16x8 vf = {lo[0], lo[1], lo[2], lo[3], hi4[0], hi4[1], hi4[2], hi4[3]}; \
            const bf16x8 pf = __builtin_bit_cast(bf16x8, u32x4{pw[4 * ss], pw[4 * ss + 1], pw[4 * ss + 2], pw[4 * ss + 3]}); \
            o[dt] = MFMA32(vf, pf, o[dt]); \
            if (i < 4 && !LAST) { kf[2 * i] = ldsr128(ka0 + i * 512); kf[2 * i + 1] = ldsr128(ka1 + i * 512); } \
            if (i < 2) { EXP1(12 + 2 * i); EXP1(13 + 2 * i); asm volatile("" : "+v"(sc)); } \
            if (i < 3) SUM1(5 + i); \
            if (i == 3 && LAST) l_run += ps0 + ps1; \
            if (i == 3 && !LAST) { l_run += ps0 + ps1; asm volatile("" : "+v"(sn), "+v"(l_run)); \
                          tmax = max3f(sn[0], sn[1], sn[2]); t2 = max3f(sn[3], sn[4], sn[5]); asm volatile("" : "+v"(tmax), "+v"(t2)); } \
            if (i == 4 && !LAST) { tmax = max3f(tmax, sn[6], sn[7]); t2 = max3f(t2, sn[8], sn[9]); tmax = max3f(tmax, sn[10], sn[11]); asm volatile("" : "+v"(tmax), "+v"(t2)); } \
            if (i == 5 && !LAST) { t2 = max3f(t2, sn[12], sn[13]); tmax = max3f(tmax, sn[14], sn[15]); tmax = max3f(tmax, t2, t2); asm volatile("" : "+v"(tmax)); } \
            if (i == 6 && !LAST) { tmax = swap_max3(tmax); asm volatile("" : "+v"(tmax)); } \
            if (i == YS0 && (!TAIL || (t) < 11)) KVDMA(voffK0, Kg + ((t) + 5) * 16384, kwr, 0); \
            if (i == YS1 && (!TAIL || (t) < 11)) KVDMA(voffK1, Kg + ((t) + 5) * 16384, kwr, 2048); \
            if (i == YS0 && TAIL && (t) == 11) BWARM(); \
            if (i == YS0 && TAIL && (t) == 12) BDMA(Bs, 0, 0); \
            if (i == YS1 && TAIL && (t) == 12) BDMA(Bs, 0, 1); \
            if (i == YS0 && TAIL && (t) == 13) BDMA(Bs, 0, 4); \
            if (i == YS1 && TAIL && (t) == 13) BDMA(Bs, 1, 0); \
            if (i == YS0 && TAIL && (t) == 14) BDMA(Bs, 1, 3); \
            if (i == YS1 && TAIL && (t) == 14) BDMA(Bs, 1, 4); \
            __builtin_amdgcn_sched_barrier(0); \
        } } while (0)
    const char* Bs = Bw + 288 * (nt >> 1) + 128 * (nt & 1);
#define BDMA(bs, c, i) QDMA(boff[i], (bs) + (c) * (256 * DM * 2), lds0 + (((c) + 1) & 3) * REG + wid * 5120 + (i) * 1024)
#define BWARM() do { unsigned ln_ = lane; asm volatile("" : "+v"(ln_)); \
        if (ln_ < 18) asm volatile("s_mov_b32 m0, %2\n\ts_nop 0\n\tglobal_load_lds_dword %0, %1" :: "v"(ln_ * 128), \
                     "s"((const char*)wo + (size_t)(1024 * sk + 512) * DM * 2 + ((bid >> 3) * 8 + wid) * 2304), "s"(lds0 + 32768 + wid * 256) : "memory"); } while (0)
    unsigned vso = VSO(3), kso = KSO(1);
#pragma unroll 1
    for (int t = 0; t < 10; t += 2) { STEP(t, sA, sB, false, false); STEP(t + 1, sB, sA, false, false); }
    STEP(10, sA, sB, true, false); STEP(11, sB, sA, true, false); STEP(12, sA, sB, true, false); STEP(13, sB, sA, true, false);
    STEP(NT - 2, sA, sB, true, false);
    const unsigned xb = lds0 + (kh ? 0 : 3 * REG) + qb * 8704;
    {
        f32x16& sc = sB;
        asm volatile("s_waitcnt vmcnt(10) lgkmcnt(0)\n\ts_barrier" ::: "memory");
        if (__builtin_expect(__any(tmax > RTHR / C2), 0)) {
            const float dl = __builtin_fmaxf(tmax, 0.f);
            const float alpha = __builtin_amdgcn_exp2f(-dl * C2);
            m_run += dl; l_run *= alpha;
#pragma unroll
            for (int r = 0; r < 16; ++r) sc[r] -= dl;
#pragma unroll
            for (int dt = 0; dt < 4; ++dt)
#pragma unroll
                for (int r = 0; r < 16; ++r) o[dt][r] *= alpha;
        }
        const unsigned va = vb + VSO(NT - 1);
        s16x4 vlo[8], vhi[8];
        unsigned pw[8];
        float ps0 = 0.f, ps1 = 0.f;
#pragma unroll
        for (int i = 0; i < 8; ++i) {
            const int ss = (i >> 1) & 1, dt = (i & 1) | ((~i >> 1) & 2);
            vlo[i] = vtr(va + ss * 4096 + dt * 512);
            vhi[i] = vtr(va + ss * 4096 + 2048 + dt * 512);
            EXP1(2 * i); EXP1(2 * i + 1);
            if (i) SUM1(i - 1);
        }
        SUM1(7);
        l_run += ps0 + ps1;
        l_run = swap_add(l_run);
        asm volatile("s_waitcnt lgkmcnt(0)\n\ts_barrier" ::: "memory");
        *(LAS float*)(uintptr_t)(xb + 8192 + lane * 4) = m_run;
        *(LAS float*)(uintptr_t)(xb + 8192 + 256 + lane * 4) = l_run;
        __builtin_amdgcn_sched_barrier(0);
#pragma unroll
        for (int i = 0; i < 8; ++i) {
            const int ss = (i >> 1) & 1, dt = (i & 1) | ((~i >> 1) & 2);
            const bf16x8 vf = {vlo[i][0], vlo[i][1], vlo[i][2], vlo[i][3], vhi[i][0], vhi[i][1], vhi[i][2], vhi[i][3]};
            const bf16x8 pf = __builtin_bit_cast(bf16x8, u32x4{pw[4 * ss], pw[4 * ss + 1], pw[4 * ss + 2], pw[4 * ss + 3]});
            o[dt] = MFMA32(vf, pf, o[dt]);
            if (i >= 4) {
#pragma unroll
                for (int j = 0; j < 2; ++j) {
                    const int r4 = 2 * (i & 1) + j, sd = 2 + ((i >> 1) & 1);
                    *(LAS f32x4*)(uintptr_t)(xb + (4 * (sd - 2) + r4) * 1024 + lane * 16) =
                        f32x4{o[sd][4 * r4], o[sd][4 * r4 + 1], o[sd][4 * r4 + 2], o[sd][4 * r4 + 3]};
                }
            }
            __builtin_amdgcn_sched_barrier(0);
        }
    }
#undef STEP
#undef EXP1
#undef SUM1
    TL(2);
    {
        f32x16 &keep0 = o[0], &keep1 = o[1];
        asm volatile("s_waitcnt lgkmcnt(0)\n\ts_barrier" ::: "memory");
        const unsigned pb = lds0 + (kh ? 3 * REG : 0) + qb * 8704;
        const float m2 = *(const LAS float*)(uintptr_t)(pb + 8192 + lane * 4);
        const float l2 = *(const LAS float*)(uintptr_t)(pb + 8192 + 256 + lane * 4);
        const float mm = __builtin_fmaxf(m_run, m2);
        const float a1 = __builtin_amdgcn_exp2f((m_run - mm) * C2), a2 = __builtin_amdgcn_exp2f((m2 - mm) * C2);
        const float inv = 1.0f / (l_run * a1 + l2 * a2);
        const float w1 = a1 * inv, w2 = a2 * inv;
        const int row = b * SQ + 32 * (qb & 1) + r32;
        char* ob = ws + ((size_t)((row >> 4) * 128 + head * 4 + 2 * kh) * 64 + hi * 16 + (row & 15)) * 16;
#define OSTORE(KEEP, i) do { u32x2 w_[4]; \
            _Pragma("unroll") for (int r4 = 0; r4 < 4; ++r4) { \
                const f32x4 v = *(const LAS f32x4*)(uintptr_t)(pb + (4 * (i) + r4) * 1024 + lane * 16); \
                const float e0 = KEEP[4 * r4] * w1 + v[0] * w2, e1 = KEEP[4 * r4 + 1] * w1 + v[1] * w2; \
                const float e2 = KEEP[4 * r4 + 2] * w1 + v[2] * w2, e3 = KEEP[4 * r4 + 3] * w1 + v[3] * w2; \
                w_[r4] = u32x2{cvtpk(e0, e1), cvtpk(e2, e3)}; } \
            _Pragma("unroll") for (int g = 0; g < 2; ++g) { \
                u32x4 o4 = {w_[g].x, w_[g].y, w_[g + 2].x, w_[g + 2].y}; \
                asm volatile("global_store_dwordx4 %0, %1, off sc1\n\ts_nop 1" :: "v"(ob + (i) * 1024 + g * 512), "v"(o4) : "memory"); } } while (0)
        OSTORE(keep0, 0); OSTORE(keep1, 1);
#undef OSTORE
    }
    TL(3);
    const char* Ag = ws + (size_t)((8 * mt + 2 * qb) * 128 + 32 * sk + kh) * 1024;
    unsigned ao0 = lane * 16 + (nt & 1) * 131072, ao1 = ao0 ^ 131072;
    unsigned bb[4];
#pragma unroll
    for (int c = 0; c < 4; ++c) { bb[c] = lds0 + ((c + 1) & 3) * REG + (32 * kh + 4 * gi + vq) * 160 + 8 * vp; asm volatile("" : "+v"(bb[c])); }
    asm volatile("" : "+v"(ao0), "+v"(ao1), "+s"(Ag));
    asm volatile("s_waitcnt vmcnt(0) lgkmcnt(0)\n\ts_barrier" ::: "memory");
    TL(4);
    unsigned dep1 = 0;
    if (wid == 0) {
        const unsigned td = rendezvous<8>(w1, tag, nt, lane, TMO, dep1);
        if (lane == 0) *(LAS unsigned*)(uintptr_t)(lds0 + 4096) = td;
    } else {
#pragma unroll
        for (int i = 0; i < 10; ++i) BDMA(Bs, 2 + i / 5, i % 5);
        if (wid == 1) {
#pragma unroll
            for (int i = 0; i < 9; ++i)
                QDMA(boff[i % 5], Bs - 32 * DM * 2 + (2 + i / 5) * (256 * DM * 2), lds0 + ((3 + i / 5) & 3) * REG + (i % 5) * 1024);
        }
    }
    asm volatile("s_waitcnt lgkmcnt(0)\n\ts_barrier" ::: "memory");
    unsigned todo = __builtin_amdgcn_readfirstlane(*(const LAS unsigned*)(uintptr_t)(lds0 + 4096));
    TL(5);
    float* part = (float*)(ws + PART_OFF);
    bool left = false;
    while (todo) {
        const int ntc = ((todo >> nt) & 1u) ? nt : __builtin_ctz(todo);
        todo &= ~(1u << ntc);
        const char* Bc = Bw + 288 * (ntc >> 1) + 128 * (ntc & 1);
        if (ntc != nt) {
            asm volatile("s_waitcnt vmcnt(0) lgkmcnt(0)\n\ts_barrier" ::: "memory");
            BOFF(ntc & 1);
            ao0 = lane * 16 + (ntc & 1) * 131072; ao1 = ao0 ^ 131072;
#pragma unroll 1
            for (int c = 0; c < 4; ++c) {
#pragma unroll
                for (int i = 0; i < 5; ++i) BDMA(Bc, c, i);
            }
            asm volatile("s_waitcnt vmcnt(0)\n\ts_barrier" ::: "memory");
        }
        u32x4 an[32];
#define ALD(s_) do { const char* ag = Ag; asm volatile("" : "+s"(ag)); ag += ((s_) >> 1) * 4096; \
            if ((s_) & 1) { \
                asm volatile("global_load_dwordx4 %0, %1, %2 offset:2048 sc1" : "=v"(an[2 * (s_)]) : "v"(ao0), "s"(ag) : "memory"); \
                asm volatile("global_load_dwordx4 %0, %1, %2 offset:2048 sc1" : "=v"(an[2 * (s_) + 1]) : "v"(ao1), "s"(ag) : "memory"); \
            } else { \
                asm volatile("global_load_dwordx4 %0, %1, %2 sc1" : "=v"(an[2 * (s_)]) : "v"(ao0), "s"(ag) : "memory"); \
                asm volatile("global_load_dwordx4 %0, %1, %2 sc1" : "=v"(an[2 * (s_) + 1]) : "v"(ao1), "s"(ag) : "memory"); } } while (0)
#pragma unroll
        for (int s2 = 0; s2 < AHEAD; ++s2) ALD(s2);
        f32x4 acc[2][5];
#pragma unroll
        for (int ct = 0; ct < 5; ++ct) { acc[0][ct] = f32x4{0.f, 0.f, 0.f, 0.f}; acc[1][ct] = f32x4{0.f, 0.f, 0.f, 0.f}; }
        s16x4 blo[2][5], bhi[2][5];
#pragma unroll
        for (int ct = 0; ct < 5; ++ct) { blo[0][ct] = vtr(bb[0] + 32 * ct); bhi[0][ct] = vtr(bb[0] + 32 * ct + 2560); }
#pragma unroll
        for (int s = 0; s < 16; ++s) {
            asm volatile("s_waitcnt vmcnt(%2)" : "+v"(an[2 * s]), "+v"(an[2 * s + 1]) : "n"(s + AHEAD <= 16 ? 2 * AHEAD - 2 : 30 - 2 * s) : "memory");
            if (s == 0) { TL(6); }
            if (s == 7) asm volatile("s_barrier" ::: "memory");
            if (s == 7 && wid == 0) BDMA(Bc, 3, 4);
            if (s == 11) asm volatile("s_barrier" ::: "memory");
            __builtin_amdgcn_sched_barrier(0);
#pragma unroll
            for (int ct = 0; ct < 5; ++ct) {
                if (s < 15) {
                    const unsigned ad = bb[((s + 1) >> 2) & 3] + ((s + 1) & 3) * 10240 + 32 * ct;
                    blo[(s + 1) & 1][ct] = vtr(ad); bhi[(s + 1) & 1][ct] = vtr(ad + 2560);
                }
                const s16x4 lo = blo[s & 1][ct], h4 = bhi[s & 1][ct];
                const bf16x8 bf = {lo[0], lo[1], lo[2], lo[3], h4[0], h4[1], h4[2], h4[3]};
                acc[0][ct] = MFMA16(bf, __builtin_bit_cast(bf16x8, an[2 * s]), acc[0][ct]);
                if (ct < 4) acc[1][ct] = MFMA16(bf, __builtin_bit_cast(bf16x8, an[2 * s + 1]), acc[1][ct]);
                if (ct == ALDPOS && s + AHEAD < 16) ALD(s + AHEAD);
                __builtin_amdgcn_sched_barrier(0);
            }
        }
        TL(7);
        float* pp = part + ((((size_t)(sk * 8 + mt) * 8 + ntc) * 4 + qb) * 9 + 4 * kh) * 256 + lane * 4;
#define PRED(K, NK, NS) do { \
            _Pragma("unroll") for (int ct = 0; ct < NS; ++ct) \
                *(LAS f32x4*)(uintptr_t)(lds0 + REG + (wid * 5 + ct) * 1024 + lane * 16) = acc[1 - K][ct]; \
            asm volatile("s_waitcnt lgkmcnt(0)\n\ts_barrier" ::: "memory"); \
            f32x4 v_[NK]; \
            _Pragma("unroll") for (int ct = 0; ct < NK; ++ct) v_[ct] = *(const LAS f32x4*)(uintptr_t)(lds0 + REG + ((wid ^ 4) * 5 + ct) * 1024 + lane * 16); \
            _Pragma("unroll") for (int ct = 0; ct < NK; ++ct) { \
                const f32x4 r_ = acc[K][ct] + v_[ct]; \
                asm volatile("global_store_dwordx4 %0, %1, off sc1\n\ts_nop 1" :: "v"(pp + (ct < 4 ? ct : 8) * 256), "v"(r_) : "memory"); } } while (0)
        if (kh == 0) PRED(0, 5, 4); else PRED(1, 4, 5);
#undef PRED
        if (wid == 0 && !left) { left = true; depart<8>(w1, dep1, lane); }
        TL(8);
        asm volatile("s_waitcnt vmcnt(0)\n\ts_barrier" ::: "memory");
        TL(9);
        unsigned dep2 = 0;
        if (wid == 0) {
            const unsigned td = rendezvous<4>(w2 + ntc * 128, tag, sk, lane, TMO2, dep2);
            if (lane == 0) *(LAS unsigned*)(uintptr_t)(lds0 + 2 * REG) = td;
        }
        asm volatile("s_waitcnt lgkmcnt(0)\n\ts_barrier" ::: "memory");
        unsigned qs = __builtin_amdgcn_readfirstlane(*(const LAS unsigned*)(uintptr_t)(lds0 + 2 * REG));
        TL(10);
        bool left2 = false;
        while (qs) {
            const int q = __builtin_ctz(qs);
            qs &= qs - 1;
            if (tid < 288) {
                const float* p = part + (((size_t)mt * 8 + ntc) * 4 + q) * 2304 + tid * 4;
                f32x4 v[2][4];
#pragma unroll
                for (int r = 0; r < 2; ++r)
#pragma unroll
                    for (int s = 0; s < 4; ++s)
                        asm volatile("global_load_dwordx4 %0, %1, off sc1" : "=v"(v[r][s]) : "v"(p + (size_t)s * 64 * 9216 + r * 1152) : "memory");
                asm volatile("s_waitcnt vmcnt(0)" : "+v"(v[0][0]), "+v"(v[0][1]), "+v"(v[0][2]), "+v"(v[0][3]),
                                                    "+v"(v[1][0]), "+v"(v[1][1]), "+v"(v[1][2]), "+v"(v[1][3]) :: "memory");
#pragma unroll
                for (int r = 0; r < 2; ++r) {
                    const f32x4 t = ((v[r][0] + v[r][1]) + v[r][2]) + v[r][3];
                    u32x2 w = {cvtpk(t[0], t[1]), cvtpk(t[2], t[3])};
                    const int pc = tid + 288 * r, tl_ = pc >> 6;
                    const int rt = (ntc ^ (tl_ >> 2)) & 1;
                    const int col = 144 * (ntc >> 1) + (tl_ == 8 ? 64 : 80 * (ntc & 1) + 16 * (tl_ & 3)) + ((pc >> 2) & 12);
                    *(u32x2*)(out + (size_t)(16 * (8 * mt + 2 * q + rt) + (pc & 15)) * DM + col) = w;
                }
            }
            if (wid == 0 && !left2) { left2 = true; depart<4>(w2 + ntc * 128, dep2, lane); }
        }
        if (wid == 0 && !left2) {
            asm volatile("s_waitcnt vmcnt(0)" ::: "memory");
            depart<4>(w2 + ntc * 128, dep2, lane);
        }
    }
    if (wid == 0 && !left) {
        asm volatile("s_waitcnt vmcnt(0)" ::: "memory");
        depart<8>(w1, dep1, lane);
    }
    TL(11);
    if (tid == 0) { asm volatile("global_store_dwordx2 %0, %1, off sc1" :: "v"(ws + CNT_OFF + 65536 + bid * 16), "v"(tl[PROBE_A]) : "memory");
                    asm volatile("global_store_dwordx2 %0, %1, off sc1" :: "v"(ws + CNT_OFF + 65536 + bid * 16 + 8), "v"(tl[PROBE_B]) : "memory"); }
    if (bid == 77 && wid == 0) {
        while (__builtin_amdgcn_s_memrealtime() - tl[11] < 400ull) __builtin_amdgcn_s_sleep(2);
        unsigned mn_ = ~0u, mx_ = 0;
        for (int i_ = 0; i_ < 4; ++i_) { u32x4 v_; asm volatile("global_load_dwordx4 %0, %1, off sc1\n\ts_waitcnt vmcnt(0)" : "=v"(v_) : "v"(ws + CNT_OFF + 65536 + (i_ * 64 + lane) * 16) : "memory");
            const unsigned a_ = v_.x - (unsigned)tl[0] + 100000u, b_ = v_.z - (unsigned)tl[0] + 100000u;
            mn_ = a_ < mn_ ? a_ : mn_; mx_ = b_ > mx_ ? b_ : mx_; }
        for (int o_ = 1; o_ < 64; o_ <<= 1) { unsigned a_ = __shfl_xor(mn_, o_), b_ = __shfl_xor(mx_, o_); mn_ = a_ < mn_ ? a_ : mn_; mx_ = b_ > mx_ ? b_ : mx_; }
        int d_ = ((int)__builtin_amdgcn_readfirstlane(mx_ - mn_) - PROBE_OFF) * 10; d_ = d_ < 0 ? 0 : d_ > 100000 ? 100000 : d_;
        while (__builtin_amdgcn_s_memrealtime() - tl[11] < 800ull + (unsigned)d_) __builtin_amdgcn_s_sleep(2);
    }
}

extern "C" void kernel_launch(void* const* d_in, const int* in_sizes, int n_in, void* d_out, int out_size,
                              void* d_ws, size_t ws_size, hipStream_t stream) {
    (void)hipFuncSetAttribute((const void*)gqa_kernel, hipFuncAttributeMaxDynamicSharedMemorySize, LDS_BYTES);
    gqa_kernel<<<NB * HKV, 512, LDS_BYTES, stream>>>((const u16*)d_in[0], (const u16*)d_in[1], (const u16*)d_in[2],
                                                     (const u16*)d_in[3], (const u16*)d_in[4], (char*)d_ws, (u16*)d_out);
}
```

```cpp
#include <hip/hip_runtime.h>
#include <hip/hip_bf16.h>
#include <stdint.h>

typedef short bf16x8 __attribute__((ext_vector_type(8)));
typedef short s16x4 __attribute__((ext_vector_type(4)));
typedef float f32x16 __attribute__((ext_vector_type(16)));
typedef float f32x4 __attribute__((ext_vector_type(4)));
typedef unsigned u32x4 __attribute__((ext_vector_type(4)));
typedef unsigned u32x2 __attribute__((ext_vector_type(2)));
typedef unsigned short u16;
#define LAS __attribute__((address_space(3)))

constexpr int NB = 16, SQ = 64, DM = 576, HQ = 32, HKV = 16, SKV = 1024, DH = 128;
constexpr int NQ = HQ * DH;
constexpr int NT = SKV / 64;
constexpr int REG = 40960;
constexpr int LDS_BYTES = 4 * REG;
constexpr size_t PART_OFF = (size_t)NB * SQ * NQ * 2;
constexpr size_t CNT_OFF = (size_t)20 << 20;
constexpr int AHEAD = 2;
constexpr unsigned long long TMO = 5000ull;
constexpr int XS0 = 5, XS1 = 7, YS0 = 5, YS1 = 7;
constexpr int PACE = 108;
constexpr float RTHR = 8.0f;
constexpr float C2 = 0.088388347648318447f * 1.4426950408889634f;

#define MFMA32(a, b, c) __builtin_amdgcn_mfma_f32_32x32x16_bf16(a, b, c, 0, 0, 0)

typedef __bf16 bf16x2_t __attribute__((ext_vector_type(2)));
typedef float f32x2 __attribute__((ext_vector_type(2)));
__device__ __forceinline__ unsigned cvtpk(float lo, float hi) {
    f32x2 v = {lo, hi};
    return __builtin_bit_cast(unsigned, __builtin_convertvector(v, bf16x2_t));
}
__device__ __forceinline__ s16x4 vtr(unsigned a) {
    return __builtin_amdgcn_ds_read_tr16_b64_v4i16((LAS s16x4*)(uintptr_t)a);
}
__device__ __forceinline__ bf16x8 ldsr128(unsigned a) { return *(const LAS bf16x8*)(uintptr_t)a; }
__device__ __forceinline__ float swap_max(float v) {
    auto rr = __builtin_amdgcn_permlane32_swap(__float_as_uint(v), __float_as_uint(v), false, false);
    return __builtin_fmaxf(__uint_as_float(rr[0]), __uint_as_float(rr[1]));
}
__device__ __forceinline__ float max3f(float a, float b, float c) { float r; asm("v_max3_f32 %0, %1, %2, %3" : "=v"(r) : "v"(a), "v"(b), "v"(c)); return r; }
__device__ __forceinline__ float fmul1(float a, float b) { float r; asm("v_mul_f32_e32 %0, %1, %2" : "=v"(r) : "s"(b), "v"(a)); return r; }
__device__ __forceinline__ float swap_max3(float v) {
    auto rr = __builtin_amdgcn_permlane32_swap(__float_as_uint(v), __float_as_uint(v), false, false);
    return max3f(__uint_as_float(rr[0]), __uint_as_float(rr[1]), __uint_as_float(rr[1]));
}
__device__ __forceinline__ float swap_add(float v) {
    auto rr = __builtin_amdgcn_permlane32_swap(__float_as_uint(v), __float_as_uint(v), false, false);
    return __uint_as_float(rr[0]) + __uint_as_float(rr[1]);
}

extern "C" __device__ unsigned long long amd_dispatch_id() __asm("llvm.amdgcn.dispatch.id");

template <unsigned N>
__device__ __forceinline__ unsigned rendezvous(const char* w, unsigned tag, unsigned own, int lane, unsigned long long tmo, unsigned& dep) {
    unsigned old = 0, todo = 1u << own;
    u32x2 v;
    if (lane == 0) asm volatile("global_atomic_add %0, %1, %2, off sc0" : "=v"(old) : "v"(w), "v"(1u) : "memory");
    asm volatile("global_load_dwordx2 %0, %1, off sc1" : "=v"(v) : "v"(w) : "memory");
    asm volatile("s_waitcnt vmcnt(0)" : "+v"(old), "+v"(v) :: "memory");
    old = __builtin_amdgcn_readfirstlane(old);
    if ((old & 255u) == N - 1) todo |= (old >> 8) & 255u;
    else {
        const unsigned long long t0 = __builtin_amdgcn_s_memrealtime();
        while ((__builtin_amdgcn_readfirstlane(v.x) & 255u) != N || __builtin_amdgcn_readfirstlane(v.y) != tag) {
            if (__builtin_amdgcn_s_memrealtime() - t0 > tmo) {
                unsigned o2 = 0;
                if (lane == 0) asm volatile("global_atomic_or %0, %1, %2, off sc0" : "=v"(o2) : "v"(w), "v"(256u << own) : "memory");
                asm volatile("s_waitcnt vmcnt(0)" : "+v"(o2) :: "memory");
                if ((__builtin_amdgcn_readfirstlane(o2) & 255u) != N) todo = 0;
                break;
            }
            asm volatile("global_load_dwordx2 %0, %1, off sc1\n\ts_waitcnt vmcnt(0)" : "=v"(v) : "v"(w) : "memory");
        }
    }
    dep = 0;
    if (lane == 0) asm volatile("global_atomic_add %0, %1, %2, off sc0" : "=v"(dep) : "v"(w), "v"(65536u) : "memory");
    return todo;
}
template <unsigned N>
__device__ __forceinline__ void depart(const char* w, unsigned dep, int lane) {
    asm volatile("" : "+v"(dep));
    if (lane == 0 && ((dep >> 16) & 255u) == N - 1)
        asm volatile("global_store_dwordx2 %0, %1, off sc1" :: "v"(w), "v"(u32x2{0u, 0u}) : "memory");
}

#define MFMA16(a, b, c) __builtin_amdgcn_mfma_f32_16x16x32_bf16(a, b, c, 0, 0, 0)
__global__ __launch_bounds__(512, 2) void gqa_kernel(const u16* __restrict__ x, const u16* __restrict__ Kc,
                                                     const u16* __restrict__ Vc, const u16* __restrict__ wq,
                                                     const u16* __restrict__ wo, char* ws, u16* __restrict__ out) {
    extern __shared__ __attribute__((aligned(16))) char lds[];
    const unsigned lds0 = (unsigned)(uintptr_t)lds;
    const int tid = threadIdx.x, lane = tid & 63, r32 = lane & 31, hi = lane >> 5;
    const int wid = __builtin_amdgcn_readfirstlane(tid >> 6);
    const int qb = wid & 3, kh = wid >> 2;
    const int bid = blockIdx.x, b = 8 * (bid & 1) + ((bid >> 3) & 7), kvh = 4 * ((bid >> 1) & 3) + (bid >> 6);
    const int head = 2 * kvh + (qb >> 1);
    const int mt = b >> 1, sk = kvh >> 2, nt = 4 * (b & 1) + (kvh & 3);
    const unsigned tag = 0x80000000u | ((unsigned)amd_dispatch_id() & 0xfffffu) |
                         ((((unsigned)((uintptr_t)__builtin_amdgcn_dispatch_ptr() >> 12) * 0x9e3779b1u) >> 21) << 20);
    const char* w1 = ws + CNT_OFF + (mt * 4 + sk) * 128;
    const char* w2 = ws + CNT_OFF + 4096 + mt * 8 * 128;
    const char* wcl = lane == 0 ? w1 : w2 + nt * 128;
    u32x2 clm = {0u, tag};
    if (wid == 1 && lane < 2)
        asm volatile("global_atomic_cmpswap_x2 %0, %1, %2, off sc0\n\ts_nop 1" : "=v"(clm) : "v"(wcl), "v"(u32x4{0u, tag, 0u, 0u}) : "memory");
    const int gi = lane >> 4, vq = (lane & 15) >> 2, vp = lane & 3;

    const unsigned vsrc = (16 * (wid >> 1) + ((lane >> 2) & 7)) * 256 + (wid & 1) * 128 + hi * 64;
    const unsigned voffV = (vsrc ^ (kh * 128)) + vp * 16;
    const unsigned voffK0 = vsrc + ((vp ^ (wid & 2)) << 4), voffK1 = vsrc + ((vp ^ ((wid & 2) | 1)) << 4);
    const char* Kg = (const char*)Kc + (size_t)(b * HKV + kvh) * SKV * DH * 2;
    const char* Vg = (const char*)Vc + (size_t)(b * HKV + kvh) * SKV * DH * 2;
    const unsigned dwo = lds0 + (wid >> 1) * 4096 + (wid & 1) * 1024;
#define KVDMA(voff, gp, dst, OFF) asm volatile("s_mov_b32 m0, %2\n\ts_nop 0\n\tglobal_load_lds_dwordx4 %0, %1 offset:" #OFF " sc1 nt" \
            :: "v"(voff), "s"(gp), "s"(dst) : "memory")
#define VSO(tn) ((int)((0x0200110au >> (8 * ((tn) & 3))) & 255) * 8192)
#define KSO(tn) ((int)((0x0f0c0705u >> (8 * ((tn) & 3))) & 255) * 8192)
#define DMA_V0(tn) KVDMA(voffV, Vg + (tn) * 16384, dwo + VSO(tn), 0)
#define DMA_V1(tn) KVDMA(voffV, Vg + (tn) * 16384, dwo + VSO(tn), 2048)
#define DMA_K0(tn) KVDMA(voffK0, Kg + (tn) * 16384, dwo + KSO(tn), 0)
#define DMA_K1(tn) KVDMA(voffK1, Kg + (tn) * 16384, dwo + KSO(tn), 2048)
#define DMA_V(tn) do { DMA_V0(tn); DMA_V1(tn); } while (0)
#define DMA_K(tn) do { DMA_K0(tn); DMA_K1(tn); } while (0)
    const char* Bw = (const char*)wo + (size_t)(1024 * sk + 32 * wid) * DM * 2;
    unsigned boff[5];
    bf16x8 qf[8];
    {
        int kb = 3 * ((b + kvh) % 3);
        const unsigned xo = (8 * wid + (lane >> 3)) * DM * 2 + (((lane & 7) ^ ((4 * wid + (lane >> 4)) & 7)) << 4);
        const unsigned wo0 = (8 * wid + hi) * NQ * 2 + ((r32 ^ (hi << 2)) << 4);
        const unsigned wo1 = (8 * wid + 2 + hi) * NQ * 2 + ((r32 ^ ((2 + hi) << 2)) << 4);
        const unsigned wo2 = wo0 + 4 * NQ * 2, wo3 = wo1 + 4 * NQ * 2;
        const char* Xs = (const char*)x + (size_t)b * SQ * DM * 2;
        const char* Ws = (const char*)wq + kvh * 512;
#define QDMA(voff, gp, dst) asm volatile("s_mov_b32 m0, %2\n\ts_nop 0\n\tglobal_load_lds_dwordx4 %0, %1" :: "v"(voff), "s"(gp), "s"(dst) : "memory")
#define QS_PIECE(slot, p) do { const unsigned s_ = lds0 + (slot) * 40960 + wid * 4096; const char* w_ = Ws + (size_t)kb * 64 * NQ * 2; \
            if ((p) == 0) QDMA(wo0, w_, s_); \
            if ((p) == 1) QDMA(wo1, w_, s_ + 1024); \
            if ((p) == 2) QDMA(wo2, w_, s_ + 2048); \
            if ((p) == 3) QDMA(wo3, w_, s_ + 3072); \
            if ((p) == 4) { QDMA(xo, Xs + kb * 128, lds0 + (slot) * 40960 + 32768 + wid * 1024); kb = kb == 8 ? 0 : kb + 1; } } while (0)
#define QS_ISSUE(slot) do { QS_PIECE(slot, 0); QS_PIECE(slot, 1); QS_PIECE(slot, 2); QS_PIECE(slot, 3); QS_PIECE(slot, 4); } while (0)
#pragma unroll 1
        for (int j = 0; j < 2; ++j) { QS_ISSUE(j); asm volatile("s_barrier" ::: "memory"); }
#define BOFF(odd) _Pragma("unroll") for (int i = 0; i < 5; ++i) { \
            unsigned ln_ = lane; asm volatile("" : "+v"(ln_)); \
            const unsigned n = 64 * i + ln_, row = (n * 205u) >> 11, c = n - 10 * row + 2 * (odd); \
            boff[i] = row * (DM * 2) + (c < 10 ? c : c - 10) * 16; \
            asm volatile("" : "+v"(boff[i])); }
        BOFF(nt & 1);
        const int pp = ((vp & 1) << 1) | (vp >> 1);
        const unsigned abase = lds0 + (8 * hi + vq) * 512 + (((4 * wid + 2 * (gi & 1) + (pp >> 1)) ^ (vq << 2)) << 4) + 8 * (pp & 1);
        const unsigned xbase = lds0 + 32768 + r32 * 128 + ((hi ^ ((r32 >> 1) & 7)) << 4);
        f32x16 acc0 = {}, acc1 = {};
        s16x4 lo[2][4], h4[2][4]; bf16x8 x0[2][4], x1[2][4];
        asm volatile("s_waitcnt vmcnt(5)\n\ts_barrier" : "+v"(clm) :: "memory");
        QS_ISSUE(2);
        if (wid == 1 && lane < 2 && clm.y != tag && (clm.x | clm.y) != 0u)
            asm volatile("global_atomic_cmpswap_x2 %0, %1, off\n\ts_nop 1" :: "v"(wcl), "v"(u32x4{0u, tag, clm.x, clm.y}) : "memory");
#pragma unroll
        for (int i = 0; i < 4; ++i) {
            lo[0][i] = vtr(abase + i * 8192); h4[0][i] = vtr(abase + i * 8192 + 2048);
            x0[0][i] = ldsr128(xbase ^ (i << 5)); x1[0][i] = ldsr128((xbase ^ (i << 5)) + 4096);
        }
#pragma unroll
        for (int st = 0; st < 9; ++st) {
            if (st == 0) asm volatile("s_waitcnt vmcnt(5)" ::: "memory");
            else if (st < 6) asm volatile("s_waitcnt vmcnt(10)" ::: "memory");
            else if (st == 6) asm volatile("s_waitcnt vmcnt(9)" ::: "memory");
            else if (st == 7) asm volatile("s_waitcnt vmcnt(6)" ::: "memory");
            asm volatile("s_waitcnt lgkmcnt(0)\n\ts_barrier" ::: "memory");
            const unsigned so = ((st + 1) & 3) * 40960;
            const int cu = st & 1, nx = cu ^ 1;
            unsigned aa = abase + so;
            asm volatile("" : "+v"(aa));
            __builtin_amdgcn_sched_barrier(0);
            if (st == 0) QS_ISSUE(3);
#pragma unroll
            for (int i = 0; i < 4; ++i) {
                const bf16x8 a0 = {lo[cu][i][0], lo[cu][i][1], lo[cu][i][2], lo[cu][i][3], h4[cu][i][0], h4[cu][i][1], h4[cu][i][2], h4[cu][i][3]};
                acc0 = MFMA32(a0, x0[cu][i], acc0);
                if (st < 8) { lo[nx][i] = vtr(aa + i * 8192); h4[nx][i] = vtr(aa + i * 8192 + 2048); }
                if (st < 5) QS_PIECE(st & 3, i);
                __builtin_amdgcn_sched_barrier(0);
                acc1 = MFMA32(a0, x1[cu][i], acc1);
                if (st < 8) { unsigned xa = (xbase ^ (i << 5)) + so; asm volatile("" : "+v"(xa)); x0[nx][i] = ldsr128(xa); x1[nx][i] = ldsr128(xa + 4096); }
                if (st < 5 && i == 3) QS_PIECE(st & 3, 4);
                if (st == 5 && i == 3) { DMA_K(0); DMA_K(1); }
                if (st == 6 && i == 3) DMA_V(0);
                if (st == 7 && i == 3) DMA_K(2);
                if (st == 8 && i == 3) DMA_K(3);
                __builtin_amdgcn_sched_barrier(0);
            }
        }
#undef QS_ISSUE
#undef QS_PIECE
#define QW(ACC, qblk) do { _Pragma("unroll") for (int s = 0; s < 2; ++s) { \
            u32x4 w_ = {cvtpk(ACC[8 * s], ACC[8 * s + 1]), cvtpk(ACC[8 * s + 2], ACC[8 * s + 3]), \
                        cvtpk(ACC[8 * s + 4], ACC[8 * s + 5]), cvtpk(ACC[8 * s + 6], ACC[8 * s + 7])}; \
            *(LAS u32x4*)(uintptr_t)(lds0 + 32768 + (2 * (wid >> 2) + (qblk)) * REG + (2 * (wid & 3) + s) * 1024 + lane * 16) = w_; } } while (0)
        QW(acc0, 0); QW(acc1, 1);
#undef QW
        DMA_V(1);
    }
    asm volatile("s_waitcnt vmcnt(8) lgkmcnt(0)\n\ts_barrier" ::: "memory");
#pragma unroll
    for (int f = 0; f < 8; ++f) qf[f] = ldsr128(lds0 + 32768 + qb * REG + f * 1024 + lane * 16);

    const unsigned kb0 = lds0 + (4 * kh + (r32 >> 3)) * 2048 + (r32 & 7) * 64 + ((hi ^ (r32 >> 3)) << 4);
    const unsigned kb1 = kb0 ^ 32;
    const unsigned vb = lds0 + kh * 8192 + (4 * hi + vq) * 64 + (gi & 1) * 32 + vp * 8;

    float m_run, l_run = 0.f, tmax = 0.f;
    unsigned tprev = (unsigned)__builtin_amdgcn_s_memrealtime() - (unsigned)PACE;
    f32x16 o[4] = {};
    f32x16 sA, sB, negm;

    bf16x8 kf[8];
    {
        bf16x8 k0[8];
        unsigned ka0 = kb0 + KSO(0), ka1 = kb1 + KSO(0);
        asm volatile("" : "+v"(ka0), "+v"(ka1));
#pragma unroll
        for (int d0 = 0; d0 < 8; ++d0) k0[d0] = ldsr128(((d0 & 1) ? ka1 : ka0) + (d0 >> 1) * 512);
#pragma unroll
        for (int d0 = 0; d0 < 8; ++d0) kf[d0] = ldsr128(((d0 & 1) ? ka1 : ka0) + 16384 + (d0 >> 1) * 512);
        __builtin_amdgcn_sched_barrier(0);
        asm volatile("s_waitcnt lgkmcnt(0)\n\ts_barrier" ::: "memory");
        sA = f32x16{};
#pragma unroll
        for (int d0 = 0; d0 < 8; ++d0) sA = MFMA32(k0[d0], qf[d0], sA);
        __builtin_amdgcn_sched_barrier(0);
        DMA_V(2); DMA_K(4);
        m_run = sA[0];
#pragma unroll
        for (int r = 1; r < 16; ++r) m_run = __builtin_fmaxf(m_run, sA[r]);
        m_run = swap_max(m_run);
#pragma unroll
        for (int r = 0; r < 16; ++r) { sA[r] -= m_run; negm[r] = -m_run; }
    }
#define EXP1(r) do { sc[r] = __builtin_amdgcn_exp2f(fmul1(sc[r], C2)); } while (0)
#define SUM1(k) do { ps0 += sc[2 * (k)]; ps1 += sc[2 * (k) + 1]; pw[k] = cvtpk(sc[2 * (k)], sc[2 * (k) + 1]); asm volatile("" : "+v"(pw[k]), "+v"(ps0), "+v"(ps1)); } while (0)
#define STEP(t, SC_, SN_, TAIL) do { \
        f32x16& sc = SC_; f32x16& sn = SN_; \
        s16x4 vlo[8], vhi[8]; \
        if (!TAIL || (t) < 12) asm volatile("s_waitcnt vmcnt(8)" ::: "memory"); \
        else if ((t) < 14) asm volatile("s_waitcnt vmcnt(7)" ::: "memory"); \
        else asm volatile("s_waitcnt vmcnt(9)" ::: "memory"); \
        if (!(TAIL && (t) >= (NT - 2))) while ((unsigned)__builtin_amdgcn_s_memrealtime() - tprev < (unsigned)PACE) __builtin_amdgcn_s_sleep(1); \
        asm volatile("s_waitcnt lgkmcnt(0)\n\ts_barrier" ::: "memory"); \
        if (!(TAIL && (t) + 1 >= (NT - 2))) tprev = (unsigned)__builtin_amdgcn_s_memrealtime(); \
        if (__builtin_expect(__any(tmax > RTHR / C2), 0)) { \
            const float dl = __builtin_fmaxf(tmax, 0.f); \
            const float alpha = __builtin_amdgcn_exp2f(-dl * C2); \
            m_run += dl; l_run *= alpha; \
            _Pragma("unroll") for (int r = 0; r < 16; ++r) { sc[r] -= dl; negm[r] = -m_run; } \
            _Pragma("unroll") for (int dt = 0; dt < 4; ++dt) \
                _Pragma("unroll") for (int r = 0; r < 16; ++r) o[dt][r] *= alpha; \
        } \
        const unsigned vwr = dwo + vso, kwr = dwo + kso;                         \
        vso = VSO(t); kso = KSO((t) + 2); \
        const unsigned va = vb + vso; \
        const unsigned ka0 = kb0 + kso, ka1 = kb1 + kso;                         \
        unsigned pw[8]; \
        float ps0 = 0.f, ps1 = 0.f, t2; \
        __builtin_amdgcn_sched_barrier(0); \
          \
        _Pragma("unroll") for (int i = 0; i < 8; ++i) { \
            sn = MFMA32(kf[i], qf[i], i == 0 ? negm : sn); \
            const int ss = i >> 2, dt = i & 3; \
            vlo[2 * dt + ss] = vtr(va + ss * 4096 + dt * 512); \
            vhi[2 * dt + ss] = vtr(va + ss * 4096 + 2048 + dt * 512); \
            if (i < 4) { EXP1(2 * i); EXP1(2 * i + 1); } else EXP1(4 + i); \
            if (i >= 1 && i <= 4) SUM1(i - 1); \
            if (i == 6) SUM1(4); \
            asm volatile("" : "+v"(sn)); \
            asm volatile("" : "+v"(sc)); \
            if (i == XS0 && (!TAIL || (t) < 13)) KVDMA(voffV, Vg + ((t) + 3) * 16384, vwr, 0); \
            if (i == XS1 && (!TAIL || (t) < 13)) KVDMA(voffV, Vg + ((t) + 3) * 16384, vwr, 2048); \
            if (i == XS0 && TAIL && (t) == 13) BDMA(Bs, 0, 2); \
            if (i == XS1 && TAIL && (t) == 13) BDMA(Bs, 0, 3); \
            if (i == XS0 && TAIL && (t) == 14) BDMA(Bs, 1, 1); \
            if (i == XS1 && TAIL && (t) == 14) BDMA(Bs, 1, 2); \
            __builtin_amdgcn_sched_barrier(0); \
        } \
          \
        _Pragma("unroll") for (int i = 0; i < 8; ++i) { \
            const int ss = i >> 2, dt = i & 3; \
            const s16x4 lo = vlo[2 * dt + ss], hi4 = vhi[2 * dt + ss]; \
            const bf16x8 vf = {lo[0], lo[1], lo[2], lo[3], hi4[0], hi4[1], hi4[2], hi4[3]}; \
            const bf16x8 pf = __builtin_bit_cast(bf16x8, u32x4{pw[4 * ss], pw[4 * ss + 1], pw[4 * ss + 2], pw[4 * ss + 3]}); \
            o[dt] = MFMA32(vf, pf, o[dt]); \
            if (i < 4) { kf[2 * i] = ldsr128(ka0 + i * 512); kf[2 * i + 1] = ldsr128(ka1 + i * 512); } \
            if (i < 2) { EXP1(12 + 2 * i); EXP1(13 + 2 * i); asm volatile("" : "+v"(sc)); } \
            if (i < 3) SUM1(5 + i); \
            if (i == 3) { l_run += ps0 + ps1; asm volatile("" : "+v"(sn), "+v"(l_run)); \
                          tmax = max3f(sn[0], sn[1], sn[2]); t2 = max3f(sn[3], sn[4], sn[5]); asm volatile("" : "+v"(tmax), "+v"(t2)); } \
            if (i == 4) { tmax = max3f(tmax, sn[6], sn[7]); t2 = max3f(t2, sn[8], sn[9]); tmax = max3f(tmax, sn[10], sn[11]); asm volatile("" : "+v"(tmax), "+v"(t2)); } \
            if (i == 5) { t2 = max3f(t2, sn[12], sn[13]); tmax = max3f(tmax, sn[14], sn[15]); tmax = max3f(tmax, t2, t2); asm volatile("" : "+v"(tmax)); } \
            if (i == 6) { tmax = swap_max3(tmax); asm volatile("" : "+v"(tmax)); } \
            if (i == YS0 && (!TAIL || (t) < 11)) KVDMA(voffK0, Kg + ((t) + 5) * 16384, kwr, 0); \
            if (i == YS1 && (!TAIL || (t) < 11)) KVDMA(voffK1, Kg + ((t) + 5) * 16384, kwr, 2048); \
            if (i == YS0 && TAIL && (t) == 11) BWARM(); \
            if (i == YS0 && TAIL && (t) == 12) BDMA(Bs, 0, 0); \
            if (i == YS1 && TAIL && (t) == 12) BDMA(Bs, 0, 1); \
            if (i == YS0 && TAIL && (t) == 13) BDMA(Bs, 0, 4); \
            if (i == YS1 && TAIL && (t) == 13) BDMA(Bs, 1, 0); \
            if (i == YS0 && TAIL && (t) == 14) BDMA(Bs, 1, 3); \
            if (i == YS1 && TAIL && (t) == 14) BDMA(Bs, 1, 4); \
            __builtin_amdgcn_sched_barrier(0); \
        } } while (0)
    const char* Bs = Bw + 288 * (nt >> 1) + 128 * (nt & 1);
#define BDMA(bs, c, i) QDMA(boff[i], (bs) + (c) * (256 * DM * 2), lds0 + (((c) + 1) & 3) * REG + wid * 5120 + (i) * 1024)
#define BWARM() do { unsigned ln_ = lane; asm volatile("" : "+v"(ln_)); \
        if (ln_ < 18) asm volatile("s_mov_b32 m0, %2\n\ts_nop 0\n\tglobal_load_lds_dword %0, %1" :: "v"(ln_ * 128), \
                     "s"((const char*)wo + (size_t)(1024 * sk + 512) * DM * 2 + ((bid >> 3) * 8 + wid) * 2304), "s"(lds0 + 32768 + wid * 256) : "memory"); } while (0)
    unsigned vso = VSO(3), kso = KSO(1);
#pragma unroll 1
    for (int t = 0; t < 10; t += 2) { STEP(t, sA, sB, false); STEP(t + 1, sB, sA, false); }
    STEP(10, sA, sB, true); STEP(11, sB, sA, true); STEP(12, sA, sB, true); STEP(13, sB, sA, true);
    STEP(NT - 2, sA, sB, true);
    const unsigned xb = lds0 + (kh ? 0 : 3 * REG) + qb * 8704;
    {
        f32x16& sc = sB;
        asm volatile("s_waitcnt vmcnt(10) lgkmcnt(0)\n\ts_barrier" ::: "memory");
        if (__builtin_expect(__any(tmax > RTHR / C2), 0)) {
            const float dl = __builtin_fmaxf(tmax, 0.f);
            const float alpha = __builtin_amdgcn_exp2f(-dl * C2);
            m_run += dl; l_run *= alpha;
#pragma unroll
            for (int r = 0; r < 16; ++r) sc[r] -= dl;
#pragma unroll
            for (int dt = 0; dt < 4; ++dt)
#pragma unroll
                for (int r = 0; r < 16; ++r) o[dt][r] *= alpha;
        }
        const unsigned va = vb + VSO(NT - 1);
        s16x4 vlo[8], vhi[8];
        unsigned pw[8];
        float ps0 = 0.f, ps1 = 0.f;
#pragma unroll
        for (int i = 0; i < 8; ++i) {
            const int ss = (i >> 1) & 1, dt = (i & 1) | ((~i >> 1) & 2);
            vlo[i] = vtr(va + ss * 4096 + dt * 512);
            vhi[i] = vtr(va + ss * 4096 + 2048 + dt * 512);
            EXP1(2 * i); EXP1(2 * i + 1);
            if (i) SUM1(i - 1);
        }
        SUM1(7);
        l_run += ps0 + ps1;
        l_run = swap_add(l_run);
        asm volatile("s_waitcnt lgkmcnt(0)\n\ts_barrier" ::: "memory");
        *(LAS float*)(uintptr_t)(xb + 8192 + lane * 4) = m_run;
        *(LAS float*)(uintptr_t)(xb + 8192 + 256 + lane * 4) = l_run;
        __builtin_amdgcn_sched_barrier(0);
#pragma unroll
        for (int i = 0; i < 8; ++i) {
            const int ss = (i >> 1) & 1, dt = (i & 1) | ((~i >> 1) & 2);
            const bf16x8 vf = {vlo[i][0], vlo[i][1], vlo[i][2], vlo[i][3], vhi[i][0], vhi[i][1], vhi[i][2], vhi[i][3]};
            const bf16x8 pf = __builtin_bit_cast(bf16x8, u32x4{pw[4 * ss], pw[4 * ss + 1], pw[4 * ss + 2], pw[4 * ss + 3]});
            o[dt] = MFMA32(vf, pf, o[dt]);
            if (i >= 4) {
#pragma unroll
                for (int j = 0; j < 2; ++j) {
                    const int r4 = 2 * (i & 1) + j, sd = 2 + ((i >> 1) & 1);
                    *(LAS f32x4*)(uintptr_t)(xb + (4 * (sd - 2) + r4) * 1024 + lane * 16) =
                        f32x4{o[sd][4 * r4], o[sd][4 * r4 + 1], o[sd][4 * r4 + 2], o[sd][4 * r4 + 3]};
                }
            }
            __builtin_amdgcn_sched_barrier(0);
        }
    }
#undef STEP
#undef EXP1
#undef SUM1
    {
        f32x16 &keep0 = o[0], &keep1 = o[1];
        asm volatile("s_waitcnt lgkmcnt(0)\n\ts_barrier" ::: "memory");
        const unsigned pb = lds0 + (kh ? 3 * REG : 0) + qb * 8704;
        const float m2 = *(const LAS float*)(uintptr_t)(pb + 8192 + lane * 4);
        const float l2 = *(const LAS float*)(uintptr_t)(pb + 8192 + 256 + lane * 4);
        const float mm = __builtin_fmaxf(m_run, m2);
        const float a1 = __builtin_amdgcn_exp2f((m_run - mm) * C2), a2 = __builtin_amdgcn_exp2f((m2 - mm) * C2);
        const float inv = 1.0f / (l_run * a1 + l2 * a2);
        const float w1 = a1 * inv, w2 = a2 * inv;
        const int row = b * SQ + 32 * (qb & 1) + r32;
        char* ob = ws + ((size_t)((row >> 4) * 128 + head * 4 + 2 * kh) * 64 + hi * 16 + (row & 15)) * 16;
#define OSTORE(KEEP, i) do { u32x2 w_[4]; \
            _Pragma("unroll") for (int r4 = 0; r4 < 4; ++r4) { \
                const f32x4 v = *(const LAS f32x4*)(uintptr_t)(pb + (4 * (i) + r4) * 1024 + lane * 16); \
                const float e0 = KEEP[4 * r4] * w1 + v[0] * w2, e1 = KEEP[4 * r4 + 1] * w1 + v[1] * w2; \
                const float e2 = KEEP[4 * r4 + 2] * w1 + v[2] * w2, e3 = KEEP[4 * r4 + 3] * w1 + v[3] * w2; \
                w_[r4] = u32x2{cvtpk(e0, e1), cvtpk(e2, e3)}; } \
            _Pragma("unroll") for (int g = 0; g < 2; ++g) { \
                u32x4 o4 = {w_[g].x, w_[g].y, w_[g + 2].x, w_[g + 2].y}; \
                asm volatile("global_store_dwordx4 %0, %1, off sc1\n\ts_nop 1" :: "v"(ob + (i) * 1024 + g * 512), "v"(o4) : "memory"); } } while (0)
        OSTORE(keep0, 0); OSTORE(keep1, 1);
#undef OSTORE
    }
    const char* Ag = ws + (size_t)((8 * mt + 2 * qb) * 128 + 32 * sk + kh) * 1024;
    unsigned ao0 = lane * 16 + (nt & 1) * 131072, ao1 = ao0 ^ 131072;
    unsigned bb[4];
#pragma unroll
    for (int c = 0; c < 4; ++c) { bb[c] = lds0 + ((c + 1) & 3) * REG + (32 * kh + 4 * gi + vq) * 160 + 8 * vp; asm volatile("" : "+v"(bb[c])); }
    asm volatile("" : "+v"(ao0), "+v"(ao1), "+s"(Ag));
    asm volatile("s_waitcnt vmcnt(0) lgkmcnt(0)\n\ts_barrier" ::: "memory");
    unsigned dep1 = 0;
    if (wid == 0) {
        const unsigned td = rendezvous<8>(w1, tag, nt, lane, TMO, dep1);
        if (lane == 0) *(LAS unsigned*)(uintptr_t)(lds0 + 4096) = td;
    } else {
#pragma unroll
        for (int i = 0; i < 10; ++i) BDMA(Bs, 2 + i / 5, i % 5);
        if (wid == 1) {
#pragma unroll
            for (int i = 0; i < 9; ++i)
                QDMA(boff[i % 5], Bs - 32 * DM * 2 + (2 + i / 5) * (256 * DM * 2), lds0 + ((3 + i / 5) & 3) * REG + (i % 5) * 1024);
        }
    }
    asm volatile("s_waitcnt lgkmcnt(0)\n\ts_barrier" ::: "memory");
    unsigned todo = __builtin_amdgcn_readfirstlane(*(const LAS unsigned*)(uintptr_t)(lds0 + 4096));
    float* part = (float*)(ws + PART_OFF);
    bool left = false;
    while (todo) {
        const int ntc = ((todo >> nt) & 1u) ? nt : __builtin_ctz(todo);
        todo &= ~(1u << ntc);
        const char* Bc = Bw + 288 * (ntc >> 1) + 128 * (ntc & 1);
        if (ntc != nt) {
            asm volatile("s_waitcnt vmcnt(0) lgkmcnt(0)\n\ts_barrier" ::: "memory");
            BOFF(ntc & 1);
            ao0 = lane * 16 + (ntc & 1) * 131072; ao1 = ao0 ^ 131072;
#pragma unroll 1
            for (int c = 0; c < 4; ++c) {
#pragma unroll
                for (int i = 0; i < 5; ++i) BDMA(Bc, c, i);
            }
            asm volatile("s_waitcnt vmcnt(0)\n\ts_barrier" ::: "memory");
        }
        u32x4 an[32];
#define ALD(s_) do { const char* ag = Ag; asm volatile("" : "+s"(ag)); ag += ((s_) >> 1) * 4096; \
            if ((s_) & 1) { \
                asm volatile("global_load_dwordx4 %0, %1, %2 offset:2048 sc1" : "=v"(an[2 * (s_)]) : "v"(ao0), "s"(ag) : "memory"); \
                asm volatile("global_load_dwordx4 %0, %1, %2 offset:2048 sc1" : "=v"(an[2 * (s_) + 1]) : "v"(ao1), "s"(ag) : "memory"); \
            } else { \
                asm volatile("global_load_dwordx4 %0, %1, %2 sc1" : "=v"(an[2 * (s_)]) : "v"(ao0), "s"(ag) : "memory"); \
                asm volatile("global_load_dwordx4 %0, %1, %2 sc1" : "=v"(an[2 * (s_) + 1]) : "v"(ao1), "s"(ag) : "memory"); } } while (0)
#pragma unroll
        for (int s2 = 0; s2 < AHEAD; ++s2) ALD(s2);
        f32x4 acc[2][5];
#pragma unroll
        for (int ct = 0; ct < 5; ++ct) { acc[0][ct] = f32x4{0.f, 0.f, 0.f, 0.f}; acc[1][ct] = f32x4{0.f, 0.f, 0.f, 0.f}; }
        s16x4 blo[2][5], bhi[2][5];
#pragma unroll
        for (int ct = 0; ct < 5; ++ct) { blo[0][ct] = vtr(bb[0] + 32 * ct); bhi[0][ct] = vtr(bb[0] + 32 * ct + 2560); }
#pragma unroll
        for (int s = 0; s < 16; ++s) {
            asm volatile("s_waitcnt vmcnt(%2)" : "+v"(an[2 * s]), "+v"(an[2 * s + 1]) : "n"(s + AHEAD <= 16 ? 2 * AHEAD - 2 : 30 - 2 * s) : "memory");
            if (s == 7) asm volatile("s_barrier" ::: "memory");
            if (s == 7 && wid == 0) BDMA(Bc, 3, 4);
            if (s == 11) asm volatile("s_barrier" ::: "memory");
            __builtin_amdgcn_sched_barrier(0);
#pragma unroll
            for (int ct = 0; ct < 5; ++ct) {
                if (s < 15) {
                    const unsigned ad = bb[((s + 1) >> 2) & 3] + ((s + 1) & 3) * 10240 + 32 * ct;
                    blo[(s + 1) & 1][ct] = vtr(ad); bhi[(s + 1) & 1][ct] = vtr(ad + 2560);
                }
                const s16x4 lo = blo[s & 1][ct], h4 = bhi[s & 1][ct];
                const bf16x8 bf = {lo[0], lo[1], lo[2], lo[3], h4[0], h4[1], h4[2], h4[3]};
                acc[0][ct] = MFMA16(bf, __builtin_bit_cast(bf16x8, an[2 * s]), acc[0][ct]);
                if (ct < 4) acc[1][ct] = MFMA16(bf, __builtin_bit_cast(bf16x8, an[2 * s + 1]), acc[1][ct]);
                if (ct == 3 && s + AHEAD < 16) ALD(s + AHEAD);
                __builtin_amdgcn_sched_barrier(0);
            }
        }
        float* pp = part + ((((size_t)(sk * 8 + mt) * 8 + ntc) * 4 + qb) * 9 + 4 * kh) * 256 + lane * 4;
#define PRED(K, NK, NS) do { \
            _Pragma("unroll") for (int ct = 0; ct < NS; ++ct) \
                *(LAS f32x4*)(uintptr_t)(lds0 + REG + (wid * 5 + ct) * 1024 + lane * 16) = acc[1 - K][ct]; \
            asm volatile("s_waitcnt lgkmcnt(0)\n\ts_barrier" ::: "memory"); \
            f32x4 v_[NK]; \
            _Pragma("unroll") for (int ct = 0; ct < NK; ++ct) v_[ct] = *(const LAS f32x4*)(uintptr_t)(lds0 + REG + ((wid ^ 4) * 5 + ct) * 1024 + lane * 16); \
            _Pragma("unroll") for (int ct = 0; ct < NK; ++ct) { \
                const f32x4 r_ = acc[K][ct] + v_[ct]; \
                asm volatile("global_store_dwordx4 %0, %1, off sc1\n\ts_nop 1" :: "v"(pp + (ct < 4 ? ct : 8) * 256), "v"(r_) : "memory"); } } while (0)
        if (kh == 0) PRED(0, 5, 4); else PRED(1, 4, 5);
#undef PRED
        if (wid == 0 && !left) { left = true; depart<8>(w1, dep1, lane); }
        asm volatile("s_waitcnt vmcnt(0)\n\ts_barrier" ::: "memory");
        unsigned dep2 = 0;
        if (wid == 0) {
            const unsigned td = rendezvous<4>(w2 + ntc * 128, tag, sk, lane, TMO, dep2);
            if (lane == 0) *(LAS unsigned*)(uintptr_t)(lds0 + 2 * REG) = td;
        }
        asm volatile("s_waitcnt lgkmcnt(0)\n\ts_barrier" ::: "memory");
        unsigned qs = __builtin_amdgcn_readfirstlane(*(const LAS unsigned*)(uintptr_t)(lds0 + 2 * REG));
        bool left2 = false;
        while (qs) {
            const int q = __builtin_ctz(qs);
            qs &= qs - 1;
            if (tid < 288) {
                const float* p = part + (((size_t)mt * 8 + ntc) * 4 + q) * 2304 + tid * 4;
                f32x4 v[2][4];
#pragma unroll
                for (int r = 0; r < 2; ++r)
#pragma unroll
                    for (int s = 0; s < 4; ++s)
                        asm volatile("global_load_dwordx4 %0, %1, off sc1" : "=v"(v[r][s]) : "v"(p + (size_t)s * 64 * 9216 + r * 1152) : "memory");
                asm volatile("s_waitcnt vmcnt(0)" : "+v"(v[0][0]), "+v"(v[0][1]), "+v"(v[0][2]), "+v"(v[0][3]),
                                                    "+v"(v[1][0]), "+v"(v[1][1]), "+v"(v[1][2]), "+v"(v[1][3]) :: "memory");
#pragma unroll
                for (int r = 0; r < 2; ++r) {
                    const f32x4 t = ((v[r][0] + v[r][1]) + v[r][2]) + v[r][3];
                    u32x2 w = {cvtpk(t[0], t[1]), cvtpk(t[2], t[3])};
                    const int pc = tid + 288 * r, tl_ = pc >> 6;
                    const int rt = (ntc ^ (tl_ >> 2)) & 1;
                    const int col = 144 * (ntc >> 1) + (tl_ == 8 ? 64 : 80 * (ntc & 1) + 16 * (tl_ & 3)) + ((pc >> 2) & 12);
                    *(u32x2*)(out + (size_t)(16 * (8 * mt + 2 * q + rt) + (pc & 15)) * DM + col) = w;
                }
            }
            if (wid == 0 && !left2) { left2 = true; depart<4>(w2 + ntc * 128, dep2, lane); }
        }
        if (wid == 0 && !left2) {
            asm volatile("s_waitcnt vmcnt(0)" ::: "memory");
            depart<4>(w2 + ntc * 128, dep2, lane);
        }
    }
    if (wid == 0 && !left) {
        asm volatile("s_waitcnt vmcnt(0)" ::: "memory");
        depart<8>(w1, dep1, lane);
    }
}

extern "C" void kernel_launch(void* const* d_in, const int* in_sizes, int n_in, void* d_out, int out_size,
                              void* d_ws, size_t ws_size, hipStream_t stream) {
    (void)hipFuncSetAttribute((const void*)gqa_kernel, hipFuncAttributeMaxDynamicSharedMemorySize, LDS_BYTES);
    gqa_kernel<<<NB * HKV, 512, LDS_BYTES, stream>>>((const u16*)d_in[0], (const u16*)d_in[1], (const u16*)d_in[2],
                                                     (const u16*)d_in[3], (const u16*)d_in[4], (char*)d_ws, (u16*)d_out);
}
```
